```python
import torch

def _softmax(x, axis=-1):
    return torch.softmax(x, dim=axis)

def _gelu(x):
    return torch.nn.functional.gelu(x, approximate='tanh')

def _silu(x):
    return torch.nn.functional.silu(x)

def _sigmoid(x):
    return torch.sigmoid(x)

def _layernorm(x, axis=-1, eps=1e-05):
    m = x.mean(dim=axis, keepdim=True)
    v = x.var(dim=axis, keepdim=True, unbiased=False)
    return (x - m) / torch.sqrt(v + eps)

def _scatter_add(tgt, idx, src, axis=0):
    return tgt.clone().index_add_(axis, idx, src)

def _segment_sum(src, seg, n):
    out = torch.zeros((n,) + src.shape[1:], dtype=src.dtype)
    return out.index_add_(0, seg, src)

def _pad(x, pw):
    flat = tuple((v for pair in reversed(pw) for v in pair))
    return torch.nn.functional.pad(x, flat)
kernel_name = 'recurrent_scan__memory_aug_recurrent_mem_tr__dense_activation_mish__prompt'

def setup_inputs():

    _w = lambda a: (lambda t: t.to(torch.bfloat16) if t.is_floating_point() else t)(torch.as_tensor(a))

    def _A_setup():
        B, S, Dm = (1, 2048, 640)
        H, Dh = (32, 128)
        M, seg = (8, 32)
        seg = min(seg, S)
        S = S // seg * seg
        return {'x': _w((lambda *s: torch.randn(*s, dtype=torch.bfloat16) * 0.05)(B, S, Dm)), 'mem0': _w((lambda *s: torch.randn(*s, dtype=torch.bfloat16) * 0.8)(B, M, Dm)), 'Wq': _w((lambda *s: torch.randn(*s, dtype=torch.bfloat16) * 0.8)(Dm, H, Dh) * Dm ** (-0.5)), 'Wk': _w((lambda *s: torch.randn(*s, dtype=torch.bfloat16) * 0.8)(Dm, H, Dh) * Dm ** (-0.5)), 'Wv': _w((lambda *s: torch.randn(*s, dtype=torch.bfloat16) * 0.8)(Dm, H, Dh) * Dm ** (-0.5)), 'Wo': _w((lambda *s: torch.randn(*s, dtype=torch.bfloat16) * 0.8)(H, Dh, Dm) * (H * Dh) ** (-0.5)), 'seg_len': seg}
    _A_in = _A_setup()

    def _B_setup():
        B, S, D, F = (1, 2048, 640, 1728)
        return {'x': _w((lambda *s: torch.randn(*s, dtype=torch.bfloat16) * 0.05)(B, S, D)), 'w1': _w((lambda *s: torch.randn(*s, dtype=torch.bfloat16) * 0.8)(D, F) * D ** (-0.5)), 'b1': _w((lambda *s: torch.randn(*s, dtype=torch.bfloat16) * 0.4)(F)), 'w2': _w((lambda *s: torch.randn(*s, dtype=torch.bfloat16) * 0.2)(F, D) * F ** (-0.5))}
    _B_in = _B_setup()
    return {'A_x': _A_in['x'], 'A_mem0': _A_in['mem0'], 'A_Wq': _A_in['Wq'], 'A_Wk': _A_in['Wk'], 'A_Wv': _A_in['Wv'], 'A_Wo': _A_in['Wo'], 'A_seg_len': _A_in['seg_len'], 'B_x': _B_in['x'], 'B_w1': _B_in['w1'], 'B_b1': _B_in['b1'], 'B_w2': _B_in['w2']}

def reference(A_x, A_mem0, A_Wq, A_Wk, A_Wv, A_Wo, A_seg_len, B_x, B_w1, B_b1, B_w2):

    def _A_ref(x, mem0, Wq, Wk, Wv, Wo, seg_len):
        B, S, Dm = x.shape
        H, Dh = (Wq.shape[1], Wq.shape[2])
        M = mem0.shape[1]
        scale = Dh ** (-0.5)
        n_seg = S // seg_len
        L = M + seg_len
        idx = torch.arange(L)
        mask = (idx[:, None] < M) | (idx[:, None] >= idx[None, :])
        mem = mem0
        outs = []
        for s in range(n_seg):
            xs = x[:, s * seg_len:(s + 1) * seg_len, :].clone()
            h = torch.concatenate([mem, xs], axis=1)
            q = torch.einsum('btd,dhe->bhte', h, Wq)
            k = torch.einsum('btd,dhe->bhte', h, Wk)
            v = torch.einsum('btd,dhe->bhte', h, Wv)
            scores = torch.einsum('bhqe,bhke->bhqk', q, k) * scale
            scores = torch.where(mask[None, None], scores, -1000000000.0)
            p = _softmax(scores, axis=-1)
            o = torch.einsum('bhqk,bhke->bhqe', p, v)
            o = torch.einsum('bhte,hed->btd', o, Wo)
            mem = o[:, :M, :].clone()
            outs.append(o[:, M:, :])
        return torch.concatenate(outs, axis=1)

    def _B_ref(x, w1, b1, w2):
        h = torch.einsum('bsd,df->bsf', x, w1) + b1
        sp = torch.log1p(torch.exp(-torch.abs(h))) + torch.where(h > 0, h, torch.zeros_like(h))
        act = h * torch.tanh(sp)
        return torch.einsum('bsf,fd->bsd', act, w2)
    x = A_x
    xn = _layernorm(x, axis=-1)
    s = _A_ref(xn, A_mem0, A_Wq, A_Wk, A_Wv, A_Wo, A_seg_len)
    g = _silu(_B_ref(xn, B_w1, B_b1, B_w2))
    return x + s * g
```

```cpp
#include <hip/hip_runtime.h>
#include <stdint.h>

typedef __bf16 bf16x2 __attribute__((ext_vector_type(2)));
typedef __bf16 bf16x4 __attribute__((ext_vector_type(4)));
typedef __bf16 bf16x8 __attribute__((ext_vector_type(8)));
typedef __bf16 bf16x16 __attribute__((ext_vector_type(16)));
typedef short s16x4 __attribute__((ext_vector_type(4)));
typedef float f32x2 __attribute__((ext_vector_type(2)));
typedef float f32x4 __attribute__((ext_vector_type(4)));
typedef unsigned u32x2 __attribute__((ext_vector_type(2)));
typedef unsigned u32x4 __attribute__((ext_vector_type(4)));
typedef unsigned u32x8 __attribute__((ext_vector_type(8)));
typedef unsigned char uchar;
typedef __amdgpu_buffer_rsrc_t rsrc_t;
#define LAS __attribute__((address_space(3)))
#define DI __device__ __forceinline__

constexpr int S_ = 2048, D_ = 640, H_ = 32, NSEG = 64, F_ = 1728;
constexpr int LDS_BYTES = 163840;
constexpr int OFF_CTRL = 0;
constexpr int OFF_XF = 65536;
constexpr int OFF_KF = OFF_XF + 128 * 20 * 1024;
constexpr int OFF_QF = OFF_KF + 64 * 32 * 8192;
constexpr int OFF_VF = OFF_QF + 64 * 32 * 8192;
constexpr int OFF_ACT = OFF_VF + 64 * 32 * 8192;
constexpr int OFF_GF = OFF_ACT + 128 * 54 * 1024;
constexpr int OFF_OT = OFF_GF + 128 * 20 * 1024;
constexpr int OFF_KM = OFF_OT + 128 * 32 * 4096;
constexpr int OFF_EX = OFF_KM + 64 * 32 * 4096;
constexpr int EX_STEP = 163840, EX_MF = 8 * 16384, EX_AST = 9 * 16384 + 2048, EX_MST = 9 * 16384 + 2304, EX_PROBE = 9 * 16384 + 6400;
constexpr int WS_USED = OFF_EX + 7 * 2097152;
constexpr int C_TMO = 0, C_POOL = 32  , C_KV = 512  , C_Q = C_KV + 4096, C_ACT = C_Q + 4096  , C_G = C_ACT + 256  ,
              C_OT = C_G + 1280  , C_KM = C_OT + 2048  , C_Q1 = C_KM + 1024  , C_P2 = C_Q1 + 256,
              C_POL = 16320  ;
static_assert(C_P2 + 32 <= C_POL, "");
constexpr unsigned SENT = 0xFFFFFFFFu;
constexpr int SC1 = 16;
constexpr unsigned OOB = 0x7FFFFF00u;
constexpr unsigned SPIN_CAP = 400000;

struct Params {
    const uchar* x; const uchar* mem0; const uchar* wq; const uchar* wk; const uchar* wv; const uchar* wo;
    const uchar* w1; const uchar* b1; const uchar* w2; uchar* out; uchar* ws;
};

DI rsrc_t mk_rsrc(const void* p, unsigned bytes) { return __builtin_amdgcn_make_buffer_rsrc((void*)p, 0, bytes, 0x00020000); }
DI u32x4 ld16(rsrc_t r, unsigned v, unsigned s) { return __builtin_amdgcn_raw_buffer_load_b128(r, v, s, SC1); }
DI u32x2 ld8(rsrc_t r, unsigned v, unsigned s) { return __builtin_amdgcn_raw_buffer_load_b64(r, v, s, SC1); }
DI unsigned ld4(rsrc_t r, unsigned v, unsigned s) { return __builtin_amdgcn_raw_buffer_load_b32(r, v, s, SC1); }
DI void st16(u32x4 d, rsrc_t r, unsigned v, unsigned s) { __builtin_amdgcn_raw_buffer_store_b128(d, r, v, s, SC1); asm volatile("s_nop 1" :: "v"(d)); }
DI void st8(u32x2 d, rsrc_t r, unsigned v, unsigned s) { __builtin_amdgcn_raw_buffer_store_b64(d, r, v, s, SC1); }
DI void st4(unsigned d, rsrc_t r, unsigned v, unsigned s) { __builtin_amdgcn_raw_buffer_store_b32(d, r, v, s, SC1); }
DI unsigned pk2(float a, float b) { f32x2 v = {a, b}; return __builtin_bit_cast(unsigned, __builtin_convertvector(v, bf16x2)); }
DI unsigned nosent(unsigned v) { return min(v, 0xFFFFFFFEu); }
DI float bf_lo(unsigned v) { return __builtin_bit_cast(float, v << 16); }
DI float bf_hi(unsigned v) { return __builtin_bit_cast(float, v & 0xFFFF0000u); }
DI float rbf(float a) { return bf_lo(pk2(a, a)); }
DI bf16x8 as8(u32x4 v) { return __builtin_bit_cast(bf16x8, v); }
DI f32x4 mfma32(u32x4 a, u32x4 b, f32x4 c) { return __builtin_amdgcn_mfma_f32_16x16x32_bf16(as8(a), as8(b), c, 0, 0, 0); }
DI f32x4 mfma16(u32x2 a, u32x2 b, f32x4 c) { return mfma32((u32x4){a.x, a.y, 0u, 0u}, (u32x4){b.x, b.y, 0u, 0u}, c); }
DI f32x4 smf(u32x4 a, u32x8 b, f32x4 c, int idx) { return __builtin_amdgcn_smfmac_f32_16x16x64_bf16(as8(a), __builtin_bit_cast(bf16x16, b), c, idx, 0, 0); }
DI void smf_a(f32x4& c, u32x4 a, u32x8 b, int idx) { asm volatile("v_smfmac_f32_16x16x64_bf16 %0, %1, %2, %3" : "+v"(c) : "v"(a), "a"(b), "v"(idx)); }
DI void smf_v(f32x4& c, u32x4 a, u32x8 b, int idx) { asm volatile("v_smfmac_f32_16x16x64_bf16 %0, %1, %2, %3" : "+v"(c) : "v"(a), "v"(b), "v"(idx)); }
DI void mfma_v(f32x4& c, u32x4 a, u32x4 b) { asm volatile("v_mfma_f32_16x16x32_bf16 %0, %1, %2, %0" : "+v"(c) : "v"(a), "v"(b)); }
DI float fmax2(float a, float b) { return __builtin_amdgcn_fmed3f(a, b, __builtin_inff()); }
DI void mfma_pad(f32x4& a, f32x4& b, f32x4& c, f32x4& d) { asm volatile("s_nop 7" : "+v"(a), "+v"(b), "+v"(c), "+v"(d)); }
DI u32x2 trd(LAS uchar* p) { return __builtin_bit_cast(u32x2, __builtin_amdgcn_ds_read_tr16_b64_v4i16((LAS s16x4*)p)); }
DI unsigned mx4(unsigned m, u32x4 v) { return max(max(max(m, v.x), max(v.y, v.z)), v.w); }
DI void mx4v(unsigned& m, u32x4 v) { asm volatile("v_max3_u32 %0, %0, %1, %2\n\tv_max3_u32 %0, %0, %3, %4" : "+v"(m) : "v"(v.x), "v"(v.y), "v"(v.z), "v"(v.w)); }
DI unsigned rt32() { unsigned long long t; asm volatile("s_memrealtime %0\n\ts_waitcnt lgkmcnt(0)" : "=s"(t) :: "memory"); return (unsigned)t; }
DI unsigned waitrt(unsigned t) { unsigned n; do n = rt32(); while ((int)(n - t) < 0); return n; }
DI void bar() { asm volatile("s_waitcnt lgkmcnt(0)" ::: "memory"); __builtin_amdgcn_s_barrier(); asm volatile("" ::: "memory"); }
template <int N> DI void wait_vm() { asm volatile("s_waitcnt vmcnt(%0)" :: "n"(N) : "memory"); }
DI void dma16(rsrc_t r, unsigned voff, unsigned soff, unsigned ldsaddr) {
    asm volatile("s_mov_b32 m0, %0\n\ts_nop 0\n\tbuffer_load_dwordx4 %1, %2, %3 offen lds" :: "s"(ldsaddr), "v"(voff), "s"(r), "s"(soff) : "memory");
}
DI float swap32_add(float a, float b) { auto r = __builtin_amdgcn_permlane32_swap(__builtin_bit_cast(unsigned, a), __builtin_bit_cast(unsigned, b), false, false);
    return __builtin_bit_cast(float, (unsigned)r[0]) + __builtin_bit_cast(float, (unsigned)r[1]); }
DI float swap16_add(float a, float b) { auto r = __builtin_amdgcn_permlane16_swap(__builtin_bit_cast(unsigned, a), __builtin_bit_cast(unsigned, b), false, false);
    return __builtin_bit_cast(float, (unsigned)r[0]) + __builtin_bit_cast(float, (unsigned)r[1]); }
DI float grp_max(float m) {
    auto r = __builtin_amdgcn_permlane32_swap(__builtin_bit_cast(unsigned, m), __builtin_bit_cast(unsigned, m), false, false);
    m = fmax2(__builtin_bit_cast(float, (unsigned)r[0]), __builtin_bit_cast(float, (unsigned)r[1]));
    auto q = __builtin_amdgcn_permlane16_swap(__builtin_bit_cast(unsigned, m), __builtin_bit_cast(unsigned, m), false, false);
    return fmax2(__builtin_bit_cast(float, (unsigned)q[0]), __builtin_bit_cast(float, (unsigned)q[1]));
}
DI int fresh_lane() { int l = __builtin_amdgcn_mbcnt_hi(~0u, __builtin_amdgcn_mbcnt_lo(~0u, 0u)); asm volatile("" : "+v"(l)); return l & 63; }
DI unsigned fresh_tid() { unsigned t = threadIdx.x; asm volatile("" : "+v"(t)); return t; }
DI unsigned ex_off(const Params& p) { return OFF_EX + ((0u - (unsigned)(size_t)(p.ws + OFF_EX)) & 0x1FFFFFu); }
DI unsigned ex_step(unsigned exo, unsigned pol, int s) { const int r = (s * 43) >> 9; return exo + r * 2097152 + (s - 12 * r) * EX_STEP + ((pol >> r) & 1) * 8192; }
DI constexpr unsigned mf_off(int ks) { return EX_MF + (ks >> 3) * 16384 + (ks & 7) * 1024; }
DI unsigned xcc_id() { return (unsigned)__builtin_amdgcn_s_getreg(20 | (3 << 11)) & 7u; }
DI unsigned mt32() { unsigned long long t; asm volatile("s_memtime %0\n\ts_waitcnt lgkmcnt(0)" : "=s"(t) :: "memory"); return (unsigned)t; }
DI unsigned read_pol(const Params& p) {
    const int lane = threadIdx.x & 63;
    const unsigned v = ((const unsigned*)p.ws)[C_POL + min(lane, 47)];
    const unsigned long long m1 = __builtin_amdgcn_ballot_w64(v == 2), m0 = __builtin_amdgcn_ballot_w64(v == 1);
    unsigned pol = 0;
#pragma unroll
    for (int r = 0; r < 6; ++r) pol |= (__builtin_popcountll((m1 >> (8 * r)) & 255) > __builtin_popcountll((m0 >> (8 * r)) & 255) ? 1u : 0u) << r;
    return pol;
}
DI void set_tmo(const Params& p) { __hip_atomic_store((unsigned*)p.ws + C_TMO, 1u, __ATOMIC_RELAXED, __HIP_MEMORY_SCOPE_AGENT); }

DI void add_ctr(const Params& p, int word, unsigned v) { (void)__hip_atomic_fetch_add((unsigned*)p.ws + word, v, __ATOMIC_RELAXED, __HIP_MEMORY_SCOPE_AGENT); }
DI void wait_ctr(const Params& p, rsrc_t rw, int word, unsigned want) {
    unsigned n = SPIN_CAP;
#pragma nounroll
    for (;;) {
        asm volatile("" ::: "memory");
        if ((unsigned)__builtin_amdgcn_readfirstlane(ld4(rw, 0, word * 4)) >= want) break;
        if (--n == 0) { set_tmo(p); break; }
        if ((n & 63) == 0 && __builtin_amdgcn_readfirstlane(ld4(rw, 0, C_TMO * 4))) break;
        __builtin_amdgcn_s_sleep(2);
    }
    asm volatile("" ::: "memory");
}

__global__ void __launch_bounds__(256) k_init(Params p) {
    const int tid = threadIdx.x, lane = tid & 63, w = tid >> 6, b = blockIdx.x;
    const unsigned exo = ex_off(p);
    if (b >= 184 || (b >= 128 && b < 136)) { __builtin_amdgcn_s_sleep(40); }
    if (b >= 136 && b < 184) {
        __shared__ unsigned lat[4];
        const int pb = b - 136;
        unsigned* a = (unsigned*)(p.ws + exo + (pb >> 3) * 2097152 + EX_PROBE + (w >> 1) * 8192 + (w & 1) * EX_STEP + (pb & 7) * 128);
        unsigned best = ~0u;
        for (int i = 0; i < 4; ++i) {
            const unsigned t0 = mt32();
            unsigned v = 0;
            if (lane == 0) v = __hip_atomic_fetch_add(a, 1u, __ATOMIC_RELAXED, __HIP_MEMORY_SCOPE_AGENT);
            asm volatile("s_waitcnt vmcnt(0)" :: "v"(v) : "memory");
            best = min(best, mt32() - t0);
        }
        if (lane == 0) lat[w] = best;
        __syncthreads();
        const int d = (int)min(lat[0], lat[1]) - (int)min(lat[2], lat[3]);
        if (tid == 0) ((unsigned*)p.ws)[C_POL + pb] = abs(d) < 120 ? 0 : 1 + ((d > 0) ^ (xcc_id() >= 4));
    }
    if (b >= 128 && b < 131) {
        const int n = (b - 128) * 256 + tid;
        if (n < 640) {
            const int ks = n >> 6, l = n & 63, i = (l & 3) | ((l >> 1) & 4), half = (l >> 2) & 1, g = l >> 4;
            u32x4 v = *(const u32x4*)(p.mem0 + (i * 640 + 64 * ks + 16 * g + 8 * half) * 2);
            v.x = nosent(v.x); v.y = nosent(v.y); v.z = nosent(v.z); v.w = nosent(v.w);
            *(u32x4*)(p.ws + exo + mf_off(ks) + l * 16) = v;
            *(u32x4*)(p.ws + exo + mf_off(ks) + l * 16 + 8192) = v;
        }
    }
    if (b < 128) {
        const int c = w * 4 + (lane >> 4), sub = lane & 15, row = b * 16 + c;
        u32x4 v[5]; float sum = 0.f;
#pragma unroll
        for (int k = 0; k < 5; ++k) {
            v[k] = *(const u32x4*)(p.x + (size_t)row * 1280 + (k * 16 + sub) * 16);
#pragma unroll
            for (int j = 0; j < 4; ++j) sum += bf_lo(v[k][j]) + bf_hi(v[k][j]);
        }
#pragma unroll
        for (int m = 8; m >= 1; m >>= 1) sum += __shfl_xor(sum, m);
        const float mean = sum * (1.f / 640.f);
        float sq = 0.f;
#pragma unroll
        for (int k = 0; k < 5; ++k)
#pragma unroll
            for (int j = 0; j < 4; ++j) { const float a = bf_lo(v[k][j]) - mean, bb = bf_hi(v[k][j]) - mean; sq += a * a + bb * bb; }
#pragma unroll
        for (int m = 8; m >= 1; m >>= 1) sq += __shfl_xor(sq, m);
        const float mb = rbf(mean), sd = rbf(sqrtf(rbf(rbf(sq * (1.f / 640.f)) + 1e-5f)));
#pragma unroll
        for (int k = 0; k < 5; ++k) {
            u32x4 o;
#pragma unroll
            for (int j = 0; j < 4; ++j) o[j] = pk2(rbf(bf_lo(v[k][j]) - mb) / sd, rbf(bf_hi(v[k][j]) - mb) / sd);
            *(u32x4*)(p.ws + OFF_XF + (size_t)b * 20480 + (k * 16 + sub) * 256 + c * 16) = o;
        }
    }
    {
        const u32x4 sv = {SENT, SENT, SENT, SENT};
        for (int i = b * 256 + tid; i < 64 * (EX_STEP / 16); i += gridDim.x * 256) {
            const int st = i / (EX_STEP / 16), o = i - st * (EX_STEP / 16), r = st / 12;
            if (st == 0 && o >= EX_MF / 16 && (o < (EX_MF + 16384) / 16 || ((o * 16) & 8191) < 2048)) continue;
            *(u32x4*)(p.ws + exo + r * 2097152 + (st - 12 * r) * EX_STEP + o * 16) = sv;
        }
        u32x4* ct = (u32x4*)(p.ws + OFF_CTRL);
        const u32x4 z = {0, 0, 0, 0};
        for (int i = b * 256 + tid; i < C_POL / 4; i += gridDim.x * 256) ct[i] = z;
    }
}

#define SB() __builtin_amdgcn_sched_barrier(0)
DI void mfma_va(f32x4& c, u32x4 a, u32x4 b) { asm volatile("v_mfma_f32_16x16x32_bf16 %0, %1, %2, %0" : "+v"(c) : "v"(a), "a"(b)); }
DI void mfma_av(f32x4& c, u32x4 a, u32x4 b) { asm volatile("v_mfma_f32_16x16x32_bf16 %0, %1, %2, %0" : "+v"(c) : "a"(a), "v"(b)); }
DI void mfma_cacc(f32x4& c, u32x4 a, u32x4 b) { asm volatile("v_mfma_f32_16x16x32_bf16 %0, %1, %2, %0" : "+a"(c) : "v"(a), "v"(b)); }
struct P1Src {
    rsrc_t r; unsigned v0, v1, so, step;
    DI void set(const Params& p, int u, int kh, int w, int lane) {
        const uchar* base; unsigned stride, col, bytes;
        if (u < 128) { base = (u & 2) ? p.wv : p.wk; stride = 8192; col = ((u >> 2) * 128 + (u & 1) * 64) * 2; bytes = 640 * 8192; }
        else if (u < 192) { base = p.wq; stride = 8192; col = (u - 128) * 128; bytes = 640 * 8192; }
        else { base = p.w1; stride = 3456; col = (u - 192) * 128; bytes = u < 219 ? 640 * 3456 : 0; }
        r = mk_rsrc(base, bytes);
        const int rl = lane >> 3, f0 = ((rl >> 1) & 1) | (((rl >> 2) & 1) << 1);
        v0 = rl * stride + (((lane & 7) ^ (2 * f0)) << 4); v1 = v0 ^ 64;
        so = (kh * 320 + 80 * w) * stride + col; step = 8 * stride;
    }
    DI void piece(int i, unsigned ldsw) const { dma16(r, (i & 1) ? v1 : v0, so + i * step, ldsw + i * 1024); }
};
DI float mish(float a, float bias) {
    const float h = a + bias;
    const float e = __builtin_amdgcn_exp2f(fminf(h, 30.f) * 1.44269504f);
    const float n = e * (e + 2.f);
    return h * n * __builtin_amdgcn_rcpf(n + 2.f);
}
#define PT(i)
constexpr int XA_ACC = 15;
template <bool ISV, int KH>
DI void p1_slab(LAS uchar* sb, const unsigned (&tb)[4], const u32x4 (&xa)[4][20], f32x4 (&acc)[4][4], const P1Src& src, unsigned ldsn) {
    u32x4 wf[2][4];
    auto rd = [&](int kk, int nt) { const u32x2 lo = trd(sb + tb[nt] + kk * 4096), hi = trd(sb + tb[nt ^ 2] + kk * 4096 + 512); return (u32x4){lo.x, lo.y, hi.x, hi.y}; };
#pragma unroll
    for (int nt = 0; nt < 4; ++nt) wf[0][nt] = rd(0, nt);
    SB();
#pragma unroll
    for (int kk = 0; kk < 10; ++kk) {
#pragma unroll
        for (int nt = 0; nt < 4; ++nt) {
            if (kk + 1 < 10) wf[(kk + 1) & 1][nt] = rd(kk + 1, nt);
            if (kk < 5 && (nt & 1)) src.piece(2 * kk + (nt >> 1), ldsn);
#pragma unroll
            for (int t = 0; t < 4; ++t) {
                const int ks = KH * 10 + kk;
                if (ISV) { if (ks < XA_ACC) mfma_av(acc[t][nt], xa[t][ks], wf[kk & 1][nt]); else mfma_v(acc[t][nt], xa[t][ks], wf[kk & 1][nt]); }
                else { if (ks < XA_ACC) mfma_va(acc[t][nt], wf[kk & 1][nt], xa[t][ks]); else mfma_v(acc[t][nt], wf[kk & 1][nt], xa[t][ks]); }
            }
            SB();
        }
    }
    asm volatile("s_nop 7" : "+v"(acc[3][3]), "+v"(acc[2][3]), "+v"(acc[1][3]), "+v"(acc[0][3]), "+v"(acc[3][2]), "+v"(acc[2][2]), "+v"(acc[1][2]), "+v"(acc[0][2]));
#pragma unroll
    for (int t = 0; t < 4; ++t) asm volatile("" : "+v"(acc[t][0]), "+v"(acc[t][1]));
}
template <bool ISV>
DI void p1_group(const Params& p, LAS uchar* lds, rsrc_t rw, unsigned lbase, const unsigned (&tb)[4], const u32x4 (&xa)[4][20], P1Src& src, int buf,
                 int chunk, int u, int un, bool first, int w, int lane, unsigned (&pt)[5]) {
    const int g = lane >> 4;
    f32x4 acc[4][4];
#pragma unroll
    for (int t = 0; t < 4; ++t)
#pragma unroll
        for (int nt = 0; nt < 4; ++nt) { acc[t][nt] = (f32x4){0.f, 0.f, 0.f, 0.f}; asm volatile("" : "+v"(acc[t][nt])); }
    PT(0);
    if (first) wait_vm<50>(); else wait_vm<10>();
    PT(1);
    bar();
    PT(2);
    src.set(p, un, 0, w, lane);
    {
        const unsigned ln = lbase + (buf == 0 ? 2 : buf - 1) * 40960;
        p1_slab<ISV, 0>(lds + buf * 40960, tb, xa, acc, src, ln);
    }
    buf = buf == 2 ? 0 : buf + 1;
    PT(3);
    wait_vm<10>();
    PT(1);
    bar();
    PT(2);
    src.set(p, un, 1, w, lane);
    {
        const unsigned ln = lbase + (buf == 0 ? 2 : buf - 1) * 40960;
        p1_slab<ISV, 1>(lds + buf * 40960, tb, xa, acc, src, ln);
    }
    PT(3);
    const int tt0 = chunk * 16 + w * 4;
    if (ISV) {
        const int h = u >> 2;
#pragma unroll
        for (int sp = 0; sp < 2; ++sp)
#pragma unroll
            for (int nt = 0; nt < 4; ++nt) {
                const f32x4 a = acc[2 * sp][nt], b = acc[2 * sp + 1][nt];
                const u32x4 o = {pk2(a[0], a[1]), pk2(a[2], a[3]), pk2(b[0], b[1]), pk2(b[2], b[3])};
                st16(o, rw, lane * 16, OFF_VF + (((tt0 >> 1) + sp) * 32 + h) * 8192 + ((u & 1) * 4 + nt) * 1024);
            }
    } else if (u < 192) {
        const int h = u < 128 ? u >> 2 : (u - 128) >> 1;
        const int ob = u < 128 ? OFF_KF : OFF_QF;
#pragma unroll
        for (int t = 0; t < 4; ++t)
#pragma unroll
            for (int m = 0; m < 2; ++m) {
                const f32x4 a = acc[t][2 * m], b = acc[t][2 * m + 1];
                const u32x4 o = {pk2(a[0], a[1]), pk2(a[2], a[3]), pk2(b[0], b[1]), pk2(b[2], b[3])};
                st16(o, rw, lane * 16, ob + (((tt0 + t) >> 1) * 32 + h) * 8192 + ((t & 1) * 4 + (u & 1) * 2 + m) * 1024);
            }
    } else {
        const int fg = u - 192;
        u32x2 bv[4];
#pragma unroll
        for (int nt = 0; nt < 4; ++nt) bv[nt] = *(const u32x2*)(p.b1 + (fg * 64 + nt * 16 + 4 * g) * 2);
#pragma unroll
        for (int t = 0; t < 4; ++t)
#pragma unroll
            for (int m = 0; m < 2; ++m) {
                const f32x4 a = acc[t][2 * m], b = acc[t][2 * m + 1];
                const u32x2 ba = bv[2 * m], bb = bv[2 * m + 1];
                const u32x4 o = {pk2(mish(a[0], bf_lo(ba.x)), mish(a[1], bf_hi(ba.x))), pk2(mish(a[2], bf_lo(ba.y)), mish(a[3], bf_hi(ba.y))),
                                 pk2(mish(b[0], bf_lo(bb.x)), mish(b[1], bf_hi(bb.x))), pk2(mish(b[2], bf_lo(bb.y)), mish(b[3], bf_hi(bb.y)))};
                st16(o, rw, lane * 16, OFF_ACT + ((tt0 + t) * 54 + fg * 2 + m) * 1024);
                SB();
            }
    }
}
DI void ld16_a(u32x4& d, rsrc_t r, unsigned v, unsigned s) { asm volatile("buffer_load_dwordx4 %0, %1, %2, %3 offen" : "=a"(d) : "v"(v), "s"(r), "s"(s) : "memory"); }
DI void ld16_v(u32x4& d, rsrc_t r, unsigned v, unsigned s) { asm volatile("buffer_load_dwordx4 %0, %1, %2, %3 offen" : "=v"(d) : "v"(v), "s"(r), "s"(s) : "memory"); }
DI void ld16_s(u32x4& d, rsrc_t r, unsigned v, unsigned s) { asm volatile("buffer_load_dwordx4 %0, %1, %2, %3 offen sc1" : "=v"(d) : "v"(v), "s"(r), "s"(s) : "memory"); }
DI void p1_task(const Params& p, LAS uchar* lds, int chunk, int u0, int nu) {
    const int lane = fresh_lane(), w = __builtin_amdgcn_readfirstlane(threadIdx.x >> 6), c = lane & 15, g = lane >> 4;
    const rsrc_t rw = mk_rsrc(p.ws, WS_USED);
    const unsigned lbase = (unsigned)(size_t)lds + w * 10240;
    P1Src src;
    u32x4 xa[4][20];
    src.set(p, u0, 0, w, lane);
#pragma unroll
    for (int i = 0; i < 10; ++i) src.piece(i, lbase);
#pragma unroll
    for (int ks = 0; ks < 20; ++ks) {
        if (ks == 10) {
            src.set(p, u0, 1, w, lane);
#pragma unroll
            for (int i = 0; i < 10; ++i) src.piece(i, lbase + 40960);
        }
#pragma unroll
        for (int t = 0; t < 4; ++t) {
            const unsigned so = OFF_XF + ((chunk * 16 + w * 4 + t) * 20 + ks) * 1024;
            if (ks < XA_ACC) ld16_a(xa[t][ks], rw, lane * 16, so); else ld16_v(xa[t][ks], rw, lane * 16, so);
        }
        SB();
    }
    const int q = c >> 2, pp = c & 3, sw = (q >> 1) | ((g & 1) << 1);
    unsigned tb[4];
#pragma unroll
    for (int nt = 0; nt < 4; ++nt) tb[nt] = (8 * g + q) * 128 + 32 * (nt ^ sw) + 16 * (pp >> 1) + 8 * (pp & 1);
    int buf = 0;
    unsigned pt[5] = {0, 0, 0, 0, mt32()};
    for (int gi = 0; gi < nu; ++gi) {
        const int u = u0 + gi, un = gi + 1 < nu ? u + 1 : 219;
        if (u < 128 && (u & 2)) p1_group<true>(p, lds, rw, lbase, tb, xa, src, buf, chunk, u, un, gi == 0, w, lane, pt);
        else p1_group<false>(p, lds, rw, lbase, tb, xa, src, buf, chunk, u, un, gi == 0, w, lane, pt);
        buf = buf == 0 ? 2 : buf - 1;
    }
    wait_vm<0>();
    bar();
    const unsigned tid = fresh_tid();
    if (tid < (unsigned)nu) {
        const int u = u0 + tid;
        add_ctr(p, u < 128 ? C_KV + (chunk * 32 + (u >> 2)) * 16 : (u < 192 ? C_Q + (chunk * 32 + ((u - 128) >> 1)) * 16 : C_ACT + chunk * 32), 1u);
    }
}

DI void down_task(const Params& p, LAS uchar* lds, int chunk, int cg) {
    const int lane = fresh_lane(), w = __builtin_amdgcn_readfirstlane(threadIdx.x >> 6), c = lane & 15, g = lane >> 4;
    const rsrc_t rw = mk_rsrc(p.ws, WS_USED), r2 = mk_rsrc(p.w2, F_ * 1280);
    const unsigned lbase = (unsigned)(size_t)lds;
    const int rl = lane >> 3, f0 = ((rl >> 1) & 1) | (((rl >> 2) & 1) << 1);
    const unsigned v0 = rl * 1280 + (((lane & 7) ^ (2 * f0)) << 4), v1 = rl * 1280 + (((lane & 7) ^ (2 * (f0 ^ 2))) << 4);
    auto issue = [&](int sl, int buf) {
#pragma unroll
        for (int i = 0; i < 6; ++i) {
            const int pc = (w & 1) * 6 + i, im = w >> 1;
            dma16(r2, (i & 1) ? v1 : v0, (sl * 96 + 8 * pc) * 1280 + cg * 256 + im * 128, lbase + buf * 24576 + im * 12288 + pc * 1024);
        }
    };
    const int tt0 = chunk * 16 + w * 4;
    issue(0, 0);
    if (w == 0) wait_ctr(p, rw, C_ACT + chunk * 32, 27);
    bar();
    u32x4 xb[2][3][4];
#pragma unroll
    for (int kk = 0; kk < 3; ++kk)
#pragma unroll
        for (int t = 0; t < 4; ++t) ld16_s(xb[0][kk][t], rw, lane * 16, OFF_ACT + ((tt0 + t) * 54 + kk) * 1024);
    issue(1, 1);
    const int q = c >> 2, pp = c & 3, sw = (q >> 1) | (((g & 1) ^ (g >> 1)) << 1);
    unsigned tb[4];
#pragma unroll
    for (int nt = 0; nt < 4; ++nt) tb[nt] = (4 * g + q) * 128 + 32 * (nt ^ sw) + 16 * (pp >> 1) + 8 * (pp & 1);
    f32x4 acc[4][8];
#pragma unroll
    for (int t = 0; t < 4; ++t)
#pragma unroll
        for (int nt = 0; nt < 8; ++nt) { acc[t][nt] = (f32x4){0.f, 0.f, 0.f, 0.f}; asm volatile("" : "+a"(acc[t][nt])); }
    int buf = 0;
    for (int s2 = 0; s2 < 9; ++s2) {
#pragma unroll
        for (int par = 0; par < 2; ++par) {
            const int sl = 2 * s2 + par;
            wait_vm<6>();
#pragma unroll
            for (int kk = 0; kk < 3; ++kk) asm volatile("" : "+v"(xb[par][kk][0]), "+v"(xb[par][kk][1]), "+v"(xb[par][kk][2]), "+v"(xb[par][kk][3]));
            bar();
            const int sn = min(sl + 1, 17);
#pragma unroll
            for (int kk = 0; kk < 3; ++kk)
#pragma unroll
                for (int t = 0; t < 4; ++t) ld16_s(xb[par ^ 1][kk][t], rw, lane * 16, OFF_ACT + ((tt0 + t) * 54 + sn * 3 + kk) * 1024);
            issue(min(sl + 2, 17), buf == 0 ? 2 : buf - 1);
            LAS uchar* sb = lds + buf * 24576;
            auto rd = [&](int i) { LAS uchar* a = sb + ((i >> 2) & 1) * 12288 + tb[i & 3] + (i >> 3) * 4096; const u32x2 lo = trd(a), hi = trd(a + 2048); return (u32x4){lo.x, lo.y, hi.x, hi.y}; };
            u32x4 wf[4];
#pragma unroll
            for (int i = 0; i < 4; ++i) wf[i] = rd(i);
            SB();
#pragma unroll
            for (int i = 0; i < 24; ++i) {
                const u32x4 cur = wf[i & 3];
#pragma unroll
                for (int t = 0; t < 4; ++t) mfma_cacc(acc[t][i & 7], cur, xb[par][i >> 3][t]);
                if (i + 4 < 24) wf[i & 3] = rd(i + 4);
                SB();
            }
            if (par) asm volatile("s_nop 7");
            SB();
            buf = buf == 2 ? 0 : buf + 1;
        }
    }
    wait_vm<0>();
#pragma unroll
    for (int kk = 0; kk < 3; ++kk) asm volatile("" :: "v"(xb[0][kk][0]), "v"(xb[0][kk][1]), "v"(xb[0][kk][2]), "v"(xb[0][kk][3]));
    SB();
#pragma unroll
    for (int t = 0; t < 4; ++t)
#pragma unroll
        for (int m = 0; m < 4; ++m) {
            float y[8];
#pragma unroll
            for (int r = 0; r < 8; ++r) { const float a = rbf(acc[t][2 * m + (r >> 2)][r & 3]); y[r] = a * __builtin_amdgcn_rcpf(1.f + __builtin_amdgcn_exp2f(-a * 1.44269504f)); }
            const u32x4 o = {pk2(y[0], y[1]), pk2(y[2], y[3]), pk2(y[4], y[5]), pk2(y[6], y[7])};
            st16(o, rw, lane * 16, OFF_GF + ((tt0 + t) * 20 + cg * 4 + m) * 1024);
        }
    wait_vm<0>();
    bar();
    if (fresh_tid() == 0) add_ctr(p, C_G + (chunk * 5 + cg) * 32, 1u);
}

constexpr float SCL = 0.08838834764831845f * 1.4426950408889634f;
DI void attn_task(const Params& p, int s, int h) {
    const int lane = fresh_lane(), c = lane & 15, g = lane >> 4;
    const rsrc_t rw = mk_rsrc(p.ws, WS_USED);
    const int sh = (s * 32 + h);
    u32x4 qf[2][4], kf[2][4], vf[8], km[4]; u32x2 vm[8];
    wait_ctr(p, rw, C_Q + ((s >> 3) * 32 + h) * 16, 2);
    wait_ctr(p, rw, C_KV + ((s >> 3) * 32 + h) * 16, 4);
#pragma unroll
    for (int i = 0; i < 8; ++i) qf[i >> 2][i & 3] = ld16(rw, lane * 16, OFF_QF + sh * 8192 + i * 1024);
#pragma unroll
    for (int i = 0; i < 8; ++i) kf[i >> 2][i & 3] = ld16(rw, lane * 16, OFF_KF + sh * 8192 + i * 1024);
#pragma unroll
    for (int i = 0; i < 8; ++i) vf[i] = ld16(rw, lane * 16, OFF_VF + sh * 8192 + i * 1024);
    wait_ctr(p, rw, C_KM + h * 32, s + 1);
#pragma unroll
    for (int i = 0; i < 4; ++i) km[i] = ld16(rw, (g * 8 + (c & 7)) * 16, OFF_KM + sh * 4096 + i * 512);
#pragma unroll
    for (int i = 0; i < 8; ++i) vm[i] = ld8(rw, (lane & 31) * 8, OFF_KM + sh * 4096 + 2048 + i * 256);
    const u32x4 one4 = {0x3F803F80u, 0x3F803F80u, 0x3F803F80u, 0x3F803F80u};
    const u32x2 one2 = {0x3F803F80u, 0x3F803F80u};
    const float NINF = -__builtin_inff();
#pragma unroll
    for (int qt = 0; qt < 2; ++qt) {
        f32x4 s0 = {0.f, 0.f, 0.f, 0.f}, s1 = s0, s2 = s0;
#pragma unroll
        for (int ks = 0; ks < 4; ++ks) {
            s0 = mfma32(kf[0][ks], qf[qt][ks], s0);
            if (qt == 1) s1 = mfma32(kf[1][ks], qf[qt][ks], s1);
            s2 = mfma32(km[ks], qf[qt][ks], s2);
        }
        float t[12], m = NINF;
#pragma unroll
        for (int r = 0; r < 4; ++r) {
            const bool diag = 4 * g + r <= c;
            t[r] = (qt == 1 || diag) ? s0[r] : NINF;
            t[4 + r] = (qt == 1 && diag) ? s1[r] : NINF;
            t[8 + r] = g >= 2 ? s2[r] : NINF;
        }
#pragma unroll
        for (int r = 0; r < 12; ++r) m = fmaxf(m, t[r]);
        m = grp_max(m) * SCL;
#pragma unroll
        for (int r = 0; r < 12; ++r) t[r] = __builtin_amdgcn_exp2f(t[r] * SCL - m);
        const u32x4 p01 = {pk2(t[0], t[1]), pk2(t[2], t[3]), pk2(t[4], t[5]), pk2(t[6], t[7])};
        const u32x2 p2 = {pk2(t[8], t[9]), pk2(t[10], t[11])};
        f32x4 l = mfma32(one4, p01, (f32x4){0.f, 0.f, 0.f, 0.f});
        l = mfma16(one2, p2, l);
        const float inv = __builtin_amdgcn_rcpf(l[0]);
#pragma unroll
        for (int tp = 0; tp < 4; ++tp) {
            f32x4 a = mfma32(vf[2 * tp], p01, (f32x4){0.f, 0.f, 0.f, 0.f}), b = mfma32(vf[2 * tp + 1], p01, (f32x4){0.f, 0.f, 0.f, 0.f});
            a = mfma16(vm[2 * tp], p2, a); b = mfma16(vm[2 * tp + 1], p2, b);
            const u32x4 o = {pk2(a[0] * inv, a[1] * inv), pk2(a[2] * inv, a[3] * inv), pk2(b[0] * inv, b[1] * inv), pk2(b[2] * inv, b[3] * inv)};
            st16(o, rw, lane * 16, OFF_OT + ((2 * s + qt) * 32 + h) * 4096 + tp * 1024);
        }
    }
    wait_vm<0>();
    if (lane == 0) add_ctr(p, C_OT + s * 32, 1u);
}

DI void wo_stage(const Params& p, unsigned ldsw, int h0, int cs, int lane) {
    const rsrc_t ro = mk_rsrc(p.wo, 4096 * 1280);
    const unsigned v = (lane >> 2) * 1280 + (lane & 3) * 16;
#pragma unroll
    for (int i = 0; i < 32; ++i) dma16(ro, v, (h0 * 128 + 16 * i) * 1280 + cs * 64, ldsw + i * 1024);
    wait_vm<0>();
}
DI void w_role(const Params& p, LAS uchar* lds, int cs, int s0, int sstep) {
    const int lane = fresh_lane(), w = __builtin_amdgcn_readfirstlane(threadIdx.x >> 6), c = lane & 15, g = lane >> 4;
    const rsrc_t rw = mk_rsrc(p.ws, WS_USED);
    u32x4 wf[8][4][2];
    {
        LAS uchar* lw = lds + w * 32768;
        const int q = c >> 2, pp = c & 3;
#pragma unroll
        for (int rd = 0; rd < 2; ++rd) {
            wo_stage(p, (unsigned)(size_t)lw, 8 * w + 4 * rd, cs, lane);
#pragma unroll
            for (int i = 0; i < 4; ++i)
#pragma unroll
                for (int tp = 0; tp < 4; ++tp)
#pragma unroll
                    for (int nt = 0; nt < 2; ++nt) {
                        LAS uchar* a = lw + i * 8192 + (32 * tp + 4 * g + q) * 64 + nt * 32 + pp * 8;
                        const u32x2 lo = trd(a), hi = trd(a + 1024);
                        wf[4 * rd + i][tp][nt] = (u32x4){lo.x, lo.y, hi.x, hi.y};
                        if (4 * rd + i < 7) asm volatile("" : "+a"(wf[4 * rd + i][tp][nt])); else asm volatile("" : "+v"(wf[4 * rd + i][tp][nt]));
                    }
            asm volatile("s_waitcnt lgkmcnt(0)" ::: "memory");
            __builtin_amdgcn_sched_barrier(0);
        }
    }
    bar();
    LAS f32x4* red = (LAS f32x4*)lds;
    for (int s = s0; s < NSEG; s += sstep) {
        f32x4 acc[2][2];
#pragma unroll
        for (int i = 0; i < 4; ++i) { acc[i >> 1][i & 1] = (f32x4){0.f, 0.f, 0.f, 0.f}; asm volatile("" : "+v"(acc[i >> 1][i & 1])); }
        const int tt = 2 * s + (w >> 1), nt = w & 1;
        if (w == 0) { wait_ctr(p, rw, C_G + ((s >> 3) * 5 + (cs >> 2)) * 32, 1); wait_ctr(p, rw, C_OT + s * 32, 32); }
        bar();
        u32x4 ob[4][2][4];
        auto fetch = [&](int i) {
#pragma unroll
            for (int t = 0; t < 2; ++t)
#pragma unroll
                for (int tp = 0; tp < 4; ++tp) ob[i & 3][t][tp] = ld16(rw, lane * 16, OFF_OT + ((2 * s + t) * 32 + 8 * w + i) * 4096 + tp * 1024);
        };
#pragma unroll
        for (int i = 0; i < 4; ++i) { fetch(i); SB(); }
        const u32x2 xv = *(const u32x2*)(p.x + (size_t)(tt * 16 + c) * 1280 + cs * 64 + nt * 32 + g * 8);
        const u32x2 gv = ld8(rw, lane * 16 + nt * 8, OFF_GF + (tt * 20 + cs) * 1024);
        SB();
#pragma unroll
        for (int i = 0; i < 8; ++i) {
#pragma unroll
            for (int tp = 0; tp < 4; ++tp)
#pragma unroll
                for (int t = 0; t < 2; ++t)
#pragma unroll
                    for (int n = 0; n < 2; ++n) { if (i < 7) mfma_av(acc[t][n], wf[i][tp][n], ob[i & 3][t][tp]); else mfma_v(acc[t][n], wf[i][tp][n], ob[i & 3][t][tp]); }
            SB();
            if (i + 4 < 8) fetch(i + 4);
            SB();
        }
        mfma_pad(acc[0][0], acc[0][1], acc[1][0], acc[1][1]);
#pragma unroll
        for (int i = 0; i < 4; ++i) red[(i * 4 + w) * 64 + lane] = acc[i >> 1][i & 1];
        bar();
        const f32x4 r = (red[(w * 4 + 0) * 64 + lane] + red[(w * 4 + 1) * 64 + lane]) + (red[(w * 4 + 2) * 64 + lane] + red[(w * 4 + 3) * 64 + lane]);
        const unsigned s01 = pk2(r[0], r[1]), s23 = pk2(r[2], r[3]);
        const unsigned m01 = pk2(bf_lo(s01) * bf_lo(gv.x), bf_hi(s01) * bf_hi(gv.x)), m23 = pk2(bf_lo(s23) * bf_lo(gv.y), bf_hi(s23) * bf_hi(gv.y));
        const u32x2 o = {pk2(bf_lo(xv.x) + bf_lo(m01), bf_hi(xv.x) + bf_hi(m01)), pk2(bf_lo(xv.y) + bf_lo(m23), bf_hi(xv.y) + bf_hi(m23))};
        *(u32x2*)(p.out + (size_t)(tt * 16 + c) * 1280 + cs * 64 + nt * 32 + g * 8) = o;
    }
}

#define V_A 8
#define V_M 6
#define KM_DELAY 30
#define PAD0 0
#define PAD_UP 6
#define PAD_MAX 16
#define D_MST 30
#define F_LATE 110
#define V_LATE 2
#define DUR_A0 140
#define DUR_M0 100
#define FT(i)
#define FDUMP(blk, s, w)
#define STAT(blk, s, w, a, b, c, d)
DI int wave_max(int d) {
    d = max(d, __builtin_amdgcn_update_dpp(0, d, 0x121, 0xf, 0xf, true));
    d = max(d, __builtin_amdgcn_update_dpp(0, d, 0x122, 0xf, 0xf, true));
    d = max(d, __builtin_amdgcn_update_dpp(0, d, 0x124, 0xf, 0xf, true));
    d = max(d, __builtin_amdgcn_update_dpp(0, d, 0x128, 0xf, 0xf, true));
    auto r = __builtin_amdgcn_permlane32_swap((unsigned)d, (unsigned)d, false, false); d = max((int)r[0], (int)r[1]);
    auto q = __builtin_amdgcn_permlane16_swap((unsigned)d, (unsigned)d, false, false); d = max((int)q[0], (int)q[1]);
    return __builtin_amdgcn_readfirstlane(d);
}
struct Sched {
    unsigned tgo, tau, durA, durM, pad;
    unsigned a, v;
    DI void fetch(rsrc_t rw, unsigned exo, unsigned pol, int s, int lane) {
        a = ld4(rw, s >= 2 ? (lane & 31) * 4 : OOB, ex_step(exo, pol, max(s - 2, 0)) + EX_AST);
        v = ld4(rw, min(lane, 39) * 4, ex_step(exo, pol, s - 1) + EX_MST);
    }
    DI void next(const Params& p, rsrc_t rw, unsigned exo, unsigned pol, int s, int lane, unsigned long long& alive) {
        if ((__builtin_amdgcn_ballot_w64(v == SENT) & alive) != 0) {
            const unsigned mo = ex_step(exo, pol, s - 1) + EX_MST;
            unsigned n = SPIN_CAP; bool bad;
#pragma nounroll
            do {
                asm volatile("" ::: "memory");
                v = ld4(rw, min(lane, 39) * 4, mo);
                bad = __builtin_amdgcn_ballot_w64(v == SENT) != 0;
            } while (bad && --n);
            if (bad) { alive = 0; set_tmo(p); }
        }
        const unsigned ref = __builtin_amdgcn_readfirstlane(v);
        const unsigned pm = ref + (unsigned)wave_max((int)(v - ref));
        if (s >= 2 && __builtin_amdgcn_ballot_w64(v & 1) == 0) durM = min(pm - tau, durM + 8);
        if (__builtin_amdgcn_ballot_w64(a - 1u >= 0xFFFFu) == 0) durA = min((unsigned)wave_max((int)a), durA + 8);
        const bool late = s == 1 || (int)(pm + V_LATE - tgo) > 0;
        pad = late || __builtin_amdgcn_ballot_w64(v & 1) != 0 ? min(pad + PAD_UP, (unsigned)PAD_MAX) : (pad ? pad - 1 : 0);
        tau = (late ? pm + F_LATE : tgo) + durA + pad;
        tgo = tau + durM + V_A + pad;
    }
};

#define PACE 8
DI void pace() { asm volatile("s_nop %0" :: "n"(PACE)); }
DI void role_M(const Params& p, LAS uchar* lds, int mi) {
    const int tid = threadIdx.x, lane = tid & 63, w = __builtin_amdgcn_readfirstlane(tid >> 6), c = lane & 15, g = lane >> 4;
    const int qh = mi & 1, cs = mi >> 1;
    const rsrc_t rw = mk_rsrc(p.ws, WS_USED);
    u32x8 wf[8][2][2];
    {
        LAS uchar* lw = lds + w * 32768;
        const int q = c >> 2, pp = c & 3;
        unsigned ra[4];
#pragma unroll
        for (int m = 0; m < 4; ++m) {
            const int k0 = m < 2 ? 8 * g + 4 * m : 32 + 8 * g + 4 * (m - 2), ga = k0 >> 4, qq = (k0 >> 2) & 3;
            const int e = 32 * ga + 2 * qq + (q & 1) + 8 * (q >> 1);
            ra[m] = e * 64 + pp * 8;
        }
#pragma unroll
        for (int rd = 0; rd < 2; ++rd) {
            wo_stage(p, (unsigned)(size_t)lw, 8 * w + 4 * rd, cs, lane);
#pragma unroll
            for (int i = 0; i < 4; ++i)
#pragma unroll
                for (int sel = 0; sel < 2; ++sel)
#pragma unroll
                    for (int nt = 0; nt < 2; ++nt) {
                        LAS uchar* a = lw + i * 8192 + sel * 1024 + nt * 32;
                        const u32x2 t0 = trd(a + ra[0]), t1 = trd(a + ra[1]), t2 = trd(a + ra[2]), t3 = trd(a + ra[3]);
                        wf[4 * rd + i][sel][nt] = (u32x8){t0.x, t0.y, t1.x, t1.y, t2.x, t2.y, t3.x, t3.y};
                        if (4 * rd + i < 6) asm volatile("" : "+a"(wf[4 * rd + i][sel][nt])); else asm volatile("" : "+v"(wf[4 * rd + i][sel][nt]));
                    }
            asm volatile("s_waitcnt lgkmcnt(0)" ::: "memory");
            SB();
        }
    }
    bar();
    if (tid < 2) *(LAS unsigned*)(lds + 4096 + tid * 4) = 0;
    bar();
    const int idx = (c & 4) ? 0xEEEE : 0x4444;
    const int dl = 32 * cs + 2 * c;
    const unsigned so = mf_off(dl >> 6) + (((dl >> 4) & 3) * 16 + qh * 8 + ((dl >> 3) & 1) * 4 + g) * 16 + (dl & 7) * 2;
    const unsigned rwo = (w * 128 + (2 * (g & 1)) * 32 + 16 * (g >> 1) + c) * 4, rro = (g * 32 + 2 * c) * 4;
    const unsigned lo16 = lane * 16, sto = lane == 0 ? mi * 4 : OOB;
    unsigned long long alive = ~0ull;
    const unsigned exo = ex_off(p), pol = read_pol(p);
    unsigned tdone = rt32();
    Sched sc = {0u, tdone - V_M, DUR_A0, DUR_M0, PAD0, 0u, 0u};
    for (int s = 0; s < NSEG - 1; ++s) {
        const unsigned eb = ex_step(exo, pol, s);
        const unsigned ob = eb + w * 32768 + qh * 1024;
        if (s > 0) { waitrt(tdone + D_MST); sc.fetch(rw, exo, pol, s, lane); sc.next(p, rw, exo, pol, s, lane, alive); }
        const unsigned tau = sc.tau;
        unsigned flag = 0;
        unsigned obs[8], mb = ex_step(exo, pol, s + 1);
#pragma unroll
        for (int i = 0; i < 8; ++i) { obs[i] = ob + (i >> 2) * 16384 + (i & 3) * 2048; asm volatile("" : "+s"(obs[i])); }
        unsigned rwa = rwo + (s & 1) * 2048, rra = rro + (s & 1) * 2048;
        f32x4 acc[2][2];
#pragma unroll
        for (int i = 0; i < 4; ++i) { acc[i >> 1][i & 1] = (f32x4){0.f, 0.f, 0.f, 0.f}; asm volatile("" : "+v"(acc[i >> 1][i & 1])); }
        asm volatile("" : "+s"(mb), "+v"(rwa), "+v"(rra));
        waitrt(tau + V_M);
        FT(0);
        u32x4 of[8];
        for (;;) {
#pragma unroll
        for (int i = 0; i < 8; ++i) { of[i] = ld16(rw, lo16, obs[i]); SB(); if (i < 7) pace(); SB(); }
        unsigned mx = 0;
        unsigned long long early = 0;
#pragma unroll
        for (int i = 0; i < 8; ++i) {
            if (i == 7) {
                mx4v(mx, of[i]);
                early = __builtin_amdgcn_ballot_w64(mx == SENT) & alive;
                asm volatile("" : "+s"(early));
                SB();
            }
#pragma unroll
            for (int j = 0; j < 4; ++j) { if (i < 6) smf_a(acc[j >> 1][j & 1], of[i], wf[i][j >> 1][j & 1], idx); else smf_v(acc[j >> 1][j & 1], of[i], wf[i][j >> 1][j & 1], idx); }
            if (i < 7) mx4v(mx, of[i]);
            if (i == 0) FT(1);
            if (i == 7) FT(2);
            SB();
        }
        if (__builtin_expect(early == 0, 1)) break;
        {
            unsigned n = SPIN_CAP; bool bad;
#pragma nounroll
            do {
                asm volatile("" ::: "memory");
#pragma unroll
                for (int i = 0; i < 8; ++i) of[i] = ld16(rw, lo16, obs[i]);
                unsigned m2 = 0;
#pragma unroll
                for (int i = 0; i < 8; ++i) m2 = mx4(m2, of[i]);
                bad = __builtin_amdgcn_ballot_w64(m2 == SENT) != 0;
            } while (bad && --n);
            if (bad) { alive = 0; set_tmo(p); }
            flag = 1;
#pragma unroll
            for (int i = 0; i < 4; ++i) { acc[i >> 1][i & 1] = (f32x4){0.f, 0.f, 0.f, 0.f}; asm volatile("" : "+v"(acc[i >> 1][i & 1])); }
            asm volatile("" ::: "memory");
        }
        }
        mfma_pad(acc[0][0], acc[0][1], acc[1][0], acc[1][1]);
        float ya[4], yb[4], y[4];
#pragma unroll
        for (int r = 0; r < 4; ++r) { ya[r] = g < 2 ? acc[0][0][r] : acc[1][0][r]; yb[r] = g < 2 ? acc[0][1][r] : acc[1][1][r]; }
        SB();
#pragma unroll
        for (int r = 0; r < 4; ++r) y[r] = swap32_add(ya[r], yb[r]);
        const float z0 = swap16_add(y[0], y[2]), z1 = swap16_add(y[1], y[3]);
        *(LAS float*)(lds + rwa) = z0;
        *(LAS float*)(lds + rwa + 128) = z1;
        FT(3);
        if (flag) *(LAS unsigned*)(lds + 4096 + (s & 1) * 4) = s + 1;
        bar();
        FT(4);
        if (w == 0) {
            LAS f32x2* r2 = (LAS f32x2*)(lds + rra);
            const f32x2 a0 = r2[0], a1 = r2[64], a2 = r2[128], a3 = r2[192];
            SB();
            const f32x2 v = (a0 + a1) + (a2 + a3);
            st4(nosent(pk2(v[0], v[1])), rw, so, mb);
            tdone = rt32();
            st4(nosent((tdone & ~1u) | (*(volatile LAS unsigned*)(lds + 4096 + (s & 1) * 4) == (unsigned)(s + 1))), rw, sto, eb + EX_MST);
        } else tdone = rt32();
        FT(5); FDUMP(32 + mi, s, w);
    }
}

constexpr int RING = 4;
constexpr int a_slot(int ks, int tile) { return ks * 16384 + tile * 2048; }
constexpr int a_xblk(int i) { return i < 3 ? a_slot(4, 6) + i * 1280 : a_slot(9, 6); }
constexpr int A_P = a_slot(9, 6) + 1280, A_P2 = A_P + 1024;
template <int W> struct AP {
    static constexpr int ROT = 3 * W, NT = W == 0 ? 0 : (W == 3 ? 2 : 3), T0 = W == 0 ? 0 : 3 * (W - 1);
    static constexpr int NL = W == 0 ? 0 : (W == 3 ? 8 : 13);
    static constexpr int QA = W == 0 ? 20 : (W == 3 ? 18 : 13);
    static constexpr bool k_lds(int t, int m) { return W == 0 ? false : (W == 3 ? (t == 1 && m >= 2) : (t == 1 || m % 3 == 2)); }
    static constexpr bool k_acc(int t, int m) { return W == 0 && m < 5; }
    static constexpr bool v_lds(int t, int ks) { return W == 3 ? ks < 4 : (ks < 4 || (ks == 4 && t == 0)); }
    static constexpr int k_idx(int t, int m) { int n = 0; for (int i = 0; i < 2 * m + t; ++i) n += k_lds(i & 1, i >> 1) ? 1 : 0; return n; }
    static constexpr int k_slot(int i) { return a_slot(5 + i / (NT ? NT : 1), T0 + i % (NT ? NT : 1)); }
    static constexpr int v_slot(int i) { return a_slot(i / (NT ? NT : 1), T0 + i % (NT ? NT : 1)); }
    static constexpr int stream(int i) { return i < NL ? k_slot(i) : v_slot(i - NL); }
    static constexpr int m_of(int ks) { return (ks + 10 - ROT % 10) % 10; }
};
DI void a_dma(const uchar* wsrc, LAS uchar* lds, int h, int w) {
    const int lane = fresh_lane();
    const rsrc_t r = mk_rsrc(wsrc, 640 * 8192);
    const unsigned v = (lane >> 4) * 8192 + (((lane & 15) ^ (2 * (((lane >> 4) & 1) | (w << 1)))) << 4);
    const unsigned lb = (unsigned)(size_t)lds;
#pragma unroll
    for (int i = 0; i < 40; ++i) dma16(r, v, (4 * (w + 4 * i)) * 8192 + h * 256, lb + (w + 4 * i) * 1024);
    wait_vm<0>();
    bar();
}
struct ATake {
    unsigned row[4], f[4];
    DI ATake() {
        const int lane = fresh_lane(), c = lane & 15, g = lane >> 4, q = c >> 2, pp = c & 3;
#pragma unroll
        for (int m = 0; m < 4; ++m) {
            const int k0 = m < 2 ? 8 * g + 4 * m : 32 + 8 * g + 4 * (m - 2), ga = k0 >> 4, qq = (k0 >> 2) & 3;
            const int r = 16 * ga + 2 * qq + (q & 1) + 8 * (q >> 1);
            row[m] = r * 256 + pp * 8; f[m] = 32 * ((r & 1) | ((r >> 1) & 6));
        }
    }
    DI u32x8 get(LAS uchar* lds, int ks, int tile) const {
        LAS uchar* a = lds + ks * 16384;
        const u32x2 t0 = trd(a + row[0] + (f[0] ^ (32 * tile))), t1 = trd(a + row[1] + (f[1] ^ (32 * tile))), t2 = trd(a + row[2] + (f[2] ^ (32 * tile))), t3 = trd(a + row[3] + (f[3] ^ (32 * tile)));
        return (u32x8){t0.x, t0.y, t1.x, t1.y, t2.x, t2.y, t3.x, t3.y};
    }
};
DI u32x8 ldsB(LAS uchar* a) { const u32x4 lo = *(LAS u32x4*)a, hi = *(LAS u32x4*)(a + 1024); return (u32x8){lo.x, lo.y, lo.z, lo.w, hi.x, hi.y, hi.z, hi.w}; }
DI void ldsB_put(LAS uchar* a, u32x8 v) { *(LAS u32x4*)a = (u32x4){v[0], v[1], v[2], v[3]}; *(LAS u32x4*)(a + 1024) = (u32x4){v[4], v[5], v[6], v[7]}; }
DI void pin(u32x8& v, bool acc) { if (acc) asm volatile("" : "+a"(v)); else asm volatile("" : "+v"(v)); }
DI void smf_x(f32x4& c, u32x4 a, u32x8 b, int idx, bool acc) { if (acc) smf_a(c, a, b, idx); else smf_v(c, a, b, idx); }

#define NEXT_KV() do { SB(); const int sn_ = min(s + 1, NSEG - 1); if ((sn_ & 7) == 0) wait_ctr(p, rw, C_KV + ((sn_ >> 3) * 32 + h) * 16, 4); \
    _Pragma("unroll") for (int i = 0; i < NPF; ++i) pf[i] = ld16(rw, lo16, pfb + sn_ * 32 * 8192 + i * 1024); SB(); } while (0)
template <int W>
DI void a_loop(const Params& p, LAS uchar* lds, int h, const u32x8 (&wq)[2][10], const u32x8 (&wk)[2][10], const u32x8 (&wv)[3][10]) {
    typedef AP<W> P;
    const int lane = fresh_lane(), c = lane & 15, g = lane >> 4;
    const rsrc_t rw = mk_rsrc(p.ws, WS_USED);
    constexpr int ROT = P::ROT, NT = P::NT, T0 = P::T0, NPF = W == 0 ? 8 : NT, NL = P::NL;
    const int idx = (c & 8) ? 0xEEEE : 0x4444;
    const unsigned lo16 = lane * 16, mo16 = (g * 16 + ((c >> 2) & 1) * 8 + (c >> 3) * 4 + (c & 3)) * 16;
    const unsigned oaddr = W == 0 ? (c >= 8 ? (g * 8 + (c & 7)) * 16 : OOB) : (c < 8 ? (c >> 2) * 1024 + (g >> 1) * 64 + (c & 3) * 16 + (g & 1) * 8 : OOB);
    const unsigned oaddr2 = c < 8 ? (c >> 2) * 1024 + (g & 1) * 128 + (g >> 1) * 64 + (c & 3) * 16 : OOB;
    const unsigned xw = a_xblk(W) + c * 40 + g * 8;
    const unsigned asto = lane == 0 ? h * 4 : OOB;
    const unsigned pfb = W == 0 ? OFF_KF + h * 8192 : OFF_VF + h * 8192 + T0 * 1024;
    u32x4 pf[NPF];
    wait_ctr(p, rw, C_KV + h * 16, 4);
#pragma unroll
    for (int i = 0; i < NPF; ++i) pf[i] = ld16(rw, lo16, pfb + i * 1024);
    unsigned long long alive = ~0ull;
    const unsigned exo = ex_off(p), pol = read_pol(p);
    Sched sc = {rt32(), 0u, DUR_A0, DUR_M0, PAD0, 0u, 0u};
    const float ninf = g < 2 ? -__builtin_inff() : 0.f;
    unsigned xra[4], xqa[4];
    if (W == 0) {
#pragma unroll
        for (int ks = 0; ks < 4; ++ks) {
            xra[ks] = a_xblk(ks) + (4 * g + (c >> 2)) * 40 + (c & 3) * 8; xqa[ks] = a_xblk(ks) + (4 * g + (c >> 2)) * 40 + (c & 1) * 8;
            asm volatile("" : "+v"(xra[ks]), "+v"(xqa[ks]));
        }
    }
    unsigned lb[3] = {lo16, lo16 + 65536, lo16 + 131072};
    asm volatile("" : "+v"(lb[0]), "+v"(lb[1]), "+v"(lb[2]));
    auto sget = [&](int i) -> u32x8 { const int a = P::stream(i); return ldsB(lds + lb[a >> 16] + (a & 65535)); };
    for (int s = 0; s < NSEG; ++s) {
        const unsigned eb = ex_step(exo, pol, s);
        unsigned mb[10], ob = eb + (h >> 2) * 16384 + (h & 3) * 2048 + T0 * 128;
#pragma unroll
        for (int m = 0; m < 10; ++m) { mb[m] = eb + mf_off((m + ROT) % 10); asm volatile("" : "+s"(mb[m])); }
        f32x4 aq[2], ak[2], va[3];
        u32x8 ring[RING];
#pragma unroll
        for (int t = 0; t < 2; ++t) { aq[t] = (f32x4){0.f, 0.f, 0.f, 0.f}; ak[t] = aq[t]; asm volatile("" : "+v"(aq[t]), "+v"(ak[t])); }
#pragma unroll
        for (int t = 0; t < 3; ++t) {
            const float z = W == 0 && t == 2 ? ninf : 0.f;
            va[t] = (f32x4){z, z, z, z}; asm volatile("" : "+v"(va[t]));
        }
        if (W) {
#pragma unroll
            for (int i = 0; i < RING; ++i) ring[i] = sget(i);
        }
        asm volatile("" : "+s"(ob));
        SB();
        const unsigned tgo = sc.tgo;
        unsigned tis = waitrt(tgo);
        FT(0);
        u32x4 mf[10];
        for (;;) {
#pragma unroll
            for (int m = 0; m < 10; ++m) {
                mf[m] = ld16(rw, mo16, mb[m]);
                SB();
                if (m < 9) pace();
                SB();
            }
            unsigned mx = 0;
            unsigned long long early = 0;
#pragma unroll
            for (int m = 0; m < 10; ++m) {
                if (m == 9) {
                    mx4v(mx, mf[m]);
                    early = __builtin_amdgcn_ballot_w64(mx == SENT) & alive;
                    asm volatile("" : "+s"(early));
                    SB();
                }
#pragma unroll
                for (int t = 0; t < 2; ++t) {
                    smf_x(aq[t], mf[m], wq[t][m], idx, 2 * m + t < P::QA - (W == 0 ? 10 : 0));
                    if (P::k_lds(t, m)) {
                        constexpr int dummy = 0; (void)dummy;
                        const int i = P::k_idx(t, m);
                        smf_v(ak[t], mf[m], ring[i % RING], idx);
                        ring[i % RING] = sget(i + RING);
                    } else smf_x(ak[t], mf[m], wk[t][m], idx, W == 0);
                }
                if (m < 9) mx4v(mx, mf[m]);
                if (m == 0) FT(1);
                if (m == 9) FT(2);
                SB();
            }
            if (__builtin_expect(early == 0, 1)) break;
            unsigned n = SPIN_CAP; bool bad;
#pragma nounroll
            do {
                asm volatile("" ::: "memory");
#pragma unroll
                for (int m = 0; m < 10; ++m) mf[m] = ld16(rw, mo16, mb[m]);
                unsigned m2 = 0;
#pragma unroll
                for (int m = 0; m < 10; ++m) m2 = mx4(m2, mf[m]);
                bad = __builtin_amdgcn_ballot_w64(m2 == SENT) != 0;
            } while (bad && --n);
            if (bad) { alive = 0; set_tmo(p); }
            tis = tgo + 1000;
#pragma unroll
            for (int t = 0; t < 2; ++t) { aq[t] = (f32x4){0.f, 0.f, 0.f, 0.f}; ak[t] = aq[t]; asm volatile("" : "+v"(aq[t]), "+v"(ak[t])); }
            if (W) {
#pragma unroll
                for (int i = 0; i < RING; ++i) ring[i] = sget(i);
            }
            asm volatile("" ::: "memory");
        }
        mfma_pad(aq[0], ak[0], aq[1], ak[1]);
#pragma unroll
        for (int t = 0; t < 2; ++t) {
            float x[4];
#pragma unroll
            for (int r = 0; r < 4; ++r) x[r] = swap32_add(aq[t][r], ak[t][r]);
            *(LAS u32x2*)(lds + xw + t * 640) = (u32x2){pk2(x[0], x[1]), pk2(x[2], x[3])};
        }
        bar();
        SB();
        FT(3);
        const unsigned kb = OFF_KM + (s * 32 + h) * 4096;
        unsigned tpub = 0;
        if (W == 0) {
            u32x4 xf[4], xd[4];
#pragma unroll
            for (int ks = 0; ks < 4; ++ks) {
                const u32x2 a = trd(lds + xqa[ks]), b = trd(lds + xqa[ks] + 640), lo = trd(lds + xra[ks]), hi = trd(lds + xra[ks] + 640);
                xd[ks] = (u32x4){a.x, a.y, b.x, b.y}; xf[ks] = (u32x4){lo.x, lo.y, hi.x, hi.y};
            }
            SB();
#pragma unroll
            for (int ks = 0; ks < 4; ++ks) { mfma_v(va[0], pf[ks], xd[ks]); mfma_v(va[1], pf[(4 + ks) % NPF], xd[ks]); mfma_v(va[2], xf[ks], xd[ks]); }
            FT(4);
            float a0, a1, a2, a3, a4, a5, tm, tn;
            unsigned e0, e1, e2;
            asm volatile("s_nop 7\n\t"
                         "v_cndmask_b32_e64 %0, %15, %11, %23\n\tv_cndmask_b32_e64 %1, %16, %12, %23\n\tv_cndmask_b32_e64 %2, %17, %13, %23\n\t"
                         "v_cndmask_b32_e64 %3, %18, %14, %23\n\tv_cndmask_b32_e64 %4, %21, %19, %23\n\tv_cndmask_b32_e64 %5, %22, %20, %23\n\t"
                         "v_max3_f32 %6, %0, %1, %2\n\tv_max3_f32 %7, %3, %4, %5\n\tv_max_f32 %6, %6, %7\n\ts_nop 1\n\t"
                         "v_max_f32_dpp %6, %6, %6 row_ror:8 row_mask:0xf bank_mask:0xf\n\t"
                         "v_mov_b32 %7, %6\n\ts_nop 1\n\tv_permlane32_swap_b32 %6, %7\n\tv_max_f32 %6, %6, %7\n\t"
                         "v_mov_b32 %7, %6\n\ts_nop 1\n\tv_permlane16_swap_b32 %6, %7\n\tv_max_f32 %6, %6, %7\n\t"
                         "v_mul_f32 %6, %24, %6\n\t"
                         "v_fma_f32 %0, %0, %24, -%6\n\tv_fma_f32 %1, %1, %24, -%6\n\tv_fma_f32 %2, %2, %24, -%6\n\t"
                         "v_fma_f32 %3, %3, %24, -%6\n\tv_fma_f32 %4, %4, %24, -%6\n\tv_fma_f32 %5, %5, %24, -%6\n\t"
                         "v_exp_f32 %0, %0\n\tv_exp_f32 %1, %1\n\tv_exp_f32 %2, %2\n\tv_exp_f32 %3, %3\n\tv_exp_f32 %4, %4\n\tv_exp_f32 %5, %5\n\t"
                         "v_cvt_pk_bf16_f32 %8, %0, %1\n\tv_cvt_pk_bf16_f32 %9, %2, %3\n\tv_cvt_pk_bf16_f32 %10, %4, %5"
                         : "=&v"(a0), "=&v"(a1), "=&v"(a2), "=&v"(a3), "=&v"(a4), "=&v"(a5), "=&v"(tm), "=&v"(tn), "=&v"(e0), "=&v"(e1), "=&v"(e2)
                         : "v"(va[0][0]), "v"(va[0][1]), "v"(va[0][2]), "v"(va[0][3]), "v"(va[1][0]), "v"(va[1][1]), "v"(va[1][2]), "v"(va[1][3]),
                           "v"(va[2][0]), "v"(va[2][1]), "v"(va[2][2]), "v"(va[2][3]), "s"(0x00FF00FF00FF00FFull), "s"(SCL));
            *(LAS u32x2*)(lds + A_P + (g * 16 + (c & 7)) * 16 + (c >> 3) * 8) = (u32x2){e0, e1};
            *(LAS unsigned*)(lds + A_P2 + (g * 16 + (c & 7)) * 8 + (c >> 3) * 4) = e2;
            FT(5);
            bar();
            FT(6);
            sc.fetch(rw, exo, pol, max(s, 1), lane);
            NEXT_KV();
            waitrt(rt32() + KM_DELAY);
            st4(s, rw, lane == 0 ? (C_KM + h * 32) * 4 : OOB, 0);
#pragma unroll
            for (int ks = 0; ks < 4; ++ks) st16(xf[ks], rw, oaddr, kb + ks * 512);
        } else {
            sc.fetch(rw, exo, pol, max(s, 1), lane);
            SB();
#pragma unroll
            for (int i = 0; i < 10 * NT - NL; ++i) {
                if (i < NL) {
                    smf_v(va[i % NT], mf[P::m_of(i / NT)], ring[(NL + i) % RING], idx);
                    if (NL + i + RING < 2 * NL) ring[(NL + i) % RING] = sget(NL + i + RING);
                }
                const int r = NL + i;
                smf_a(va[r % NT], mf[P::m_of(r / NT)], wv[r % NT][r / NT], idx);
                SB();
            }
            asm volatile("s_nop 7" : "+v"(va[0]), "+v"(va[1]), "+v"(va[2]));
            float sa[4], sb[4];
#pragma unroll
            for (int r = 0; r < 4; ++r) { sa[r] = swap32_add(va[0][r], va[1][r]); if (W != 3) sb[r] = swap32_add(va[2][r], va[2][r]); }
            u32x4 v01 = {pk2(sa[0], sa[1]), pk2(sa[2], sa[3]), 0u, 0u}, v22 = v01;
            if (W != 3) v22 = (u32x4){pk2(sb[0], sb[1]), pk2(sb[2], sb[3]), 0u, 0u};
            u32x4 one4 = {0x3F803F80u, 0x3F803F80u, 0x3F803F80u, 0x3F803F80u}, p2 = {0u, 0u, 0u, 0u}, p2s = p2;
            f32x4 l = {0.f, 0.f, 0.f, 0.f}, o[3] = {l, l, l};
            asm volatile("" : "+v"(v01), "+v"(v22), "+v"(one4), "+v"(p2), "+v"(p2s), "+v"(l), "+v"(o[0]), "+v"(o[1]), "+v"(o[2]));
            SB();
            FT(5);
            bar();
            FT(6);
            SB();
            if (s < NSEG - 1) {
                const u32x4 p01 = *(LAS u32x4*)(lds + A_P + lane * 16);
                const u32x2 q2 = *(LAS u32x2*)(lds + A_P2 + lane * 8), q2s = *(LAS u32x2*)(lds + A_P2 + (lane ^ 32) * 8);
                p2.x = q2.x; p2.y = q2.y; p2s.x = q2s.x; p2s.y = q2s.y;
                SB();
                mfma_v(l, one4, p01);
                mfma_v(o[0], pf[0], p01);
                mfma_v(l, one4, p2);
                mfma_v(o[1], pf[1 % NPF], p01);
                mfma_v(o[0], v01, p2s);
                mfma_v(o[1], v01, p2);
                float inv;
                if (W != 3) {
                    mfma_v(o[2], pf[2 % NPF], p01);
                    asm volatile("v_rcp_f32 %0, %1" : "=v"(inv) : "v"(l[0]));
                    mfma_v(o[2], v22, p2);
                    asm volatile("s_nop 3" : "+v"(o[0]), "+v"(o[1]), "+v"(inv));
                } else {
                    asm volatile("s_nop 1\n\tv_rcp_f32 %0, %3\n\ts_nop 5" : "=&v"(inv), "+v"(o[0]), "+v"(o[1]) : "v"(l[0]));
                }
                const f32x4 n0 = o[0] * inv, n1 = o[1] * inv;
                SB();
                const unsigned a0 = pk2(n0[0], n0[1]), b0 = pk2(n1[0], n1[1]), a1 = pk2(n0[2], n0[3]), b1 = pk2(n1[2], n1[3]);
                SB();
                auto r0 = __builtin_amdgcn_permlane16_swap(a0, b0, false, false);
                auto r1 = __builtin_amdgcn_permlane16_swap(a1, b1, false, false);
                st16((u32x4){nosent(r0[0]), nosent(r1[0]), nosent(r0[1]), nosent(r1[1])}, rw, oaddr2, ob);
                if (W != 3) {
                    asm volatile("" : "+v"(o[2]));
                    const f32x4 n2 = o[2] * inv;
                    st8((u32x2){nosent(pk2(n2[0], n2[1])), nosent(pk2(n2[2], n2[3]))}, rw, oaddr, ob + 256);
                }
            }
            tpub = rt32();
            NEXT_KV();
            if (W == 1) st4(tis - tgo <= 4u ? tpub - tgo : 0u, rw, asto, eb + EX_AST);
            st8((u32x2){v01.x, v01.y}, rw, lane * 8, kb + 2048 + T0 * 256);
            if (W != 3) st8((u32x2){v22.x, v22.y}, rw, g < 2 ? lane * 8 : OOB, kb + 2048 + T0 * 256 + 512);
        }
        FT(7); FDUMP(h, s, W);
        STAT(h, s, W, tis, tpub, tgo, tries);
        if (s > 0) sc.next(p, rw, exo, pol, s, lane, alive);
    }
    wait_vm<0>();
    bar();
    if (W == 0) st4(NSEG, rw, lane == 0 ? (C_KM + h * 32) * 4 : OOB, 0);
    __builtin_amdgcn_endpgm();
}

template <int W>
DI void a_wave(const Params& p, LAS uchar* lds, int h) {
    typedef AP<W> P;
    u32x8 wq[2][10], wk[2][10], wv[3][10];
    a_dma(p.wq, lds, h, W);
    {
        const ATake tk;
#pragma unroll
        for (int m = 0; m < 10; ++m) {
#pragma unroll
            for (int t = 0; t < 2; ++t) { wq[t][m] = tk.get(lds, (m + P::ROT) % 10, 2 * W + t); pin(wq[t][m], 2 * m + t < P::QA - (W == 0 ? 10 : 0)); }
            if (m & 1) SB();
        }
        bar(); SB();
    }
    a_dma(p.wk, lds, h, W);
    {
        const ATake tk;
#pragma unroll
        for (int m = 0; m < 10; ++m) {
#pragma unroll
            for (int t = 0; t < 2; ++t) { wk[t][m] = tk.get(lds, (m + P::ROT) % 10, 2 * W + t); pin(wk[t][m], W == 0); }
            if (m & 1) SB();
        }
        bar(); SB();
    }
    a_dma(p.wv, lds, h, W);
#pragma unroll
    for (int half = 0; half < 2; ++half) {
        const ATake tk;
#pragma unroll
        for (int ks = 5 * half; ks < 5 * half + 5; ++ks) {
#pragma unroll
            for (int t = 0; t < P::NT; ++t) { wv[t][ks] = tk.get(lds, ks, P::T0 + t); pin(wv[t][ks], true); }
            SB();
        }
        bar(); SB();
        const int lane = fresh_lane();
        if (half == 0) {
#pragma unroll
            for (int i = 0; i < P::NL; ++i) ldsB_put(lds + P::v_slot(i) + lane * 16, wv[i % (P::NT ? P::NT : 1)][i / (P::NT ? P::NT : 1)]);
        } else {
#pragma unroll
            for (int i = 0; i < 20; ++i) if (P::k_lds(i & 1, i >> 1)) ldsB_put(lds + P::k_slot(P::k_idx(i & 1, i >> 1)) + lane * 16, wk[i & 1][i >> 1]);
        }
        SB();
    }
    bar();
    SB();
    a_loop<W>(p, lds, h, wq, wk, wv);
}
DI void role_A(const Params& p, LAS uchar* lds, int h) {
    const int w = __builtin_amdgcn_readfirstlane(threadIdx.x >> 6);
    if (w == 0) a_wave<0>(p, lds, h);
    else if (w == 1) a_wave<1>(p, lds, h);
    else if (w == 2) a_wave<2>(p, lds, h);
    else a_wave<3>(p, lds, h);
}
DI int take_slot(const Params& p, LAS uchar* lds) {
    if (threadIdx.x == 0) {
        unsigned x = xcc_id(), t;
        for (int i = 0; i < 8; ++i) {
            t = __hip_atomic_fetch_add((unsigned*)p.ws + C_POOL + x * 32, 1u, __ATOMIC_RELAXED, __HIP_MEMORY_SCOPE_AGENT);
            if (t < 32) break;
            x = (x + 1) & 7;
        }
        *(LAS unsigned*)lds = x * 32 + t;
    }
    bar();
    const int slot = __builtin_amdgcn_readfirstlane(*(LAS unsigned*)lds);
    bar();
    return slot;
}
DI int take_ticket(const Params& p, LAS uchar* lds, int word) {
    if (fresh_tid() == 0) *(LAS unsigned*)(lds + LDS_BYTES - 16) = __hip_atomic_fetch_add((unsigned*)p.ws + word, 1u, __ATOMIC_RELAXED, __HIP_MEMORY_SCOPE_AGENT);
    bar();
    const int t = __builtin_amdgcn_readfirstlane(*(LAS unsigned*)(lds + LDS_BYTES - 16));
    bar();
    return t;
}
#define TSTAT(i, a, b)
constexpr int N_T = 24, N_WG = 5;
DI void background(const Params& p, LAS uchar* lds, int x) {
    const bool big = x >= 4;
    const int nh = big ? 6 : 2, h0 = big ? 8 + 6 * (x - 4) : 2 * x, nq = big ? 3 : 1, q0 = big ? 4 + 3 * (x - 4) : x;
    const int e0 = 4 * nh, e1 = e0 + 7 * nh, e2 = e1 + (x < 7 ? 8 : 0), e3 = e2 + 8 * nq, e4 = e3 + (big ? 8 : 2);
    for (;;) {
        const int t = take_ticket(p, lds, C_Q1 + x * 32);
        if (t >= e4) break;
        if (t < e3) {
            int chunk, u0, nu = 4;
            if (t < e0) { chunk = 0; u0 = 4 * h0 + t; nu = 1; }
            else if (t < e1) { const int i = big ? ((t - e0) * 43) >> 8 : (t - e0) >> 1; chunk = 1 + i; u0 = 4 * (h0 + (t - e0) - nh * i); }
            else if (t < e2) { chunk = t - e1; u0 = 192 + 4 * x; if (x == 6) nu = 3; }
            else { chunk = (t - e2) & 7; u0 = 128 + 4 * (q0 + ((t - e2) >> 3)); }
            p1_task(p, lds, chunk, u0, nu);
            TSTAT(x * 128 + t, t0, u0 < 128 ? (nu == 1 ? 0 : 1) : (u0 < 192 ? 2 : 3));
        } else {
            down_task(p, lds, big ? t - e3 : 2 * x + t - e3, big ? x - 4 : 4);
            TSTAT(x * 128 + t, t0, 4);
        }
    }
    if (!big) return;
    const int t = take_ticket(p, lds, C_P2);
    if (t >= N_T + 20 * N_WG) return;
    if (t < N_T) {
        const int w = __builtin_amdgcn_readfirstlane(threadIdx.x >> 6);
        for (int s = t >> 3; s < NSEG; s += N_T / 8) attn_task(p, s, (t & 7) * 4 + w);
    } else { const int gq = ((t - N_T) * 205) >> 12; w_role(p, lds, (t - N_T) - 20 * gq, gq, N_WG); }
    TSTAT(1024 + t, t0, t < N_T ? 5 : 6);
}
__global__ void __launch_bounds__(256, 1) k_main(Params p) {
    extern __shared__ __attribute__((aligned(16))) uchar smem[];
    const int slot = take_slot(p, (LAS uchar*)smem), x = slot >> 5, t = slot & 31;
    if (x < 4 && t < 8) role_A(p, (LAS uchar*)smem, x * 8 + t);
    else if (x < 4 && t < 18) role_M(p, (LAS uchar*)smem, x * 10 + t - 8);
    else background(p, (LAS uchar*)smem, x);
}

extern "C" void kernel_launch(void* const* d_in, const int* in_sizes, int n_in, void* d_out, int out_size, void* d_ws, size_t ws_size, hipStream_t stream) {
    Params p;
    p.x = (const uchar*)d_in[0]; p.mem0 = (const uchar*)d_in[1]; p.wq = (const uchar*)d_in[2]; p.wk = (const uchar*)d_in[3];
    p.wv = (const uchar*)d_in[4]; p.wo = (const uchar*)d_in[5]; p.w1 = (const uchar*)d_in[8]; p.b1 = (const uchar*)d_in[9];
    p.w2 = (const uchar*)d_in[10]; p.out = (uchar*)d_out; p.ws = (uchar*)d_ws;
    (void)hipFuncSetAttribute((const void*)k_main, hipFuncAttributeMaxDynamicSharedMemorySize, LDS_BYTES);
    k_init<<<256, 256, 0, stream>>>(p);
    k_main<<<256, 256, LDS_BYTES, stream>>>(p);
}
```

```cpp
#include <hip/hip_runtime.h>
#include <stdint.h>
#define NOWARM

typedef __bf16 bf16x2 __attribute__((ext_vector_type(2)));
typedef __bf16 bf16x4 __attribute__((ext_vector_type(4)));
typedef __bf16 bf16x8 __attribute__((ext_vector_type(8)));
typedef __bf16 bf16x16 __attribute__((ext_vector_type(16)));
typedef short s16x4 __attribute__((ext_vector_type(4)));
typedef float f32x2 __attribute__((ext_vector_type(2)));
typedef float f32x4 __attribute__((ext_vector_type(4)));
typedef unsigned u32x2 __attribute__((ext_vector_type(2)));
typedef unsigned u32x4 __attribute__((ext_vector_type(4)));
typedef unsigned u32x8 __attribute__((ext_vector_type(8)));
typedef unsigned char uchar;
typedef __amdgpu_buffer_rsrc_t rsrc_t;
#define LAS __attribute__((address_space(3)))
#define DI __device__ __forceinline__

constexpr int S_ = 2048, D_ = 640, H_ = 32, NSEG = 64, F_ = 1728;
constexpr int LDS_BYTES = 163840;
constexpr int OFF_CTRL = 0;
constexpr int OFF_XF = 65536;
constexpr int OFF_KF = OFF_XF + 128 * 20 * 1024;
constexpr int OFF_QF = OFF_KF + 64 * 32 * 8192;
constexpr int OFF_VF = OFF_QF + 64 * 32 * 8192;
constexpr int OFF_ACT = OFF_VF + 64 * 32 * 8192;
constexpr int OFF_GF = OFF_ACT + 128 * 54 * 1024;
constexpr int OFF_OT = OFF_GF + 128 * 20 * 1024;
constexpr int OFF_KM = OFF_OT + 128 * 32 * 4096;
constexpr int OFF_EX = OFF_KM + 64 * 32 * 4096;
constexpr int EX_STEP = 163840, EX_MF = 8 * 16384, EX_AST = 9 * 16384 + 2048, EX_MST = 9 * 16384 + 2304, EX_PROBE = 9 * 16384 + 6400;
constexpr int WS_USED = OFF_EX + 7 * 2097152;
constexpr int C_TMO = 0, C_POOL = 32  , C_KV = 512  , C_Q = C_KV + 4096, C_ACT = C_Q + 4096  , C_G = C_ACT + 256  ,
              C_OT = C_G + 1280  , C_KM = C_OT + 2048  , C_Q1 = C_KM + 1024  , C_P2 = C_Q1 + 256, C_KVH = C_P2 + 32  , C_X = C_KVH + 512  , C_PZ = C_X + 512  ,
              C_POL = 16320  ;
static_assert(C_PZ + 128 <= C_POL, "");
constexpr unsigned SENT = 0xFFFFFFFFu;
constexpr int SC1 = 16;
constexpr unsigned OOB = 0x7FFFFF00u;
constexpr unsigned SPIN_CAP = 400000;

struct Params {
    const uchar* x; const uchar* mem0; const uchar* wq; const uchar* wk; const uchar* wv; const uchar* wo;
    const uchar* w1; const uchar* b1; const uchar* w2; uchar* out; uchar* ws;
};

DI rsrc_t mk_rsrc(const void* p, unsigned bytes) { return __builtin_amdgcn_make_buffer_rsrc((void*)p, 0, bytes, 0x00020000); }
DI u32x4 ld16(rsrc_t r, unsigned v, unsigned s) { return __builtin_amdgcn_raw_buffer_load_b128(r, v, s, SC1); }
DI u32x2 ld8(rsrc_t r, unsigned v, unsigned s) { return __builtin_amdgcn_raw_buffer_load_b64(r, v, s, SC1); }
DI unsigned ld4(rsrc_t r, unsigned v, unsigned s) { return __builtin_amdgcn_raw_buffer_load_b32(r, v, s, SC1); }
DI void st16(u32x4 d, rsrc_t r, unsigned v, unsigned s) { __builtin_amdgcn_raw_buffer_store_b128(d, r, v, s, SC1); asm volatile("s_nop 1" :: "v"(d)); }
DI void st8(u32x2 d, rsrc_t r, unsigned v, unsigned s) { __builtin_amdgcn_raw_buffer_store_b64(d, r, v, s, SC1); }
DI void st4(unsigned d, rsrc_t r, unsigned v, unsigned s) { __builtin_amdgcn_raw_buffer_store_b32(d, r, v, s, SC1); }
DI unsigned pk2(float a, float b) { f32x2 v = {a, b}; return __builtin_bit_cast(unsigned, __builtin_convertvector(v, bf16x2)); }
DI unsigned nosent(unsigned v) { return min(v, 0xFFFFFFFEu); }
DI float bf_lo(unsigned v) { return __builtin_bit_cast(float, v << 16); }
DI float bf_hi(unsigned v) { return __builtin_bit_cast(float, v & 0xFFFF0000u); }
DI float rbf(float a) { return bf_lo(pk2(a, a)); }
DI bf16x8 as8(u32x4 v) { return __builtin_bit_cast(bf16x8, v); }
DI f32x4 mfma32(u32x4 a, u32x4 b, f32x4 c) { return __builtin_amdgcn_mfma_f32_16x16x32_bf16(as8(a), as8(b), c, 0, 0, 0); }
DI f32x4 mfma16(u32x2 a, u32x2 b, f32x4 c) { return mfma32((u32x4){a.x, a.y, 0u, 0u}, (u32x4){b.x, b.y, 0u, 0u}, c); }
DI f32x4 smf(u32x4 a, u32x8 b, f32x4 c, int idx) { return __builtin_amdgcn_smfmac_f32_16x16x64_bf16(as8(a), __builtin_bit_cast(bf16x16, b), c, idx, 0, 0); }
DI void smf_a(f32x4& c, u32x4 a, u32x8 b, int idx) { asm volatile("v_smfmac_f32_16x16x64_bf16 %0, %1, %2, %3" : "+v"(c) : "v"(a), "a"(b), "v"(idx)); }
DI void smf_v(f32x4& c, u32x4 a, u32x8 b, int idx) { asm volatile("v_smfmac_f32_16x16x64_bf16 %0, %1, %2, %3" : "+v"(c) : "v"(a), "v"(b), "v"(idx)); }
DI void mfma_v(f32x4& c, u32x4 a, u32x4 b) { asm volatile("v_mfma_f32_16x16x32_bf16 %0, %1, %2, %0" : "+v"(c) : "v"(a), "v"(b)); }
DI void mfma_0(f32x4& c, u32x4 a, u32x4 b) { asm volatile("v_mfma_f32_16x16x32_bf16 %0, %1, %2, 0" : "=&v"(c) : "v"(a), "v"(b)); }
DI float fmax2(float a, float b) { return __builtin_amdgcn_fmed3f(a, b, __builtin_inff()); }
DI void mfma_pad(f32x4& a, f32x4& b, f32x4& c, f32x4& d) { asm volatile("s_nop 7" : "+v"(a), "+v"(b), "+v"(c), "+v"(d)); }
DI u32x2 trd(LAS uchar* p) { return __builtin_bit_cast(u32x2, __builtin_amdgcn_ds_read_tr16_b64_v4i16((LAS s16x4*)p)); }
DI unsigned mx4(unsigned m, u32x4 v) { return max(max(max(m, v.x), max(v.y, v.z)), v.w); }
DI void mx4v(unsigned& m, u32x4 v) { asm volatile("v_max3_u32 %0, %0, %1, %2\n\tv_max3_u32 %0, %0, %3, %4" : "+v"(m) : "v"(v.x), "v"(v.y), "v"(v.z), "v"(v.w)); }
DI unsigned rt32() { unsigned long long t; asm volatile("s_memrealtime %0\n\ts_waitcnt lgkmcnt(0)" : "=s"(t) :: "memory"); return (unsigned)t; }
DI unsigned waitrt(unsigned t) { unsigned n; do n = rt32(); while ((int)(n - t) < 0); return n; }
DI void bar() { asm volatile("s_waitcnt lgkmcnt(0)" ::: "memory"); __builtin_amdgcn_s_barrier(); asm volatile("" ::: "memory"); }
template <int N> DI void wait_vm() { asm volatile("s_waitcnt vmcnt(%0)" :: "n"(N) : "memory"); }
DI void dma16(rsrc_t r, unsigned voff, unsigned soff, unsigned ldsaddr) {
    asm volatile("s_mov_b32 m0, %0\n\ts_nop 0\n\tbuffer_load_dwordx4 %1, %2, %3 offen lds" :: "s"(ldsaddr), "v"(voff), "s"(r), "s"(soff) : "memory");
}
DI float swap32_add(float a, float b) { auto r = __builtin_amdgcn_permlane32_swap(__builtin_bit_cast(unsigned, a), __builtin_bit_cast(unsigned, b), false, false);
    return __builtin_bit_cast(float, (unsigned)r[0]) + __builtin_bit_cast(float, (unsigned)r[1]); }
DI float swap16_add(float a, float b) { auto r = __builtin_amdgcn_permlane16_swap(__builtin_bit_cast(unsigned, a), __builtin_bit_cast(unsigned, b), false, false);
    return __builtin_bit_cast(float, (unsigned)r[0]) + __builtin_bit_cast(float, (unsigned)r[1]); }
DI float grp_max(float m) {
    auto r = __builtin_amdgcn_permlane32_swap(__builtin_bit_cast(unsigned, m), __builtin_bit_cast(unsigned, m), false, false);
    m = fmax2(__builtin_bit_cast(float, (unsigned)r[0]), __builtin_bit_cast(float, (unsigned)r[1]));
    auto q = __builtin_amdgcn_permlane16_swap(__builtin_bit_cast(unsigned, m), __builtin_bit_cast(unsigned, m), false, false);
    return fmax2(__builtin_bit_cast(float, (unsigned)q[0]), __builtin_bit_cast(float, (unsigned)q[1]));
}
DI int fresh_lane() { int l = __builtin_amdgcn_mbcnt_hi(~0u, __builtin_amdgcn_mbcnt_lo(~0u, 0u)); asm volatile("" : "+v"(l)); return l & 63; }
DI unsigned fresh_tid() { unsigned t = threadIdx.x; asm volatile("" : "+v"(t)); return t; }
DI unsigned ex_off(const Params& p) { return OFF_EX + ((0u - (unsigned)(size_t)(p.ws + OFF_EX)) & 0x1FFFFFu); }
DI unsigned ex_step(unsigned exo, unsigned pol, int s) { const int r = (s * 43) >> 9; return exo + r * 2097152 + (s - 12 * r) * EX_STEP + ((pol >> r) & 1) * 8192; }
DI constexpr unsigned mf_off(int ks) { return EX_MF + (ks >> 3) * 16384 + (ks & 7) * 1024; }
DI unsigned xcc_id() { return (unsigned)__builtin_amdgcn_s_getreg(20 | (3 << 11)) & 7u; }
DI unsigned mt32() { unsigned long long t; asm volatile("s_memtime %0\n\ts_waitcnt lgkmcnt(0)" : "=s"(t) :: "memory"); return (unsigned)t; }
DI unsigned read_pol(const Params& p) {
    const int lane = threadIdx.x & 63;
    const unsigned v = ((const unsigned*)p.ws)[C_POL + min(lane, 47)];
    const unsigned long long m1 = __builtin_amdgcn_ballot_w64(v == 2), m0 = __builtin_amdgcn_ballot_w64(v == 1);
    unsigned pol = 0;
#pragma unroll
    for (int r = 0; r < 6; ++r) pol |= (__builtin_popcountll((m1 >> (8 * r)) & 255) > __builtin_popcountll((m0 >> (8 * r)) & 255) ? 1u : 0u) << r;
    return pol;
}
DI void set_tmo(const Params& p) { __hip_atomic_store((unsigned*)p.ws + C_TMO, 1u, __ATOMIC_RELAXED, __HIP_MEMORY_SCOPE_AGENT); }

DI float row16_sum(float v) {
    v += __builtin_bit_cast(float, __builtin_amdgcn_update_dpp(0, __builtin_bit_cast(int, v), 0x121, 0xf, 0xf, true));
    v += __builtin_bit_cast(float, __builtin_amdgcn_update_dpp(0, __builtin_bit_cast(int, v), 0x122, 0xf, 0xf, true));
    v += __builtin_bit_cast(float, __builtin_amdgcn_update_dpp(0, __builtin_bit_cast(int, v), 0x124, 0xf, 0xf, true));
    v += __builtin_bit_cast(float, __builtin_amdgcn_update_dpp(0, __builtin_bit_cast(int, v), 0x128, 0xf, 0xf, true));
    return v;
}
DI void add_ctr(const Params& p, int word, unsigned v) { (void)__hip_atomic_fetch_add((unsigned*)p.ws + word, v, __ATOMIC_RELAXED, __HIP_MEMORY_SCOPE_AGENT); }
DI void wait_ctr(const Params& p, rsrc_t rw, int word, unsigned want) {
    unsigned n = SPIN_CAP;
#pragma nounroll
    for (;;) {
        asm volatile("" ::: "memory");
        if ((unsigned)__builtin_amdgcn_readfirstlane(ld4(rw, 0, word * 4)) >= want) break;
        if (--n == 0) { set_tmo(p); break; }
        if ((n & 63) == 0 && __builtin_amdgcn_readfirstlane(ld4(rw, 0, C_TMO * 4))) break;
        __builtin_amdgcn_s_sleep(1);
    }
    asm volatile("" ::: "memory");
}

__global__ void __launch_bounds__(256) k_init(Params p) {
    const int tid = threadIdx.x, lane = tid & 63, w = tid >> 6, b = blockIdx.x;
    if (b < 48) {
        __shared__ unsigned lat[4];
        unsigned* a = (unsigned*)(p.ws + ex_off(p) + (b >> 3) * 2097152 + EX_PROBE + (w >> 1) * 8192 + (w & 1) * EX_STEP + (b & 7) * 128);
        unsigned best = ~0u;
        for (int i = 0; i < 3; ++i) {
            const unsigned t0 = mt32();
            unsigned v = 0;
            if (lane == 0) v = __hip_atomic_fetch_add(a, 1u, __ATOMIC_RELAXED, __HIP_MEMORY_SCOPE_AGENT);
            asm volatile("s_waitcnt vmcnt(0)" :: "v"(v) : "memory");
            best = min(best, mt32() - t0);
        }
        if (lane == 0) lat[w] = best;
        __syncthreads();
        const int d = (int)min(lat[0], lat[1]) - (int)min(lat[2], lat[3]);
        if (tid == 0) ((unsigned*)p.ws)[C_POL + b] = abs(d) < 120 ? 0 : 1 + ((d > 0) ^ (xcc_id() >= 4));
    } else {
        const int i = (b - 48) * 256 + tid;
        if (i < C_POL / 4) ((u32x4*)(p.ws + OFF_CTRL))[i] = (u32x4){0u, 0u, 0u, 0u};
    }
}

DI void ln_tile(const Params& p, int b) {
    const int lane = fresh_lane(), w = __builtin_amdgcn_readfirstlane(threadIdx.x >> 6);
    const rsrc_t rw = mk_rsrc(p.ws, WS_USED);
    const int c = w * 4 + (lane >> 4), sub = lane & 15, row = b * 16 + c;
    u32x4 v[5]; float sum = 0.f;
#pragma unroll
    for (int k = 0; k < 5; ++k) v[k] = *(const u32x4*)(p.x + (size_t)row * 1280 + (k * 16 + sub) * 16);
#pragma unroll
    for (int k = 0; k < 5; ++k)
#pragma unroll
        for (int j = 0; j < 4; ++j) sum += bf_lo(v[k][j]) + bf_hi(v[k][j]);
    const float mean = row16_sum(sum) * (1.f / 640.f);
    float sq = 0.f;
#pragma unroll
    for (int k = 0; k < 5; ++k)
#pragma unroll
        for (int j = 0; j < 4; ++j) { const float a = bf_lo(v[k][j]) - mean, bb = bf_hi(v[k][j]) - mean; sq += a * a + bb * bb; }
    const float mb = rbf(mean), sd = rbf(sqrtf(rbf(rbf(row16_sum(sq) * (1.f / 640.f)) + 1e-5f))), ri = 1.f / sd;
#pragma unroll
    for (int k = 0; k < 5; ++k) {
        u32x4 o;
#pragma unroll
        for (int j = 0; j < 4; ++j) { const unsigned d = pk2(bf_lo(v[k][j]) - mb, bf_hi(v[k][j]) - mb); o[j] = pk2(bf_lo(d) * ri, bf_hi(d) * ri); }
        st16(o, rw, (k * 16 + sub) * 256 + c * 16, OFF_XF + b * 20480);
    }
    wait_vm<0>();
    bar();
    if (fresh_tid() == 0) add_ctr(p, C_X + (b >> 3) * 32, 1u);
}
DI void poison(const Params& p, int j, int n0, int n1, int word) {
    const int tid = fresh_tid();
    const rsrc_t rw = mk_rsrc(p.ws, WS_USED);
    const unsigned exo = ex_off(p), pol = read_pol(p);
    const u32x4 sv = {SENT, SENT, SENT, SENT};
    for (int n = n0 + j; n < n1; n += 56) {
        const int s = (n * 3277) >> 16, i = n - 20 * s;
        u32x4 v = sv;
        if (s == 0 && i >= 16 && (i < 18 || (i == 18 && tid < 128))) {
            const int f = (i - 16) * 256 + tid, ks = f >> 6, l = f & 63, r = (l & 3) | ((l >> 1) & 4), half = (l >> 2) & 1, g = l >> 4;
            v = *(const u32x4*)(p.mem0 + (r * 640 + 64 * ks + 16 * g + 8 * half) * 2);
            v.x = nosent(v.x); v.y = nosent(v.y); v.z = nosent(v.z); v.w = nosent(v.w);
        }
        st16(v, rw, tid * 16, ex_step(exo, pol, s) + (i >> 1) * 16384 + (i & 1) * 4096);
    }
    wait_vm<0>();
    bar();
    if (fresh_tid() == 0) add_ctr(p, word, 1u);
}

#define SB() __builtin_amdgcn_sched_barrier(0)
DI void mfma_va(f32x4& c, u32x4 a, u32x4 b) { asm volatile("v_mfma_f32_16x16x32_bf16 %0, %1, %2, %0" : "+v"(c) : "v"(a), "a"(b)); }
DI void mfma_av(f32x4& c, u32x4 a, u32x4 b) { asm volatile("v_mfma_f32_16x16x32_bf16 %0, %1, %2, %0" : "+v"(c) : "a"(a), "v"(b)); }
DI void mfma_cacc(f32x4& c, u32x4 a, u32x4 b) { asm volatile("v_mfma_f32_16x16x32_bf16 %0, %1, %2, %0" : "+a"(c) : "v"(a), "v"(b)); }
struct P1Src {
    rsrc_t r; unsigned v0, v1, so, step;
    DI void set(const Params& p, int u, int kh, int w, int lane) {
        const uchar* base; unsigned stride, col, bytes;
        if (u < 128) { base = (u & 2) ? p.wv : p.wk; stride = 8192; col = ((u >> 2) * 128 + (u & 1) * 64) * 2; bytes = 640 * 8192; }
        else if (u < 192) { base = p.wq; stride = 8192; col = (u - 128) * 128; bytes = 640 * 8192; }
        else { base = p.w1; stride = 3456; col = (u - 192) * 128; bytes = u < 219 ? 640 * 3456 : 0; }
        r = mk_rsrc(base, bytes);
        const int rl = lane >> 3, f0 = ((rl >> 1) & 1) | (((rl >> 2) & 1) << 1);
        v0 = rl * stride + (((lane & 7) ^ (2 * f0)) << 4); v1 = v0 ^ 64;
        so = (kh * 320 + 80 * w) * stride + col; step = 8 * stride;
    }
    DI void piece(int i, unsigned ldsw) const { dma16(r, (i & 1) ? v1 : v0, so + i * step, ldsw + i * 1024); }
};
DI float mish(float a, float bias) {
    const float h = a + bias;
    const float e = __builtin_amdgcn_exp2f(fminf(h, 30.f) * 1.44269504f);
    const float n = e * (e + 2.f);
    return h * n * __builtin_amdgcn_rcpf(n + 2.f);
}
#define PT(i)
constexpr int XA_ACC = 15;
template <bool ISV, int KH>
DI void p1_slab(LAS uchar* sb, const unsigned (&tb)[4], const u32x4 (&xa)[4][20], f32x4 (&acc)[4][4], const P1Src& src, unsigned ldsn) {
    u32x4 wf[2][4];
    auto rd = [&](int kk, int nt) { const u32x2 lo = trd(sb + tb[nt] + kk * 4096), hi = trd(sb + tb[nt ^ 2] + kk * 4096 + 512); return (u32x4){lo.x, lo.y, hi.x, hi.y}; };
#pragma unroll
    for (int nt = 0; nt < 4; ++nt) wf[0][nt] = rd(0, nt);
    SB();
#pragma unroll
    for (int kk = 0; kk < 10; ++kk) {
#pragma unroll
        for (int nt = 0; nt < 4; ++nt) {
            if (kk + 1 < 10) wf[(kk + 1) & 1][nt] = rd(kk + 1, nt);
            if (kk < 5 && (nt & 1)) src.piece(2 * kk + (nt >> 1), ldsn);
#pragma unroll
            for (int t = 0; t < 4; ++t) {
                const int ks = KH * 10 + kk;
                if (ISV) { if (ks < XA_ACC) mfma_av(acc[t][nt], xa[t][ks], wf[kk & 1][nt]); else mfma_v(acc[t][nt], xa[t][ks], wf[kk & 1][nt]); }
                else { if (ks < XA_ACC) mfma_va(acc[t][nt], wf[kk & 1][nt], xa[t][ks]); else mfma_v(acc[t][nt], wf[kk & 1][nt], xa[t][ks]); }
            }
            SB();
        }
    }
    asm volatile("s_nop 7" : "+v"(acc[3][3]), "+v"(acc[2][3]), "+v"(acc[1][3]), "+v"(acc[0][3]), "+v"(acc[3][2]), "+v"(acc[2][2]), "+v"(acc[1][2]), "+v"(acc[0][2]));
#pragma unroll
    for (int t = 0; t < 4; ++t) asm volatile("" : "+v"(acc[t][0]), "+v"(acc[t][1]));
}
template <bool ISV>
DI void p1_group(const Params& p, LAS uchar* lds, rsrc_t rw, unsigned lbase, const unsigned (&tb)[4], const u32x4 (&xa)[4][20], P1Src& src, int buf,
                 int chunk, int u, int un, bool first, int w, int lane, unsigned (&pt)[5]) {
    const int g = lane >> 4;
    f32x4 acc[4][4];
#pragma unroll
    for (int t = 0; t < 4; ++t)
#pragma unroll
        for (int nt = 0; nt < 4; ++nt) { acc[t][nt] = (f32x4){0.f, 0.f, 0.f, 0.f}; asm volatile("" : "+v"(acc[t][nt])); }
    PT(0);
    if (first) wait_vm<50>(); else wait_vm<10>();
    PT(1);
    bar();
    PT(2);
    src.set(p, un, 0, w, lane);
    {
        const unsigned ln = lbase + (buf == 0 ? 2 : buf - 1) * 40960;
        p1_slab<ISV, 0>(lds + buf * 40960, tb, xa, acc, src, ln);
    }
    buf = buf == 2 ? 0 : buf + 1;
    PT(3);
    wait_vm<10>();
    PT(1);
    bar();
    PT(2);
    src.set(p, un, 1, w, lane);
    {
        const unsigned ln = lbase + (buf == 0 ? 2 : buf - 1) * 40960;
        p1_slab<ISV, 1>(lds + buf * 40960, tb, xa, acc, src, ln);
    }
    PT(3);
    const int tt0 = chunk * 16 + w * 4;
    if (ISV) {
        const int h = u >> 2;
#pragma unroll
        for (int sp = 0; sp < 2; ++sp)
#pragma unroll
            for (int nt = 0; nt < 4; ++nt) {
                const f32x4 a = acc[2 * sp][nt], b = acc[2 * sp + 1][nt];
                const u32x4 o = {pk2(a[0], a[1]), pk2(a[2], a[3]), pk2(b[0], b[1]), pk2(b[2], b[3])};
                st16(o, rw, lane * 16, OFF_VF + (((tt0 >> 1) + sp) * 32 + h) * 8192 + ((u & 1) * 4 + nt) * 1024);
            }
    } else if (u < 192) {
        const int h = u < 128 ? u >> 2 : (u - 128) >> 1;
        const int ob = u < 128 ? OFF_KF : OFF_QF;
#pragma unroll
        for (int t = 0; t < 4; ++t)
#pragma unroll
            for (int m = 0; m < 2; ++m) {
                const f32x4 a = acc[t][2 * m], b = acc[t][2 * m + 1];
                const u32x4 o = {pk2(a[0], a[1]), pk2(a[2], a[3]), pk2(b[0], b[1]), pk2(b[2], b[3])};
                st16(o, rw, lane * 16, ob + (((tt0 + t) >> 1) * 32 + h) * 8192 + ((t & 1) * 4 + (u & 1) * 2 + m) * 1024);
            }
    } else {
        const int fg = u - 192;
        u32x2 bv[4];
#pragma unroll
        for (int nt = 0; nt < 4; ++nt) bv[nt] = *(const u32x2*)(p.b1 + (fg * 64 + nt * 16 + 4 * g) * 2);
#pragma unroll
        for (int t = 0; t < 4; ++t)
#pragma unroll
            for (int m = 0; m < 2; ++m) {
                const f32x4 a = acc[t][2 * m], b = acc[t][2 * m + 1];
                const u32x2 ba = bv[2 * m], bb = bv[2 * m + 1];
                const u32x4 o = {pk2(mish(a[0], bf_lo(ba.x)), mish(a[1], bf_hi(ba.x))), pk2(mish(a[2], bf_lo(ba.y)), mish(a[3], bf_hi(ba.y))),
                                 pk2(mish(b[0], bf_lo(bb.x)), mish(b[1], bf_hi(bb.x))), pk2(mish(b[2], bf_lo(bb.y)), mish(b[3], bf_hi(bb.y)))};
                st16(o, rw, lane * 16, OFF_ACT + ((tt0 + t) * 54 + fg * 2 + m) * 1024);
                SB();
            }
    }
}
DI void ld16_a(u32x4& d, rsrc_t r, unsigned v, unsigned s) { asm volatile("buffer_load_dwordx4 %0, %1, %2, %3 offen sc1" : "=a"(d) : "v"(v), "s"(r), "s"(s) : "memory"); }
DI void ld16_v(u32x4& d, rsrc_t r, unsigned v, unsigned s) { asm volatile("buffer_load_dwordx4 %0, %1, %2, %3 offen sc1" : "=v"(d) : "v"(v), "s"(r), "s"(s) : "memory"); }
DI void ld16_s(u32x4& d, rsrc_t r, unsigned v, unsigned s) { asm volatile("buffer_load_dwordx4 %0, %1, %2, %3 offen sc1" : "=v"(d) : "v"(v), "s"(r), "s"(s) : "memory"); }
DI void p1_task(const Params& p, LAS uchar* lds, int chunk, int u0, int nu) {
    const int lane = fresh_lane(), w = __builtin_amdgcn_readfirstlane(threadIdx.x >> 6), c = lane & 15, g = lane >> 4;
    const rsrc_t rw = mk_rsrc(p.ws, WS_USED);
    const unsigned lbase = (unsigned)(size_t)lds + w * 10240;
    P1Src src;
    u32x4 xa[4][20];
    src.set(p, u0, 0, w, lane);
    if (w == 0) { wait_ctr(p, rw, C_X + chunk * 64, 8); wait_ctr(p, rw, C_X + chunk * 64 + 32, 8); }
    bar();
#pragma unroll
    for (int i = 0; i < 10; ++i) src.piece(i, lbase);
#pragma unroll
    for (int ks = 0; ks < 20; ++ks) {
        if (ks == 10) {
            src.set(p, u0, 1, w, lane);
#pragma unroll
            for (int i = 0; i < 10; ++i) src.piece(i, lbase + 40960);
        }
#pragma unroll
        for (int t = 0; t < 4; ++t) {
            const unsigned so = OFF_XF + ((chunk * 16 + w * 4 + t) * 20 + ks) * 1024;
            if (ks < XA_ACC) ld16_a(xa[t][ks], rw, lane * 16, so); else ld16_v(xa[t][ks], rw, lane * 16, so);
        }
        SB();
    }
    const int q = c >> 2, pp = c & 3, sw = (q >> 1) | ((g & 1) << 1);
    unsigned tb[4];
#pragma unroll
    for (int nt = 0; nt < 4; ++nt) tb[nt] = (8 * g + q) * 128 + 32 * (nt ^ sw) + 16 * (pp >> 1) + 8 * (pp & 1);
    int buf = 0;
    unsigned pt[5] = {0, 0, 0, 0, mt32()};
    for (int gi = 0; gi < nu; ++gi) {
        const int u = u0 + gi, un = gi + 1 < nu ? u + 1 : 219;
        if (u < 128 && (u & 2)) p1_group<true>(p, lds, rw, lbase, tb, xa, src, buf, chunk, u, un, gi == 0, w, lane, pt);
        else p1_group<false>(p, lds, rw, lbase, tb, xa, src, buf, chunk, u, un, gi == 0, w, lane, pt);
        buf = buf == 0 ? 2 : buf - 1;
    }
    wait_vm<0>();
    bar();
    const unsigned tid = fresh_tid();
    if (tid < (unsigned)nu) {
        const int u = u0 + tid;
        add_ctr(p, u < 128 ? C_KV + (chunk * 32 + (u >> 2)) * 16 : (u < 192 ? C_Q + (chunk * 32 + ((u - 128) >> 1)) * 16 : C_ACT + chunk * 32), 1u);
    }
}

#define KST(i)
#define AST_(i)
template <bool ISV>
DI void kv0_task(const Params& p, LAS uchar* lds, int u) {
    const int lane = fresh_lane(), w = __builtin_amdgcn_readfirstlane(threadIdx.x >> 6), c = lane & 15, g = lane >> 4;
    const rsrc_t rw = mk_rsrc(p.ws, WS_USED);
    const unsigned lbase = (unsigned)(size_t)lds + w * 10240;
    P1Src src;
    u32x4 xa[2][20];
    auto fetch = [&](int half) {
#pragma unroll
        for (int ks = 0; ks < 20; ++ks)
#pragma unroll
            for (int t = 0; t < 2; ++t) ld16_v(xa[t][ks], rw, lane * 16, OFF_XF + ((half * 8 + w * 2 + t) * 20 + ks) * 1024);
    };
#pragma unroll
    for (int kh = 0; kh < 2; ++kh) {
        src.set(p, u, kh, w, lane);
#pragma unroll
        for (int i = 0; i < 10; ++i) src.piece(i, lbase + kh * 40960);
    }
    KST(8);
    KST(9);
    if (w == 0) wait_ctr(p, rw, C_X, 8);
    bar();
    KST(10);
    fetch(0);
    const int q = c >> 2, pp = c & 3, sw = (q >> 1) | ((g & 1) << 1), h = u >> 2;
    unsigned tb[4];
#pragma unroll
    for (int nt = 0; nt < 4; ++nt) tb[nt] = (8 * g + q) * 128 + 32 * (nt ^ sw) + 16 * (pp >> 1) + 8 * (pp & 1);
    auto rd = [&](int i) {
        const int ks = i >> 2, nt = i & 3; LAS uchar* sb = lds + (ks / 10) * 40960 + (ks % 10) * 4096;
        const u32x2 lo = trd(sb + tb[nt]), hi = trd(sb + tb[nt ^ 2] + 512); return (u32x4){lo.x, lo.y, hi.x, hi.y};
    };
    for (int half = 0; half < 2; ++half) {
        f32x4 acc[2][4];
#pragma unroll
        for (int i = 0; i < 8; ++i) { acc[i >> 2][i & 3] = (f32x4){0.f, 0.f, 0.f, 0.f}; asm volatile("" : "+v"(acc[i >> 2][i & 3])); }
        KST(half * 4 + 0);
        wait_vm<0>();
#pragma unroll
        for (int ks = 0; ks < 20; ++ks) asm volatile("" : "+v"(xa[0][ks]), "+v"(xa[1][ks]));
        bar();
        KST(half * 4 + 1);
        u32x4 wf[8];
#pragma unroll
        for (int i = 0; i < 8; ++i) wf[i] = rd(i);
        SB();
#pragma unroll
        for (int i = 0; i < 80; ++i) {
#pragma unroll
            for (int t = 0; t < 2; ++t) { if (ISV) mfma_v(acc[t][i & 3], xa[t][i >> 2], wf[i & 7]); else mfma_v(acc[t][i & 3], wf[i & 7], xa[t][i >> 2]); }
            if (i + 8 < 80) wf[i & 7] = rd(i + 8);
            SB();
        }
        asm volatile("s_nop 7" : "+v"(acc[0][0]), "+v"(acc[0][1]), "+v"(acc[0][2]), "+v"(acc[0][3]), "+v"(acc[1][0]), "+v"(acc[1][1]), "+v"(acc[1][2]), "+v"(acc[1][3]));
        KST(half * 4 + 2);
        const unsigned ob = (ISV ? OFF_VF : OFF_KF) + ((half * 4 + w) * 32 + h) * 8192;
#pragma unroll
        for (int i = 0; i < 4; ++i) {
            const f32x4 a = ISV ? acc[0][i] : acc[i >> 1][2 * (i & 1)], b = ISV ? acc[1][i] : acc[i >> 1][2 * (i & 1) + 1];
            const u32x4 o = {pk2(a[0], a[1]), pk2(a[2], a[3]), pk2(b[0], b[1]), pk2(b[2], b[3])};
            st16(o, rw, lane * 16, ob + (ISV ? (u & 1) * 4 + i : (i >> 1) * 4 + (u & 1) * 2 + (i & 1)) * 1024);
        }
        wait_vm<0>();
        bar();
        KST(half * 4 + 3);
        if (fresh_tid() == 0) add_ctr(p, (half ? C_KV : C_KVH) + h * 16, 1u);
        if (w == 0) wait_ctr(p, rw, C_X + 32, 8);
        bar();
        fetch(1);
    }
    wait_vm<0>();
#pragma unroll
    for (int ks = 0; ks < 20; ++ks) asm volatile("" :: "v"(xa[0][ks]), "v"(xa[1][ks]));
}

DI void down_task(const Params& p, LAS uchar* lds, int chunk, int cg) {
    const int lane = fresh_lane(), w = __builtin_amdgcn_readfirstlane(threadIdx.x >> 6), c = lane & 15, g = lane >> 4;
    const rsrc_t rw = mk_rsrc(p.ws, WS_USED), r2 = mk_rsrc(p.w2, F_ * 1280);
    const unsigned lbase = (unsigned)(size_t)lds;
    const int rl = lane >> 3, f0 = ((rl >> 1) & 1) | (((rl >> 2) & 1) << 1);
    const unsigned v0 = rl * 1280 + (((lane & 7) ^ (2 * f0)) << 4), v1 = rl * 1280 + (((lane & 7) ^ (2 * (f0 ^ 2))) << 4);
    auto issue = [&](int sl, int buf) {
#pragma unroll
        for (int i = 0; i < 6; ++i) {
            const int pc = (w & 1) * 6 + i, im = w >> 1;
            dma16(r2, (i & 1) ? v1 : v0, (sl * 96 + 8 * pc) * 1280 + cg * 256 + im * 128, lbase + buf * 24576 + im * 12288 + pc * 1024);
        }
    };
    const int tt0 = chunk * 16 + w * 4;
    issue(0, 0);
    if (w == 0) wait_ctr(p, rw, C_ACT + chunk * 32, 27);
    bar();
    u32x4 xb[2][3][4];
#pragma unroll
    for (int kk = 0; kk < 3; ++kk)
#pragma unroll
        for (int t = 0; t < 4; ++t) ld16_s(xb[0][kk][t], rw, lane * 16, OFF_ACT + ((tt0 + t) * 54 + kk) * 1024);
    issue(1, 1);
    const int q = c >> 2, pp = c & 3, sw = (q >> 1) | (((g & 1) ^ (g >> 1)) << 1);
    unsigned tb[4];
#pragma unroll
    for (int nt = 0; nt < 4; ++nt) tb[nt] = (4 * g + q) * 128 + 32 * (nt ^ sw) + 16 * (pp >> 1) + 8 * (pp & 1);
    f32x4 acc[4][8];
#pragma unroll
    for (int t = 0; t < 4; ++t)
#pragma unroll
        for (int nt = 0; nt < 8; ++nt) { acc[t][nt] = (f32x4){0.f, 0.f, 0.f, 0.f}; asm volatile("" : "+a"(acc[t][nt])); }
    int buf = 0;
    for (int s2 = 0; s2 < 9; ++s2) {
#pragma unroll
        for (int par = 0; par < 2; ++par) {
            const int sl = 2 * s2 + par;
            wait_vm<6>();
#pragma unroll
            for (int kk = 0; kk < 3; ++kk) asm volatile("" : "+v"(xb[par][kk][0]), "+v"(xb[par][kk][1]), "+v"(xb[par][kk][2]), "+v"(xb[par][kk][3]));
            bar();
            const int sn = min(sl + 1, 17);
#pragma unroll
            for (int kk = 0; kk < 3; ++kk)
#pragma unroll
                for (int t = 0; t < 4; ++t) ld16_s(xb[par ^ 1][kk][t], rw, lane * 16, OFF_ACT + ((tt0 + t) * 54 + sn * 3 + kk) * 1024);
            issue(min(sl + 2, 17), buf == 0 ? 2 : buf - 1);
            LAS uchar* sb = lds + buf * 24576;
            auto rd = [&](int i) { LAS uchar* a = sb + ((i >> 2) & 1) * 12288 + tb[i & 3] + (i >> 3) * 4096; const u32x2 lo = trd(a), hi = trd(a + 2048); return (u32x4){lo.x, lo.y, hi.x, hi.y}; };
            u32x4 wf[4];
#pragma unroll
            for (int i = 0; i < 4; ++i) wf[i] = rd(i);
            SB();
#pragma unroll
            for (int i = 0; i < 24; ++i) {
                const u32x4 cur = wf[i & 3];
#pragma unroll
                for (int t = 0; t < 4; ++t) mfma_cacc(acc[t][i & 7], cur, xb[par][i >> 3][t]);
                if (i + 4 < 24) wf[i & 3] = rd(i + 4);
                SB();
            }
            if (par) asm volatile("s_nop 7");
            SB();
            buf = buf == 2 ? 0 : buf + 1;
        }
    }
    wait_vm<0>();
#pragma unroll
    for (int kk = 0; kk < 3; ++kk) asm volatile("" :: "v"(xb[0][kk][0]), "v"(xb[0][kk][1]), "v"(xb[0][kk][2]), "v"(xb[0][kk][3]));
    SB();
#pragma unroll
    for (int t = 0; t < 4; ++t)
#pragma unroll
        for (int m = 0; m < 4; ++m) {
            float y[8];
#pragma unroll
            for (int r = 0; r < 8; ++r) { const float a = rbf(acc[t][2 * m + (r >> 2)][r & 3]); y[r] = a * __builtin_amdgcn_rcpf(1.f + __builtin_amdgcn_exp2f(-a * 1.44269504f)); }
            const u32x4 o = {pk2(y[0], y[1]), pk2(y[2], y[3]), pk2(y[4], y[5]), pk2(y[6], y[7])};
            st16(o, rw, lane * 16, OFF_GF + ((tt0 + t) * 20 + cg * 4 + m) * 1024);
        }
    wait_vm<0>();
    bar();
    if (fresh_tid() == 0) add_ctr(p, C_G + (chunk * 5 + cg) * 32, 1u);
}

constexpr float SCL = 0.08838834764831845f * 1.4426950408889634f;
DI void attn_task(const Params& p, int s, int h) {
    const int lane = fresh_lane(), c = lane & 15, g = lane >> 4;
    const rsrc_t rw = mk_rsrc(p.ws, WS_USED);
    const int sh = (s * 32 + h);
    u32x4 qf[2][4], kf[2][4], vf[8], km[4]; u32x2 vm[8];
    wait_ctr(p, rw, C_Q + ((s >> 3) * 32 + h) * 16, 2);
    wait_ctr(p, rw, C_KV + ((s >> 3) * 32 + h) * 16, 4);
#pragma unroll
    for (int i = 0; i < 8; ++i) qf[i >> 2][i & 3] = ld16(rw, lane * 16, OFF_QF + sh * 8192 + i * 1024);
#pragma unroll
    for (int i = 0; i < 8; ++i) kf[i >> 2][i & 3] = ld16(rw, lane * 16, OFF_KF + sh * 8192 + i * 1024);
#pragma unroll
    for (int i = 0; i < 8; ++i) vf[i] = ld16(rw, lane * 16, OFF_VF + sh * 8192 + i * 1024);
    wait_ctr(p, rw, C_KM + h * 32, s + 1);
#pragma unroll
    for (int i = 0; i < 4; ++i) km[i] = ld16(rw, (g * 8 + (c & 7)) * 16, OFF_KM + sh * 4096 + i * 512);
#pragma unroll
    for (int i = 0; i < 8; ++i) vm[i] = ld8(rw, (lane & 31) * 8, OFF_KM + sh * 4096 + 2048 + i * 256);
    const u32x4 one4 = {0x3F803F80u, 0x3F803F80u, 0x3F803F80u, 0x3F803F80u};
    const u32x2 one2 = {0x3F803F80u, 0x3F803F80u};
    const float NINF = -__builtin_inff();
#pragma unroll
    for (int qt = 0; qt < 2; ++qt) {
        f32x4 s0 = {0.f, 0.f, 0.f, 0.f}, s1 = s0, s2 = s0;
#pragma unroll
        for (int ks = 0; ks < 4; ++ks) {
            s0 = mfma32(kf[0][ks], qf[qt][ks], s0);
            if (qt == 1) s1 = mfma32(kf[1][ks], qf[qt][ks], s1);
            s2 = mfma32(km[ks], qf[qt][ks], s2);
        }
        float t[12], m = NINF;
#pragma unroll
        for (int r = 0; r < 4; ++r) {
            const bool diag = 4 * g + r <= c;
            t[r] = (qt == 1 || diag) ? s0[r] : NINF;
            t[4 + r] = (qt == 1 && diag) ? s1[r] : NINF;
            t[8 + r] = g >= 2 ? s2[r] : NINF;
        }
#pragma unroll
        for (int r = 0; r < 12; ++r) m = fmaxf(m, t[r]);
        m = grp_max(m) * SCL;
#pragma unroll
        for (int r = 0; r < 12; ++r) t[r] = __builtin_amdgcn_exp2f(t[r] * SCL - m);
        const u32x4 p01 = {pk2(t[0], t[1]), pk2(t[2], t[3]), pk2(t[4], t[5]), pk2(t[6], t[7])};
        const u32x2 p2 = {pk2(t[8], t[9]), pk2(t[10], t[11])};
        f32x4 l = mfma32(one4, p01, (f32x4){0.f, 0.f, 0.f, 0.f});
        l = mfma16(one2, p2, l);
        const float inv = __builtin_amdgcn_rcpf(l[0]);
#pragma unroll
        for (int tp = 0; tp < 4; ++tp) {
            f32x4 a = mfma32(vf[2 * tp], p01, (f32x4){0.f, 0.f, 0.f, 0.f}), b = mfma32(vf[2 * tp + 1], p01, (f32x4){0.f, 0.f, 0.f, 0.f});
            a = mfma16(vm[2 * tp], p2, a); b = mfma16(vm[2 * tp + 1], p2, b);
            const u32x4 o = {pk2(a[0] * inv, a[1] * inv), pk2(a[2] * inv, a[3] * inv), pk2(b[0] * inv, b[1] * inv), pk2(b[2] * inv, b[3] * inv)};
            st16(o, rw, lane * 16, OFF_OT + ((2 * s + qt) * 32 + h) * 4096 + tp * 1024);
        }
    }
    wait_vm<0>();
    if (lane == 0) add_ctr(p, C_OT + s * 32, 1u);
}

DI void wo_stage(const Params& p, unsigned ldsw, int h0, int cs, int lane) {
    const rsrc_t ro = mk_rsrc(p.wo, 4096 * 1280);
    const unsigned v = (lane >> 2) * 1280 + (lane & 3) * 16;
#pragma unroll
    for (int i = 0; i < 16; ++i) dma16(ro, v, (h0 * 128 + 16 * i) * 1280 + cs * 64, ldsw + i * 1024);
}
DI void w_role(const Params& p, LAS uchar* lds, int cs, int s0, int sstep) {
    const int lane = fresh_lane(), w = __builtin_amdgcn_readfirstlane(threadIdx.x >> 6), c = lane & 15, g = lane >> 4;
    const rsrc_t rw = mk_rsrc(p.ws, WS_USED);
    u32x4 wf[8][4][2];
    {
        LAS uchar* lw = lds + w * 32768;
        const int q = c >> 2, pp = c & 3;
        wo_stage(p, (unsigned)(size_t)lw, 8 * w, cs, lane);
        wo_stage(p, (unsigned)(size_t)lw + 16384, 8 * w + 2, cs, lane);
#pragma unroll
        for (int rd = 0; rd < 4; ++rd) {
            if (rd < 3) wait_vm<16>(); else wait_vm<0>();
#pragma unroll
            for (int i = 0; i < 2; ++i)
#pragma unroll
                for (int tp = 0; tp < 4; ++tp)
#pragma unroll
                    for (int nt = 0; nt < 2; ++nt) {
                        LAS uchar* a = lw + (rd & 1) * 16384 + i * 8192 + (32 * tp + 4 * g + q) * 64 + nt * 32 + pp * 8;
                        const u32x2 lo = trd(a), hi = trd(a + 1024);
                        wf[2 * rd + i][tp][nt] = (u32x4){lo.x, lo.y, hi.x, hi.y};
                        if (2 * rd + i < 7) asm volatile("" : "+a"(wf[2 * rd + i][tp][nt])); else asm volatile("" : "+v"(wf[2 * rd + i][tp][nt]));
                    }
            asm volatile("s_waitcnt lgkmcnt(0)" ::: "memory");
            SB();
            if (rd < 2) wo_stage(p, (unsigned)(size_t)lw + (rd & 1) * 16384, 8 * w + 2 * rd + 4, cs, lane);
        }
    }
    bar();
    LAS f32x4* red = (LAS f32x4*)lds;
    for (int s = s0; s < NSEG; s += sstep) {
        f32x4 acc[2][2];
#pragma unroll
        for (int i = 0; i < 4; ++i) { acc[i >> 1][i & 1] = (f32x4){0.f, 0.f, 0.f, 0.f}; asm volatile("" : "+v"(acc[i >> 1][i & 1])); }
        const int tt = 2 * s + (w >> 1), nt = w & 1;
        if (w == 0) { wait_ctr(p, rw, C_G + ((s >> 3) * 5 + (cs >> 2)) * 32, 1); wait_ctr(p, rw, C_OT + s * 32, 32); }
        bar();
        u32x4 ob[4][2][4];
        auto fetch = [&](int i) {
#pragma unroll
            for (int t = 0; t < 2; ++t)
#pragma unroll
                for (int tp = 0; tp < 4; ++tp) ob[i & 3][t][tp] = ld16(rw, lane * 16, OFF_OT + ((2 * s + t) * 32 + 8 * w + i) * 4096 + tp * 1024);
        };
#pragma unroll
        for (int i = 0; i < 4; ++i) { fetch(i); SB(); }
        const u32x2 xv = *(const u32x2*)(p.x + (size_t)(tt * 16 + c) * 1280 + cs * 64 + nt * 32 + g * 8);
        const u32x2 gv = ld8(rw, lane * 16 + nt * 8, OFF_GF + (tt * 20 + cs) * 1024);
        SB();
#pragma unroll
        for (int i = 0; i < 8; ++i) {
#pragma unroll
            for (int tp = 0; tp < 4; ++tp)
#pragma unroll
                for (int t = 0; t < 2; ++t)
#pragma unroll
                    for (int n = 0; n < 2; ++n) { if (i < 7) mfma_av(acc[t][n], wf[i][tp][n], ob[i & 3][t][tp]); else mfma_v(acc[t][n], wf[i][tp][n], ob[i & 3][t][tp]); }
            SB();
            if (i + 4 < 8) fetch(i + 4);
            SB();
        }
        mfma_pad(acc[0][0], acc[0][1], acc[1][0], acc[1][1]);
#pragma unroll
        for (int i = 0; i < 4; ++i) red[(i * 4 + w) * 64 + lane] = acc[i >> 1][i & 1];
        bar();
        const f32x4 r = (red[(w * 4 + 0) * 64 + lane] + red[(w * 4 + 1) * 64 + lane]) + (red[(w * 4 + 2) * 64 + lane] + red[(w * 4 + 3) * 64 + lane]);
        const unsigned s01 = pk2(r[0], r[1]), s23 = pk2(r[2], r[3]);
        const unsigned m01 = pk2(bf_lo(s01) * bf_lo(gv.x), bf_hi(s01) * bf_hi(gv.x)), m23 = pk2(bf_lo(s23) * bf_lo(gv.y), bf_hi(s23) * bf_hi(gv.y));
        const u32x2 o = {pk2(bf_lo(xv.x) + bf_lo(m01), bf_hi(xv.x) + bf_hi(m01)), pk2(bf_lo(xv.y) + bf_lo(m23), bf_hi(xv.y) + bf_hi(m23))};
        *(u32x2*)(p.out + (size_t)(tt * 16 + c) * 1280 + cs * 64 + nt * 32 + g * 8) = o;
    }
}

template <int V> struct IC { static constexpr int value = V; };
template <int N, int I = 0, class F> DI void static_for(F&& f) { if constexpr (I < N) { f(IC<I>{}); static_for<N, I + 1>(f); } }
#define V_A 88
#define V_M 56
#define KM_DELAY 240
#define PAD0 0
#define PAD_UP 0
#define PAD_MAX 48
#define RISE 64
#define DECAY 8
#ifndef M_DELAY
#define M_DELAY 200
#endif
#define D_MST 240
#define F_LATE 1000
#define V_LATE 8
#define DUR_A0 1060
#define DUR_M0 760
#define FT(i)
#define FDUMP(blk, s, w)
#define STAT(blk, s, w, a, b, c, d)
DI int wave_max(int d) {
    d = max(d, __builtin_amdgcn_update_dpp(0, d, 0x121, 0xf, 0xf, true));
    d = max(d, __builtin_amdgcn_update_dpp(0, d, 0x122, 0xf, 0xf, true));
    d = max(d, __builtin_amdgcn_update_dpp(0, d, 0x124, 0xf, 0xf, true));
    d = max(d, __builtin_amdgcn_update_dpp(0, d, 0x128, 0xf, 0xf, true));
    auto r = __builtin_amdgcn_permlane32_swap((unsigned)d, (unsigned)d, false, false); d = max((int)r[0], (int)r[1]);
    auto q = __builtin_amdgcn_permlane16_swap((unsigned)d, (unsigned)d, false, false); d = max((int)q[0], (int)q[1]);
    return __builtin_amdgcn_readfirstlane(d);
}
struct Clock {
    unsigned r0, m0, cq, qc;
    DI void edge(unsigned& r, unsigned& m) {
        unsigned long long a, b;
        const unsigned first = rt32();
        do asm volatile("s_memrealtime %0\n\ts_memtime %1\n\ts_waitcnt lgkmcnt(0)" : "=s"(a), "=s"(b) :: "memory"); while ((unsigned)a == first);
        r = (unsigned)a << 3; m = (unsigned)b - 24;
    }
    DI void start() { edge(r0, m0); cq = 3000; qc = 22370; }
    DI void sync() {
        unsigned r, m; edge(r, m);
        const float f = (float)(m - m0) * __builtin_amdgcn_rcpf((float)(r - r0));
        cq = __builtin_amdgcn_readfirstlane((unsigned)(f * 1024.f));
        qc = __builtin_amdgcn_readfirstlane((unsigned)(65536.f * __builtin_amdgcn_rcpf(f)));
        r0 = r; m0 = m;
    }
    DI unsigned at(unsigned m) const { return r0 + (unsigned)(((unsigned long long)(m - m0) * qc) >> 16); }
    DI unsigned now() const {
        unsigned long long a, b;
        asm volatile("s_memrealtime %0\n\ts_memtime %1\n\ts_waitcnt lgkmcnt(0)" : "=s"(a), "=s"(b) :: "memory");
        const unsigned lo = (unsigned)a << 3;
        return lo + __builtin_amdgcn_readfirstlane(min(max((int)(at((unsigned)b) - lo), 0), 31));
    }
    DI unsigned cycles(unsigned t) const { const int d = __builtin_amdgcn_readfirstlane(min(max((int)(t - r0), 0), 1 << 20)); return m0 + (((unsigned)d * cq) >> 10) - 26; }
};
DI unsigned waitmt(unsigned t) { unsigned n; do n = mt32(); while ((int)(n - t) < 0); return n; }
struct Sched {
    unsigned tgo, tau, durA, durM, pad;
    unsigned a, v;
    DI void fetch(rsrc_t rw, unsigned exo, unsigned pol, int s, int lane) {
        a = ld4(rw, s >= 2 ? (lane & 31) * 4 : OOB, ex_step(exo, pol, max(s - 2, 0)) + EX_AST);
        v = ld4(rw, min(lane, 39) * 4, ex_step(exo, pol, s - 1) + EX_MST);
    }
    DI void next(const Params& p, rsrc_t rw, unsigned exo, unsigned pol, int s, int lane, unsigned long long& alive) {
        if ((__builtin_amdgcn_ballot_w64(v == SENT) & alive) != 0) {
            const unsigned mo = ex_step(exo, pol, s - 1) + EX_MST;
            unsigned n = SPIN_CAP; bool bad;
#pragma nounroll
            do {
                asm volatile("" ::: "memory");
                v = ld4(rw, min(lane, 39) * 4, mo);
                bad = __builtin_amdgcn_ballot_w64(v == SENT) != 0;
            } while (bad && --n);
            if (bad) { alive = 0; set_tmo(p); }
        }
        const unsigned ref = __builtin_amdgcn_readfirstlane(v);
        const unsigned pm = ref + (unsigned)wave_max((int)(v - ref));
        if (s >= 2 && __builtin_amdgcn_ballot_w64(v & 1) == 0) durM += (min((int)(pm - tau - durM), RISE) + 2) >> 2;
        if (__builtin_amdgcn_ballot_w64(a - 1u >= 0xFFFFu) == 0) durA += (min(wave_max((int)a) - (int)durA, RISE) + 2) >> 2;
        const bool late = s == 1 || (int)(pm + V_LATE - tgo) > 0;
        pad = late || __builtin_amdgcn_ballot_w64(v & 1) != 0 ? min(pad + PAD_UP, (unsigned)PAD_MAX) : (pad > 4 ? pad - 4 : 0);
        tau = (late ? pm + F_LATE : tgo) + durA + pad;
        tgo = tau + durM + V_A + pad;
    }
};

#define PACE 8
DI void pace() { asm volatile("s_nop %0" :: "n"(PACE)); }
DI void role_M(const Params& p, LAS uchar* lds, int mi) {
    const int tid = threadIdx.x, lane = tid & 63, w = __builtin_amdgcn_readfirstlane(tid >> 6), c = lane & 15, g = lane >> 4;
    const int qh = mi & 1, cs = mi >> 1;
    const rsrc_t rw = mk_rsrc(p.ws, WS_USED);
    u32x8 wf[8][2][2];
    waitrt(rt32() + M_DELAY);
    {
        LAS uchar* lw = lds + w * 32768;
        const int q = c >> 2, pp = c & 3;
        unsigned ra[4];
#pragma unroll
        for (int m = 0; m < 4; ++m) {
            const int k0 = m < 2 ? 8 * g + 4 * m : 32 + 8 * g + 4 * (m - 2), ga = k0 >> 4, qq = (k0 >> 2) & 3;
            const int e = 32 * ga + 2 * qq + (q & 1) + 8 * (q >> 1);
            ra[m] = e * 64 + pp * 8;
        }
        wo_stage(p, (unsigned)(size_t)lw, 8 * w, cs, lane);
        wo_stage(p, (unsigned)(size_t)lw + 16384, 8 * w + 2, cs, lane);
#pragma unroll
        for (int rd = 0; rd < 4; ++rd) {
            if (rd < 3) wait_vm<16>(); else wait_vm<0>();
#pragma unroll
            for (int i = 0; i < 2; ++i)
#pragma unroll
                for (int sel = 0; sel < 2; ++sel)
#pragma unroll
                    for (int nt = 0; nt < 2; ++nt) {
                        LAS uchar* a = lw + (rd & 1) * 16384 + i * 8192 + sel * 1024 + nt * 32;
                        const u32x2 t0 = trd(a + ra[0]), t1 = trd(a + ra[1]), t2 = trd(a + ra[2]), t3 = trd(a + ra[3]);
                        wf[2 * rd + i][sel][nt] = (u32x8){t0.x, t0.y, t1.x, t1.y, t2.x, t2.y, t3.x, t3.y};
                        if (2 * rd + i < 6) asm volatile("" : "+a"(wf[2 * rd + i][sel][nt])); else asm volatile("" : "+v"(wf[2 * rd + i][sel][nt]));
                    }
            asm volatile("s_waitcnt lgkmcnt(0)" ::: "memory");
            SB();
            if (rd < 2) wo_stage(p, (unsigned)(size_t)lw + (rd & 1) * 16384, 8 * w + 2 * rd + 4, cs, lane);
        }
    }
    bar();
    if (tid < 2) *(LAS unsigned*)(lds + 4096 + tid * 4) = 0;
    bar();
    const int idx = (c & 4) ? 0xEEEE : 0x4444;
    const int dl = 32 * cs + 2 * c;
    const unsigned so = mf_off(dl >> 6) + (((dl >> 4) & 3) * 16 + qh * 8 + ((dl >> 3) & 1) * 4 + g) * 16 + (dl & 7) * 2;
    const unsigned rwo = (w * 128 + (2 * (g & 1)) * 32 + 16 * (g >> 1) + c) * 4, rro = (g * 32 + 2 * c) * 4;
    const unsigned lo16 = lane * 16, sto = lane == 0 ? mi * 4 : OOB;
    unsigned long long alive = ~0ull;
    const unsigned exo = ex_off(p), pol = read_pol(p);
    wait_ctr(p, rw, C_PZ, 56);
    Clock ck; ck.start();
    unsigned tdone = ck.now();
    Sched sc = {0u, tdone - V_M, DUR_A0, DUR_M0, PAD0, 0u, 0u};
    for (int s = 0; s < NSEG - 1; ++s) {
        const unsigned eb = ex_step(exo, pol, s);
        const unsigned ob = eb + w * 32768 + qh * 1024;
        if (s == 3) wait_ctr(p, rw, C_PZ + 32, 56);
        if (s > 0) { waitmt(ck.cycles(tdone + D_MST)); sc.fetch(rw, exo, pol, s, lane); ck.sync(); sc.next(p, rw, exo, pol, s, lane, alive); }
        const unsigned tau = sc.tau;
        unsigned flag = 0;
        unsigned obs[8], mb = ex_step(exo, pol, s + 1);
#pragma unroll
        for (int i = 0; i < 8; ++i) { obs[i] = ob + (i >> 2) * 16384 + (i & 3) * 2048; asm volatile("" : "+s"(obs[i])); }
        unsigned rwa = rwo + (s & 1) * 2048, rra = rro + (s & 1) * 2048;
        f32x4 acc[2][2];
#pragma unroll
        for (int i = 0; i < 4; ++i) { acc[i >> 1][i & 1] = (f32x4){0.f, 0.f, 0.f, 0.f}; asm volatile("" : "+v"(acc[i >> 1][i & 1])); }
        unsigned tw = ck.cycles(tau + V_M);
        asm volatile("" : "+s"(mb), "+v"(rwa), "+v"(rra), "+s"(tw));
        waitmt(tw);
        FT(0);
        u32x4 of[8];
#pragma unroll
        for (int i = 0; i < 8; ++i) { of[i] = ld16(rw, lo16, obs[i]); SB(); if (i < 7) pace(); SB(); }
        for (;;) {
            unsigned mx = 0;
            unsigned long long early = 0;
#pragma unroll
            for (int i = 0; i < 8; ++i) {
                if (i == 7) {
                    mx4v(mx, of[i]);
                    early = __builtin_amdgcn_ballot_w64(mx == SENT) & alive;
                    asm volatile("" : "+s"(early));
                    SB();
                }
#pragma unroll
                for (int j = 0; j < 4; ++j) { if (i < 6) smf_a(acc[j >> 1][j & 1], of[i], wf[i][j >> 1][j & 1], idx); else smf_v(acc[j >> 1][j & 1], of[i], wf[i][j >> 1][j & 1], idx); }
                if (i < 7) mx4v(mx, of[i]);
                if (i == 0) FT(1);
                if (i == 7) FT(2);
                SB();
            }
            if (__builtin_expect(early == 0, 1)) break;
            unsigned n = SPIN_CAP; bool bad;
#pragma nounroll
            do {
                asm volatile("" ::: "memory");
#pragma unroll
                for (int i = 0; i < 8; ++i) of[i] = ld16(rw, lo16, obs[i]);
                unsigned m2 = 0;
#pragma unroll
                for (int i = 0; i < 8; ++i) m2 = mx4(m2, of[i]);
                bad = __builtin_amdgcn_ballot_w64(m2 == SENT) != 0;
            } while (bad && --n);
            if (bad) { alive = 0; set_tmo(p); }
            flag = 1;
#pragma unroll
            for (int i = 0; i < 4; ++i) { acc[i >> 1][i & 1] = (f32x4){0.f, 0.f, 0.f, 0.f}; asm volatile("" : "+v"(acc[i >> 1][i & 1])); }
            asm volatile("" ::: "memory");
        }
        mfma_pad(acc[0][0], acc[0][1], acc[1][0], acc[1][1]);
        float ya[4], yb[4], y[4];
#pragma unroll
        for (int r = 0; r < 4; ++r) { ya[r] = g < 2 ? acc[0][0][r] : acc[1][0][r]; yb[r] = g < 2 ? acc[0][1][r] : acc[1][1][r]; }
        SB();
#pragma unroll
        for (int r = 0; r < 4; ++r) y[r] = swap32_add(ya[r], yb[r]);
        const float z0 = swap16_add(y[0], y[2]), z1 = swap16_add(y[1], y[3]);
        *(LAS float*)(lds + rwa) = z0;
        *(LAS float*)(lds + rwa + 128) = z1;
        FT(3);
        if (flag) *(LAS unsigned*)(lds + 4096 + (s & 1) * 4) = s + 1;
        bar();
        FT(4);
        if (w == 0) {
            LAS f32x2* r2 = (LAS f32x2*)(lds + rra);
            const f32x2 a0 = r2[0], a1 = r2[64], a2 = r2[128], a3 = r2[192];
            SB();
            const f32x2 v = (a0 + a1) + (a2 + a3);
            st4(nosent(pk2(v[0], v[1])), rw, so, mb);
            tdone = ck.now();
            st4(nosent((tdone & ~1u) | (*(volatile LAS unsigned*)(lds + 4096 + (s & 1) * 4) == (unsigned)(s + 1))), rw, sto, eb + EX_MST);
        } else tdone = ck.now();
        FT(5); FDUMP(32 + mi, s, w);
    }
}

constexpr int RING = 4, VPRE = 4;
constexpr int a_slot(int ks, int tile) { return ks * 16384 + tile * 2048; }
constexpr int a_xblk(int i) { return i < 3 ? a_slot(4, 6) + i * 1280 : a_slot(9, 6); }
constexpr int A_P = a_slot(9, 6) + 1280, A_P2 = A_P + 1024;
template <int W> struct AP {
    static constexpr int ROT = 3 * W, NT = W == 0 ? 0 : (W == 3 ? 2 : 3), T0 = W == 0 ? 0 : 3 * (W - 1);
    static constexpr int NL = W == 0 ? 0 : (W == 3 ? 8 : 13);
    static constexpr int QA = W == 0 ? 20 : (W == 3 ? 19 : 14);
    static constexpr bool k_lds(int t, int m) { return W == 0 ? false : (W == 3 ? (t == 1 && m >= 2) : (t == 1 || m % 3 == 2)); }
    static constexpr bool k_acc(int t, int m) { return W == 0 && m < 5; }
    static constexpr bool v_lds(int t, int ks) { return W == 3 ? ks < 4 : (ks < 4 || (ks == 4 && t == 0)); }
    static constexpr int k_idx(int t, int m) { int n = 0; for (int i = 0; i < 2 * m + t; ++i) n += k_lds(i & 1, i >> 1) ? 1 : 0; return n; }
    static constexpr int k_slot(int i) { return a_slot(5 + i / (NT ? NT : 1), T0 + i % (NT ? NT : 1)); }
    static constexpr int v_slot(int i) { return a_slot(i / (NT ? NT : 1), T0 + i % (NT ? NT : 1)); }
    static constexpr int stream(int i) { return i < NL ? k_slot(i) : v_slot(i - NL); }
    static constexpr int m_of(int ks) { return (ks + 10 - ROT % 10) % 10; }
};
DI void a_dma(const uchar* wsrc, LAS uchar* lds, int h, int w, int half) {
    const int lane = fresh_lane();
    const rsrc_t r = mk_rsrc(wsrc, 640 * 8192);
    const unsigned v = (lane >> 4) * 8192 + (((lane & 15) ^ (2 * (((lane >> 4) & 1) | (w << 1)))) << 4);
    const unsigned lb = (unsigned)(size_t)lds;
#pragma unroll
    for (int i = 20 * half; i < 20 * half + 20; ++i) dma16(r, v, (4 * (w + 4 * i)) * 8192 + h * 256, lb + (w + 4 * i) * 1024);
}
struct ATake {
    unsigned row[4], f[4];
    DI ATake() {
        const int lane = fresh_lane(), c = lane & 15, g = lane >> 4, q = c >> 2, pp = c & 3;
#pragma unroll
        for (int m = 0; m < 4; ++m) {
            const int k0 = m < 2 ? 8 * g + 4 * m : 32 + 8 * g + 4 * (m - 2), ga = k0 >> 4, qq = (k0 >> 2) & 3;
            const int r = 16 * ga + 2 * qq + (q & 1) + 8 * (q >> 1);
            row[m] = r * 256 + pp * 8; f[m] = 32 * ((r & 1) | ((r >> 1) & 6));
        }
    }
    DI u32x8 get(LAS uchar* lds, int ks, int tile) const {
        LAS uchar* a = lds + ks * 16384;
        const u32x2 t0 = trd(a + row[0] + (f[0] ^ (32 * tile))), t1 = trd(a + row[1] + (f[1] ^ (32 * tile))), t2 = trd(a + row[2] + (f[2] ^ (32 * tile))), t3 = trd(a + row[3] + (f[3] ^ (32 * tile)));
        return (u32x8){t0.x, t0.y, t1.x, t1.y, t2.x, t2.y, t3.x, t3.y};
    }
};
DI u32x8 ldsB(LAS uchar* a) { const u32x4 lo = *(LAS u32x4*)a, hi = *(LAS u32x4*)(a + 1024); return (u32x8){lo.x, lo.y, lo.z, lo.w, hi.x, hi.y, hi.z, hi.w}; }
DI void ldsB_put(LAS uchar* a, u32x8 v) { *(LAS u32x4*)a = (u32x4){v[0], v[1], v[2], v[3]}; *(LAS u32x4*)(a + 1024) = (u32x4){v[4], v[5], v[6], v[7]}; }
DI void pin(u32x8& v, bool acc) { if (acc) asm volatile("" : "+a"(v)); else asm volatile("" : "+v"(v)); }
DI void smf_x(f32x4& c, u32x4 a, u32x8 b, int idx, bool acc) { if (acc) smf_a(c, a, b, idx); else smf_v(c, a, b, idx); }

#define NEXT_KV() do { SB(); const int sn_ = min(s + 1, NSEG - 1); kvn = ld4(rw, 0, kvw * 4); \
    _Pragma("unroll") for (int i = 0; i < NPF; ++i) pf[i] = ld16(rw, lo16, pfb + sn_ * 32 * 8192 + i * 1024); SB(); } while (0)
template <int W>
DI void a_loop(const Params& p, LAS uchar* lds, int h, const u32x8 (&wq)[2][10], const u32x8 (&wk)[2][10], const u32x8 (&wv)[3][10]) {
    typedef AP<W> P;
    const int lane = fresh_lane(), c = lane & 15, g = lane >> 4;
    const rsrc_t rw = mk_rsrc(p.ws, WS_USED);
    constexpr int ROT = P::ROT, NT = P::NT, T0 = P::T0, NPF = W == 0 ? 8 : NT, NL = P::NL;
    const int idx = (c & 8) ? 0xEEEE : 0x4444;
    const unsigned lo16 = lane * 16, mo16 = (g * 16 + ((c >> 2) & 1) * 8 + (c >> 3) * 4 + (c & 3)) * 16;
    const unsigned oaddr = W == 0 ? (c >= 8 ? (g * 8 + (c & 7)) * 16 : OOB) : (c < 8 ? (c >> 2) * 1024 + (g >> 1) * 64 + (c & 3) * 16 + (g & 1) * 8 : OOB);
    const unsigned oaddr2 = c < 8 ? (c >> 2) * 1024 + (g & 1) * 128 + (g >> 1) * 64 + (c & 3) * 16 : OOB;
    const unsigned xw = a_xblk(W) + c * 40 + g * 8;
    const unsigned asto = lane == 0 ? h * 4 : OOB;
    const unsigned pfb = W == 0 ? OFF_KF + h * 8192 : OFF_VF + h * 8192 + T0 * 1024;
    u32x4 pf[NPF];
    unsigned long long alive = ~0ull;
    const unsigned exo = ex_off(p), pol = read_pol(p);
    Clock ck; ck.start();
    wait_ctr(p, rw, C_PZ, 56);
    wait_ctr(p, rw, C_KVH + h * 16, 4);
#pragma unroll
    for (int i = 0; i < NPF; ++i) pf[i] = ld16(rw, lo16, pfb + i * 1024);
    Sched sc = {ck.now(), 0u, DUR_A0, DUR_M0, PAD0, 0u, 0u};
    const float ninf = g < 2 ? -__builtin_inff() : 0.f;
    unsigned xra[4], xqa[4];
    if (W == 0) {
#pragma unroll
        for (int ks = 0; ks < 4; ++ks) {
            xra[ks] = a_xblk(ks) + (4 * g + (c >> 2)) * 40 + (c & 3) * 8; xqa[ks] = a_xblk(ks) + (4 * g + (c >> 2)) * 40 + (c & 1) * 8;
            asm volatile("" : "+v"(xra[ks]), "+v"(xqa[ks]));
        }
    }
    u32x4 one4 = {0x3F803F80u, 0x3F803F80u, 0x3F803F80u, 0x3F803F80u}, p2 = {0u, 0u, 0u, 0u}, p2s = p2, p2h = p2;
    unsigned pa[3] = {A_P + lo16, A_P2 + lane * 8u, A_P2 + (lane ^ 32) * 8u};
    if (W) asm volatile("" : "+a"(one4), "+v"(p2), "+v"(p2s), "+v"(p2h), "+v"(pa[0]), "+v"(pa[1]), "+v"(pa[2]));
    unsigned lb[3] = {lo16, lo16 + 65536, lo16 + 131072};
    asm volatile("" : "+v"(lb[0]), "+v"(lb[1]), "+v"(lb[2]));
    auto sget = [&](int i) -> u32x8 { const int a = P::stream(i); return ldsB(lds + lb[a >> 16] + (a & 65535)); };
    for (int s = 0; s < NSEG; ++s) {
        const unsigned eb = ex_step(exo, pol, s);
        unsigned mb[10], ob = eb + (h >> 2) * 16384 + (h & 3) * 2048 + T0 * 128;
#pragma unroll
        for (int m = 0; m < 10; ++m) { mb[m] = eb + mf_off((m + ROT) % 10); asm volatile("" : "+s"(mb[m])); }
        f32x4 aq[2], ak[2], va[3];
        u32x8 ring[RING];
#pragma unroll
        for (int t = 0; t < 2; ++t) { aq[t] = (f32x4){0.f, 0.f, 0.f, 0.f}; ak[t] = aq[t]; asm volatile("" : "+v"(aq[t]), "+v"(ak[t])); }
#pragma unroll
        for (int t = 0; t < 3; ++t) {
            const float z = W == 0 && t == 2 ? ninf : 0.f;
            va[t] = (f32x4){z, z, z, z}; asm volatile("" : "+v"(va[t]));
        }
        if (W) {
#pragma unroll
            for (int i = 0; i < RING; ++i) ring[i] = sget(i);
        }
        const unsigned tgo = sc.tgo;
        unsigned tw = ck.cycles(tgo);
        asm volatile("" : "+s"(ob), "+s"(tw));
        SB();
        unsigned mis = waitmt(tw);
        FT(0);
        u32x4 mf[10];
#pragma unroll
        for (int m = 0; m < 10; ++m) {
            mf[m] = ld16(rw, mo16, mb[m]);
            SB();
            if (m < 9) pace();
            SB();
        }
        for (;;) {
            unsigned mx = 0;
            unsigned long long early = 0;
#pragma unroll
            for (int m = 0; m < 10; ++m) {
                if (m == 9) {
                    mx4v(mx, mf[m]);
                    early = __builtin_amdgcn_ballot_w64(mx == SENT) & alive;
                    asm volatile("" : "+s"(early));
                    SB();
                }
#pragma unroll
                for (int t = 0; t < 2; ++t) {
                    smf_x(aq[t], mf[m], wq[t][m], idx, 2 * m + t < P::QA - (W == 0 ? 10 : 0));
                    if (P::k_lds(t, m)) {
                        const int i = P::k_idx(t, m);
                        smf_v(ak[t], mf[m], ring[i % RING], idx);
                        ring[i % RING] = sget(i + RING);
                    } else smf_x(ak[t], mf[m], wk[t][m], idx, W == 0);
                }
                if (m < 9) mx4v(mx, mf[m]);
                if (m == 0) FT(1);
                if (m == 9) FT(2);
                SB();
            }
            if (__builtin_expect(early == 0, 1)) break;
            unsigned n = SPIN_CAP; bool bad;
#pragma nounroll
            do {
                asm volatile("" ::: "memory");
#pragma unroll
                for (int m = 0; m < 10; ++m) mf[m] = ld16(rw, mo16, mb[m]);
                unsigned m2 = 0;
#pragma unroll
                for (int m = 0; m < 10; ++m) m2 = mx4(m2, mf[m]);
                bad = __builtin_amdgcn_ballot_w64(m2 == SENT) != 0;
            } while (bad && --n);
            if (bad) { alive = 0; set_tmo(p); }
            mis = tw + 100000;
#pragma unroll
            for (int t = 0; t < 2; ++t) { aq[t] = (f32x4){0.f, 0.f, 0.f, 0.f}; ak[t] = aq[t]; asm volatile("" : "+v"(aq[t]), "+v"(ak[t])); }
            if (W) {
#pragma unroll
                for (int i = 0; i < RING; ++i) ring[i] = sget(i);
            }
            asm volatile("" ::: "memory");
        }
        {
            float x[4], y[4];
            asm volatile("s_nop 2" : "+v"(aq[0]), "+v"(ak[0]));
#pragma unroll
            for (int r = 0; r < 4; ++r) x[r] = swap32_add(aq[0][r], ak[0][r]);
            const u32x2 x2 = {pk2(x[0], x[1]), pk2(x[2], x[3])};
            SB();
            asm volatile("" : "+v"(aq[1]), "+v"(ak[1]));
#pragma unroll
            for (int r = 0; r < 4; ++r) y[r] = swap32_add(aq[1][r], ak[1][r]);
            *(LAS u32x2*)(lds + xw) = x2;
            *(LAS u32x2*)(lds + xw + 640) = (u32x2){pk2(y[0], y[1]), pk2(y[2], y[3])};
        }
        SB();
        if (W) {
#pragma unroll
            for (int i = 0; i < VPRE; ++i) { const int r = 10 * NT - 1 - i; smf_a(va[r % NT], mf[P::m_of(r / NT)], wv[r % NT][r / NT], idx); }
        }
        bar();
        SB();
        FT(3);
        const unsigned kb = OFF_KM + (s * 32 + h) * 4096;
        const int kvw = s < 2 ? C_KVH + h * 16 : C_KV + ((min(s + 2, NSEG - 1) >> 3) * 32 + h) * 16;
        unsigned tpub = 0, kvn;
        if (W == 0) {
            u32x4 xf[4], xd[4];
#pragma unroll
            for (int ks = 0; ks < 4; ++ks) {
                const u32x2 a = trd(lds + xqa[ks]), b = trd(lds + xqa[ks] + 640), lo = trd(lds + xra[ks]), hi = trd(lds + xra[ks] + 640);
                xd[ks] = (u32x4){a.x, a.y, b.x, b.y}; xf[ks] = (u32x4){lo.x, lo.y, hi.x, hi.y};
            }
            SB();
#pragma unroll
            for (int ks = 0; ks < 4; ++ks) { mfma_v(va[0], pf[ks], xd[ks]); mfma_v(va[1], pf[(4 + ks) % NPF], xd[ks]); mfma_v(va[2], xf[ks], xd[ks]); }
            FT(4);
            float a0, a1, a2, a3, a4, a5, tm, tn;
            unsigned e0, e1, e2;
            asm volatile("s_nop 7\n\t"
                         "v_cndmask_b32_e64 %0, %15, %11, %23\n\tv_cndmask_b32_e64 %1, %16, %12, %23\n\tv_cndmask_b32_e64 %2, %17, %13, %23\n\t"
                         "v_cndmask_b32_e64 %3, %18, %14, %23\n\tv_cndmask_b32_e64 %4, %21, %19, %23\n\tv_cndmask_b32_e64 %5, %22, %20, %23\n\t"
                         "v_max3_f32 %6, %0, %1, %2\n\tv_max3_f32 %7, %3, %4, %5\n\tv_max_f32 %6, %6, %7\n\ts_nop 1\n\t"
                         "v_max_f32_dpp %6, %6, %6 row_ror:8 row_mask:0xf bank_mask:0xf\n\t"
                         "v_mov_b32 %7, %6\n\ts_nop 1\n\tv_permlane32_swap_b32 %6, %7\n\tv_max_f32 %6, %6, %7\n\t"
                         "v_mov_b32 %7, %6\n\ts_nop 1\n\tv_permlane16_swap_b32 %6, %7\n\tv_max_f32 %6, %6, %7\n\t"
                         "v_mul_f32 %6, %24, %6\n\t"
                         "v_fma_f32 %0, %0, %24, -%6\n\tv_fma_f32 %1, %1, %24, -%6\n\tv_fma_f32 %2, %2, %24, -%6\n\t"
                         "v_fma_f32 %3, %3, %24, -%6\n\tv_fma_f32 %4, %4, %24, -%6\n\tv_fma_f32 %5, %5, %24, -%6\n\t"
                         "v_exp_f32 %0, %0\n\tv_exp_f32 %1, %1\n\tv_exp_f32 %2, %2\n\tv_exp_f32 %3, %3\n\tv_exp_f32 %4, %4\n\tv_exp_f32 %5, %5\n\t"
                         "v_cvt_pk_bf16_f32 %8, %0, %1\n\tv_cvt_pk_bf16_f32 %9, %2, %3\n\tv_cvt_pk_bf16_f32 %10, %4, %5"
                         : "=&v"(a0), "=&v"(a1), "=&v"(a2), "=&v"(a3), "=&v"(a4), "=&v"(a5), "=&v"(tm), "=&v"(tn), "=&v"(e0), "=&v"(e1), "=&v"(e2)
                         : "v"(va[0][0]), "v"(va[0][1]), "v"(va[0][2]), "v"(va[0][3]), "v"(va[1][0]), "v"(va[1][1]), "v"(va[1][2]), "v"(va[1][3]),
                           "v"(va[2][0]), "v"(va[2][1]), "v"(va[2][2]), "v"(va[2][3]), "s"(0x00FF00FF00FF00FFull), "s"(SCL));
            *(LAS u32x2*)(lds + A_P + (g * 16 + (c & 7)) * 16 + (c >> 3) * 8) = (u32x2){e0, e1};
            *(LAS unsigned*)(lds + A_P2 + (g * 16 + (c & 7)) * 8 + (c >> 3) * 4) = e2;
            FT(5);
            bar();
            FT(6);
            sc.fetch(rw, exo, pol, max(s, 1), lane);
            NEXT_KV();
            waitmt(mt32() + KM_DELAY * 3);
            st4(s, rw, lane == 0 ? (C_KM + h * 32) * 4 : OOB, 0);
#pragma unroll
            for (int ks = 0; ks < 4; ++ks) st16(xf[ks], rw, oaddr, kb + ks * 512);
        } else {
#pragma unroll
            for (int i = 0; i < (10 * NT - NL - VPRE > NL ? 10 * NT - NL - VPRE : NL); ++i) {
                if (i < NL) {
                    smf_v(va[i % NT], mf[P::m_of(i / NT)], ring[(NL + i) % RING], idx);
                    if (NL + i + RING < 2 * NL) ring[(NL + i) % RING] = sget(NL + i + RING);
                }
                const int r = NL + i;
                if (r < 10 * NT - VPRE) smf_a(va[r % NT], mf[P::m_of(r / NT)], wv[r % NT][r / NT], idx);
                SB();
            }
            asm volatile("s_nop 7" : "+v"(va[0]), "+v"(va[1]), "+v"(va[2]));
            float sa[4], sb[4];
#pragma unroll
            for (int r = 0; r < 4; ++r) { sa[r] = swap32_add(va[0][r], va[1][r]); if (W != 3) sb[r] = swap32_add(va[2][r], va[2][r]); }
            u32x4 vv = {pk2(sa[0], sa[1]), pk2(sa[2], sa[3]), 0u, 0u};
            if (W != 3) { vv.z = pk2(sb[0], sb[1]); vv.w = pk2(sb[2], sb[3]); }
            asm volatile("" : "+v"(vv));
            SB();
            FT(5);
            bar();
            FT(6);
            SB();
            if (s < NSEG - 1) {
                const u32x4 p01 = *(LAS u32x4*)(lds + pa[0]);
                const u32x2 q2 = *(LAS u32x2*)(lds + pa[1]), q2s = *(LAS u32x2*)(lds + pa[2]);
                p2.x = q2.x; p2.y = q2.y; p2s.x = q2s.x; p2s.y = q2s.y;
                if (W != 3) { const u32x2 q2h = *(volatile LAS u32x2*)(lds + pa[1]); p2h.z = q2h.x; p2h.w = q2h.y; }
                SB();
                f32x4 l, o[3];
                asm volatile("v_mfma_f32_16x16x32_bf16 %0, %1, %2, 0" : "=&v"(l) : "a"(one4), "v"(p01));
                mfma_0(o[0], pf[0], p01);
                mfma_av(l, one4, p2);
                mfma_0(o[1], pf[1 % NPF], p01);
                mfma_v(o[0], vv, p2s);
                mfma_v(o[1], vv, p2);
                float inv;
                if (W != 3) {
                    mfma_0(o[2], pf[2 % NPF], p01);
                    asm volatile("v_rcp_f32 %0, %1" : "=v"(inv) : "v"(l[0]));
                    mfma_v(o[2], vv, p2h);
                    asm volatile("s_nop 3" : "+v"(o[0]), "+v"(o[1]), "+v"(inv));
                } else {
                    asm volatile("s_nop 1\n\tv_rcp_f32 %0, %3\n\ts_nop 5" : "=&v"(inv), "+v"(o[0]), "+v"(o[1]) : "v"(l[0]));
                }
                const f32x4 n0 = o[0] * inv, n1 = o[1] * inv;
                SB();
                const unsigned a0 = pk2(n0[0], n0[1]), b0 = pk2(n1[0], n1[1]), a1 = pk2(n0[2], n0[3]), b1 = pk2(n1[2], n1[3]);
                SB();
                auto r0 = __builtin_amdgcn_permlane16_swap(a0, b0, false, false);
                auto r1 = __builtin_amdgcn_permlane16_swap(a1, b1, false, false);
                st16((u32x4){nosent(r0[0]), nosent(r1[0]), nosent(r0[1]), nosent(r1[1])}, rw, oaddr2, ob);
                if (W != 3) {
                    asm volatile("" : "+v"(o[2]));
                    const f32x4 n2 = o[2] * inv;
                    st8((u32x2){nosent(pk2(n2[0], n2[1])), nosent(pk2(n2[2], n2[3]))}, rw, oaddr, ob + 256);
                }
            }
            tpub = ck.now();
            sc.fetch(rw, exo, pol, max(s, 1), lane);
            NEXT_KV();
            if (W == 1) st4(mis - tw <= 120u ? tpub - tgo : 0u, rw, asto, eb + EX_AST);
            st8((u32x2){vv.x, vv.y}, rw, lane * 8, kb + 2048 + T0 * 256);
            if (W != 3) st8((u32x2){vv.z, vv.w}, rw, g < 2 ? lane * 8 : OOB, kb + 2048 + T0 * 256 + 512);
        }
        FT(7); FDUMP(h, s, W);
        STAT(h, s, W, ck.at(mis), tpub, tgo, tries);
        ck.sync();
        if (s > 0) sc.next(p, rw, exo, pol, s, lane, alive);
        if ((unsigned)__builtin_amdgcn_readfirstlane(kvn) < 4u) wait_ctr(p, rw, kvw, 4);
        if (s == 2) wait_ctr(p, rw, C_PZ + 32, 56);
    }
    wait_vm<0>();
    bar();
    if (W == 0) st4(NSEG, rw, lane == 0 ? (C_KM + h * 32) * 4 : OOB, 0);
    __builtin_amdgcn_endpgm();
}

template <int W>
DI void a_wave(const Params& p, LAS uchar* lds, int h) {
    typedef AP<W> P;
    u32x8 wq[2][10], wk[2][10], wv[3][10];
    a_dma(p.wq, lds, h, W, 0);
    a_dma(p.wq, lds, h, W, 1);
    static_for<6>([&](auto sc) {
        constexpr int mat = sc.value >> 1, half = sc.value & 1;
        if (sc.value < 5) wait_vm<20>(); else wait_vm<0>();
        bar();
        AST_(sc.value);
        const ATake tk;
#pragma unroll
        for (int ks = 5 * half; ks < 5 * half + 5; ++ks) {
            const int m = P::m_of(ks);
#pragma unroll
            for (int t = 0; t < (mat < 2 ? 2 : P::NT); ++t) {
                if (mat == 0) { wq[t][m] = tk.get(lds, ks, 2 * W + t); pin(wq[t][m], 2 * m + t < P::QA - (W == 0 ? 10 : 0)); }
                if (mat == 1) { wk[t][m] = tk.get(lds, ks, 2 * W + t); pin(wk[t][m], W == 0); }
                if (mat == 2) { wv[t][ks] = tk.get(lds, ks, P::T0 + t); pin(wv[t][ks], true); }
            }
            SB();
        }
        bar(); SB();
        if (mat < 2) a_dma(mat == 0 ? p.wk : p.wv, lds, h, W, half);
        else {
            const int lane = fresh_lane();
            if (half == 0) {
#pragma unroll
                for (int i = 0; i < P::NL; ++i) ldsB_put(lds + P::v_slot(i) + lane * 16, wv[i % (P::NT ? P::NT : 1)][i / (P::NT ? P::NT : 1)]);
            } else {
#pragma unroll
                for (int i = 0; i < 20; ++i) if (P::k_lds(i & 1, i >> 1)) ldsB_put(lds + P::k_slot(P::k_idx(i & 1, i >> 1)) + lane * 16, wk[i & 1][i >> 1]);
            }
        }
        SB();
    });
    bar();
    SB();
    a_loop<W>(p, lds, h, wq, wk, wv);
}
DI void role_A(const Params& p, LAS uchar* lds, int h) {
    const int w = __builtin_amdgcn_readfirstlane(threadIdx.x >> 6);
    if (w == 0) a_wave<0>(p, lds, h);
    else if (w == 1) a_wave<1>(p, lds, h);
    else if (w == 2) a_wave<2>(p, lds, h);
    else a_wave<3>(p, lds, h);
}
DI int take_slot(const Params& p, LAS uchar* lds) {
    if (threadIdx.x == 0) {
        unsigned x = xcc_id(), t;
        for (int i = 0; i < 8; ++i) {
            t = __hip_atomic_fetch_add((unsigned*)p.ws + C_POOL + x * 32, 1u, __ATOMIC_RELAXED, __HIP_MEMORY_SCOPE_AGENT);
            if (t < 32) break;
            x = (x + 1) & 7;
        }
        *(LAS unsigned*)lds = x * 32 + t;
    }
    bar();
    const int slot = __builtin_amdgcn_readfirstlane(*(LAS unsigned*)lds);
    bar();
    return slot;
}
DI int take_ticket(const Params& p, LAS uchar* lds, int word) {
    if (fresh_tid() == 0) *(LAS unsigned*)(lds + LDS_BYTES - 16) = __hip_atomic_fetch_add((unsigned*)p.ws + word, 1u, __ATOMIC_RELAXED, __HIP_MEMORY_SCOPE_AGENT);
    bar();
    const int t = __builtin_amdgcn_readfirstlane(*(LAS unsigned*)(lds + LDS_BYTES - 16));
    bar();
    return t;
}
#define TSTAT(i, a, b)
#define SMALL_DELAY 300
constexpr int N_T = 24, N_WG = 5;
DI void warm_l2(const Params& p, int x, int j) {
    const int lane = fresh_lane(), id = j * 4 + __builtin_amdgcn_readfirstlane(threadIdx.x >> 6);
    u32x4 sink[46];
#pragma unroll
    for (int i = 0; i < 46; ++i) {
        const int n = min(id + 56 * i, 2559), row = n >> 1;
        const rsrc_t r = mk_rsrc(row < 640 ? p.wk : p.wv, 640 * 8192);
        asm volatile("buffer_load_dwordx4 %0, %1, %2, %3 offen" : "=v"(sink[i]) : "v"(lane * 16), "s"(r), "s"((row < 640 ? row : row - 640) * 8192 + x * 2048 + (n & 1) * 1024) : "memory");
    }
    wait_vm<0>();
#pragma unroll
    for (int i = 0; i < 46; ++i) asm volatile("" :: "v"(sink[i]));
}
DI void background(const Params& p, LAS uchar* lds, int x, int j) {
    const bool big = x >= 4;
    const int nh = big ? 6 : 2, h0 = big ? 8 + 6 * (x - 4) : 2 * x, nq = big ? 3 : 1, q0 = big ? 4 + 3 * (x - 4) : x;
    if (!big) {
        const int id = j * 4 + x;
        ln_tile(p, id);
        poison(p, id, 0, 80, C_PZ);
        ln_tile(p, id + 56);
        if (id < 16) ln_tile(p, id + 112);
        poison(p, id, 80, 1280, C_PZ + 32);
    } else {
        if (j & 2) kv0_task<true>(p, lds, 32 * (x - 4) + j); else kv0_task<false>(p, lds, 32 * (x - 4) + j);
        TSTAT(x * 128 + 96 + j, t0, 0);
    }
    const int e0 = 0, ea = e0 + 2 * nh, e1 = ea + 6 * nh, e2 = e1 + (x < 7 ? 8 : 0), e3 = e2 + 8 * nq, e4 = e3 + (big ? 8 : 2);
    for (;;) {
        const int t = take_ticket(p, lds, C_Q1 + x * 32);
        if (t >= e4) break;
        if (t < e3) {
            int chunk, u0, nu = 4;
            if (t < ea) { chunk = 1; u0 = 4 * h0 + 2 * (t - e0); nu = 2; }
            else if (t < e1) { const int i = big ? ((t - ea) * 43) >> 8 : (t - ea) >> 1; chunk = 2 + i; u0 = 4 * (h0 + (t - ea) - nh * i); }
            else if (t < e2) { chunk = t - e1; u0 = 192 + 4 * x; if (x == 6) nu = 3; }
            else { chunk = (t - e2) & 7; u0 = 128 + 4 * (q0 + ((t - e2) >> 3)); }
            p1_task(p, lds, chunk, u0, nu);
            TSTAT(x * 128 + t, t0, u0 < 128 ? (nu == 1 ? 0 : 1) : (u0 < 192 ? 2 : 3));
        } else {
            down_task(p, lds, big ? t - e3 : 2 * x + t - e3, big ? x - 4 : 4);
            TSTAT(x * 128 + t, t0, 4);
        }
    }
    if (!big) return;
    const int t = take_ticket(p, lds, C_P2);
    if (t >= N_T + 20 * N_WG) return;
    if (t < N_T) {
        const int w = __builtin_amdgcn_readfirstlane(threadIdx.x >> 6);
        for (int s = t >> 3; s < NSEG; s += N_T / 8) attn_task(p, s, (t & 7) * 4 + w);
    } else { const int gq = ((t - N_T) * 205) >> 12; w_role(p, lds, (t - N_T) - 20 * gq, gq, N_WG); }
    TSTAT(1024 + t, t0, t < N_T ? 5 : 6);
}
__global__ void __launch_bounds__(256, 1) k_main(Params p) {
    extern __shared__ __attribute__((aligned(16))) uchar smem[];
    const int slot = take_slot(p, (LAS uchar*)smem), x = slot >> 5, t = slot & 31;
    if (x < 4 && t < 8) role_A(p, (LAS uchar*)smem, x * 8 + t);
    else if (x < 4 && t < 18) role_M(p, (LAS uchar*)smem, x * 10 + t - 8);
    else background(p, (LAS uchar*)smem, x, x < 4 ? t - 18 : t);
}

extern "C" void kernel_launch(void* const* d_in, const int* in_sizes, int n_in, void* d_out, int out_size, void* d_ws, size_t ws_size, hipStream_t stream) {
    Params p;
    p.x = (const uchar*)d_in[0]; p.mem0 = (const uchar*)d_in[1]; p.wq = (const uchar*)d_in[2]; p.wk = (const uchar*)d_in[3];
    p.wv = (const uchar*)d_in[4]; p.wo = (const uchar*)d_in[5]; p.w1 = (const uchar*)d_in[8]; p.b1 = (const uchar*)d_in[9];
    p.w2 = (const uchar*)d_in[10]; p.out = (uchar*)d_out; p.ws = (uchar*)d_ws;
    (void)hipFuncSetAttribute((const void*)k_main, hipFuncAttributeMaxDynamicSharedMemorySize, LDS_BYTES);
    k_init<<<64, 256, 0, stream>>>(p);
    k_main<<<256, 256, LDS_BYTES, stream>>>(p);
}
```

```cpp
#include <hip/hip_runtime.h>
#include <stdint.h>

typedef __bf16 bf16x2 __attribute__((ext_vector_type(2)));
typedef __bf16 bf16x4 __attribute__((ext_vector_type(4)));
typedef __bf16 bf16x8 __attribute__((ext_vector_type(8)));
typedef __bf16 bf16x16 __attribute__((ext_vector_type(16)));
typedef short s16x4 __attribute__((ext_vector_type(4)));
typedef float f32x2 __attribute__((ext_vector_type(2)));
typedef float f32x4 __attribute__((ext_vector_type(4)));
typedef unsigned u32x2 __attribute__((ext_vector_type(2)));
typedef unsigned u32x4 __attribute__((ext_vector_type(4)));
typedef unsigned u32x8 __attribute__((ext_vector_type(8)));
typedef unsigned char uchar;
typedef __amdgpu_buffer_rsrc_t rsrc_t;
#define LAS __attribute__((address_space(3)))
#define DI __device__ __forceinline__

constexpr int S_ = 2048, D_ = 640, H_ = 32, NSEG = 64, F_ = 1728;
constexpr int LDS_BYTES = 163840;
constexpr int OFF_CTRL = 0;
constexpr int OFF_XF = 65536;
constexpr int OFF_KF = OFF_XF + 128 * 20 * 1024;
constexpr int OFF_QF = OFF_KF + 64 * 32 * 8192;
constexpr int OFF_VF = OFF_QF + 64 * 32 * 8192;
constexpr int OFF_ACT = OFF_VF + 64 * 32 * 8192;
constexpr int OFF_GF = OFF_ACT + 128 * 54 * 1024;
constexpr int OFF_OT = OFF_GF + 128 * 20 * 1024;
constexpr int OFF_KM = OFF_OT + 128 * 32 * 4096;
constexpr int OFF_EX = OFF_KM + 64 * 32 * 4096;
constexpr int EX_STEP = 163840, EX_MF = 8 * 16384, EX_AST = 9 * 16384 + 2048, EX_MST = 9 * 16384 + 2304, EX_PROBE = 9 * 16384 + 6400;
constexpr int WS_USED = OFF_EX + 7 * 2097152;
constexpr int C_TMO = 0, C_POOL = 32  , C_KV = 512  , C_Q = C_KV + 4096, C_ACT = C_Q + 4096  , C_G = C_ACT + 256  ,
              C_OT = C_G + 1280  , C_KM = C_OT + 2048  , C_Q1 = C_KM + 1024  , C_P2 = C_Q1 + 256, C_KVH = C_P2 + 32  , C_X = C_KVH + 512  , C_PZ = C_X + 512  , C_WT = C_PZ + 64  ,
              C_POL = 16320  ;
static_assert(C_WT + 640 <= C_POL, "");
constexpr unsigned SENT = 0xFFFFFFFFu;
constexpr int SC1 = 16;
constexpr unsigned OOB = 0x7FFFFF00u;
constexpr unsigned SPIN_CAP = 400000;

struct Params {
    const uchar* x; const uchar* mem0; const uchar* wq; const uchar* wk; const uchar* wv; const uchar* wo;
    const uchar* w1; const uchar* b1; const uchar* w2; uchar* out; uchar* ws;
};

DI rsrc_t mk_rsrc(const void* p, unsigned bytes) { return __builtin_amdgcn_make_buffer_rsrc((void*)p, 0, bytes, 0x00020000); }
DI u32x4 ld16(rsrc_t r, unsigned v, unsigned s) { return __builtin_amdgcn_raw_buffer_load_b128(r, v, s, SC1); }
DI u32x2 ld8(rsrc_t r, unsigned v, unsigned s) { return __builtin_amdgcn_raw_buffer_load_b64(r, v, s, SC1); }
DI unsigned ld4(rsrc_t r, unsigned v, unsigned s) { return __builtin_amdgcn_raw_buffer_load_b32(r, v, s, SC1); }
DI void st16(u32x4 d, rsrc_t r, unsigned v, unsigned s) { __builtin_amdgcn_raw_buffer_store_b128(d, r, v, s, SC1); asm volatile("s_nop 1" :: "v"(d)); }
DI void st8(u32x2 d, rsrc_t r, unsigned v, unsigned s) { __builtin_amdgcn_raw_buffer_store_b64(d, r, v, s, SC1); }
DI void st4(unsigned d, rsrc_t r, unsigned v, unsigned s) { __builtin_amdgcn_raw_buffer_store_b32(d, r, v, s, SC1); }
DI unsigned pk2(float a, float b) { f32x2 v = {a, b}; return __builtin_bit_cast(unsigned, __builtin_convertvector(v, bf16x2)); }
DI unsigned nosent(unsigned v) { return min(v, 0xFFFFFFFEu); }
DI float bf_lo(unsigned v) { return __builtin_bit_cast(float, v << 16); }
DI float bf_hi(unsigned v) { return __builtin_bit_cast(float, v & 0xFFFF0000u); }
DI float rbf(float a) { return bf_lo(pk2(a, a)); }
DI bf16x8 as8(u32x4 v) { return __builtin_bit_cast(bf16x8, v); }
DI f32x4 mfma32(u32x4 a, u32x4 b, f32x4 c) { return __builtin_amdgcn_mfma_f32_16x16x32_bf16(as8(a), as8(b), c, 0, 0, 0); }
DI f32x4 mfma16(u32x2 a, u32x2 b, f32x4 c) { return mfma32((u32x4){a.x, a.y, 0u, 0u}, (u32x4){b.x, b.y, 0u, 0u}, c); }
DI f32x4 smf(u32x4 a, u32x8 b, f32x4 c, int idx) { return __builtin_amdgcn_smfmac_f32_16x16x64_bf16(as8(a), __builtin_bit_cast(bf16x16, b), c, idx, 0, 0); }
DI void smf_a(f32x4& c, u32x4 a, u32x8 b, int idx) { asm volatile("v_smfmac_f32_16x16x64_bf16 %0, %1, %2, %3" : "+v"(c) : "v"(a), "a"(b), "v"(idx)); }
DI void smf_v(f32x4& c, u32x4 a, u32x8 b, int idx) { asm volatile("v_smfmac_f32_16x16x64_bf16 %0, %1, %2, %3" : "+v"(c) : "v"(a), "v"(b), "v"(idx)); }
DI void mfma_v(f32x4& c, u32x4 a, u32x4 b) { asm volatile("v_mfma_f32_16x16x32_bf16 %0, %1, %2, %0" : "+v"(c) : "v"(a), "v"(b)); }
DI void mfma_0(f32x4& c, u32x4 a, u32x4 b) { asm volatile("v_mfma_f32_16x16x32_bf16 %0, %1, %2, 0" : "=&v"(c) : "v"(a), "v"(b)); }
DI float fmax2(float a, float b) { return __builtin_amdgcn_fmed3f(a, b, __builtin_inff()); }
DI void mfma_pad(f32x4& a, f32x4& b, f32x4& c, f32x4& d) { asm volatile("s_nop 7" : "+v"(a), "+v"(b), "+v"(c), "+v"(d)); }
DI u32x2 trd(LAS uchar* p) { return __builtin_bit_cast(u32x2, __builtin_amdgcn_ds_read_tr16_b64_v4i16((LAS s16x4*)p)); }
DI unsigned mx4(unsigned m, u32x4 v) { return max(max(max(m, v.x), max(v.y, v.z)), v.w); }
DI void mx4v(unsigned& m, u32x4 v) { asm volatile("v_max3_u32 %0, %0, %1, %2\n\tv_max3_u32 %0, %0, %3, %4" : "+v"(m) : "v"(v.x), "v"(v.y), "v"(v.z), "v"(v.w)); }
DI unsigned rt32() { unsigned long long t; asm volatile("s_memrealtime %0\n\ts_waitcnt lgkmcnt(0)" : "=s"(t) :: "memory"); return (unsigned)t; }
DI unsigned waitrt(unsigned t) { unsigned n; do n = rt32(); while ((int)(n - t) < 0); return n; }
DI void bar() { asm volatile("s_waitcnt lgkmcnt(0)" ::: "memory"); __builtin_amdgcn_s_barrier(); asm volatile("" ::: "memory"); }
template <int N> DI void wait_vm() { asm volatile("s_waitcnt vmcnt(%0)" :: "n"(N) : "memory"); }
DI void dma16(rsrc_t r, unsigned voff, unsigned soff, unsigned ldsaddr) {
    asm volatile("s_mov_b32 m0, %0\n\ts_nop 0\n\tbuffer_load_dwordx4 %1, %2, %3 offen lds" :: "s"(ldsaddr), "v"(voff), "s"(r), "s"(soff) : "memory");
}
DI float swap32_add(float a, float b) { auto r = __builtin_amdgcn_permlane32_swap(__builtin_bit_cast(unsigned, a), __builtin_bit_cast(unsigned, b), false, false);
    return __builtin_bit_cast(float, (unsigned)r[0]) + __builtin_bit_cast(float, (unsigned)r[1]); }
DI float swap16_add(float a, float b) { auto r = __builtin_amdgcn_permlane16_swap(__builtin_bit_cast(unsigned, a), __builtin_bit_cast(unsigned, b), false, false);
    return __builtin_bit_cast(float, (unsigned)r[0]) + __builtin_bit_cast(float, (unsigned)r[1]); }
DI float grp_max(float m) {
    auto r = __builtin_amdgcn_permlane32_swap(__builtin_bit_cast(unsigned, m), __builtin_bit_cast(unsigned, m), false, false);
    m = fmax2(__builtin_bit_cast(float, (unsigned)r[0]), __builtin_bit_cast(float, (unsigned)r[1]));
    auto q = __builtin_amdgcn_permlane16_swap(__builtin_bit_cast(unsigned, m), __builtin_bit_cast(unsigned, m), false, false);
    return fmax2(__builtin_bit_cast(float, (unsigned)q[0]), __builtin_bit_cast(float, (unsigned)q[1]));
}
DI int fresh_lane() { int l = __builtin_amdgcn_mbcnt_hi(~0u, __builtin_amdgcn_mbcnt_lo(~0u, 0u)); asm volatile("" : "+v"(l)); return l & 63; }
DI unsigned fresh_tid() { unsigned t = threadIdx.x; asm volatile("" : "+v"(t)); return t; }
DI unsigned ex_off(const Params& p) { return OFF_EX + ((0u - (unsigned)(size_t)(p.ws + OFF_EX)) & 0x1FFFFFu); }
DI unsigned ex_step(unsigned exo, unsigned pol, int s) { const int r = (s * 43) >> 9; return exo + r * 2097152 + (s - 12 * r) * EX_STEP + ((pol >> r) & 1) * 8192; }
DI constexpr unsigned mf_off(int ks) { return EX_MF + (ks >> 3) * 16384 + (ks & 7) * 1024; }
DI unsigned xcc_id() { return (unsigned)__builtin_amdgcn_s_getreg(20 | (3 << 11)) & 7u; }
DI unsigned mt32() { unsigned long long t; asm volatile("s_memtime %0\n\ts_waitcnt lgkmcnt(0)" : "=s"(t) :: "memory"); return (unsigned)t; }
DI unsigned read_pol(const Params& p) {
    const int lane = threadIdx.x & 63;
    const unsigned v = ((const unsigned*)p.ws)[C_POL + min(lane, 47)];
    const unsigned long long m1 = __builtin_amdgcn_ballot_w64(v == 2), m0 = __builtin_amdgcn_ballot_w64(v == 1);
    unsigned pol = 0;
#pragma unroll
    for (int r = 0; r < 6; ++r) pol |= (__builtin_popcountll((m1 >> (8 * r)) & 255) > __builtin_popcountll((m0 >> (8 * r)) & 255) ? 1u : 0u) << r;
    return pol;
}
DI void set_tmo(const Params& p) { __hip_atomic_store((unsigned*)p.ws + C_TMO, 1u, __ATOMIC_RELAXED, __HIP_MEMORY_SCOPE_AGENT); }

DI float row16_sum(float v) {
    v += __builtin_bit_cast(float, __builtin_amdgcn_update_dpp(0, __builtin_bit_cast(int, v), 0x121, 0xf, 0xf, true));
    v += __builtin_bit_cast(float, __builtin_amdgcn_update_dpp(0, __builtin_bit_cast(int, v), 0x122, 0xf, 0xf, true));
    v += __builtin_bit_cast(float, __builtin_amdgcn_update_dpp(0, __builtin_bit_cast(int, v), 0x124, 0xf, 0xf, true));
    v += __builtin_bit_cast(float, __builtin_amdgcn_update_dpp(0, __builtin_bit_cast(int, v), 0x128, 0xf, 0xf, true));
    return v;
}
DI void add_ctr(const Params& p, int word, unsigned v) { (void)__hip_atomic_fetch_add((unsigned*)p.ws + word, v, __ATOMIC_RELAXED, __HIP_MEMORY_SCOPE_AGENT); }
DI void wait_ctr(const Params& p, rsrc_t rw, int word, unsigned want) {
    unsigned n = SPIN_CAP;
#pragma nounroll
    for (;;) {
        asm volatile("" ::: "memory");
        if ((unsigned)__builtin_amdgcn_readfirstlane(ld4(rw, 0, word * 4)) >= want) break;
        if (--n == 0) { set_tmo(p); break; }
        if ((n & 63) == 0 && __builtin_amdgcn_readfirstlane(ld4(rw, 0, C_TMO * 4))) break;
        __builtin_amdgcn_s_sleep(1);
    }
    asm volatile("" ::: "memory");
}

__global__ void __launch_bounds__(256) k_init(Params p) {
    const int tid = threadIdx.x, lane = tid & 63, w = tid >> 6, b = blockIdx.x;
    if (b < 48) {
        __shared__ unsigned lat[4];
        unsigned* a = (unsigned*)(p.ws + ex_off(p) + (b >> 3) * 2097152 + EX_PROBE + (w >> 1) * 8192 + (w & 1) * EX_STEP + (b & 7) * 128);
        unsigned best = ~0u;
        for (int i = 0; i < 3; ++i) {
            const unsigned t0 = mt32();
            unsigned v = 0;
            if (lane == 0) v = __hip_atomic_fetch_add(a, 1u, __ATOMIC_RELAXED, __HIP_MEMORY_SCOPE_AGENT);
            asm volatile("s_waitcnt vmcnt(0)" :: "v"(v) : "memory");
            best = min(best, mt32() - t0);
        }
        if (lane == 0) lat[w] = best;
        __syncthreads();
        const int d = (int)min(lat[0], lat[1]) - (int)min(lat[2], lat[3]);
        if (tid == 0) ((unsigned*)p.ws)[C_POL + b] = abs(d) < 120 ? 0 : 1 + ((d > 0) ^ (xcc_id() >= 4));
    } else {
        const int i = (b - 48) * 256 + tid;
        if (i < C_POL / 4) ((u32x4*)(p.ws + OFF_CTRL))[i] = (u32x4){0u, 0u, 0u, 0u};
    }
}

DI void ln_tile(const Params& p, int b) {
    const int lane = fresh_lane(), w = __builtin_amdgcn_readfirstlane(threadIdx.x >> 6);
    const rsrc_t rw = mk_rsrc(p.ws, WS_USED);
    const int c = w * 4 + (lane >> 4), sub = lane & 15, row = b * 16 + c;
    u32x4 v[5]; float sum = 0.f;
#pragma unroll
    for (int k = 0; k < 5; ++k) v[k] = *(const u32x4*)(p.x + (size_t)row * 1280 + (k * 16 + sub) * 16);
#pragma unroll
    for (int k = 0; k < 5; ++k)
#pragma unroll
        for (int j = 0; j < 4; ++j) sum += bf_lo(v[k][j]) + bf_hi(v[k][j]);
    const float mean = row16_sum(sum) * (1.f / 640.f);
    float sq = 0.f;
#pragma unroll
    for (int k = 0; k < 5; ++k)
#pragma unroll
        for (int j = 0; j < 4; ++j) { const float a = bf_lo(v[k][j]) - mean, bb = bf_hi(v[k][j]) - mean; sq += a * a + bb * bb; }
    const float mb = rbf(mean), sd = rbf(sqrtf(rbf(rbf(row16_sum(sq) * (1.f / 640.f)) + 1e-5f))), ri = 1.f / sd;
#pragma unroll
    for (int k = 0; k < 5; ++k) {
        u32x4 o;
#pragma unroll
        for (int j = 0; j < 4; ++j) { const unsigned d = pk2(bf_lo(v[k][j]) - mb, bf_hi(v[k][j]) - mb); o[j] = pk2(bf_lo(d) * ri, bf_hi(d) * ri); }
        st16(o, rw, (k * 16 + sub) * 256 + c * 16, OFF_XF + b * 20480);
    }
    wait_vm<0>();
    bar();
    if (fresh_tid() == 0) add_ctr(p, C_X + (b >> 3) * 32, 1u);
}
DI void poison(const Params& p, int j, int n0, int n1, int word) {
    const int tid = fresh_tid();
    const rsrc_t rw = mk_rsrc(p.ws, WS_USED);
    const unsigned exo = ex_off(p), pol = read_pol(p);
    const u32x4 sv = {SENT, SENT, SENT, SENT};
    for (int n = n0 + j; n < n1; n += 56) {
        const int s = (n * 3277) >> 16, i = n - 20 * s;
        u32x4 v = sv;
        if (s == 0 && i >= 16 && (i < 18 || (i == 18 && tid < 128))) {
            const int f = (i - 16) * 256 + tid, ks = f >> 6, l = f & 63, r = (l & 3) | ((l >> 1) & 4), half = (l >> 2) & 1, g = l >> 4;
            v = *(const u32x4*)(p.mem0 + (r * 640 + 64 * ks + 16 * g + 8 * half) * 2);
            v.x = nosent(v.x); v.y = nosent(v.y); v.z = nosent(v.z); v.w = nosent(v.w);
        }
        st16(v, rw, tid * 16, ex_step(exo, pol, s) + (i >> 1) * 16384 + (i & 1) * 4096);
    }
    wait_vm<0>();
    bar();
    if (fresh_tid() == 0) add_ctr(p, word, 1u);
}

#define SB() __builtin_amdgcn_sched_barrier(0)
DI void mfma_va(f32x4& c, u32x4 a, u32x4 b) { asm volatile("v_mfma_f32_16x16x32_bf16 %0, %1, %2, %0" : "+v"(c) : "v"(a), "a"(b)); }
DI void mfma_av(f32x4& c, u32x4 a, u32x4 b) { asm volatile("v_mfma_f32_16x16x32_bf16 %0, %1, %2, %0" : "+v"(c) : "a"(a), "v"(b)); }
DI void mfma_cacc(f32x4& c, u32x4 a, u32x4 b) { asm volatile("v_mfma_f32_16x16x32_bf16 %0, %1, %2, %0" : "+a"(c) : "v"(a), "v"(b)); }
struct P1Src {
    rsrc_t r; unsigned v0, v1, so, step;
    DI void set(const Params& p, int u, int kh, int w, int lane) {
        const uchar* base; unsigned stride, col, bytes;
        if (u < 128) { base = (u & 2) ? p.wv : p.wk; stride = 8192; col = ((u >> 2) * 128 + (u & 1) * 64) * 2; bytes = 640 * 8192; }
        else if (u < 192) { base = p.wq; stride = 8192; col = (u - 128) * 128; bytes = 640 * 8192; }
        else { base = p.w1; stride = 3456; col = (u - 192) * 128; bytes = u < 219 ? 640 * 3456 : 0; }
        r = mk_rsrc(base, bytes);
        const int rl = lane >> 3, f0 = ((rl >> 1) & 1) | (((rl >> 2) & 1) << 1);
        v0 = rl * stride + (((lane & 7) ^ (2 * f0)) << 4); v1 = v0 ^ 64;
        so = (kh * 320 + 80 * w) * stride + col; step = 8 * stride;
    }
    DI void piece(int i, unsigned ldsw) const { dma16(r, (i & 1) ? v1 : v0, so + i * step, ldsw + i * 1024); }
};
DI float mish(float a, float bias) {
    const float h = a + bias;
    const float e = __builtin_amdgcn_exp2f(fminf(h, 30.f) * 1.44269504f);
    const float n = e * (e + 2.f);
    return h * n * __builtin_amdgcn_rcpf(n + 2.f);
}
#define PT(i)
constexpr int XA_ACC = 15;
template <bool ISV, int KH>
DI void p1_slab(LAS uchar* sb, const unsigned (&tb)[4], const u32x4 (&xa)[4][20], f32x4 (&acc)[4][4], const P1Src& src, unsigned ldsn) {
    u32x4 wf[2][4];
    auto rd = [&](int kk, int nt) { const u32x2 lo = trd(sb + tb[nt] + kk * 4096), hi = trd(sb + tb[nt ^ 2] + kk * 4096 + 512); return (u32x4){lo.x, lo.y, hi.x, hi.y}; };
#pragma unroll
    for (int nt = 0; nt < 4; ++nt) wf[0][nt] = rd(0, nt);
    SB();
#pragma unroll
    for (int kk = 0; kk < 10; ++kk) {
#pragma unroll
        for (int nt = 0; nt < 4; ++nt) {
            if (kk + 1 < 10) wf[(kk + 1) & 1][nt] = rd(kk + 1, nt);
            if (kk < 5 && (nt & 1)) src.piece(2 * kk + (nt >> 1), ldsn);
#pragma unroll
            for (int t = 0; t < 4; ++t) {
                const int ks = KH * 10 + kk;
                if (ISV) { if (ks < XA_ACC) mfma_av(acc[t][nt], xa[t][ks], wf[kk & 1][nt]); else mfma_v(acc[t][nt], xa[t][ks], wf[kk & 1][nt]); }
                else { if (ks < XA_ACC) mfma_va(acc[t][nt], wf[kk & 1][nt], xa[t][ks]); else mfma_v(acc[t][nt], wf[kk & 1][nt], xa[t][ks]); }
            }
            SB();
        }
    }
    asm volatile("s_nop 7" : "+v"(acc[3][3]), "+v"(acc[2][3]), "+v"(acc[1][3]), "+v"(acc[0][3]), "+v"(acc[3][2]), "+v"(acc[2][2]), "+v"(acc[1][2]), "+v"(acc[0][2]));
#pragma unroll
    for (int t = 0; t < 4; ++t) asm volatile("" : "+v"(acc[t][0]), "+v"(acc[t][1]));
}
template <bool ISV>
DI void p1_group(const Params& p, LAS uchar* lds, rsrc_t rw, unsigned lbase, const unsigned (&tb)[4], const u32x4 (&xa)[4][20], P1Src& src, int buf,
                 int chunk, int u, int un, bool first, int w, int lane, unsigned (&pt)[5]) {
    const int g = lane >> 4;
    f32x4 acc[4][4];
#pragma unroll
    for (int t = 0; t < 4; ++t)
#pragma unroll
        for (int nt = 0; nt < 4; ++nt) { acc[t][nt] = (f32x4){0.f, 0.f, 0.f, 0.f}; asm volatile("" : "+v"(acc[t][nt])); }
    PT(0);
    if (first) wait_vm<50>(); else wait_vm<10>();
    PT(1);
    bar();
    PT(2);
    src.set(p, un, 0, w, lane);
    {
        const unsigned ln = lbase + (buf == 0 ? 2 : buf - 1) * 40960;
        p1_slab<ISV, 0>(lds + buf * 40960, tb, xa, acc, src, ln);
    }
    buf = buf == 2 ? 0 : buf + 1;
    PT(3);
    wait_vm<10>();
    PT(1);
    bar();
    PT(2);
    src.set(p, un, 1, w, lane);
    {
        const unsigned ln = lbase + (buf == 0 ? 2 : buf - 1) * 40960;
        p1_slab<ISV, 1>(lds + buf * 40960, tb, xa, acc, src, ln);
    }
    PT(3);
    const int tt0 = chunk * 16 + w * 4;
    if (ISV) {
        const int h = u >> 2;
#pragma unroll
        for (int sp = 0; sp < 2; ++sp)
#pragma unroll
            for (int nt = 0; nt < 4; ++nt) {
                const f32x4 a = acc[2 * sp][nt], b = acc[2 * sp + 1][nt];
                const u32x4 o = {pk2(a[0], a[1]), pk2(a[2], a[3]), pk2(b[0], b[1]), pk2(b[2], b[3])};
                st16(o, rw, lane * 16, OFF_VF + (((tt0 >> 1) + sp) * 32 + h) * 8192 + ((u & 1) * 4 + nt) * 1024);
            }
    } else if (u < 192) {
        const int h = u < 128 ? u >> 2 : (u - 128) >> 1;
        const int ob = u < 128 ? OFF_KF : OFF_QF;
#pragma unroll
        for (int t = 0; t < 4; ++t)
#pragma unroll
            for (int m = 0; m < 2; ++m) {
                const f32x4 a = acc[t][2 * m], b = acc[t][2 * m + 1];
                const u32x4 o = {pk2(a[0], a[1]), pk2(a[2], a[3]), pk2(b[0], b[1]), pk2(b[2], b[3])};
                st16(o, rw, lane * 16, ob + (((tt0 + t) >> 1) * 32 + h) * 8192 + ((t & 1) * 4 + (u & 1) * 2 + m) * 1024);
            }
    } else {
        const int fg = u - 192;
        u32x2 bv[4];
#pragma unroll
        for (int nt = 0; nt < 4; ++nt) bv[nt] = *(const u32x2*)(p.b1 + (fg * 64 + nt * 16 + 4 * g) * 2);
#pragma unroll
        for (int t = 0; t < 4; ++t)
#pragma unroll
            for (int m = 0; m < 2; ++m) {
                const f32x4 a = acc[t][2 * m], b = acc[t][2 * m + 1];
                const u32x2 ba = bv[2 * m], bb = bv[2 * m + 1];
                const u32x4 o = {pk2(mish(a[0], bf_lo(ba.x)), mish(a[1], bf_hi(ba.x))), pk2(mish(a[2], bf_lo(ba.y)), mish(a[3], bf_hi(ba.y))),
                                 pk2(mish(b[0], bf_lo(bb.x)), mish(b[1], bf_hi(bb.x))), pk2(mish(b[2], bf_lo(bb.y)), mish(b[3], bf_hi(bb.y)))};
                st16(o, rw, lane * 16, OFF_ACT + ((tt0 + t) * 54 + fg * 2 + m) * 1024);
                SB();
            }
    }
}
DI void ld16_a(u32x4& d, rsrc_t r, unsigned v, unsigned s) { asm volatile("buffer_load_dwordx4 %0, %1, %2, %3 offen sc1" : "=a"(d) : "v"(v), "s"(r), "s"(s) : "memory"); }
DI void ld16_v(u32x4& d, rsrc_t r, unsigned v, unsigned s) { asm volatile("buffer_load_dwordx4 %0, %1, %2, %3 offen sc1" : "=v"(d) : "v"(v), "s"(r), "s"(s) : "memory"); }
DI void ld16_s(u32x4& d, rsrc_t r, unsigned v, unsigned s) { asm volatile("buffer_load_dwordx4 %0, %1, %2, %3 offen sc1" : "=v"(d) : "v"(v), "s"(r), "s"(s) : "memory"); }
DI void p1_task(const Params& p, LAS uchar* lds, int chunk, int u0, int nu) {
    const int lane = fresh_lane(), w = __builtin_amdgcn_readfirstlane(threadIdx.x >> 6), c = lane & 15, g = lane >> 4;
    const rsrc_t rw = mk_rsrc(p.ws, WS_USED);
    const unsigned lbase = (unsigned)(size_t)lds + w * 10240;
    P1Src src;
    u32x4 xa[4][20];
    src.set(p, u0, 0, w, lane);
    if (w == 0) { wait_ctr(p, rw, C_X + chunk * 64, 8); wait_ctr(p, rw, C_X + chunk * 64 + 32, 8); }
    bar();
#pragma unroll
    for (int i = 0; i < 10; ++i) src.piece(i, lbase);
#pragma unroll
    for (int ks = 0; ks < 20; ++ks) {
        if (ks == 10) {
            src.set(p, u0, 1, w, lane);
#pragma unroll
            for (int i = 0; i < 10; ++i) src.piece(i, lbase + 40960);
        }
#pragma unroll
        for (int t = 0; t < 4; ++t) {
            const unsigned so = OFF_XF + ((chunk * 16 + w * 4 + t) * 20 + ks) * 1024;
            if (ks < XA_ACC) ld16_a(xa[t][ks], rw, lane * 16, so); else ld16_v(xa[t][ks], rw, lane * 16, so);
        }
        SB();
    }
    const int q = c >> 2, pp = c & 3, sw = (q >> 1) | ((g & 1) << 1);
    unsigned tb[4];
#pragma unroll
    for (int nt = 0; nt < 4; ++nt) tb[nt] = (8 * g + q) * 128 + 32 * (nt ^ sw) + 16 * (pp >> 1) + 8 * (pp & 1);
    int buf = 0;
    unsigned pt[5] = {0, 0, 0, 0, mt32()};
    for (int gi = 0; gi < nu; ++gi) {
        const int u = u0 + gi, un = gi + 1 < nu ? u + 1 : 219;
        if (u < 128 && (u & 2)) p1_group<true>(p, lds, rw, lbase, tb, xa, src, buf, chunk, u, un, gi == 0, w, lane, pt);
        else p1_group<false>(p, lds, rw, lbase, tb, xa, src, buf, chunk, u, un, gi == 0, w, lane, pt);
        buf = buf == 0 ? 2 : buf - 1;
    }
    wait_vm<0>();
    bar();
    const unsigned tid = fresh_tid();
    if (tid < (unsigned)nu) {
        const int u = u0 + tid;
        add_ctr(p, u < 128 ? C_KV + (chunk * 32 + (u >> 2)) * 16 : (u < 192 ? C_Q + (chunk * 32 + ((u - 128) >> 1)) * 16 : C_ACT + chunk * 32), 1u);
    }
}

#define KST(i)
#define AST_(i)
template <bool ISV>
DI void kv0_task(const Params& p, LAS uchar* lds, int u) {
    const int lane = fresh_lane(), w = __builtin_amdgcn_readfirstlane(threadIdx.x >> 6), c = lane & 15, g = lane >> 4;
    const rsrc_t rw = mk_rsrc(p.ws, WS_USED);
    const unsigned lbase = (unsigned)(size_t)lds + w * 10240;
    P1Src src;
    u32x4 xa[2][20];
    auto fetch = [&](int half) {
#pragma unroll
        for (int ks = 0; ks < 20; ++ks)
#pragma unroll
            for (int t = 0; t < 2; ++t) ld16_v(xa[t][ks], rw, lane * 16, OFF_XF + ((half * 8 + w * 2 + t) * 20 + ks) * 1024);
    };
#pragma unroll
    for (int kh = 0; kh < 2; ++kh) {
        src.set(p, u, kh, w, lane);
#pragma unroll
        for (int i = 0; i < 10; ++i) src.piece(i, lbase + kh * 40960);
    }
    KST(8);
    KST(9);
    if (w == 0) wait_ctr(p, rw, C_X, 8);
    bar();
    KST(10);
    fetch(0);
    const int q = c >> 2, pp = c & 3, sw = (q >> 1) | ((g & 1) << 1), h = u >> 2;
    unsigned tb[4];
#pragma unroll
    for (int nt = 0; nt < 4; ++nt) tb[nt] = (8 * g + q) * 128 + 32 * (nt ^ sw) + 16 * (pp >> 1) + 8 * (pp & 1);
    auto rd = [&](int i) {
        const int ks = i >> 2, nt = i & 3; LAS uchar* sb = lds + (ks / 10) * 40960 + (ks % 10) * 4096;
        const u32x2 lo = trd(sb + tb[nt]), hi = trd(sb + tb[nt ^ 2] + 512); return (u32x4){lo.x, lo.y, hi.x, hi.y};
    };
    for (int half = 0; half < 2; ++half) {
        f32x4 acc[2][4];
#pragma unroll
        for (int i = 0; i < 8; ++i) { acc[i >> 2][i & 3] = (f32x4){0.f, 0.f, 0.f, 0.f}; asm volatile("" : "+v"(acc[i >> 2][i & 3])); }
        KST(half * 4 + 0);
        wait_vm<0>();
#pragma unroll
        for (int ks = 0; ks < 20; ++ks) asm volatile("" : "+v"(xa[0][ks]), "+v"(xa[1][ks]));
        bar();
        KST(half * 4 + 1);
        u32x4 wf[8];
#pragma unroll
        for (int i = 0; i < 8; ++i) wf[i] = rd(i);
        SB();
#pragma unroll
        for (int i = 0; i < 80; ++i) {
#pragma unroll
            for (int t = 0; t < 2; ++t) { if (ISV) mfma_v(acc[t][i & 3], xa[t][i >> 2], wf[i & 7]); else mfma_v(acc[t][i & 3], wf[i & 7], xa[t][i >> 2]); }
            if (i + 8 < 80) wf[i & 7] = rd(i + 8);
            SB();
        }
        asm volatile("s_nop 7" : "+v"(acc[0][0]), "+v"(acc[0][1]), "+v"(acc[0][2]), "+v"(acc[0][3]), "+v"(acc[1][0]), "+v"(acc[1][1]), "+v"(acc[1][2]), "+v"(acc[1][3]));
        KST(half * 4 + 2);
        const unsigned ob = (ISV ? OFF_VF : OFF_KF) + ((half * 4 + w) * 32 + h) * 8192;
#pragma unroll
        for (int i = 0; i < 4; ++i) {
            const f32x4 a = ISV ? acc[0][i] : acc[i >> 1][2 * (i & 1)], b = ISV ? acc[1][i] : acc[i >> 1][2 * (i & 1) + 1];
            const u32x4 o = {pk2(a[0], a[1]), pk2(a[2], a[3]), pk2(b[0], b[1]), pk2(b[2], b[3])};
            st16(o, rw, lane * 16, ob + (ISV ? (u & 1) * 4 + i : (i >> 1) * 4 + (u & 1) * 2 + (i & 1)) * 1024);
        }
        wait_vm<0>();
        bar();
        KST(half * 4 + 3);
        if (fresh_tid() == 0) add_ctr(p, (half ? C_KV : C_KVH) + h * 16, 1u);
        if (w == 0) wait_ctr(p, rw, C_X + 32, 8);
        bar();
        fetch(1);
    }
    wait_vm<0>();
#pragma unroll
    for (int ks = 0; ks < 20; ++ks) asm volatile("" :: "v"(xa[0][ks]), "v"(xa[1][ks]));
}

DI void down_task(const Params& p, LAS uchar* lds, int chunk, int cg) {
    const int lane = fresh_lane(), w = __builtin_amdgcn_readfirstlane(threadIdx.x >> 6), c = lane & 15, g = lane >> 4;
    const rsrc_t rw = mk_rsrc(p.ws, WS_USED), r2 = mk_rsrc(p.w2, F_ * 1280);
    const unsigned lbase = (unsigned)(size_t)lds;
    const int rl = lane >> 3, f0 = ((rl >> 1) & 1) | (((rl >> 2) & 1) << 1);
    const unsigned v0 = rl * 1280 + (((lane & 7) ^ (2 * f0)) << 4), v1 = rl * 1280 + (((lane & 7) ^ (2 * (f0 ^ 2))) << 4);
    auto issue = [&](int sl, int buf) {
#pragma unroll
        for (int i = 0; i < 6; ++i) {
            const int pc = (w & 1) * 6 + i, im = w >> 1;
            dma16(r2, (i & 1) ? v1 : v0, (sl * 96 + 8 * pc) * 1280 + cg * 256 + im * 128, lbase + buf * 24576 + im * 12288 + pc * 1024);
        }
    };
    const int tt0 = chunk * 16 + w * 4;
    issue(0, 0);
    if (w == 0) wait_ctr(p, rw, C_ACT + chunk * 32, 27);
    bar();
    u32x4 xb[2][3][4];
#pragma unroll
    for (int kk = 0; kk < 3; ++kk)
#pragma unroll
        for (int t = 0; t < 4; ++t) ld16_s(xb[0][kk][t], rw, lane * 16, OFF_ACT + ((tt0 + t) * 54 + kk) * 1024);
    issue(1, 1);
    const int q = c >> 2, pp = c & 3, sw = (q >> 1) | (((g & 1) ^ (g >> 1)) << 1);
    unsigned tb[4];
#pragma unroll
    for (int nt = 0; nt < 4; ++nt) tb[nt] = (4 * g + q) * 128 + 32 * (nt ^ sw) + 16 * (pp >> 1) + 8 * (pp & 1);
    f32x4 acc[4][8];
#pragma unroll
    for (int t = 0; t < 4; ++t)
#pragma unroll
        for (int nt = 0; nt < 8; ++nt) { acc[t][nt] = (f32x4){0.f, 0.f, 0.f, 0.f}; asm volatile("" : "+a"(acc[t][nt])); }
    int buf = 0;
    for (int s2 = 0; s2 < 9; ++s2) {
#pragma unroll
        for (int par = 0; par < 2; ++par) {
            const int sl = 2 * s2 + par;
            wait_vm<6>();
#pragma unroll
            for (int kk = 0; kk < 3; ++kk) asm volatile("" : "+v"(xb[par][kk][0]), "+v"(xb[par][kk][1]), "+v"(xb[par][kk][2]), "+v"(xb[par][kk][3]));
            bar();
            const int sn = min(sl + 1, 17);
#pragma unroll
            for (int kk = 0; kk < 3; ++kk)
#pragma unroll
                for (int t = 0; t < 4; ++t) ld16_s(xb[par ^ 1][kk][t], rw, lane * 16, OFF_ACT + ((tt0 + t) * 54 + sn * 3 + kk) * 1024);
            issue(min(sl + 2, 17), buf == 0 ? 2 : buf - 1);
            LAS uchar* sb = lds + buf * 24576;
            auto rd = [&](int i) { LAS uchar* a = sb + ((i >> 2) & 1) * 12288 + tb[i & 3] + (i >> 3) * 4096; const u32x2 lo = trd(a), hi = trd(a + 2048); return (u32x4){lo.x, lo.y, hi.x, hi.y}; };
            u32x4 wf[4];
#pragma unroll
            for (int i = 0; i < 4; ++i) wf[i] = rd(i);
            SB();
#pragma unroll
            for (int i = 0; i < 24; ++i) {
                const u32x4 cur = wf[i & 3];
#pragma unroll
                for (int t = 0; t < 4; ++t) mfma_cacc(acc[t][i & 7], cur, xb[par][i >> 3][t]);
                if (i + 4 < 24) wf[i & 3] = rd(i + 4);
                SB();
            }
            if (par) asm volatile("s_nop 7");
            SB();
            buf = buf == 2 ? 0 : buf + 1;
        }
    }
    wait_vm<0>();
#pragma unroll
    for (int kk = 0; kk < 3; ++kk) asm volatile("" :: "v"(xb[0][kk][0]), "v"(xb[0][kk][1]), "v"(xb[0][kk][2]), "v"(xb[0][kk][3]));
    SB();
#pragma unroll
    for (int t = 0; t < 4; ++t)
#pragma unroll
        for (int m = 0; m < 4; ++m) {
            float y[8];
#pragma unroll
            for (int r = 0; r < 8; ++r) { const float a = rbf(acc[t][2 * m + (r >> 2)][r & 3]); y[r] = a * __builtin_amdgcn_rcpf(1.f + __builtin_amdgcn_exp2f(-a * 1.44269504f)); }
            const u32x4 o = {pk2(y[0], y[1]), pk2(y[2], y[3]), pk2(y[4], y[5]), pk2(y[6], y[7])};
            st16(o, rw, lane * 16, OFF_GF + ((tt0 + t) * 20 + cg * 4 + m) * 1024);
        }
    wait_vm<0>();
    bar();
    if (fresh_tid() == 0) add_ctr(p, C_G + (chunk * 5 + cg) * 32, 1u);
}

#define XST(cond, idx)
constexpr float SCL = 0.08838834764831845f * 1.4426950408889634f;
DI void attn_task(const Params& p, int s, int h, bool new_chunk) {
    const int lane = fresh_lane(), c = lane & 15, g = lane >> 4;
    const rsrc_t rw = mk_rsrc(p.ws, WS_USED);
    const int sh = (s * 32 + h);
    u32x4 qf[2][4], kf[2][4], vf[8], km[4]; u32x2 vm[8];
    if (new_chunk) {
        wait_ctr(p, rw, C_Q + ((s >> 3) * 32 + h) * 16, 2);
        wait_ctr(p, rw, C_KV + ((s >> 3) * 32 + h) * 16, 4);
    }
#pragma unroll
    for (int i = 0; i < 8; ++i) qf[i >> 2][i & 3] = ld16(rw, lane * 16, OFF_QF + sh * 8192 + i * 1024);
#pragma unroll
    for (int i = 0; i < 8; ++i) kf[i >> 2][i & 3] = ld16(rw, lane * 16, OFF_KF + sh * 8192 + i * 1024);
#pragma unroll
    for (int i = 0; i < 8; ++i) vf[i] = ld16(rw, lane * 16, OFF_VF + sh * 8192 + i * 1024);
    const float NINF = -__builtin_inff();
    const f32x4 zero = {0.f, 0.f, 0.f, 0.f};
    float t[2][12], m[2];
    {
        f32x4 s0[2] = {zero, zero}, s1 = zero;
#pragma unroll
        for (int ks = 0; ks < 4; ++ks) { s0[0] = mfma32(kf[0][ks], qf[0][ks], s0[0]); s0[1] = mfma32(kf[0][ks], qf[1][ks], s0[1]); s1 = mfma32(kf[1][ks], qf[1][ks], s1); }
#pragma unroll
        for (int r = 0; r < 4; ++r) {
            const bool diag = 4 * g + r <= c;
            t[0][r] = diag ? s0[0][r] * SCL : NINF; t[1][r] = s0[1][r] * SCL; t[1][4 + r] = diag ? s1[r] * SCL : NINF;
        }
        m[0] = fmaxf(fmaxf(t[0][0], t[0][1]), fmaxf(t[0][2], t[0][3]));
        m[1] = fmaxf(fmaxf(fmaxf(t[1][0], t[1][1]), fmaxf(t[1][2], t[1][3])), fmaxf(fmaxf(t[1][4], t[1][5]), fmaxf(t[1][6], t[1][7])));
#pragma unroll
        for (int r = 0; r < 4; ++r) asm volatile("" : "+v"(t[0][r]), "+v"(t[1][r]), "+v"(t[1][4 + r]));
        asm volatile("" : "+v"(m[0]), "+v"(m[1]));
#pragma unroll
        for (int i = 0; i < 8; ++i) asm volatile("" : "+v"(vf[i]));
    }
    XST(s == 63, 64 + h * 4);
    wait_ctr(p, rw, C_KM + h * 32, s + 1);
    XST(s == 63, 64 + h * 4 + 1);
#pragma unroll
    for (int i = 0; i < 4; ++i) km[i] = ld16(rw, (g * 8 + (c & 7)) * 16, OFF_KM + sh * 4096 + i * 512);
#pragma unroll
    for (int i = 0; i < 8; ++i) vm[i] = ld8(rw, (lane & 31) * 8, OFF_KM + sh * 4096 + 2048 + i * 256);
    SB();
    const u32x4 one4 = {0x3F803F80u, 0x3F803F80u, 0x3F803F80u, 0x3F803F80u};
    const u32x2 one2 = {0x3F803F80u, 0x3F803F80u};
    const float z2 = g >= 2 ? 0.f : NINF;
    f32x4 s2[2] = {{z2, z2, z2, z2}, {z2, z2, z2, z2}};
#pragma unroll
    for (int ks = 0; ks < 4; ++ks)
#pragma unroll
        for (int qt = 0; qt < 2; ++qt) s2[qt] = mfma32(km[ks], qf[qt][ks], s2[qt]);
    u32x4 p01[2]; u32x2 p2[2];
#pragma unroll
    for (int qt = 0; qt < 2; ++qt) {
#pragma unroll
        for (int r = 0; r < 4; ++r) t[qt][8 + r] = s2[qt][r] * SCL;
        m[qt] = fmaxf(fmaxf(m[qt], fmaxf(t[qt][8], t[qt][9])), fmaxf(t[qt][10], t[qt][11]));
    }
#pragma unroll
    for (int qt = 0; qt < 2; ++qt) m[qt] = grp_max(m[qt]);
#pragma unroll
    for (int qt = 0; qt < 2; ++qt) {
#pragma unroll
        for (int r = 0; r < 12; ++r) if (qt == 1 || r < 4 || r >= 8) t[qt][r] = __builtin_amdgcn_exp2f(t[qt][r] - m[qt]);
        p01[qt] = (u32x4){pk2(t[qt][0], t[qt][1]), pk2(t[qt][2], t[qt][3]), qt ? pk2(t[qt][4], t[qt][5]) : 0u, qt ? pk2(t[qt][6], t[qt][7]) : 0u};
        p2[qt] = (u32x2){pk2(t[qt][8], t[qt][9]), pk2(t[qt][10], t[qt][11])};
    }
    float inv[2];
#pragma unroll
    for (int qt = 0; qt < 2; ++qt) inv[qt] = __builtin_amdgcn_rcpf(mfma16(one2, p2[qt], mfma32(one4, p01[qt], zero))[0]);
#pragma unroll
    for (int tp = 0; tp < 4; ++tp)
#pragma unroll
        for (int qt = 0; qt < 2; ++qt) {
            const f32x4 a = mfma16(vm[2 * tp], p2[qt], mfma32(vf[2 * tp], p01[qt], zero)) * inv[qt], b = mfma16(vm[2 * tp + 1], p2[qt], mfma32(vf[2 * tp + 1], p01[qt], zero)) * inv[qt];
            const u32x4 o = {pk2(a[0], a[1]), pk2(a[2], a[3]), pk2(b[0], b[1]), pk2(b[2], b[3])};
            st16(o, rw, lane * 16, OFF_OT + ((2 * s + qt) * 32 + h) * 4096 + tp * 1024);
        }
    XST(s == 63, 64 + h * 4 + 2);
    wait_vm<0>();
    XST(s == 63, 64 + h * 4 + 3);
    if (lane == 0) add_ctr(p, C_OT + s * 32, 1u);
}

constexpr int N_T = 24, N_WG = 5, W_FIX = 10;
DI void wo_stage(const Params& p, unsigned ldsw, int h0, int cs, int lane) {
    const rsrc_t ro = mk_rsrc(p.wo, 4096 * 1280);
    const unsigned v = (lane >> 2) * 1280 + (lane & 3) * 16;
#pragma unroll
    for (int i = 0; i < 16; ++i) dma16(ro, v, (h0 * 128 + 16 * i) * 1280 + cs * 64, ldsw + i * 1024);
}
DI void w_role(const Params& p, LAS uchar* lds, int cs, int rank) {
    const int lane = fresh_lane(), w = __builtin_amdgcn_readfirstlane(threadIdx.x >> 6), c = lane & 15, g = lane >> 4;
    const rsrc_t rw = mk_rsrc(p.ws, WS_USED);
    u32x4 wf[8][4][2];
    {
        LAS uchar* lw = lds + w * 32768;
        const int q = c >> 2, pp = c & 3;
        wo_stage(p, (unsigned)(size_t)lw, 8 * w, cs, lane);
        wo_stage(p, (unsigned)(size_t)lw + 16384, 8 * w + 2, cs, lane);
#pragma unroll
        for (int rd = 0; rd < 4; ++rd) {
            if (rd < 3) wait_vm<16>(); else wait_vm<0>();
#pragma unroll
            for (int i = 0; i < 2; ++i)
#pragma unroll
                for (int tp = 0; tp < 4; ++tp)
#pragma unroll
                    for (int nt = 0; nt < 2; ++nt) {
                        LAS uchar* a = lw + (rd & 1) * 16384 + i * 8192 + (32 * tp + 4 * g + q) * 64 + nt * 32 + pp * 8;
                        const u32x2 lo = trd(a), hi = trd(a + 1024);
                        wf[2 * rd + i][tp][nt] = (u32x4){lo.x, lo.y, hi.x, hi.y};
                        if (2 * rd + i < 7) asm volatile("" : "+a"(wf[2 * rd + i][tp][nt])); else asm volatile("" : "+v"(wf[2 * rd + i][tp][nt]));
                    }
            asm volatile("s_waitcnt lgkmcnt(0)" ::: "memory");
            SB();
            if (rd < 2) wo_stage(p, (unsigned)(size_t)lw + (rd & 1) * 16384, 8 * w + 2 * rd + 4, cs, lane);
        }
    }
    bar();
    LAS f32x2* red = (LAS f32x2*)lds;
    volatile LAS int* nxt = (volatile LAS int*)(lds + 16384);
    const rsrc_t rx = mk_rsrc(p.x, S_ * 1280), ro = mk_rsrc(p.out, S_ * 1280);
    unsigned tk = 0;
    const unsigned wto = lane == 0 ? (C_WT + cs * 32) * 4 : OOB;
    auto draw = [&]() { tk = 1; asm volatile("buffer_atomic_add %0, %1, %2, 0 offen sc0" : "+v"(tk) : "v"(wto), "s"(rw) : "memory"); };
    auto tile_of = [&](int ticket, int cur) { return ticket < 2 * NSEG - W_FIX ? ticket : (cur < 2 * NSEG - W_FIX ? 2 * NSEG - W_FIX + rank : cur + N_WG); };
    u32x4 ob[8][4];
    int tt = 0;
    bool have = false;
    if (w == 0) { draw(); asm volatile("s_waitcnt vmcnt(0)" : "+v"(tk)); *nxt = tile_of(__builtin_amdgcn_readfirstlane(tk), 0); draw(); }
    bar();
    tt = __builtin_amdgcn_readfirstlane(*nxt);
    while (tt < 2 * NSEG) {
        if (!have) {
            if (w == 0) { wait_ctr(p, rw, C_G + ((tt >> 4) * 5 + (cs >> 2)) * 32, 1); wait_ctr(p, rw, C_OT + (tt >> 1) * 32, 32); }
            bar();
            XST(w == 0 && tt >= 126, 256 + ((tt - 126) * 20 + cs) * 4);
#pragma unroll
            for (int i = 0; i < 32; ++i) ld16_s(ob[i >> 2][i & 3], rw, lane * 16, OFF_OT + (tt * 32 + 8 * w) * 4096 + i * 1024);
        }
        f32x4 acc[2][2];
#pragma unroll
        for (int i = 0; i < 4; ++i) { acc[i >> 1][i & 1] = (f32x4){0.f, 0.f, 0.f, 0.f}; asm volatile("" : "+v"(acc[i >> 1][i & 1])); }
        const unsigned xo = c * 1280 + g * 8, xs = tt * 16 * 1280 + cs * 64 + (w >> 1) * 32 + (w & 1) * 4;
        unsigned xv, gv;
        asm volatile("buffer_load_dword %0, %1, %2, %3 offen" : "=v"(xv) : "v"(xo), "s"(rx), "s"(xs) : "memory");
        asm volatile("buffer_load_dword %0, %1, %2, %3 offen sc1" : "=v"(gv) : "v"(lane * 16), "s"(rw), "s"(OFF_GF + (tt * 20 + cs) * 1024 + w * 4) : "memory");
        if (w == 0) {
            asm volatile("s_waitcnt vmcnt(32)" : "+v"(tk));
            const int tn = tile_of(__builtin_amdgcn_readfirstlane(tk), tt);
            int rd = 0;
            if (tn < 2 * NSEG) {
                const unsigned f = ld4(rw, (lane == 0 ? C_G + ((tn >> 4) * 5 + (cs >> 2)) * 32 : C_OT + (tn >> 1) * 32) * 4, 0);
                rd = __builtin_amdgcn_ballot_w64(f < (lane == 0 ? 1u : 32u)) == 0 ? 0x10000 : 0;
            }
            nxt[1] = tn | rd;
        }
        bar();
        XST(w == 0 && tt >= 126, 256 + ((tt - 126) * 20 + cs) * 4 + 1);
        const int nx = __builtin_amdgcn_readfirstlane(nxt[1]);
        have = (nx >> 16) != 0;
        const rsrc_t rn = mk_rsrc(p.ws, have ? WS_USED : 0);
        const unsigned nb = OFF_OT + ((nx & 127) * 32 + 8 * w) * 4096;
        if (w == 0) draw();
#pragma unroll
        for (int i = 0; i < 8; ++i) {
            asm volatile("s_waitcnt vmcnt(28)" : "+v"(ob[i][0]), "+v"(ob[i][1]), "+v"(ob[i][2]), "+v"(ob[i][3]));
#pragma unroll
            for (int tp = 0; tp < 4; ++tp)
#pragma unroll
                for (int n = 0; n < 2; ++n) { if (i < 7) mfma_av(acc[tp & 1][n], wf[i][tp][n], ob[i][tp]); else mfma_v(acc[tp & 1][n], wf[i][tp][n], ob[i][tp]); }
#pragma unroll
            for (int tp = 0; tp < 4; ++tp) ld16_s(ob[i][tp], rn, lane * 16, nb + (i * 4 + tp) * 1024);
            SB();
        }
        mfma_pad(acc[0][0], acc[0][1], acc[1][0], acc[1][1]);
#pragma unroll
        for (int n = 0; n < 2; ++n) {
            const f32x4 a = acc[0][n] + acc[1][n];
            red[((2 * n) * 4 + w) * 64 + lane] = (f32x2){a[0], a[1]};
            red[((2 * n + 1) * 4 + w) * 64 + lane] = (f32x2){a[2], a[3]};
        }
        bar();
        const f32x2 r = (red[(w * 4 + 0) * 64 + lane] + red[(w * 4 + 1) * 64 + lane]) + (red[(w * 4 + 2) * 64 + lane] + red[(w * 4 + 3) * 64 + lane]);
        asm volatile("s_waitcnt vmcnt(32)" : "+v"(xv), "+v"(gv));
        const unsigned sv = pk2(r[0], r[1]), m = pk2(bf_lo(sv) * bf_lo(gv), bf_hi(sv) * bf_hi(gv));
        __builtin_amdgcn_raw_buffer_store_b32(pk2(bf_lo(xv) + bf_lo(m), bf_hi(xv) + bf_hi(m)), ro, xo, xs, 0);
        XST(w == 0 && tt >= 126, 256 + ((tt - 126) * 20 + cs) * 4 + 2);
        if (!have) {
            wait_vm<0>();
#pragma unroll
            for (int i = 0; i < 8; ++i) asm volatile("" : "+v"(ob[i][0]), "+v"(ob[i][1]), "+v"(ob[i][2]), "+v"(ob[i][3]));
        }
        tt = nx & 0xFFFF;
    }
}

template <int V> struct IC { static constexpr int value = V; };
template <int N, int I = 0, class F> DI void static_for(F&& f) { if constexpr (I < N) { f(IC<I>{}); static_for<N, I + 1>(f); } }
#define V_A 88
#define V_M 56
#define KM_DELAY 240
#define PAD0 0
#define PAD_UP 0
#define PAD_MAX 48
#define RISE 64
#define DECAY 8
#ifndef M_DELAY
#define M_DELAY 200
#endif
#define D_MST 240
#define F_LATE 1000
#define V_LATE 8
#define DUR_A0 1060
#define DUR_M0 760
#define FT(i)
#define FDUMP(blk, s, w)
#define STAT(blk, s, w, a, b, c, d)
DI int wave_max(int d) {
    d = max(d, __builtin_amdgcn_update_dpp(0, d, 0x121, 0xf, 0xf, true));
    d = max(d, __builtin_amdgcn_update_dpp(0, d, 0x122, 0xf, 0xf, true));
    d = max(d, __builtin_amdgcn_update_dpp(0, d, 0x124, 0xf, 0xf, true));
    d = max(d, __builtin_amdgcn_update_dpp(0, d, 0x128, 0xf, 0xf, true));
    auto r = __builtin_amdgcn_permlane32_swap((unsigned)d, (unsigned)d, false, false); d = max((int)r[0], (int)r[1]);
    auto q = __builtin_amdgcn_permlane16_swap((unsigned)d, (unsigned)d, false, false); d = max((int)q[0], (int)q[1]);
    return __builtin_amdgcn_readfirstlane(d);
}
struct Clock {
    unsigned r0, m0, cq, qc;
    DI void edge(unsigned& r, unsigned& m) {
        unsigned long long a, b;
        const unsigned first = rt32();
        do asm volatile("s_memrealtime %0\n\ts_memtime %1\n\ts_waitcnt lgkmcnt(0)" : "=s"(a), "=s"(b) :: "memory"); while ((unsigned)a == first);
        r = (unsigned)a << 3; m = (unsigned)b - 24;
    }
    DI void start() { edge(r0, m0); cq = 3000; qc = 22370; }
    DI void sync() {
        unsigned r, m; edge(r, m);
        const float f = (float)(m - m0) * __builtin_amdgcn_rcpf((float)(r - r0));
        cq = __builtin_amdgcn_readfirstlane((unsigned)(f * 1024.f));
        qc = __builtin_amdgcn_readfirstlane((unsigned)(65536.f * __builtin_amdgcn_rcpf(f)));
        r0 = r; m0 = m;
    }
    DI unsigned at(unsigned m) const { return r0 + (unsigned)(((unsigned long long)(m - m0) * qc) >> 16); }
    DI unsigned now() const {
        unsigned long long a, b;
        asm volatile("s_memrealtime %0\n\ts_memtime %1\n\ts_waitcnt lgkmcnt(0)" : "=s"(a), "=s"(b) :: "memory");
        const unsigned lo = (unsigned)a << 3;
        return lo + __builtin_amdgcn_readfirstlane(min(max((int)(at((unsigned)b) - lo), 0), 31));
    }
    DI unsigned cycles(unsigned t) const { const int d = __builtin_amdgcn_readfirstlane(min(max((int)(t - r0), 0), 1 << 20)); return m0 + (((unsigned)d * cq) >> 10) - 26; }
};
DI unsigned waitmt(unsigned t) { unsigned n; do n = mt32(); while ((int)(n - t) < 0); return n; }
struct Sched {
    unsigned tgo, tau, durA, durM, pad;
    unsigned a, v, z;
    DI void fetch(rsrc_t rw, unsigned exo, unsigned pol, int s, int lane) {
        a = ld4(rw, s >= 2 ? (lane & 31) * 4 : OOB, ex_step(exo, pol, max(s - 2, 0)) + EX_AST);
        v = ld4(rw, min(lane, 39) * 4, ex_step(exo, pol, s - 1) + EX_MST);
    }
    DI void next(const Params& p, rsrc_t rw, unsigned exo, unsigned pol, int s, int lane, unsigned long long& alive) {
        if ((__builtin_amdgcn_ballot_w64(v == SENT) & alive) != 0) {
            const unsigned mo = ex_step(exo, pol, s - 1) + EX_MST;
            unsigned n = SPIN_CAP; bool bad;
#pragma nounroll
            do {
                asm volatile("" ::: "memory");
                v = ld4(rw, min(lane, 39) * 4, mo);
                bad = __builtin_amdgcn_ballot_w64(v == SENT) != 0;
            } while (bad && --n);
            if (bad) { alive = 0; set_tmo(p); }
        }
        const unsigned ref = __builtin_amdgcn_readfirstlane(v);
        const unsigned pm = ref + (unsigned)wave_max((int)(v - ref));
        if (s >= 2 && __builtin_amdgcn_ballot_w64(v & 1) == 0) durM += (min((int)(pm - tau - durM), RISE) + 2) >> 2;
        if (__builtin_amdgcn_ballot_w64(a - 1u >= 0xFFFFu) == 0) durA += (min(wave_max((int)a) - (int)durA, RISE) + 2) >> 2;
        const bool late = s == 1 || (int)(pm + V_LATE - tgo) > 0;
        pad = late || __builtin_amdgcn_ballot_w64(v & 1) != 0 ? min(pad + PAD_UP, (unsigned)PAD_MAX) : (pad > 4 ? pad - 4 : 0);
        tau = (late ? pm + F_LATE : tgo) + durA + pad;
        tgo = tau + durM + V_A + pad;
    }
};

#define PACE 8
DI void pace() { asm volatile("s_nop %0" :: "n"(PACE)); }
DI void role_M(const Params& p, LAS uchar* lds, int mi) {
    const int tid = threadIdx.x, lane = tid & 63, w = __builtin_amdgcn_readfirstlane(tid >> 6), c = lane & 15, g = lane >> 4;
    const int qh = mi & 1, cs = mi >> 1;
    const rsrc_t rw = mk_rsrc(p.ws, WS_USED);
    u32x8 wf[8][2][2];
    waitrt(rt32() + M_DELAY);
    {
        LAS uchar* lw = lds + w * 32768;
        const int q = c >> 2, pp = c & 3;
        unsigned ra[4];
#pragma unroll
        for (int m = 0; m < 4; ++m) {
            const int k0 = m < 2 ? 8 * g + 4 * m : 32 + 8 * g + 4 * (m - 2), ga = k0 >> 4, qq = (k0 >> 2) & 3;
            const int e = 32 * ga + 2 * qq + (q & 1) + 8 * (q >> 1);
            ra[m] = e * 64 + pp * 8;
        }
        wo_stage(p, (unsigned)(size_t)lw, 8 * w, cs, lane);
        wo_stage(p, (unsigned)(size_t)lw + 16384, 8 * w + 2, cs, lane);
#pragma unroll
        for (int rd = 0; rd < 4; ++rd) {
            if (rd < 3) wait_vm<16>(); else wait_vm<0>();
#pragma unroll
            for (int i = 0; i < 2; ++i)
#pragma unroll
                for (int sel = 0; sel < 2; ++sel)
#pragma unroll
                    for (int nt = 0; nt < 2; ++nt) {
                        LAS uchar* a = lw + (rd & 1) * 16384 + i * 8192 + sel * 1024 + nt * 32;
                        const u32x2 t0 = trd(a + ra[0]), t1 = trd(a + ra[1]), t2 = trd(a + ra[2]), t3 = trd(a + ra[3]);
                        wf[2 * rd + i][sel][nt] = (u32x8){t0.x, t0.y, t1.x, t1.y, t2.x, t2.y, t3.x, t3.y};
                        if (2 * rd + i < 6) asm volatile("" : "+a"(wf[2 * rd + i][sel][nt])); else asm volatile("" : "+v"(wf[2 * rd + i][sel][nt]));
                    }
            asm volatile("s_waitcnt lgkmcnt(0)" ::: "memory");
            SB();
            if (rd < 2) wo_stage(p, (unsigned)(size_t)lw + (rd & 1) * 16384, 8 * w + 2 * rd + 4, cs, lane);
        }
    }
    bar();
    if (tid < 2) *(LAS unsigned*)(lds + 4096 + tid * 4) = 0;
    bar();
    const int idx = (c & 4) ? 0xEEEE : 0x4444;
    const int dl = 32 * cs + 2 * c;
    const unsigned so = mf_off(dl >> 6) + (((dl >> 4) & 3) * 16 + qh * 8 + ((dl >> 3) & 1) * 4 + g) * 16 + (dl & 7) * 2;
    const unsigned rwo = (w * 128 + (2 * (g & 1)) * 32 + 16 * (g >> 1) + c) * 4, rro = (g * 32 + 2 * c) * 4;
    const unsigned lo16 = lane * 16, sto = lane == 0 ? mi * 4 : OOB;
    unsigned long long alive = ~0ull;
    const unsigned exo = ex_off(p), pol = read_pol(p);
    wait_ctr(p, rw, C_PZ, 56);
    Clock ck; ck.start();
    unsigned tdone = ck.now();
    Sched sc = {0u, tdone - V_M, DUR_A0, DUR_M0, PAD0, 0u, 0u, 0u};
    for (int s = 0; s < NSEG - 1; ++s) {
        const unsigned eb = ex_step(exo, pol, s);
        const unsigned ob = eb + w * 32768 + qh * 1024;
        if (s == 3) wait_ctr(p, rw, C_PZ + 32, 56);
        if (s > 0) { waitmt(ck.cycles(tdone + D_MST)); sc.fetch(rw, exo, pol, s, lane); ck.sync(); sc.next(p, rw, exo, pol, s, lane, alive); }
        const unsigned tau = sc.tau;
        unsigned flag = 0;
        unsigned obs[8], mb = ex_step(exo, pol, s + 1);
#pragma unroll
        for (int i = 0; i < 8; ++i) { obs[i] = ob + (i >> 2) * 16384 + (i & 3) * 2048; asm volatile("" : "+s"(obs[i])); }
        unsigned rwa = rwo + (s & 1) * 2048, rra = rro + (s & 1) * 2048;
        f32x4 acc[2][2];
#pragma unroll
        for (int i = 0; i < 4; ++i) { acc[i >> 1][i & 1] = (f32x4){0.f, 0.f, 0.f, 0.f}; asm volatile("" : "+v"(acc[i >> 1][i & 1])); }
        unsigned tw = ck.cycles(tau + V_M);
        asm volatile("" : "+s"(mb), "+v"(rwa), "+v"(rra), "+s"(tw));
        waitmt(tw);
        FT(0);
        u32x4 of[8];
#pragma unroll
        for (int i = 0; i < 8; ++i) { of[i] = ld16(rw, lo16, obs[i]); SB(); if (i < 7) pace(); SB(); }
        for (;;) {
            unsigned mx = 0;
            unsigned long long early = 0;
#pragma unroll
            for (int i = 0; i < 8; ++i) {
                if (i == 7) {
                    mx4v(mx, of[i]);
                    early = __builtin_amdgcn_ballot_w64(mx == SENT) & alive;
                    asm volatile("" : "+s"(early));
                    SB();
                }
#pragma unroll
                for (int j = 0; j < 4; ++j) { if (i < 6) smf_a(acc[j >> 1][j & 1], of[i], wf[i][j >> 1][j & 1], idx); else smf_v(acc[j >> 1][j & 1], of[i], wf[i][j >> 1][j & 1], idx); }
                if (i < 7) mx4v(mx, of[i]);
                if (i == 0) FT(1);
                if (i == 7) FT(2);
                SB();
            }
            if (__builtin_expect(early == 0, 1)) break;
            unsigned n = SPIN_CAP; bool bad;
#pragma nounroll
            do {
                asm volatile("" ::: "memory");
#pragma unroll
                for (int i = 0; i < 8; ++i) of[i] = ld16(rw, lo16, obs[i]);
                unsigned m2 = 0;
#pragma unroll
                for (int i = 0; i < 8; ++i) m2 = mx4(m2, of[i]);
                bad = __builtin_amdgcn_ballot_w64(m2 == SENT) != 0;
            } while (bad && --n);
            if (bad) { alive = 0; set_tmo(p); }
            flag = 1;
#pragma unroll
            for (int i = 0; i < 4; ++i) { acc[i >> 1][i & 1] = (f32x4){0.f, 0.f, 0.f, 0.f}; asm volatile("" : "+v"(acc[i >> 1][i & 1])); }
            asm volatile("" ::: "memory");
        }
        mfma_pad(acc[0][0], acc[0][1], acc[1][0], acc[1][1]);
        float ya[4], yb[4], y[4];
#pragma unroll
        for (int r = 0; r < 4; ++r) { ya[r] = g < 2 ? acc[0][0][r] : acc[1][0][r]; yb[r] = g < 2 ? acc[0][1][r] : acc[1][1][r]; }
        SB();
#pragma unroll
        for (int r = 0; r < 4; ++r) y[r] = swap32_add(ya[r], yb[r]);
        const float z0 = swap16_add(y[0], y[2]), z1 = swap16_add(y[1], y[3]);
        *(LAS float*)(lds + rwa) = z0;
        *(LAS float*)(lds + rwa + 128) = z1;
        FT(3);
        if (flag) *(LAS unsigned*)(lds + 4096 + (s & 1) * 4) = s + 1;
        bar();
        FT(4);
        if (w == 0) {
            LAS f32x2* r2 = (LAS f32x2*)(lds + rra);
            const f32x2 a0 = r2[0], a1 = r2[64], a2 = r2[128], a3 = r2[192];
            SB();
            const f32x2 v = (a0 + a1) + (a2 + a3);
            st4(nosent(pk2(v[0], v[1])), rw, so, mb);
            tdone = ck.now();
            st4(nosent((tdone & ~1u) | (*(volatile LAS unsigned*)(lds + 4096 + (s & 1) * 4) == (unsigned)(s + 1))), rw, sto, eb + EX_MST);
        } else tdone = ck.now();
        FT(5); FDUMP(32 + mi, s, w);
    }
}

constexpr int RING = 4, VPRE = 4;
constexpr int a_slot(int ks, int tile) { return ks * 16384 + tile * 2048; }
constexpr int a_xblk(int i) { return i < 3 ? a_slot(4, 6) + i * 1280 : a_slot(9, 6); }
constexpr int A_P = a_slot(9, 6) + 1280, A_P2 = A_P + 1024;
template <int W> struct AP {
    static constexpr int ROT = 3 * W, NT = W == 0 ? 0 : (W == 3 ? 2 : 3), T0 = W == 0 ? 0 : 3 * (W - 1);
    static constexpr int NL = W == 0 ? 0 : (W == 3 ? 8 : 13);
    static constexpr int QA = W == 0 ? 20 : (W == 3 ? 19 : 14);
    static constexpr bool k_lds(int t, int m) { return W == 0 ? false : (W == 3 ? (t == 1 && m >= 2) : (t == 1 || m % 3 == 2)); }
    static constexpr bool k_acc(int t, int m) { return W == 0 && m < 5; }
    static constexpr bool v_lds(int t, int ks) { return W == 3 ? ks < 4 : (ks < 4 || (ks == 4 && t == 0)); }
    static constexpr int k_idx(int t, int m) { int n = 0; for (int i = 0; i < 2 * m + t; ++i) n += k_lds(i & 1, i >> 1) ? 1 : 0; return n; }
    static constexpr int k_slot(int i) { return a_slot(5 + i / (NT ? NT : 1), T0 + i % (NT ? NT : 1)); }
    static constexpr int v_slot(int i) { return a_slot(i / (NT ? NT : 1), T0 + i % (NT ? NT : 1)); }
    static constexpr int stream(int i) { return i < NL ? k_slot(i) : v_slot(i - NL); }
    static constexpr int m_of(int ks) { return (ks + 10 - ROT % 10) % 10; }
};
DI void a_dma(const uchar* wsrc, LAS uchar* lds, int h, int w, int half) {
    const int lane = fresh_lane();
    const rsrc_t r = mk_rsrc(wsrc, 640 * 8192);
    const unsigned v = (lane >> 4) * 8192 + (((lane & 15) ^ (2 * (((lane >> 4) & 1) | (w << 1)))) << 4);
    const unsigned lb = (unsigned)(size_t)lds;
#pragma unroll
    for (int i = 20 * half; i < 20 * half + 20; ++i) dma16(r, v, (4 * (w + 4 * i)) * 8192 + h * 256, lb + (w + 4 * i) * 1024);
}
struct ATake {
    unsigned row[4], f[4];
    DI ATake() {
        const int lane = fresh_lane(), c = lane & 15, g = lane >> 4, q = c >> 2, pp = c & 3;
#pragma unroll
        for (int m = 0; m < 4; ++m) {
            const int k0 = m < 2 ? 8 * g + 4 * m : 32 + 8 * g + 4 * (m - 2), ga = k0 >> 4, qq = (k0 >> 2) & 3;
            const int r = 16 * ga + 2 * qq + (q & 1) + 8 * (q >> 1);
            row[m] = r * 256 + pp * 8; f[m] = 32 * ((r & 1) | ((r >> 1) & 6));
        }
    }
    DI u32x8 get(LAS uchar* lds, int ks, int tile) const {
        LAS uchar* a = lds + ks * 16384;
        const u32x2 t0 = trd(a + row[0] + (f[0] ^ (32 * tile))), t1 = trd(a + row[1] + (f[1] ^ (32 * tile))), t2 = trd(a + row[2] + (f[2] ^ (32 * tile))), t3 = trd(a + row[3] + (f[3] ^ (32 * tile)));
        return (u32x8){t0.x, t0.y, t1.x, t1.y, t2.x, t2.y, t3.x, t3.y};
    }
};
DI u32x8 ldsB(LAS uchar* a) { const u32x4 lo = *(LAS u32x4*)a, hi = *(LAS u32x4*)(a + 1024); return (u32x8){lo.x, lo.y, lo.z, lo.w, hi.x, hi.y, hi.z, hi.w}; }
DI void ldsB_put(LAS uchar* a, u32x8 v) { *(LAS u32x4*)a = (u32x4){v[0], v[1], v[2], v[3]}; *(LAS u32x4*)(a + 1024) = (u32x4){v[4], v[5], v[6], v[7]}; }
DI void pin(u32x8& v, bool acc) { if (acc) asm volatile("" : "+a"(v)); else asm volatile("" : "+v"(v)); }
DI void smf_x(f32x4& c, u32x4 a, u32x8 b, int idx, bool acc) { if (acc) smf_a(c, a, b, idx); else smf_v(c, a, b, idx); }

#define NEXT_KV() do { SB(); const int sn_ = min(s + 1, NSEG - 1); kvn = ld4(rw, 0, kvw * 4); \
    _Pragma("unroll") for (int i = 0; i < NPF; ++i) pf[i] = ld16(rw, lo16, pfb + sn_ * 32 * 8192 + i * 1024); SB(); } while (0)
template <int W>
DI void a_loop(const Params& p, LAS uchar* lds, int h, const u32x8 (&wq)[2][10], const u32x8 (&wk)[2][10], const u32x8 (&wv)[3][10]) {
    typedef AP<W> P;
    const int lane = fresh_lane(), c = lane & 15, g = lane >> 4;
    const rsrc_t rw = mk_rsrc(p.ws, WS_USED);
    constexpr int ROT = P::ROT, NT = P::NT, T0 = P::T0, NPF = W == 0 ? 8 : NT, NL = P::NL;
    const int idx = (c & 8) ? 0xEEEE : 0x4444;
    const unsigned lo16 = lane * 16, mo16 = (g * 16 + ((c >> 2) & 1) * 8 + (c >> 3) * 4 + (c & 3)) * 16;
    const unsigned oaddr = W == 0 ? (c >= 8 ? (g * 8 + (c & 7)) * 16 : OOB) : (c < 8 ? (c >> 2) * 1024 + (g >> 1) * 64 + (c & 3) * 16 + (g & 1) * 8 : OOB);
    const unsigned oaddr2 = c < 8 ? (c >> 2) * 1024 + (g & 1) * 128 + (g >> 1) * 64 + (c & 3) * 16 : OOB;
    const unsigned xw = a_xblk(W) + c * 40 + g * 8;
    const unsigned asto = lane == 0 ? h * 4 : OOB;
    const unsigned pfb = W == 0 ? OFF_KF + h * 8192 : OFF_VF + h * 8192 + T0 * 1024;
    u32x4 pf[NPF];
    unsigned long long alive = ~0ull;
    const unsigned exo = ex_off(p), pol = read_pol(p);
    Clock ck; ck.start();
    wait_ctr(p, rw, C_PZ, 56);
    wait_ctr(p, rw, C_KVH + h * 16, 4);
#pragma unroll
    for (int i = 0; i < NPF; ++i) pf[i] = ld16(rw, lo16, pfb + i * 1024);
    Sched sc = {ck.now(), 0u, DUR_A0, DUR_M0, PAD0, 0u, 0u, 0u};
    const float ninf = g < 2 ? -__builtin_inff() : 0.f;
    unsigned xra[4], xqa[4];
    if (W == 0) {
#pragma unroll
        for (int ks = 0; ks < 4; ++ks) {
            xra[ks] = a_xblk(ks) + (4 * g + (c >> 2)) * 40 + (c & 3) * 8; xqa[ks] = a_xblk(ks) + (4 * g + (c >> 2)) * 40 + (c & 1) * 8;
            asm volatile("" : "+v"(xra[ks]), "+v"(xqa[ks]));
        }
    }
    u32x4 one4 = {0x3F803F80u, 0x3F803F80u, 0x3F803F80u, 0x3F803F80u}, p2 = {0u, 0u, 0u, 0u}, p2s = p2, p2h = p2;
    unsigned pa[3] = {A_P + lo16, A_P2 + lane * 8u, A_P2 + (lane ^ 32) * 8u};
    if (W) asm volatile("" : "+a"(one4), "+v"(p2), "+v"(p2s), "+v"(p2h), "+v"(pa[0]), "+v"(pa[1]), "+v"(pa[2]));
    unsigned lb[3] = {lo16, lo16 + 65536, lo16 + 131072};
    asm volatile("" : "+v"(lb[0]), "+v"(lb[1]), "+v"(lb[2]));
    auto sget = [&](int i) -> u32x8 { const int a = P::stream(i); return ldsB(lds + lb[a >> 16] + (a & 65535)); };
    for (int s = 0; s < NSEG; ++s) {
        const unsigned eb = ex_step(exo, pol, s);
        unsigned mb[10], ob = eb + (h >> 2) * 16384 + (h & 3) * 2048 + T0 * 128;
#pragma unroll
        for (int m = 0; m < 10; ++m) { mb[m] = eb + mf_off((m + ROT) % 10); asm volatile("" : "+s"(mb[m])); }
        f32x4 aq[2], ak[2], va[3];
        u32x8 ring[RING];
#pragma unroll
        for (int t = 0; t < 2; ++t) { aq[t] = (f32x4){0.f, 0.f, 0.f, 0.f}; ak[t] = aq[t]; asm volatile("" : "+v"(aq[t]), "+v"(ak[t])); }
#pragma unroll
        for (int t = 0; t < 3; ++t) {
            const float z = W == 0 && t == 2 ? ninf : 0.f;
            va[t] = (f32x4){z, z, z, z}; asm volatile("" : "+v"(va[t]));
        }
        if (W) {
#pragma unroll
            for (int i = 0; i < RING; ++i) ring[i] = sget(i);
        }
        const unsigned tgo = sc.tgo;
        unsigned tw = ck.cycles(tgo);
        asm volatile("" : "+s"(ob), "+s"(tw));
        SB();
        unsigned mis = waitmt(tw);
        FT(0);
        u32x4 mf[10];
#pragma unroll
        for (int m = 0; m < 10; ++m) {
            mf[m] = ld16(rw, mo16, mb[m]);
            SB();
            if (m < 9) pace();
            SB();
        }
        for (;;) {
            unsigned mx = 0;
            unsigned long long early = 0;
#pragma unroll
            for (int m = 0; m < 10; ++m) {
                if (m == 9) {
                    mx4v(mx, mf[m]);
                    early = __builtin_amdgcn_ballot_w64(mx == SENT) & alive;
                    asm volatile("" : "+s"(early));
                    SB();
                }
#pragma unroll
                for (int t = 0; t < 2; ++t) {
                    smf_x(aq[t], mf[m], wq[t][m], idx, 2 * m + t < P::QA - (W == 0 ? 10 : 0));
                    if (P::k_lds(t, m)) {
                        const int i = P::k_idx(t, m);
                        smf_v(ak[t], mf[m], ring[i % RING], idx);
                        ring[i % RING] = sget(i + RING);
                    } else smf_x(ak[t], mf[m], wk[t][m], idx, W == 0);
                }
                if (m < 9) mx4v(mx, mf[m]);
                if (m == 0) FT(1);
                if (m == 9) FT(2);
                SB();
            }
            if (__builtin_expect(early == 0, 1)) break;
            unsigned n = SPIN_CAP; bool bad;
#pragma nounroll
            do {
                asm volatile("" ::: "memory");
#pragma unroll
                for (int m = 0; m < 10; ++m) mf[m] = ld16(rw, mo16, mb[m]);
                unsigned m2 = 0;
#pragma unroll
                for (int m = 0; m < 10; ++m) m2 = mx4(m2, mf[m]);
                bad = __builtin_amdgcn_ballot_w64(m2 == SENT) != 0;
            } while (bad && --n);
            if (bad) { alive = 0; set_tmo(p); }
            mis = tw + 100000;
#pragma unroll
            for (int t = 0; t < 2; ++t) { aq[t] = (f32x4){0.f, 0.f, 0.f, 0.f}; ak[t] = aq[t]; asm volatile("" : "+v"(aq[t]), "+v"(ak[t])); }
            if (W) {
#pragma unroll
                for (int i = 0; i < RING; ++i) ring[i] = sget(i);
            }
            asm volatile("" ::: "memory");
        }
        {
            float x[4], y[4];
            asm volatile("s_nop 2" : "+v"(aq[0]), "+v"(ak[0]));
#pragma unroll
            for (int r = 0; r < 4; ++r) x[r] = swap32_add(aq[0][r], ak[0][r]);
            const u32x2 x2 = {pk2(x[0], x[1]), pk2(x[2], x[3])};
            SB();
            asm volatile("" : "+v"(aq[1]), "+v"(ak[1]));
#pragma unroll
            for (int r = 0; r < 4; ++r) y[r] = swap32_add(aq[1][r], ak[1][r]);
            *(LAS u32x2*)(lds + xw) = x2;
            *(LAS u32x2*)(lds + xw + 640) = (u32x2){pk2(y[0], y[1]), pk2(y[2], y[3])};
        }
        SB();
        if (W) {
#pragma unroll
            for (int i = 0; i < VPRE; ++i) { const int r = 10 * NT - 1 - i; smf_a(va[r % NT], mf[P::m_of(r / NT)], wv[r % NT][r / NT], idx); }
        }
        bar();
        SB();
        FT(3);
        const unsigned kb = OFF_KM + (s * 32 + h) * 4096;
        const int kvw = s < 2 ? C_KVH + h * 16 : C_KV + ((min(s + 2, NSEG - 1) >> 3) * 32 + h) * 16;
        unsigned tpub = 0, kvn;
        if (W == 0) {
            u32x4 xf[4], xd[4];
#pragma unroll
            for (int ks = 0; ks < 4; ++ks) {
                const u32x2 a = trd(lds + xqa[ks]), b = trd(lds + xqa[ks] + 640), lo = trd(lds + xra[ks]), hi = trd(lds + xra[ks] + 640);
                xd[ks] = (u32x4){a.x, a.y, b.x, b.y}; xf[ks] = (u32x4){lo.x, lo.y, hi.x, hi.y};
            }
            SB();
#pragma unroll
            for (int ks = 0; ks < 4; ++ks) { mfma_v(va[0], pf[ks], xd[ks]); mfma_v(va[1], pf[(4 + ks) % NPF], xd[ks]); mfma_v(va[2], xf[ks], xd[ks]); }
            FT(4);
            float a0, a1, a2, a3, a4, a5, tm, tn;
            unsigned e0, e1, e2;
            asm volatile("s_nop 7\n\t"
                         "v_cndmask_b32_e64 %0, %15, %11, %23\n\tv_cndmask_b32_e64 %1, %16, %12, %23\n\tv_cndmask_b32_e64 %2, %17, %13, %23\n\t"
                         "v_cndmask_b32_e64 %3, %18, %14, %23\n\tv_cndmask_b32_e64 %4, %21, %19, %23\n\tv_cndmask_b32_e64 %5, %22, %20, %23\n\t"
                         "v_max3_f32 %6, %0, %1, %2\n\tv_max3_f32 %7, %3, %4, %5\n\tv_max_f32 %6, %6, %7\n\ts_nop 1\n\t"
                         "v_max_f32_dpp %6, %6, %6 row_ror:8 row_mask:0xf bank_mask:0xf\n\t"
                         "v_mov_b32 %7, %6\n\ts_nop 1\n\tv_permlane32_swap_b32 %6, %7\n\tv_max_f32 %6, %6, %7\n\t"
                         "v_mov_b32 %7, %6\n\ts_nop 1\n\tv_permlane16_swap_b32 %6, %7\n\tv_max_f32 %6, %6, %7\n\t"
                         "v_mul_f32 %6, %24, %6\n\t"
                         "v_fma_f32 %0, %0, %24, -%6\n\tv_fma_f32 %1, %1, %24, -%6\n\tv_fma_f32 %2, %2, %24, -%6\n\t"
                         "v_fma_f32 %3, %3, %24, -%6\n\tv_fma_f32 %4, %4, %24, -%6\n\tv_fma_f32 %5, %5, %24, -%6\n\t"
                         "v_exp_f32 %0, %0\n\tv_exp_f32 %1, %1\n\tv_exp_f32 %2, %2\n\tv_exp_f32 %3, %3\n\tv_exp_f32 %4, %4\n\tv_exp_f32 %5, %5\n\t"
                         "v_cvt_pk_bf16_f32 %8, %0, %1\n\tv_cvt_pk_bf16_f32 %9, %2, %3\n\tv_cvt_pk_bf16_f32 %10, %4, %5"
                         : "=&v"(a0), "=&v"(a1), "=&v"(a2), "=&v"(a3), "=&v"(a4), "=&v"(a5), "=&v"(tm), "=&v"(tn), "=&v"(e0), "=&v"(e1), "=&v"(e2)
                         : "v"(va[0][0]), "v"(va[0][1]), "v"(va[0][2]), "v"(va[0][3]), "v"(va[1][0]), "v"(va[1][1]), "v"(va[1][2]), "v"(va[1][3]),
                           "v"(va[2][0]), "v"(va[2][1]), "v"(va[2][2]), "v"(va[2][3]), "s"(0x00FF00FF00FF00FFull), "s"(SCL));
            *(LAS u32x2*)(lds + A_P + (g * 16 + (c & 7)) * 16 + (c >> 3) * 8) = (u32x2){e0, e1};
            *(LAS unsigned*)(lds + A_P2 + (g * 16 + (c & 7)) * 8 + (c >> 3) * 4) = e2;
            FT(5);
            bar();
            FT(6);
            sc.fetch(rw, exo, pol, max(s, 1), lane);
            NEXT_KV();
            if (s < NSEG - 1) waitmt(mt32() + KM_DELAY * 3);
            st4(s, rw, lane == 0 ? (C_KM + h * 32) * 4 : OOB, 0);
#pragma unroll
            for (int ks = 0; ks < 4; ++ks) st16(xf[ks], rw, oaddr, kb + ks * 512);
        } else {
#pragma unroll
            for (int i = 0; i < (10 * NT - NL - VPRE > NL ? 10 * NT - NL - VPRE : NL); ++i) {
                if (i < NL) {
                    smf_v(va[i % NT], mf[P::m_of(i / NT)], ring[(NL + i) % RING], idx);
                    if (NL + i + RING < 2 * NL) ring[(NL + i) % RING] = sget(NL + i + RING);
                }
                const int r = NL + i;
                if (r < 10 * NT - VPRE) smf_a(va[r % NT], mf[P::m_of(r / NT)], wv[r % NT][r / NT], idx);
                SB();
            }
            asm volatile("s_nop 7" : "+v"(va[0]), "+v"(va[1]), "+v"(va[2]));
            float sa[4], sb[4];
#pragma unroll
            for (int r = 0; r < 4; ++r) { sa[r] = swap32_add(va[0][r], va[1][r]); if (W != 3) sb[r] = swap32_add(va[2][r], va[2][r]); }
            u32x4 vv = {pk2(sa[0], sa[1]), pk2(sa[2], sa[3]), 0u, 0u};
            if (W != 3) { vv.z = pk2(sb[0], sb[1]); vv.w = pk2(sb[2], sb[3]); }
            asm volatile("" : "+v"(vv));
            SB();
            FT(5);
            bar();
            FT(6);
            SB();
            if (s < NSEG - 1) {
                const u32x4 p01 = *(LAS u32x4*)(lds + pa[0]);
                const u32x2 q2 = *(LAS u32x2*)(lds + pa[1]), q2s = *(LAS u32x2*)(lds + pa[2]);
                p2.x = q2.x; p2.y = q2.y; p2s.x = q2s.x; p2s.y = q2s.y;
                if (W != 3) { const u32x2 q2h = *(volatile LAS u32x2*)(lds + pa[1]); p2h.z = q2h.x; p2h.w = q2h.y; }
                SB();
                f32x4 l, o[3];
                asm volatile("v_mfma_f32_16x16x32_bf16 %0, %1, %2, 0" : "=&v"(l) : "a"(one4), "v"(p01));
                mfma_0(o[0], pf[0], p01);
                mfma_av(l, one4, p2);
                mfma_0(o[1], pf[1 % NPF], p01);
                mfma_v(o[0], vv, p2s);
                mfma_v(o[1], vv, p2);
                float inv;
                if (W != 3) {
                    mfma_0(o[2], pf[2 % NPF], p01);
                    asm volatile("v_rcp_f32 %0, %1" : "=v"(inv) : "v"(l[0]));
                    mfma_v(o[2], vv, p2h);
                    asm volatile("s_nop 3" : "+v"(o[0]), "+v"(o[1]), "+v"(inv));
                } else {
                    asm volatile("s_nop 1\n\tv_rcp_f32 %0, %3\n\ts_nop 5" : "=&v"(inv), "+v"(o[0]), "+v"(o[1]) : "v"(l[0]));
                }
                const f32x4 n0 = o[0] * inv, n1 = o[1] * inv;
                SB();
                const unsigned a0 = pk2(n0[0], n0[1]), b0 = pk2(n1[0], n1[1]), a1 = pk2(n0[2], n0[3]), b1 = pk2(n1[2], n1[3]);
                SB();
                auto r0 = __builtin_amdgcn_permlane16_swap(a0, b0, false, false);
                auto r1 = __builtin_amdgcn_permlane16_swap(a1, b1, false, false);
                st16((u32x4){nosent(r0[0]), nosent(r1[0]), nosent(r0[1]), nosent(r1[1])}, rw, oaddr2, ob);
                if (W != 3) {
                    asm volatile("" : "+v"(o[2]));
                    const f32x4 n2 = o[2] * inv;
                    st8((u32x2){nosent(pk2(n2[0], n2[1])), nosent(pk2(n2[2], n2[3]))}, rw, oaddr, ob + 256);
                }
            }
            tpub = ck.now();
            sc.fetch(rw, exo, pol, max(s, 1), lane);
            NEXT_KV();
            if (W == 1) st4(mis - tw <= 120u ? tpub - tgo : 0u, rw, asto, eb + EX_AST);
            st8((u32x2){vv.x, vv.y}, rw, lane * 8, kb + 2048 + T0 * 256);
            if (W != 3) st8((u32x2){vv.z, vv.w}, rw, g < 2 ? lane * 8 : OOB, kb + 2048 + T0 * 256 + 512);
        }
        FT(7); FDUMP(h, s, W);
        STAT(h, s, W, ck.at(mis), tpub, tgo, tries);
        if (s == NSEG - 1) break;
        ck.sync();
        if (s > 0) sc.next(p, rw, exo, pol, s, lane, alive);
        if ((unsigned)__builtin_amdgcn_readfirstlane(kvn) < 4u) wait_ctr(p, rw, kvw, 4);
        if (s == 2) wait_ctr(p, rw, C_PZ + 32, 56);
    }
    wait_vm<0>();
    bar();
    XST(W == 0, h);
    if (W == 0) st4(NSEG, rw, lane == 0 ? (C_KM + h * 32) * 4 : OOB, 0);
    __builtin_amdgcn_endpgm();
}

template <int W>
DI void a_wave(const Params& p, LAS uchar* lds, int h) {
    typedef AP<W> P;
    u32x8 wq[2][10], wk[2][10], wv[3][10];
    a_dma(p.wq, lds, h, W, 0);
    a_dma(p.wq, lds, h, W, 1);
    static_for<6>([&](auto sc) {
        constexpr int mat = sc.value >> 1, half = sc.value & 1;
        if (sc.value < 5) wait_vm<20>(); else wait_vm<0>();
        bar();
        AST_(sc.value);
        const ATake tk;
#pragma unroll
        for (int ks = 5 * half; ks < 5 * half + 5; ++ks) {
            const int m = P::m_of(ks);
#pragma unroll
            for (int t = 0; t < (mat < 2 ? 2 : P::NT); ++t) {
                if (mat == 0) { wq[t][m] = tk.get(lds, ks, 2 * W + t); pin(wq[t][m], 2 * m + t < P::QA - (W == 0 ? 10 : 0)); }
                if (mat == 1) { wk[t][m] = tk.get(lds, ks, 2 * W + t); pin(wk[t][m], W == 0); }
                if (mat == 2) { wv[t][ks] = tk.get(lds, ks, P::T0 + t); pin(wv[t][ks], true); }
            }
            SB();
        }
        bar(); SB();
        if (mat < 2) a_dma(mat == 0 ? p.wk : p.wv, lds, h, W, half);
        else {
            const int lane = fresh_lane();
            if (half == 0) {
#pragma unroll
                for (int i = 0; i < P::NL; ++i) ldsB_put(lds + P::v_slot(i) + lane * 16, wv[i % (P::NT ? P::NT : 1)][i / (P::NT ? P::NT : 1)]);
            } else {
#pragma unroll
                for (int i = 0; i < 20; ++i) if (P::k_lds(i & 1, i >> 1)) ldsB_put(lds + P::k_slot(P::k_idx(i & 1, i >> 1)) + lane * 16, wk[i & 1][i >> 1]);
            }
        }
        SB();
    });
    bar();
    SB();
    a_loop<W>(p, lds, h, wq, wk, wv);
}
DI void role_A(const Params& p, LAS uchar* lds, int h) {
    const int w = __builtin_amdgcn_readfirstlane(threadIdx.x >> 6);
    if (w == 0) a_wave<0>(p, lds, h);
    else if (w == 1) a_wave<1>(p, lds, h);
    else if (w == 2) a_wave<2>(p, lds, h);
    else a_wave<3>(p, lds, h);
}
DI int take_slot(const Params& p, LAS uchar* lds) {
    if (threadIdx.x == 0) {
        unsigned x = xcc_id(), t;
        for (int i = 0; i < 8; ++i) {
            t = __hip_atomic_fetch_add((unsigned*)p.ws + C_POOL + x * 32, 1u, __ATOMIC_RELAXED, __HIP_MEMORY_SCOPE_AGENT);
            if (t < 32) break;
            x = (x + 1) & 7;
        }
        *(LAS unsigned*)lds = x * 32 + t;
    }
    bar();
    const int slot = __builtin_amdgcn_readfirstlane(*(LAS unsigned*)lds);
    bar();
    return slot;
}
DI int take_ticket(const Params& p, LAS uchar* lds, int word) {
    if (fresh_tid() == 0) *(LAS unsigned*)(lds + LDS_BYTES - 16) = __hip_atomic_fetch_add((unsigned*)p.ws + word, 1u, __ATOMIC_RELAXED, __HIP_MEMORY_SCOPE_AGENT);
    bar();
    const int t = __builtin_amdgcn_readfirstlane(*(LAS unsigned*)(lds + LDS_BYTES - 16));
    bar();
    return t;
}
#define TSTAT(i, a, b)
#define SMALL_DELAY 300
DI void warm_l2(const Params& p, int x, int j) {
    const int lane = fresh_lane(), id = j * 4 + __builtin_amdgcn_readfirstlane(threadIdx.x >> 6);
    u32x4 sink[46];
#pragma unroll
    for (int i = 0; i < 46; ++i) {
        const int n = min(id + 56 * i, 2559), row = n >> 1;
        const rsrc_t r = mk_rsrc(row < 640 ? p.wk : p.wv, 640 * 8192);
        asm volatile("buffer_load_dwordx4 %0, %1, %2, %3 offen" : "=v"(sink[i]) : "v"(lane * 16), "s"(r), "s"((row < 640 ? row : row - 640) * 8192 + x * 2048 + (n & 1) * 1024) : "memory");
    }
    wait_vm<0>();
#pragma unroll
    for (int i = 0; i < 46; ++i) asm volatile("" :: "v"(sink[i]));
}
DI void background(const Params& p, LAS uchar* lds, int x, int j) {
    const bool big = x >= 4;
    if (!big) {
        const int id = j * 4 + x;
        ln_tile(p, id);
        poison(p, id, 0, 80, C_PZ);
        warm_l2(p, x, j);
        ln_tile(p, id + 56);
        if (id < 16) ln_tile(p, id + 112);
        poison(p, id, 80, 1280, C_PZ + 32);
    } else {
        if (j & 2) kv0_task<true>(p, lds, 32 * (x - 4) + j); else kv0_task<false>(p, lds, 32 * (x - 4) + j);
        TSTAT((x - 4) * 128 + j, t0, 0);
    }
    if (!big) return;
    const int y = x - 4, nw = y < 3 ? 16 : 8;
    const int ea = 16, e1 = ea + 48, e2 = e1 + nw, e3 = e2 + 32, e4 = e3 + 10;
    for (;;) {
        const int t = take_ticket(p, lds, C_Q1 + x * 32);
        if (t >= e4) break;
        if (t < e3) {
            int chunk, u0, nu = 4;
            if (t < ea) { chunk = 1; u0 = 32 * y + 2 * t; nu = 2; }
            else if (t < e1) { chunk = 2 + ((t - ea) >> 3); u0 = 32 * y + 4 * ((t - ea) & 7); }
            else if (t < e2) { const int i = t - e1; chunk = y < 3 ? i >> 1 : i; u0 = 192 + 8 * y + (y < 3 ? 4 * (i & 1) : 0); if (y == 3) nu = 3; }
            else { chunk = (t - e2) >> 2; u0 = 128 + 16 * y + 4 * ((t - e2) & 3); }
            p1_task(p, lds, chunk, u0, nu);
            TSTAT(x * 128 + t, t0, u0 < 128 ? 1 : (u0 < 192 ? 2 : 3));
        } else {
            const int i = t - e3;
            down_task(p, lds, i < 8 ? i : 2 * y + i - 8, i < 8 ? y : 4);
            TSTAT(x * 128 + t, t0, 4);
        }
    }
    const int t = take_ticket(p, lds, C_P2);
    if (t >= N_T + 20 * N_WG) return;
    if (t < N_T) {
        const int w = __builtin_amdgcn_readfirstlane(threadIdx.x >> 6);
        for (int s = t >> 3; s < NSEG; s += N_T / 8) attn_task(p, s, (t & 7) * 4 + w, (s & 7) < N_T / 8);
    } else { const int gq = ((t - N_T) * 205) >> 12; w_role(p, lds, (t - N_T) - 20 * gq, gq); }
    TSTAT(1024 + t, t0, t < N_T ? 5 : 6);
}
__global__ void __launch_bounds__(256, 1) k_main(Params p) {
    extern __shared__ __attribute__((aligned(16))) uchar smem[];
    const int slot = take_slot(p, (LAS uchar*)smem), x = slot >> 5, t = slot & 31;
    if (x < 4 && t < 8) role_A(p, (LAS uchar*)smem, x * 8 + t);
    else if (x < 4 && t < 18) role_M(p, (LAS uchar*)smem, x * 10 + t - 8);
    else background(p, (LAS uchar*)smem, x, x < 4 ? t - 18 : t);
}

extern "C" void kernel_launch(void* const* d_in, const int* in_sizes, int n_in, void* d_out, int out_size, void* d_ws, size_t ws_size, hipStream_t stream) {
    Params p;
    p.x = (const uchar*)d_in[0]; p.mem0 = (const uchar*)d_in[1]; p.wq = (const uchar*)d_in[2]; p.wk = (const uchar*)d_in[3];
    p.wv = (const uchar*)d_in[4]; p.wo = (const uchar*)d_in[5]; p.w1 = (const uchar*)d_in[8]; p.b1 = (const uchar*)d_in[9];
    p.w2 = (const uchar*)d_in[10]; p.out = (uchar*)d_out; p.ws = (uchar*)d_ws;
    (void)hipFuncSetAttribute((const void*)k_main, hipFuncAttributeMaxDynamicSharedMemorySize, LDS_BYTES);
    k_init<<<64, 256, 0, stream>>>(p);
    k_main<<<256, 256, LDS_BYTES, stream>>>(p);
}
```

```cpp
#include <hip/hip_runtime.h>
#include <stdint.h>

typedef __bf16 bf16x2 __attribute__((ext_vector_type(2)));
typedef __bf16 bf16x4 __attribute__((ext_vector_type(4)));
typedef __bf16 bf16x8 __attribute__((ext_vector_type(8)));
typedef __bf16 bf16x16 __attribute__((ext_vector_type(16)));
typedef short s16x4 __attribute__((ext_vector_type(4)));
typedef float f32x2 __attribute__((ext_vector_type(2)));
typedef float f32x4 __attribute__((ext_vector_type(4)));
typedef unsigned u32x2 __attribute__((ext_vector_type(2)));
typedef unsigned u32x4 __attribute__((ext_vector_type(4)));
typedef unsigned u32x8 __attribute__((ext_vector_type(8)));
typedef unsigned char uchar;
typedef __amdgpu_buffer_rsrc_t rsrc_t;
#define LAS __attribute__((address_space(3)))
#define DI __device__ __forceinline__

constexpr int S_ = 2048, D_ = 640, H_ = 32, NSEG = 64, F_ = 1728;
constexpr int LDS_BYTES = 163840;
constexpr int OFF_CTRL = 0;
constexpr int OFF_XF = 65536;
constexpr int OFF_KF = OFF_XF + 128 * 20 * 1024;
constexpr int OFF_QF = OFF_KF + 64 * 32 * 8192;
constexpr int OFF_VF = OFF_QF + 64 * 32 * 8192;
constexpr int OFF_ACT = OFF_VF + 64 * 32 * 8192;
constexpr int OFF_GF = OFF_ACT + 128 * 54 * 1024;
constexpr int OFF_OT = OFF_GF + 128 * 20 * 1024;
constexpr int OFF_KM = OFF_OT + 128 * 32 * 4096;
constexpr int OFF_EX = OFF_KM + 64 * 32 * 4096;
constexpr int EX_STEP = 163840, EX_MF = 8 * 16384, EX_AST = 9 * 16384 + 2048, EX_MST = 9 * 16384 + 2304, EX_PROBE = 9 * 16384 + 7168;
constexpr int WS_USED = OFF_EX + 7 * 2097152;
constexpr int C_TMO = 0, C_POOL = 32  , C_KV = 512  , C_Q = C_KV + 4096, C_ACT = C_Q + 4096  , C_G = C_ACT + 256  ,
              C_OT = C_G + 1280  , C_KM = C_OT + 2048  , C_Q1 = C_KM + 1024  , C_P2 = C_Q1 + 256, C_KVH = C_P2 + 32  , C_X = C_KVH + 512  , C_PZ = C_X + 512  , C_WT = C_PZ + 64  , C_OF = C_WT + 640  ,
              C_POL = 16320  ;
static_assert(C_OF + 128 <= C_POL, "");
constexpr unsigned SENT = 0xFFFFFFFFu;
constexpr int SC1 = 16;
constexpr unsigned OOB = 0x7FFFFF00u;
constexpr unsigned SPIN_CAP = 400000;

struct Params {
    const uchar* x; const uchar* mem0; const uchar* wq; const uchar* wk; const uchar* wv; const uchar* wo;
    const uchar* w1; const uchar* b1; const uchar* w2; uchar* out; uchar* ws;
};

DI rsrc_t mk_rsrc(const void* p, unsigned bytes) { return __builtin_amdgcn_make_buffer_rsrc((void*)p, 0, bytes, 0x00020000); }
DI u32x4 ld16(rsrc_t r, unsigned v, unsigned s) { return __builtin_amdgcn_raw_buffer_load_b128(r, v, s, SC1); }
DI u32x2 ld8(rsrc_t r, unsigned v, unsigned s) { return __builtin_amdgcn_raw_buffer_load_b64(r, v, s, SC1); }
DI unsigned ld4(rsrc_t r, unsigned v, unsigned s) { return __builtin_amdgcn_raw_buffer_load_b32(r, v, s, SC1); }
DI void st16(u32x4 d, rsrc_t r, unsigned v, unsigned s) { __builtin_amdgcn_raw_buffer_store_b128(d, r, v, s, SC1); asm volatile("s_nop 1" :: "v"(d)); }
DI void st8(u32x2 d, rsrc_t r, unsigned v, unsigned s) { __builtin_amdgcn_raw_buffer_store_b64(d, r, v, s, SC1); }
DI void st4(unsigned d, rsrc_t r, unsigned v, unsigned s) { __builtin_amdgcn_raw_buffer_store_b32(d, r, v, s, SC1); }
DI unsigned pk2(float a, float b) { f32x2 v = {a, b}; return __builtin_bit_cast(unsigned, __builtin_convertvector(v, bf16x2)); }
DI unsigned nosent(unsigned v) { return min(v, 0xFFFFFFFEu); }
DI float bf_lo(unsigned v) { return __builtin_bit_cast(float, v << 16); }
DI float bf_hi(unsigned v) { return __builtin_bit_cast(float, v & 0xFFFF0000u); }
DI float rbf(float a) { return bf_lo(pk2(a, a)); }
DI bf16x8 as8(u32x4 v) { return __builtin_bit_cast(bf16x8, v); }
DI f32x4 mfma32(u32x4 a, u32x4 b, f32x4 c) { return __builtin_amdgcn_mfma_f32_16x16x32_bf16(as8(a), as8(b), c, 0, 0, 0); }
DI f32x4 mfma16(u32x2 a, u32x2 b, f32x4 c) { return mfma32((u32x4){a.x, a.y, 0u, 0u}, (u32x4){b.x, b.y, 0u, 0u}, c); }
DI f32x4 smf(u32x4 a, u32x8 b, f32x4 c, int idx) { return __builtin_amdgcn_smfmac_f32_16x16x64_bf16(as8(a), __builtin_bit_cast(bf16x16, b), c, idx, 0, 0); }
DI void smf_a(f32x4& c, u32x4 a, u32x8 b, int idx) { asm volatile("v_smfmac_f32_16x16x64_bf16 %0, %1, %2, %3" : "+v"(c) : "v"(a), "a"(b), "v"(idx)); }
DI void smf_v(f32x4& c, u32x4 a, u32x8 b, int idx) { asm volatile("v_smfmac_f32_16x16x64_bf16 %0, %1, %2, %3" : "+v"(c) : "v"(a), "v"(b), "v"(idx)); }
DI void mfma_v(f32x4& c, u32x4 a, u32x4 b) { asm volatile("v_mfma_f32_16x16x32_bf16 %0, %1, %2, %0" : "+v"(c) : "v"(a), "v"(b)); }
DI void mfma_0(f32x4& c, u32x4 a, u32x4 b) { asm volatile("v_mfma_f32_16x16x32_bf16 %0, %1, %2, 0" : "=&v"(c) : "v"(a), "v"(b)); }
DI float fmax2(float a, float b) { return __builtin_amdgcn_fmed3f(a, b, __builtin_inff()); }
DI void mfma_pad(f32x4& a, f32x4& b, f32x4& c, f32x4& d) { asm volatile("s_nop 7" : "+v"(a), "+v"(b), "+v"(c), "+v"(d)); }
DI u32x2 trd(LAS uchar* p) { return __builtin_bit_cast(u32x2, __builtin_amdgcn_ds_read_tr16_b64_v4i16((LAS s16x4*)p)); }
DI unsigned mx4(unsigned m, u32x4 v) { return max(max(max(m, v.x), max(v.y, v.z)), v.w); }
DI void mx4v(unsigned& m, u32x4 v) { asm volatile("v_max3_u32 %0, %0, %1, %2\n\tv_max3_u32 %0, %0, %3, %4" : "+v"(m) : "v"(v.x), "v"(v.y), "v"(v.z), "v"(v.w)); }
DI unsigned long long nan_lanes(const f32x4& a) { return __builtin_amdgcn_ballot_w64(__builtin_isunordered(a[0], a[1]) | __builtin_isunordered(a[2], a[3])); }
template <int N> DI bool refetch(u32x4 (&f)[N], rsrc_t r, unsigned v, const unsigned (&so)[N], unsigned& budget) {
    bool redo = false;
#pragma nounroll
    for (;;) {
        unsigned m = 0;
#pragma unroll
        for (int i = 0; i < N; ++i) m = max(max(max(m, f[i].x), max(f[i].y, f[i].z)), f[i].w);
        if (__builtin_amdgcn_ballot_w64(m == SENT) == 0 || budget == 0) break;
        --budget; redo = true;
        asm volatile("" ::: "memory");
#pragma unroll
        for (int i = 0; i < N; ++i) f[i] = __builtin_amdgcn_raw_buffer_load_b128(r, v, so[i], SC1);
    }
    return redo;
}
DI unsigned rt32() { unsigned long long t; asm volatile("s_memrealtime %0\n\ts_waitcnt lgkmcnt(0)" : "=s"(t) :: "memory"); return (unsigned)t; }
DI unsigned waitrt(unsigned t) { unsigned n; do n = rt32(); while ((int)(n - t) < 0); return n; }
DI void bar() { asm volatile("s_waitcnt lgkmcnt(0)" ::: "memory"); __builtin_amdgcn_s_barrier(); asm volatile("" ::: "memory"); }
template <int N> DI void wait_vm() { asm volatile("s_waitcnt vmcnt(%0)" :: "n"(N) : "memory"); }
DI void dma16(rsrc_t r, unsigned voff, unsigned soff, unsigned ldsaddr) {
    asm volatile("s_mov_b32 m0, %0\n\ts_nop 0\n\tbuffer_load_dwordx4 %1, %2, %3 offen lds" :: "s"(ldsaddr), "v"(voff), "s"(r), "s"(soff) : "memory");
}
DI float swap32_add(float a, float b) { auto r = __builtin_amdgcn_permlane32_swap(__builtin_bit_cast(unsigned, a), __builtin_bit_cast(unsigned, b), false, false);
    return __builtin_bit_cast(float, (unsigned)r[0]) + __builtin_bit_cast(float, (unsigned)r[1]); }
DI float swap16_add(float a, float b) { auto r = __builtin_amdgcn_permlane16_swap(__builtin_bit_cast(unsigned, a), __builtin_bit_cast(unsigned, b), false, false);
    return __builtin_bit_cast(float, (unsigned)r[0]) + __builtin_bit_cast(float, (unsigned)r[1]); }
DI float grp_max(float m) {
    auto r = __builtin_amdgcn_permlane32_swap(__builtin_bit_cast(unsigned, m), __builtin_bit_cast(unsigned, m), false, false);
    m = fmax2(__builtin_bit_cast(float, (unsigned)r[0]), __builtin_bit_cast(float, (unsigned)r[1]));
    auto q = __builtin_amdgcn_permlane16_swap(__builtin_bit_cast(unsigned, m), __builtin_bit_cast(unsigned, m), false, false);
    return fmax2(__builtin_bit_cast(float, (unsigned)q[0]), __builtin_bit_cast(float, (unsigned)q[1]));
}
DI int fresh_lane() { int l = __builtin_amdgcn_mbcnt_hi(~0u, __builtin_amdgcn_mbcnt_lo(~0u, 0u)); asm volatile("" : "+v"(l)); return l & 63; }
DI unsigned fresh_tid() { unsigned t = threadIdx.x; asm volatile("" : "+v"(t)); return t; }
DI unsigned ex_off(const Params& p) { return OFF_EX + ((0u - (unsigned)(size_t)(p.ws + OFF_EX)) & 0x1FFFFFu); }
DI unsigned ex_step(unsigned exo, unsigned pol, int s) { const int r = (s * 43) >> 9; return exo + r * 2097152 + (s - 12 * r) * EX_STEP + ((pol >> r) & 1) * 8192; }
DI constexpr unsigned mf_off(int ks) { return EX_MF + (ks >> 3) * 16384 + (ks & 7) * 1024; }
DI unsigned xcc_id() { return (unsigned)__builtin_amdgcn_s_getreg(20 | (3 << 11)) & 7u; }
DI unsigned mt32() { unsigned long long t; asm volatile("s_memtime %0\n\ts_waitcnt lgkmcnt(0)" : "=s"(t) :: "memory"); return (unsigned)t; }
DI unsigned read_pol(const Params& p) {
    const int lane = threadIdx.x & 63;
    const unsigned v = ((const unsigned*)p.ws)[C_POL + min(lane, 47)];
    const unsigned long long m1 = __builtin_amdgcn_ballot_w64(v == 2), m0 = __builtin_amdgcn_ballot_w64(v == 1);
    unsigned pol = 0;
#pragma unroll
    for (int r = 0; r < 6; ++r) pol |= (__builtin_popcountll((m1 >> (8 * r)) & 255) > __builtin_popcountll((m0 >> (8 * r)) & 255) ? 1u : 0u) << r;
    return pol;
}
DI void set_tmo(const Params& p) { __hip_atomic_store((unsigned*)p.ws + C_TMO, 1u, __ATOMIC_RELAXED, __HIP_MEMORY_SCOPE_AGENT); }

DI float row16_sum(float v) {
    v += __builtin_bit_cast(float, __builtin_amdgcn_update_dpp(0, __builtin_bit_cast(int, v), 0x121, 0xf, 0xf, true));
    v += __builtin_bit_cast(float, __builtin_amdgcn_update_dpp(0, __builtin_bit_cast(int, v), 0x122, 0xf, 0xf, true));
    v += __builtin_bit_cast(float, __builtin_amdgcn_update_dpp(0, __builtin_bit_cast(int, v), 0x124, 0xf, 0xf, true));
    v += __builtin_bit_cast(float, __builtin_amdgcn_update_dpp(0, __builtin_bit_cast(int, v), 0x128, 0xf, 0xf, true));
    return v;
}
DI void add_ctr(const Params& p, int word, unsigned v) { (void)__hip_atomic_fetch_add((unsigned*)p.ws + word, v, __ATOMIC_RELAXED, __HIP_MEMORY_SCOPE_AGENT); }
DI void wait_ctr(const Params& p, rsrc_t rw, int word, unsigned want) {
    unsigned n = SPIN_CAP;
#pragma nounroll
    for (;;) {
        asm volatile("" ::: "memory");
        if ((unsigned)__builtin_amdgcn_readfirstlane(ld4(rw, 0, word * 4)) >= want) break;
        if (--n == 0) { set_tmo(p); break; }
        if ((n & 63) == 0 && __builtin_amdgcn_readfirstlane(ld4(rw, 0, C_TMO * 4))) break;
        __builtin_amdgcn_s_sleep(1);
    }
    asm volatile("" ::: "memory");
}

__global__ void __launch_bounds__(256) k_init(Params p) {
    const int tid = threadIdx.x, lane = tid & 63, w = tid >> 6, b = blockIdx.x;
    if (b < 48) {
        __shared__ unsigned lat[4];
        unsigned* a = (unsigned*)(p.ws + ex_off(p) + (b >> 3) * 2097152 + EX_PROBE + (w >> 1) * 8192 + (w & 1) * EX_STEP + (b & 7) * 128);
        unsigned best = ~0u;
        for (int i = 0; i < 3; ++i) {
            const unsigned t0 = mt32();
            unsigned v = 0;
            if (lane == 0) v = __hip_atomic_fetch_add(a, 1u, __ATOMIC_RELAXED, __HIP_MEMORY_SCOPE_AGENT);
            asm volatile("s_waitcnt vmcnt(0)" :: "v"(v) : "memory");
            best = min(best, mt32() - t0);
        }
        if (lane == 0) lat[w] = best;
        __syncthreads();
        const int d = (int)min(lat[0], lat[1]) - (int)min(lat[2], lat[3]);
        if (tid == 0) ((unsigned*)p.ws)[C_POL + b] = abs(d) < 120 ? 0 : 1 + ((d > 0) ^ (xcc_id() >= 4));
    } else {
        const int i = (b - 48) * 256 + tid;
        if (i < C_POL / 4) ((u32x4*)(p.ws + OFF_CTRL))[i] = (u32x4){0u, 0u, 0u, 0u};
    }
}

DI void ln_tile(const Params& p, int b) {
    const int lane = fresh_lane(), w = __builtin_amdgcn_readfirstlane(threadIdx.x >> 6);
    const rsrc_t rw = mk_rsrc(p.ws, WS_USED);
    const int c = w * 4 + (lane >> 4), sub = lane & 15, row = b * 16 + c;
    u32x4 v[5]; float sum = 0.f;
#pragma unroll
    for (int k = 0; k < 5; ++k) v[k] = *(const u32x4*)(p.x + (size_t)row * 1280 + (k * 16 + sub) * 16);
#pragma unroll
    for (int k = 0; k < 5; ++k)
#pragma unroll
        for (int j = 0; j < 4; ++j) sum += bf_lo(v[k][j]) + bf_hi(v[k][j]);
    const float mean = row16_sum(sum) * (1.f / 640.f);
    float sq = 0.f;
#pragma unroll
    for (int k = 0; k < 5; ++k)
#pragma unroll
        for (int j = 0; j < 4; ++j) { const float a = bf_lo(v[k][j]) - mean, bb = bf_hi(v[k][j]) - mean; sq += a * a + bb * bb; }
    const float mb = rbf(mean), sd = rbf(sqrtf(rbf(rbf(row16_sum(sq) * (1.f / 640.f)) + 1e-5f))), ri = 1.f / sd;
#pragma unroll
    for (int k = 0; k < 5; ++k) {
        u32x4 o;
#pragma unroll
        for (int j = 0; j < 4; ++j) { const unsigned d = pk2(bf_lo(v[k][j]) - mb, bf_hi(v[k][j]) - mb); o[j] = pk2(bf_lo(d) * ri, bf_hi(d) * ri); }
        st16(o, rw, (k * 16 + sub) * 256 + c * 16, OFF_XF + b * 20480);
    }
    wait_vm<0>();
    bar();
    if (fresh_tid() == 0) add_ctr(p, C_X + (b >> 3) * 32, 1u);
}
DI void poison(const Params& p, int j, int n0, int n1, int word) {
    const int tid = fresh_tid();
    const rsrc_t rw = mk_rsrc(p.ws, WS_USED);
    const unsigned exo = ex_off(p), pol = read_pol(p);
    const u32x4 sv = {SENT, SENT, SENT, SENT};
    for (int n = n0 + j; n < n1; n += 56) {
        const int s = min((n * 3277) >> 16, 63), i = n - 20 * s;
        u32x4 v = sv;
        if (s == 0 && i >= 16 && (i < 18 || (i == 18 && tid < 128))) {
            const int f = (i - 16) * 256 + tid, ks = f >> 6, l = f & 63, r = (l & 3) | ((l >> 1) & 4), half = (l >> 2) & 1, g = l >> 4;
            v = *(const u32x4*)(p.mem0 + (r * 640 + 64 * ks + 16 * g + 8 * half) * 2);
            v.x = nosent(v.x); v.y = nosent(v.y); v.z = nosent(v.z); v.w = nosent(v.w);
        }
        st16(v, rw, tid * 16, n < 1280 ? ex_step(exo, pol, s) + (i >> 1) * 16384 + (i & 1) * 4096
                                       : (n < 1312 ? OFF_KM + 63 * 131072 + (n - 1280) * 4096 : OFF_OT + 126 * 131072 + (n - 1312) * 4096));
    }
    wait_vm<0>();
    bar();
    if (fresh_tid() == 0) add_ctr(p, word, 1u);
}

#define SB() __builtin_amdgcn_sched_barrier(0)
DI void mfma_va(f32x4& c, u32x4 a, u32x4 b) { asm volatile("v_mfma_f32_16x16x32_bf16 %0, %1, %2, %0" : "+v"(c) : "v"(a), "a"(b)); }
DI void mfma_av(f32x4& c, u32x4 a, u32x4 b) { asm volatile("v_mfma_f32_16x16x32_bf16 %0, %1, %2, %0" : "+v"(c) : "a"(a), "v"(b)); }
DI void mfma_cacc(f32x4& c, u32x4 a, u32x4 b) { asm volatile("v_mfma_f32_16x16x32_bf16 %0, %1, %2, %0" : "+a"(c) : "v"(a), "v"(b)); }
struct P1Src {
    rsrc_t r; unsigned v0, v1, so, step;
    DI void set(const Params& p, int u, int kh, int w, int lane) {
        const uchar* base; unsigned stride, col, bytes;
        if (u < 128) { base = (u & 2) ? p.wv : p.wk; stride = 8192; col = ((u >> 2) * 128 + (u & 1) * 64) * 2; bytes = 640 * 8192; }
        else if (u < 192) { base = p.wq; stride = 8192; col = (u - 128) * 128; bytes = 640 * 8192; }
        else { base = p.w1; stride = 3456; col = (u - 192) * 128; bytes = u < 219 ? 640 * 3456 : 0; }
        r = mk_rsrc(base, bytes);
        const int rl = lane >> 3, f0 = ((rl >> 1) & 1) | (((rl >> 2) & 1) << 1);
        v0 = rl * stride + (((lane & 7) ^ (2 * f0)) << 4); v1 = v0 ^ 64;
        so = (kh * 320 + 80 * w) * stride + col; step = 8 * stride;
    }
    DI void piece(int i, unsigned ldsw) const { dma16(r, (i & 1) ? v1 : v0, so + i * step, ldsw + i * 1024); }
};
DI float mish(float a, float bias) {
    const float h = a + bias;
    const float e = __builtin_amdgcn_exp2f(fminf(h, 30.f) * 1.44269504f);
    const float n = e * (e + 2.f);
    return h * n * __builtin_amdgcn_rcpf(n + 2.f);
}
#define PT(i)
constexpr int XA_ACC = 15;
template <bool ISV, int KH>
DI void p1_slab(LAS uchar* sb, const unsigned (&tb)[4], const u32x4 (&xa)[4][20], f32x4 (&acc)[4][4], const P1Src& src, unsigned ldsn) {
    u32x4 wf[2][4];
    auto rd = [&](int kk, int nt) { const u32x2 lo = trd(sb + tb[nt] + kk * 4096), hi = trd(sb + tb[nt ^ 2] + kk * 4096 + 512); return (u32x4){lo.x, lo.y, hi.x, hi.y}; };
#pragma unroll
    for (int nt = 0; nt < 4; ++nt) wf[0][nt] = rd(0, nt);
    SB();
#pragma unroll
    for (int kk = 0; kk < 10; ++kk) {
#pragma unroll
        for (int nt = 0; nt < 4; ++nt) {
            if (kk + 1 < 10) wf[(kk + 1) & 1][nt] = rd(kk + 1, nt);
            if (kk < 5 && (nt & 1)) src.piece(2 * kk + (nt >> 1), ldsn);
#pragma unroll
            for (int t = 0; t < 4; ++t) {
                const int ks = KH * 10 + kk;
                if (ISV) { if (ks < XA_ACC) mfma_av(acc[t][nt], xa[t][ks], wf[kk & 1][nt]); else mfma_v(acc[t][nt], xa[t][ks], wf[kk & 1][nt]); }
                else { if (ks < XA_ACC) mfma_va(acc[t][nt], wf[kk & 1][nt], xa[t][ks]); else mfma_v(acc[t][nt], wf[kk & 1][nt], xa[t][ks]); }
            }
            SB();
        }
    }
    asm volatile("s_nop 7" : "+v"(acc[3][3]), "+v"(acc[2][3]), "+v"(acc[1][3]), "+v"(acc[0][3]), "+v"(acc[3][2]), "+v"(acc[2][2]), "+v"(acc[1][2]), "+v"(acc[0][2]));
#pragma unroll
    for (int t = 0; t < 4; ++t) asm volatile("" : "+v"(acc[t][0]), "+v"(acc[t][1]));
}
template <bool ISV>
DI void p1_group(const Params& p, LAS uchar* lds, rsrc_t rw, unsigned lbase, const unsigned (&tb)[4], const u32x4 (&xa)[4][20], P1Src& src, int buf,
                 int chunk, int u, int un, bool first, int w, int lane, unsigned (&pt)[5]) {
    const int g = lane >> 4;
    f32x4 acc[4][4];
#pragma unroll
    for (int t = 0; t < 4; ++t)
#pragma unroll
        for (int nt = 0; nt < 4; ++nt) { acc[t][nt] = (f32x4){0.f, 0.f, 0.f, 0.f}; asm volatile("" : "+v"(acc[t][nt])); }
    PT(0);
    if (first) wait_vm<50>(); else wait_vm<10>();
    PT(1);
    bar();
    PT(2);
    src.set(p, un, 0, w, lane);
    {
        const unsigned ln = lbase + (buf == 0 ? 2 : buf - 1) * 40960;
        p1_slab<ISV, 0>(lds + buf * 40960, tb, xa, acc, src, ln);
    }
    buf = buf == 2 ? 0 : buf + 1;
    PT(3);
    wait_vm<10>();
    PT(1);
    bar();
    PT(2);
    src.set(p, un, 1, w, lane);
    {
        const unsigned ln = lbase + (buf == 0 ? 2 : buf - 1) * 40960;
        p1_slab<ISV, 1>(lds + buf * 40960, tb, xa, acc, src, ln);
    }
    PT(3);
    const int tt0 = chunk * 16 + w * 4;
    if (ISV) {
        const int h = u >> 2;
#pragma unroll
        for (int sp = 0; sp < 2; ++sp)
#pragma unroll
            for (int nt = 0; nt < 4; ++nt) {
                const f32x4 a = acc[2 * sp][nt], b = acc[2 * sp + 1][nt];
                const u32x4 o = {pk2(a[0], a[1]), pk2(a[2], a[3]), pk2(b[0], b[1]), pk2(b[2], b[3])};
                st16(o, rw, lane * 16, OFF_VF + (((tt0 >> 1) + sp) * 32 + h) * 8192 + ((u & 1) * 4 + nt) * 1024);
            }
    } else if (u < 192) {
        const int h = u < 128 ? u >> 2 : (u - 128) >> 1;
        const int ob = u < 128 ? OFF_KF : OFF_QF;
#pragma unroll
        for (int t = 0; t < 4; ++t)
#pragma unroll
            for (int m = 0; m < 2; ++m) {
                const f32x4 a = acc[t][2 * m], b = acc[t][2 * m + 1];
                const u32x4 o = {pk2(a[0], a[1]), pk2(a[2], a[3]), pk2(b[0], b[1]), pk2(b[2], b[3])};
                st16(o, rw, lane * 16, ob + (((tt0 + t) >> 1) * 32 + h) * 8192 + ((t & 1) * 4 + (u & 1) * 2 + m) * 1024);
            }
    } else {
        const int fg = u - 192;
        u32x2 bv[4];
#pragma unroll
        for (int nt = 0; nt < 4; ++nt) bv[nt] = *(const u32x2*)(p.b1 + (fg * 64 + nt * 16 + 4 * g) * 2);
#pragma unroll
        for (int t = 0; t < 4; ++t)
#pragma unroll
            for (int m = 0; m < 2; ++m) {
                const f32x4 a = acc[t][2 * m], b = acc[t][2 * m + 1];
                const u32x2 ba = bv[2 * m], bb = bv[2 * m + 1];
                const u32x4 o = {pk2(mish(a[0], bf_lo(ba.x)), mish(a[1], bf_hi(ba.x))), pk2(mish(a[2], bf_lo(ba.y)), mish(a[3], bf_hi(ba.y))),
                                 pk2(mish(b[0], bf_lo(bb.x)), mish(b[1], bf_hi(bb.x))), pk2(mish(b[2], bf_lo(bb.y)), mish(b[3], bf_hi(bb.y)))};
                st16(o, rw, lane * 16, OFF_ACT + ((tt0 + t) * 54 + fg * 2 + m) * 1024);
                SB();
            }
    }
}
DI void ld16_a(u32x4& d, rsrc_t r, unsigned v, unsigned s) { asm volatile("buffer_load_dwordx4 %0, %1, %2, %3 offen sc1" : "=a"(d) : "v"(v), "s"(r), "s"(s) : "memory"); }
DI void ld16_v(u32x4& d, rsrc_t r, unsigned v, unsigned s) { asm volatile("buffer_load_dwordx4 %0, %1, %2, %3 offen sc1" : "=v"(d) : "v"(v), "s"(r), "s"(s) : "memory"); }
DI void ld16_s(u32x4& d, rsrc_t r, unsigned v, unsigned s) { asm volatile("buffer_load_dwordx4 %0, %1, %2, %3 offen sc1" : "=v"(d) : "v"(v), "s"(r), "s"(s) : "memory"); }
DI void p1_task(const Params& p, LAS uchar* lds, int chunk, int u0, int nu) {
    const int lane = fresh_lane(), w = __builtin_amdgcn_readfirstlane(threadIdx.x >> 6), c = lane & 15, g = lane >> 4;
    const rsrc_t rw = mk_rsrc(p.ws, WS_USED);
    const unsigned lbase = (unsigned)(size_t)lds + w * 10240;
    P1Src src;
    u32x4 xa[4][20];
    src.set(p, u0, 0, w, lane);
    if (w == 0) { wait_ctr(p, rw, C_X + chunk * 64, 8); wait_ctr(p, rw, C_X + chunk * 64 + 32, 8); }
    bar();
#pragma unroll
    for (int i = 0; i < 10; ++i) src.piece(i, lbase);
#pragma unroll
    for (int ks = 0; ks < 20; ++ks) {
        if (ks == 10) {
            src.set(p, u0, 1, w, lane);
#pragma unroll
            for (int i = 0; i < 10; ++i) src.piece(i, lbase + 40960);
        }
#pragma unroll
        for (int t = 0; t < 4; ++t) {
            const unsigned so = OFF_XF + ((chunk * 16 + w * 4 + t) * 20 + ks) * 1024;
            if (ks < XA_ACC) ld16_a(xa[t][ks], rw, lane * 16, so); else ld16_v(xa[t][ks], rw, lane * 16, so);
        }
        SB();
    }
    const int q = c >> 2, pp = c & 3, sw = (q >> 1) | ((g & 1) << 1);
    unsigned tb[4];
#pragma unroll
    for (int nt = 0; nt < 4; ++nt) tb[nt] = (8 * g + q) * 128 + 32 * (nt ^ sw) + 16 * (pp >> 1) + 8 * (pp & 1);
    int buf = 0;
    unsigned pt[5] = {0, 0, 0, 0, mt32()};
    for (int gi = 0; gi < nu; ++gi) {
        const int u = u0 + gi, un = gi + 1 < nu ? u + 1 : 219;
        if (u < 128 && (u & 2)) p1_group<true>(p, lds, rw, lbase, tb, xa, src, buf, chunk, u, un, gi == 0, w, lane, pt);
        else p1_group<false>(p, lds, rw, lbase, tb, xa, src, buf, chunk, u, un, gi == 0, w, lane, pt);
        buf = buf == 0 ? 2 : buf - 1;
    }
    wait_vm<0>();
    bar();
    const unsigned tid = fresh_tid();
    if (tid < (unsigned)nu) {
        const int u = u0 + tid;
        add_ctr(p, u < 128 ? C_KV + (chunk * 32 + (u >> 2)) * 16 : (u < 192 ? C_Q + (chunk * 32 + ((u - 128) >> 1)) * 16 : C_ACT + chunk * 32), 1u);
    }
}

#define KST(i)
#define AST_(i)
template <bool ISV>
DI void kv0_task(const Params& p, LAS uchar* lds, int u) {
    const int lane = fresh_lane(), w = __builtin_amdgcn_readfirstlane(threadIdx.x >> 6), c = lane & 15, g = lane >> 4;
    const rsrc_t rw = mk_rsrc(p.ws, WS_USED);
    const unsigned lbase = (unsigned)(size_t)lds + w * 10240;
    P1Src src;
    u32x4 xa[2][20];
    auto fetch = [&](int half) {
#pragma unroll
        for (int ks = 0; ks < 20; ++ks)
#pragma unroll
            for (int t = 0; t < 2; ++t) ld16_v(xa[t][ks], rw, lane * 16, OFF_XF + ((half * 8 + w * 2 + t) * 20 + ks) * 1024);
    };
#pragma unroll
    for (int kh = 0; kh < 2; ++kh) {
        src.set(p, u, kh, w, lane);
#pragma unroll
        for (int i = 0; i < 10; ++i) src.piece(i, lbase + kh * 40960);
    }
    KST(8);
    KST(9);
    if (w == 0) wait_ctr(p, rw, C_X, 8);
    bar();
    KST(10);
    fetch(0);
    const int q = c >> 2, pp = c & 3, sw = (q >> 1) | ((g & 1) << 1), h = u >> 2;
    unsigned tb[4];
#pragma unroll
    for (int nt = 0; nt < 4; ++nt) tb[nt] = (8 * g + q) * 128 + 32 * (nt ^ sw) + 16 * (pp >> 1) + 8 * (pp & 1);
    auto rd = [&](int i) {
        const int ks = i >> 2, nt = i & 3; LAS uchar* sb = lds + (ks / 10) * 40960 + (ks % 10) * 4096;
        const u32x2 lo = trd(sb + tb[nt]), hi = trd(sb + tb[nt ^ 2] + 512); return (u32x4){lo.x, lo.y, hi.x, hi.y};
    };
    for (int half = 0; half < 2; ++half) {
        f32x4 acc[2][4];
#pragma unroll
        for (int i = 0; i < 8; ++i) { acc[i >> 2][i & 3] = (f32x4){0.f, 0.f, 0.f, 0.f}; asm volatile("" : "+v"(acc[i >> 2][i & 3])); }
        KST(half * 4 + 0);
        wait_vm<0>();
#pragma unroll
        for (int ks = 0; ks < 20; ++ks) asm volatile("" : "+v"(xa[0][ks]), "+v"(xa[1][ks]));
        bar();
        KST(half * 4 + 1);
        u32x4 wf[8];
#pragma unroll
        for (int i = 0; i < 8; ++i) wf[i] = rd(i);
        SB();
#pragma unroll
        for (int i = 0; i < 80; ++i) {
#pragma unroll
            for (int t = 0; t < 2; ++t) { if (ISV) mfma_v(acc[t][i & 3], xa[t][i >> 2], wf[i & 7]); else mfma_v(acc[t][i & 3], wf[i & 7], xa[t][i >> 2]); }
            if (i + 8 < 80) wf[i & 7] = rd(i + 8);
            SB();
        }
        asm volatile("s_nop 7" : "+v"(acc[0][0]), "+v"(acc[0][1]), "+v"(acc[0][2]), "+v"(acc[0][3]), "+v"(acc[1][0]), "+v"(acc[1][1]), "+v"(acc[1][2]), "+v"(acc[1][3]));
        KST(half * 4 + 2);
        const unsigned ob = (ISV ? OFF_VF : OFF_KF) + ((half * 4 + w) * 32 + h) * 8192;
#pragma unroll
        for (int i = 0; i < 4; ++i) {
            const f32x4 a = ISV ? acc[0][i] : acc[i >> 1][2 * (i & 1)], b = ISV ? acc[1][i] : acc[i >> 1][2 * (i & 1) + 1];
            const u32x4 o = {pk2(a[0], a[1]), pk2(a[2], a[3]), pk2(b[0], b[1]), pk2(b[2], b[3])};
            st16(o, rw, lane * 16, ob + (ISV ? (u & 1) * 4 + i : (i >> 1) * 4 + (u & 1) * 2 + (i & 1)) * 1024);
        }
        wait_vm<0>();
        bar();
        KST(half * 4 + 3);
        if (fresh_tid() == 0) add_ctr(p, (half ? C_KV : C_KVH) + h * 16, 1u);
        if (w == 0) wait_ctr(p, rw, C_X + 32, 8);
        bar();
        fetch(1);
    }
    wait_vm<0>();
#pragma unroll
    for (int ks = 0; ks < 20; ++ks) asm volatile("" :: "v"(xa[0][ks]), "v"(xa[1][ks]));
}

template <bool WO>
DI void gemm_task(const Params& p, LAS uchar* lds, int chunk, int cg) {
    constexpr int KS = WO ? 4 : 3, NS = WO ? 32 : 18, NP = 2 * KS, IMG = KS * 4096, NKF = KS * NS, XOFF = WO ? OFF_OT : OFF_ACT;
    const int lane = fresh_lane(), w = __builtin_amdgcn_readfirstlane(threadIdx.x >> 6), c = lane & 15, g = lane >> 4;
    const rsrc_t rw = mk_rsrc(p.ws, WS_USED), r2 = mk_rsrc(WO ? p.wo : p.w2, NKF * 32 * 1280);
    const unsigned lbase = (unsigned)(size_t)lds + (w >> 1) * IMG + (w & 1) * NP * 1024;
    const int rl = lane >> 3, f0 = ((rl >> 1) & 1) | (((rl >> 2) & 1) << 1);
    const unsigned v0 = rl * 1280 + (((lane & 7) ^ (2 * f0)) << 4), v1 = v0 ^ 64;
    const unsigned wbase = (w & 1) * NP * 8 * 1280 + cg * 256 + (w >> 1) * 128;
    auto piece = [&](int sl, int buf, int i) { dma16(r2, (i & 1) ? v1 : v0, wbase + (sl * KS * 32 + 8 * i) * 1280, lbase + buf * 2 * IMG + i * 1024); };
    const int tt0 = chunk * 16 + w * 4;
    u32x4 xb[2][KS][4];
    auto frag = [&](int par, int sl, int i) { ld16_s(xb[par][i >> 2][i & 3], rw, lane * 16, XOFF + ((tt0 + (i & 3)) * NKF + sl * KS + (i >> 2)) * 1024); };
#pragma unroll
    for (int i = 0; i < NP; ++i) piece(0, 0, i);
    if (w == 0) {
        if (WO) {
            wait_ctr(p, rw, C_G + (chunk * 5 + cg) * 32, 1);
            for (int i = 0; i < 8; ++i) wait_ctr(p, rw, C_OT + (chunk * 8 + i) * 32, 32);
        } else wait_ctr(p, rw, C_ACT + chunk * 32, 27);
    }
    bar();
#pragma unroll
    for (int i = 0; i < 4 * KS; ++i) frag(0, 0, i);
#pragma unroll
    for (int i = 0; i < NP; ++i) piece(1, 1, i);
    const int q = c >> 2, pp = c & 3, sw = (q >> 1) | (((g & 1) ^ (g >> 1)) << 1);
    unsigned tb[4];
#pragma unroll
    for (int nt = 0; nt < 4; ++nt) tb[nt] = (4 * g + q) * 128 + 32 * (nt ^ sw) + 16 * (pp >> 1) + 8 * (pp & 1);
    f32x4 acc[4][8];
#pragma unroll
    for (int t = 0; t < 4; ++t)
#pragma unroll
        for (int nt = 0; nt < 8; ++nt) { acc[t][nt] = (f32x4){0.f, 0.f, 0.f, 0.f}; asm volatile("" : "+a"(acc[t][nt])); }
    int buf = 0;
    for (int s2 = 0; s2 < NS / 2; ++s2) {
#pragma unroll
        for (int par = 0; par < 2; ++par) {
            const int sl = 2 * s2 + par;
            wait_vm<NP>();
#pragma unroll
            for (int kk = 0; kk < KS; ++kk) asm volatile("" : "+v"(xb[par][kk][0]), "+v"(xb[par][kk][1]), "+v"(xb[par][kk][2]), "+v"(xb[par][kk][3]));
            bar();
            const int sn = min(sl + 1, NS - 1), sp = min(sl + 2, NS - 1), bn = buf == 0 ? 2 : buf - 1;
            LAS uchar* sb = lds + buf * 2 * IMG;
            auto rd = [&](int i) { LAS uchar* a = sb + ((i >> 2) & 1) * IMG + tb[i & 3] + (i >> 3) * 4096; const u32x2 lo = trd(a), hi = trd(a + 2048); return (u32x4){lo.x, lo.y, hi.x, hi.y}; };
            u32x4 wf[4];
#pragma unroll
            for (int i = 0; i < 4; ++i) wf[i] = rd(i);
            SB();
#pragma unroll
            for (int i = 0; i < 8 * KS; ++i) {
                const u32x4 cur = wf[i & 3];
                mfma_cacc(acc[0][i & 7], cur, xb[par][i >> 3][0]);
                if (i < 4 * KS) frag(par ^ 1, sn, i); else if (i < 4 * KS + NP) piece(sp, bn, i - 4 * KS);
#pragma unroll
                for (int t = 1; t < 4; ++t) mfma_cacc(acc[t][i & 7], cur, xb[par][i >> 3][t]);
                if (i + 4 < 8 * KS) wf[i & 3] = rd(i + 4);
                SB();
            }
            if (par) asm volatile("s_nop 7");
            SB();
            buf = buf == 2 ? 0 : buf + 1;
        }
    }
    wait_vm<0>();
#pragma unroll
    for (int kk = 0; kk < KS; ++kk) asm volatile("" :: "v"(xb[0][kk][0]), "v"(xb[0][kk][1]), "v"(xb[0][kk][2]), "v"(xb[0][kk][3]));
    SB();
#pragma unroll
    for (int t = 0; t < 4; ++t) {
        if (WO) {
            const size_t xo = (size_t)((tt0 + t) * 16 + c) * 1280 + cg * 256 + g * 8;
            u32x4 gv[4]; u32x2 xv[8];
#pragma unroll
            for (int m = 0; m < 4; ++m) gv[m] = ld16(rw, lane * 16, OFF_GF + ((tt0 + t) * 20 + cg * 4 + m) * 1024);
#pragma unroll
            for (int nt = 0; nt < 8; ++nt) xv[nt] = *(const u32x2*)(p.x + xo + nt * 32);
#pragma unroll
            for (int nt = 0; nt < 8; ++nt) {
                const f32x4 a = acc[t][nt];
                const unsigned g0 = gv[nt >> 1][2 * (nt & 1)], g1 = gv[nt >> 1][2 * (nt & 1) + 1], s0 = pk2(a[0], a[1]), s1 = pk2(a[2], a[3]);
                const unsigned m0 = pk2(bf_lo(s0) * bf_lo(g0), bf_hi(s0) * bf_hi(g0)), m1 = pk2(bf_lo(s1) * bf_lo(g1), bf_hi(s1) * bf_hi(g1));
                *(u32x2*)(p.out + xo + nt * 32) = (u32x2){pk2(bf_lo(xv[nt].x) + bf_lo(m0), bf_hi(xv[nt].x) + bf_hi(m0)), pk2(bf_lo(xv[nt].y) + bf_lo(m1), bf_hi(xv[nt].y) + bf_hi(m1))};
            }
        } else {
#pragma unroll
            for (int m = 0; m < 4; ++m) {
                float y[8];
#pragma unroll
                for (int r = 0; r < 8; ++r) { const float a = rbf(acc[t][2 * m + (r >> 2)][r & 3]); y[r] = a * __builtin_amdgcn_rcpf(1.f + __builtin_amdgcn_exp2f(-a * 1.44269504f)); }
                const u32x4 o = {pk2(y[0], y[1]), pk2(y[2], y[3]), pk2(y[4], y[5]), pk2(y[6], y[7])};
                st16(o, rw, lane * 16, OFF_GF + ((tt0 + t) * 20 + cg * 4 + m) * 1024);
            }
        }
        SB();
    }
    wait_vm<0>();
    bar();
    if (!WO && fresh_tid() == 0) add_ctr(p, C_G + (chunk * 5 + cg) * 32, 1u);
}

#define XST(cond, idx)
constexpr float SCL = 0.08838834764831845f * 1.4426950408889634f;
DI void attn_task(const Params& p, int s, int h, bool new_chunk) {
    const int lane = fresh_lane(), c = lane & 15, g = lane >> 4;
    const rsrc_t rw = mk_rsrc(p.ws, WS_USED);
    const int sh = (s * 32 + h);
    u32x4 qf[2][4], kf[2][4], vf[8], km[4]; u32x2 vm[8];
    if (new_chunk) {
        wait_ctr(p, rw, C_Q + ((s >> 3) * 32 + h) * 16, 2);
        wait_ctr(p, rw, C_KV + ((s >> 3) * 32 + h) * 16, 4);
    }
#pragma unroll
    for (int i = 0; i < 8; ++i) qf[i >> 2][i & 3] = ld16(rw, lane * 16, OFF_QF + sh * 8192 + i * 1024);
#pragma unroll
    for (int i = 0; i < 8; ++i) kf[i >> 2][i & 3] = ld16(rw, lane * 16, OFF_KF + sh * 8192 + i * 1024);
#pragma unroll
    for (int i = 0; i < 8; ++i) vf[i] = ld16(rw, lane * 16, OFF_VF + sh * 8192 + i * 1024);
    const float NINF = -__builtin_inff();
    const f32x4 zero = {0.f, 0.f, 0.f, 0.f};
    float t[2][12], m[2];
    {
        f32x4 s0[2] = {zero, zero}, s1 = zero;
#pragma unroll
        for (int ks = 0; ks < 4; ++ks) { s0[0] = mfma32(kf[0][ks], qf[0][ks], s0[0]); s0[1] = mfma32(kf[0][ks], qf[1][ks], s0[1]); s1 = mfma32(kf[1][ks], qf[1][ks], s1); }
#pragma unroll
        for (int r = 0; r < 4; ++r) {
            const bool diag = 4 * g + r <= c;
            t[0][r] = diag ? s0[0][r] * SCL : NINF; t[1][r] = s0[1][r] * SCL; t[1][4 + r] = diag ? s1[r] * SCL : NINF;
        }
        m[0] = fmaxf(fmaxf(t[0][0], t[0][1]), fmaxf(t[0][2], t[0][3]));
        m[1] = fmaxf(fmaxf(fmaxf(t[1][0], t[1][1]), fmaxf(t[1][2], t[1][3])), fmaxf(fmaxf(t[1][4], t[1][5]), fmaxf(t[1][6], t[1][7])));
#pragma unroll
        for (int r = 0; r < 4; ++r) asm volatile("" : "+v"(t[0][r]), "+v"(t[1][r]), "+v"(t[1][4 + r]));
        asm volatile("" : "+v"(m[0]), "+v"(m[1]));
#pragma unroll
        for (int i = 0; i < 8; ++i) asm volatile("" : "+v"(vf[i]));
    }
    u32x4 one4 = {0x3F803F80u, 0x3F803F80u, 0x3F803F80u, 0x3F803F80u};
    const float z2 = g >= 2 ? 0.f : NINF;
    f32x4 s2[2] = {{z2, z2, z2, z2}, {z2, z2, z2, z2}};
    asm volatile("" : "+v"(s2[0]), "+v"(s2[1]));
    XST(s == 63, 64 + h * 4);
    wait_ctr(p, rw, C_KM + h * 32 + (s == NSEG - 1), s + 1);
    XST(s == 63, 64 + h * 4 + 1);
    for (unsigned n = SPIN_CAP;;) {
        asm volatile("" ::: "memory");
#pragma unroll
        for (int i = 0; i < 4; ++i) km[i] = ld16(rw, (g * 8 + (c & 7)) * 16, OFF_KM + sh * 4096 + i * 512);
#pragma unroll
        for (int i = 0; i < 8; ++i) vm[i] = ld8(rw, (lane & 31) * 8, OFF_KM + sh * 4096 + 2048 + i * 256);
        if (s < NSEG - 1) break;
        unsigned mx = 0;
#pragma unroll
        for (int i = 0; i < 4; ++i) mx = mx4(mx, km[i]);
#pragma unroll
        for (int i = 0; i < 8; ++i) mx = max(mx, max(vm[i].x, vm[i].y));
        if (__builtin_amdgcn_ballot_w64(mx == SENT) == 0) break;
        if (--n == 0) { set_tmo(p); break; }
    }
    SB();
    SB();
#pragma unroll
    for (int ks = 0; ks < 4; ++ks)
#pragma unroll
        for (int qt = 0; qt < 2; ++qt) mfma_v(s2[qt], km[ks], qf[qt][ks]);
    asm volatile("s_nop 7" : "+v"(s2[0]), "+v"(s2[1]));
    u32x4 p01[2], p2[2];
#pragma unroll
    for (int qt = 0; qt < 2; ++qt) {
#pragma unroll
        for (int r = 0; r < 4; ++r) t[qt][8 + r] = s2[qt][r] * SCL;
        m[qt] = fmaxf(fmaxf(m[qt], fmaxf(t[qt][8], t[qt][9])), fmaxf(t[qt][10], t[qt][11]));
    }
#pragma unroll
    for (int qt = 0; qt < 2; ++qt) m[qt] = grp_max(m[qt]);
#pragma unroll
    for (int qt = 0; qt < 2; ++qt) {
#pragma unroll
        for (int r = 0; r < 12; ++r) if (qt == 1 || r < 4 || r >= 8) t[qt][r] = __builtin_amdgcn_exp2f(t[qt][r] - m[qt]);
        p01[qt] = (u32x4){pk2(t[qt][0], t[qt][1]), pk2(t[qt][2], t[qt][3]), qt ? pk2(t[qt][4], t[qt][5]) : 0u, qt ? pk2(t[qt][6], t[qt][7]) : 0u};
        p2[qt] = (u32x4){pk2(t[qt][8], t[qt][9]), pk2(t[qt][10], t[qt][11]), 0u, 0u};
    }
    u32x4 vq[4];
#pragma unroll
    for (int i = 0; i < 4; ++i) vq[i] = (u32x4){vm[2 * i].x, vm[2 * i].y, vm[2 * i + 1].x, vm[2 * i + 1].y};
    u32x4 p2h[2] = {{0u, 0u, p2[0].x, p2[0].y}, {0u, 0u, p2[1].x, p2[1].y}};
    asm volatile("s_nop 1" : "+v"(p01[0]), "+v"(p01[1]), "+v"(p2[0]), "+v"(p2[1]), "+v"(p2h[0]), "+v"(p2h[1]), "+v"(vq[0]), "+v"(vq[1]), "+v"(vq[2]), "+v"(vq[3]), "+v"(one4));
    f32x4 l[2], o[2][8];
#pragma unroll
    for (int qt = 0; qt < 2; ++qt) mfma_0(l[qt], one4, p01[qt]);
#pragma unroll
    for (int qt = 0; qt < 2; ++qt) mfma_v(l[qt], one4, p2[qt]);
#pragma unroll
    for (int et = 0; et < 8; ++et)
#pragma unroll
        for (int qt = 0; qt < 2; ++qt) { mfma_0(o[qt][et], vf[et], p01[qt]); mfma_v(o[qt][et], vq[et >> 1], et & 1 ? p2h[qt] : p2[qt]); }
    asm volatile("s_nop 3" : "+v"(l[0]), "+v"(l[1]), "+v"(o[0][0]), "+v"(o[0][1]), "+v"(o[1][0]), "+v"(o[1][1]));
    const float inv[2] = {__builtin_amdgcn_rcpf(l[0][0]), __builtin_amdgcn_rcpf(l[1][0])};
#pragma unroll
    for (int tp = 0; tp < 4; ++tp) {
        asm volatile("" : "+v"(o[0][2 * tp]), "+v"(o[0][2 * tp + 1]), "+v"(o[1][2 * tp]), "+v"(o[1][2 * tp + 1]));
#pragma unroll
        for (int qt = 0; qt < 2; ++qt) {
            const f32x4 a = o[qt][2 * tp] * inv[qt], b = o[qt][2 * tp + 1] * inv[qt];
            const u32x4 ov = {nosent(pk2(a[0], a[1])), nosent(pk2(a[2], a[3])), nosent(pk2(b[0], b[1])), nosent(pk2(b[2], b[3]))};
            st16(ov, rw, lane * 16, OFF_OT + ((2 * s + qt) * 32 + h) * 4096 + tp * 1024);
        }
        SB();
    }
    if (s == NSEG - 1) st4(1u, rw, lane == 0 ? (C_OF + (h >> 3) * 32 + (h & 7)) * 4 : OOB, 0);
    XST(s == 63, 64 + h * 4 + 2);
    wait_vm<0>();
    XST(s == 63, 64 + h * 4 + 3);
    if (lane == 0) add_ctr(p, C_OT + s * 32, 1u);
}

constexpr int N_T = 24, N_WG = 4, W_FIRST = 64;
DI void wo_stage(const Params& p, unsigned ldsw, int h0, int cs, int lane) {
    const rsrc_t ro = mk_rsrc(p.wo, 4096 * 1280);
    const unsigned v = (lane >> 2) * 1280 + (lane & 3) * 16;
#pragma unroll
    for (int i = 0; i < 16; ++i) dma16(ro, v, (h0 * 128 + 16 * i) * 1280 + cs * 64, ldsw + i * 1024);
}
DI void w_role(const Params& p, LAS uchar* lds, int cs, int rank) {
    const int lane = fresh_lane(), w = __builtin_amdgcn_readfirstlane(threadIdx.x >> 6), c = lane & 15, g = lane >> 4;
    const rsrc_t rw = mk_rsrc(p.ws, WS_USED);
    u32x4 wf[8][4][2];
    {
        LAS uchar* lw = lds + w * 32768;
        const int q = c >> 2, pp = c & 3;
        wo_stage(p, (unsigned)(size_t)lw, 8 * w, cs, lane);
        wo_stage(p, (unsigned)(size_t)lw + 16384, 8 * w + 2, cs, lane);
#pragma unroll
        for (int rd = 0; rd < 4; ++rd) {
            if (rd < 3) wait_vm<16>(); else wait_vm<0>();
#pragma unroll
            for (int i = 0; i < 2; ++i)
#pragma unroll
                for (int tp = 0; tp < 4; ++tp)
#pragma unroll
                    for (int nt = 0; nt < 2; ++nt) {
                        LAS uchar* a = lw + (rd & 1) * 16384 + i * 8192 + (32 * tp + 4 * g + q) * 64 + nt * 32 + pp * 8;
                        const u32x2 lo = trd(a), hi = trd(a + 1024);
                        wf[2 * rd + i][tp][nt] = (u32x4){lo.x, lo.y, hi.x, hi.y};
                        if (2 * rd + i < 7) asm volatile("" : "+a"(wf[2 * rd + i][tp][nt])); else asm volatile("" : "+v"(wf[2 * rd + i][tp][nt]));
                    }
            asm volatile("s_waitcnt lgkmcnt(0)" ::: "memory");
            SB();
            if (rd < 2) wo_stage(p, (unsigned)(size_t)lw + (rd & 1) * 16384, 8 * w + 2 * rd + 4, cs, lane);
        }
    }
    bar();
    LAS f32x2* red = (LAS f32x2*)lds;
    volatile LAS int* nxt = (volatile LAS int*)(lds + 16384);
    const rsrc_t rx = mk_rsrc(p.x, S_ * 1280), ro = mk_rsrc(p.out, S_ * 1280);
    unsigned tk = 0;
    const unsigned wto = lane == 0 ? (C_WT + cs * 32) * 4 : OOB;
    auto draw = [&]() { tk = 1; asm volatile("buffer_atomic_add %0, %1, %2, 0 offen sc0" : "+v"(tk) : "v"(wto), "s"(rw) : "memory"); };
    auto tile_of = [&](int ticket, int cur) { return ticket + W_FIRST < 120 ? ticket + W_FIRST : (cur < 120 ? 120 + rank : (cur < 124 ? (rank < 2 ? 126 : 122) + rank : 128)); };
    u32x4 ob[8][4];
    int tt = 0;
    bool have = false, careful = false;
    if (w == 0) { draw(); asm volatile("s_waitcnt vmcnt(0)" : "+v"(tk)); *nxt = tile_of(__builtin_amdgcn_readfirstlane(tk), 0); draw(); }
    bar();
    tt = __builtin_amdgcn_readfirstlane(*nxt);
    while (tt < 2 * NSEG) {
        if (!have) {
            if (w == 0) { wait_ctr(p, rw, C_G + ((tt >> 4) * 5 + (cs >> 2)) * 32, 1); if (tt < 126 || careful) wait_ctr(p, rw, C_OT + (tt >> 1) * 32, 32); }
            bar();
            if (tt >= 126 && !careful) {
                for (unsigned n = SPIN_CAP;;) {
                    asm volatile("" ::: "memory");
                    if (__builtin_amdgcn_ballot_w64(ld4(rw, (lane & 7) * 4, (C_OF + w * 32) * 4) == 0) == 0) break;
                    if (--n == 0) { set_tmo(p); break; }
                }
            }
            XST(w == 0 && tt >= 126, 256 + ((tt - 126) * 20 + cs) * 4);
#pragma unroll
            for (int i = 0; i < 32; ++i) ld16_s(ob[i >> 2][i & 3], rw, lane * 16, OFF_OT + (tt * 32 + 8 * w) * 4096 + i * 1024);
        }
        f32x4 acc[2][2];
#pragma unroll
        for (int i = 0; i < 4; ++i) { acc[i >> 1][i & 1] = (f32x4){0.f, 0.f, 0.f, 0.f}; asm volatile("" : "+v"(acc[i >> 1][i & 1])); }
        const unsigned xo = c * 1280 + g * 8, xs = tt * 16 * 1280 + cs * 64 + (w >> 1) * 32 + (w & 1) * 4;
        unsigned xv, gv;
        asm volatile("buffer_load_dword %0, %1, %2, %3 offen" : "=v"(xv) : "v"(xo), "s"(rx), "s"(xs) : "memory");
        asm volatile("buffer_load_dword %0, %1, %2, %3 offen sc1" : "=v"(gv) : "v"(lane * 16), "s"(rw), "s"(OFF_GF + (tt * 20 + cs) * 1024 + w * 4) : "memory");
        if (w == 0) {
            asm volatile("s_waitcnt vmcnt(32)" : "+v"(tk));
            const int tn = tile_of(__builtin_amdgcn_readfirstlane(tk), tt);
            int rd = 0;
            if (tn < 126) {
                const unsigned f = ld4(rw, (lane == 0 ? C_G + ((tn >> 4) * 5 + (cs >> 2)) * 32 : C_OT + (tn >> 1) * 32) * 4, 0);
                rd = __builtin_amdgcn_ballot_w64(f < (lane == 0 ? 1u : 32u)) == 0 ? 0x10000 : 0;
            }
            nxt[1] = tn | rd;
        }
        bar();
        XST(w == 0 && tt >= 126, 256 + ((tt - 126) * 20 + cs) * 4 + 1);
        const int nx = __builtin_amdgcn_readfirstlane(nxt[1]);
        have = (nx >> 16) != 0;
        const rsrc_t rn = mk_rsrc(p.ws, have ? WS_USED : 0);
        const unsigned nb = OFF_OT + ((nx & 127) * 32 + 8 * w) * 4096;
        if (w == 0) draw();
#pragma unroll
        for (int i = 0; i < 8; ++i) {
            asm volatile("s_waitcnt vmcnt(28)" : "+v"(ob[i][0]), "+v"(ob[i][1]), "+v"(ob[i][2]), "+v"(ob[i][3]));
#pragma unroll
            for (int tp = 0; tp < 4; ++tp)
#pragma unroll
                for (int n = 0; n < 2; ++n) { if (i < 7) mfma_av(acc[tp & 1][n], wf[i][tp][n], ob[i][tp]); else mfma_v(acc[tp & 1][n], wf[i][tp][n], ob[i][tp]); }
#pragma unroll
            for (int tp = 0; tp < 4; ++tp) ld16_s(ob[i][tp], rn, lane * 16, nb + (i * 4 + tp) * 1024);
            SB();
        }
        mfma_pad(acc[0][0], acc[0][1], acc[1][0], acc[1][1]);
#pragma unroll
        for (int n = 0; n < 2; ++n) {
            const f32x4 a = acc[0][n] + acc[1][n];
            red[((2 * n) * 4 + w) * 64 + lane] = (f32x2){a[0], a[1]};
            red[((2 * n + 1) * 4 + w) * 64 + lane] = (f32x2){a[2], a[3]};
        }
        bar();
        const f32x2 r = (red[(w * 4 + 0) * 64 + lane] + red[(w * 4 + 1) * 64 + lane]) + (red[(w * 4 + 2) * 64 + lane] + red[(w * 4 + 3) * 64 + lane]);
        asm volatile("s_waitcnt vmcnt(32)" : "+v"(xv), "+v"(gv));
        const bool redo = tt >= 126 && !careful && __builtin_amdgcn_ballot_w64(r[0] != r[0]) != 0;
        const unsigned sv = pk2(r[0], r[1]), m = pk2(bf_lo(sv) * bf_lo(gv), bf_hi(sv) * bf_hi(gv));
        __builtin_amdgcn_raw_buffer_store_b32(pk2(bf_lo(xv) + bf_lo(m), bf_hi(xv) + bf_hi(m)), ro, xo, xs, 0);
        XST(w == 0 && tt >= 126, 256 + ((tt - 126) * 20 + cs) * 4 + 2);
        if (!have) {
            wait_vm<0>();
#pragma unroll
            for (int i = 0; i < 8; ++i) asm volatile("" : "+v"(ob[i][0]), "+v"(ob[i][1]), "+v"(ob[i][2]), "+v"(ob[i][3]));
        }
        careful = redo;
        if (!redo) tt = nx & 0xFFFF;
    }
}

template <int V> struct IC { static constexpr int value = V; };
template <int N, int I = 0, class F> DI void static_for(F&& f) { if constexpr (I < N) { f(IC<I>{}); static_for<N, I + 1>(f); } }
DI void smf_x(f32x4& c, u32x4 a, u32x8 b, int idx, bool acc) { if (acc) smf_a(c, a, b, idx); else smf_v(c, a, b, idx); }
#define TRY()
#define V_A 80
#define V_M 52
#define KM_DELAY 240
#define PAD0 0
#define PAD_UP 0
#define PAD_MAX 48
#define RISE 64
#define DECAY 8
#ifndef M_DELAY
#define M_DELAY 200
#endif
#define L_SMALL 96
#define F_SMALL 280
#ifndef M_TAIL
#define M_TAIL 40
#endif
#define D_MST 300
#define F_LATE 1000
#define V_LATE 8
#define DUR_A0 1060
#define DUR_M0 760
#define FT(i)
#define FDUMP(blk, s, w)
#define STAT(blk, s, w, a, b, c, d)
DI int wave_max(int d) {
    d = max(d, __builtin_amdgcn_update_dpp(0, d, 0x121, 0xf, 0xf, true));
    d = max(d, __builtin_amdgcn_update_dpp(0, d, 0x122, 0xf, 0xf, true));
    d = max(d, __builtin_amdgcn_update_dpp(0, d, 0x124, 0xf, 0xf, true));
    d = max(d, __builtin_amdgcn_update_dpp(0, d, 0x128, 0xf, 0xf, true));
    auto r = __builtin_amdgcn_permlane32_swap((unsigned)d, (unsigned)d, false, false); d = max((int)r[0], (int)r[1]);
    auto q = __builtin_amdgcn_permlane16_swap((unsigned)d, (unsigned)d, false, false); d = max((int)q[0], (int)q[1]);
    return __builtin_amdgcn_readfirstlane(d);
}
struct Clock {
    unsigned r0, m0, cq, qc;
    DI void edge(unsigned& r, unsigned& m) {
        unsigned long long a, b;
        const unsigned first = rt32();
        do asm volatile("s_memrealtime %0\n\ts_memtime %1\n\ts_waitcnt lgkmcnt(0)" : "=s"(a), "=s"(b) :: "memory"); while ((unsigned)a == first);
        r = (unsigned)a << 3; m = (unsigned)b - 24;
    }
    DI void start() { edge(r0, m0); cq = 3000; qc = 22370; }
    DI void sync() {
        unsigned r, m; edge(r, m);
        const float f = (float)(m - m0) * __builtin_amdgcn_rcpf((float)(r - r0));
        asm volatile("" : "+s"(r), "+s"(m));
        cq = __builtin_amdgcn_readfirstlane((unsigned)(f * 1024.f));
        qc = __builtin_amdgcn_readfirstlane((unsigned)(65536.f * __builtin_amdgcn_rcpf(f)));
        r0 = r; m0 = m;
    }
    DI unsigned at(unsigned m) const { return r0 + (unsigned)(((unsigned long long)(m - m0) * qc) >> 16); }
    DI static void sample(unsigned long long& a, unsigned long long& b) { asm volatile("s_memrealtime %0\n\ts_memtime %1\n\ts_waitcnt lgkmcnt(0)" : "=s"(a), "=s"(b) :: "memory"); }
    DI unsigned value(unsigned long long a, unsigned long long b) const {
        const unsigned lo = (unsigned)a << 3;
        return lo + __builtin_amdgcn_readfirstlane(min(max((int)(at((unsigned)b) - lo), 0), 31));
    }
    DI unsigned now() const { unsigned long long a, b; sample(a, b); return value(a, b); }
    DI unsigned cycles(unsigned t) const { const int d = __builtin_amdgcn_readfirstlane(min(max((int)(t - r0), 0), 1 << 20)); return m0 + (((unsigned)d * cq) >> 10) - 26; }
};
DI unsigned waitmt(unsigned t) { unsigned n; do n = mt32(); while ((int)(n - t) < 0); return n; }
#define MST_SH 2
DI unsigned mst_off(int m) { return (m >> MST_SH) * 128 + (m & ((1 << MST_SH) - 1)) * 4; }
struct Sched {
    unsigned tgo, tau, durA, durM, pad;
    unsigned a, v;
    DI void fetch_a(rsrc_t rw, unsigned exo, unsigned pol, int s, int lane) { a = ld4(rw, s >= 2 ? (lane & 31) * 4 : OOB, ex_step(exo, pol, max(s - 2, 0)) + EX_AST); }
    DI void fetch_m(rsrc_t rw, unsigned exo, unsigned pol, int s, int lane) { v = ld4(rw, mst_off(min(lane, 39)), ex_step(exo, pol, s - 1) + EX_MST); }
    DI void take_a() { const int m = wave_max((int)a); durA += __builtin_amdgcn_ballot_w64(a - 1u >= 0xFFFFu) == 0 ? (min(m - (int)durA, RISE) + 2) >> 2 : 0; }
    DI void next(const Params& p, rsrc_t rw, unsigned exo, unsigned pol, int s, int lane, unsigned long long& alive) {
        if ((__builtin_amdgcn_ballot_w64(v == SENT) & alive) != 0) {
            const unsigned mo = ex_step(exo, pol, s - 1) + EX_MST;
            unsigned n = SPIN_CAP; bool bad;
#pragma nounroll
            do {
                asm volatile("" ::: "memory");
                v = ld4(rw, mst_off(min(lane, 39)), mo);
                bad = __builtin_amdgcn_ballot_w64(v == SENT) != 0;
            } while (bad && --n);
            if (bad) { alive = 0; set_tmo(p); }
        }
        const unsigned ref = __builtin_amdgcn_readfirstlane(v);
        const unsigned pm = ref + (unsigned)wave_max((int)(v - ref));
        if (s >= 2 && __builtin_amdgcn_ballot_w64(v & 1) == 0) durM += (min((int)(pm - tau - durM), RISE) + 2) >> 2;
        const bool late = s == 1 || (int)(pm + V_LATE - tgo) > 0;
        pad = late || __builtin_amdgcn_ballot_w64(v & 1) != 0 ? min(pad + PAD_UP, (unsigned)PAD_MAX) : (pad > 4 ? pad - 4 : 0);
        tau = (late ? pm + F_LATE : tgo) + durA + pad;
        tgo = tau + durM + V_A + pad;
    }
};

#define PACE 8
DI void pace() { asm volatile("s_nop %0" :: "n"(PACE)); }
DI void role_M(const Params& p, LAS uchar* lds, int mi) {
    const int tid = threadIdx.x, lane = tid & 63, w = __builtin_amdgcn_readfirstlane(tid >> 6), c = lane & 15, g = lane >> 4;
    const int qh = mi & 1, cs = mi >> 1;
    const rsrc_t rw = mk_rsrc(p.ws, WS_USED);
    u32x8 wf[8][2][2];
    waitrt(rt32() + M_DELAY);
    {
        LAS uchar* lw = lds + w * 32768;
        const int q = c >> 2, pp = c & 3;
        unsigned ra[4];
#pragma unroll
        for (int m = 0; m < 4; ++m) {
            const int k0 = m < 2 ? 8 * g + 4 * m : 32 + 8 * g + 4 * (m - 2), ga = k0 >> 4, qq = (k0 >> 2) & 3;
            const int e = 32 * ga + 2 * qq + (q & 1) + 8 * (q >> 1);
            ra[m] = e * 64 + pp * 8;
        }
        wo_stage(p, (unsigned)(size_t)lw, 8 * w, cs, lane);
        wo_stage(p, (unsigned)(size_t)lw + 16384, 8 * w + 2, cs, lane);
#pragma unroll
        for (int rd = 0; rd < 4; ++rd) {
            if (rd < 3) wait_vm<16>(); else wait_vm<0>();
#pragma unroll
            for (int i = 0; i < 2; ++i)
#pragma unroll
                for (int sel = 0; sel < 2; ++sel)
#pragma unroll
                    for (int nt = 0; nt < 2; ++nt) {
                        LAS uchar* a = lw + (rd & 1) * 16384 + i * 8192 + sel * 1024 + nt * 32;
                        const u32x2 t0 = trd(a + ra[0]), t1 = trd(a + ra[1]), t2 = trd(a + ra[2]), t3 = trd(a + ra[3]);
                        wf[2 * rd + i][sel][nt] = (u32x8){t0.x, t0.y, t1.x, t1.y, t2.x, t2.y, t3.x, t3.y};
                        if (2 * rd + i < 6) asm volatile("" : "+a"(wf[2 * rd + i][sel][nt])); else asm volatile("" : "+v"(wf[2 * rd + i][sel][nt]));
                    }
            asm volatile("s_waitcnt lgkmcnt(0)" ::: "memory");
            SB();
            if (rd < 2) wo_stage(p, (unsigned)(size_t)lw + (rd & 1) * 16384, 8 * w + 2 * rd + 4, cs, lane);
        }
    }
    bar();
    if (tid < 2) *(LAS unsigned*)(lds + 4096 + tid * 4) = 0;
    bar();
    const int idx = (c & 4) ? 0xEEEE : 0x4444;
    const int dl = 32 * cs + 2 * c;
    const unsigned so = mf_off(dl >> 6) + (((dl >> 4) & 3) * 16 + qh * 8 + ((dl >> 3) & 1) * 4 + g) * 16 + (dl & 7) * 2;
    const unsigned rwo = (w * 128 + (2 * (g & 1)) * 32 + 16 * (g >> 1) + c) * 4, rro = (g * 32 + 2 * c) * 4;
    const unsigned lo16 = lane * 16, sto = lane == 0 ? mst_off(mi) : OOB;
    unsigned long long alive = ~0ull;
    unsigned budget = SPIN_CAP;
    const unsigned exo = ex_off(p), pol = read_pol(p);
    wait_ctr(p, rw, C_PZ, 56);
    Clock ck; ck.start();
    unsigned tdone = ck.now();
    Sched sc = {0u, tdone - V_M, DUR_A0, DUR_M0, PAD0, 0u, 0u};
    for (int s = 0; s < NSEG - 1; ++s) {
        const unsigned eb = ex_step(exo, pol, s);
        const unsigned ob = eb + w * 32768 + qh * 1024;
        if (s == 3) wait_ctr(p, rw, C_PZ + 32, 56);
        if (s > 0) {
            sc.fetch_a(rw, exo, pol, s, lane);
            ck.sync();
            sc.take_a();
            SB();
            waitmt(ck.cycles(tdone + D_MST));
            sc.fetch_m(rw, exo, pol, s, lane);
            sc.next(p, rw, exo, pol, s, lane, alive);
        }
        const unsigned tau = sc.tau;
        unsigned obs[8], mb = ex_step(exo, pol, s + 1);
#pragma unroll
        for (int i = 0; i < 8; ++i) { obs[i] = ob + (i >> 2) * 16384 + (i & 3) * 2048; asm volatile("" : "+s"(obs[i])); }
        unsigned rwa = rwo + (s & 1) * 2048, rra = rro + (s & 1) * 2048;
        f32x4 acc[2][2];
#pragma unroll
        for (int i = 0; i < 4; ++i) { acc[i >> 1][i & 1] = (f32x4){0.f, 0.f, 0.f, 0.f}; asm volatile("" : "+v"(acc[i >> 1][i & 1])); }
        unsigned tw = ck.cycles(tau + V_M);
        asm volatile("" : "+s"(mb), "+v"(rwa), "+v"(rra), "+s"(tw));
        waitmt(tw);
        FT(0);
        u32x4 of[8];
#pragma unroll
        for (int i = 0; i < 8; ++i) { of[i] = ld16(rw, lo16, obs[i]); SB(); if (i < 7) pace(); SB(); }
        auto mix = [&]() {
#pragma unroll
            for (int i = 0; i < 8; ++i) {
                smf_x(acc[0][1], of[i], wf[i][0][1], idx, i < 6);
                smf_x(acc[1][0], of[i], wf[i][1][0], idx, i < 6);
                smf_x(acc[0][0], of[i], wf[i][0][0], idx, i < 6);
                smf_x(acc[1][1], of[i], wf[i][1][1], idx, i < 6);
                if (i == 0) FT(1);
                if (i == 7) FT(2);
                SB();
            }
            asm volatile("s_nop 2" : "+v"(acc[0][1]), "+v"(acc[1][0]));
            float u[4], v[4], y[4];
#pragma unroll
            for (int r = 0; r < 4; ++r) {
                const float fa = acc[1][0][r], fb = acc[0][1][r];
                auto q = __builtin_amdgcn_permlane32_swap(__builtin_bit_cast(unsigned, fa), __builtin_bit_cast(unsigned, fb), false, false);
                u[r] = __builtin_bit_cast(float, (unsigned)q[0]); v[r] = __builtin_bit_cast(float, (unsigned)q[1]);
            }
            SB();
            asm volatile("s_nop 0" : "+v"(acc[0][0]), "+v"(acc[1][1]));
#pragma unroll
            for (int r = 0; r < 4; ++r) { float lo = acc[0][0][r] + v[r], hi = acc[1][1][r] + u[r]; asm("" : "+v"(lo), "+v"(hi)); y[r] = g < 2 ? lo : hi; }
            const float z0 = swap16_add(y[0], y[2]), z1 = swap16_add(y[1], y[3]);
            *(LAS float*)(lds + rwa) = z0;
            *(LAS float*)(lds + rwa + 128) = z1;
            SB();
            return __builtin_amdgcn_ballot_w64(__builtin_isunordered(z0, z1));
        };
        if (__builtin_expect(mix() != 0, 0)) {
            if (refetch<8>(of, rw, lo16, obs, budget)) {
                TRY();
                *(LAS unsigned*)(lds + 4096 + (s & 1) * 4) = s + 1;
#pragma unroll
                for (int i = 0; i < 4; ++i) { acc[i >> 1][i & 1] = (f32x4){0.f, 0.f, 0.f, 0.f}; asm volatile("" : "+v"(acc[i >> 1][i & 1])); }
                asm volatile("s_nop 1" ::: "memory");
                mix();
            }
        }
        FT(3);
        bar();
        FT(4);
        if (__builtin_expect(w == 0, 1)) {
            LAS f32x2* r2 = (LAS f32x2*)(lds + rra);
            const f32x2 a0 = r2[0], a1 = r2[64], a2 = r2[128], a3 = r2[192];
            const unsigned fl = *(LAS unsigned*)(lds + 4096 + (s & 1) * 4);
            SB();
            unsigned long long ca, cb;
            Clock::sample(ca, cb);
            SB();
            const f32x2 v = (a0 + a1) + (a2 + a3);
            st4(nosent(pk2(v[0], v[1])), rw, so, mb);
            SB();
            tdone = ck.value(ca, cb) + M_TAIL;
            st4(nosent((tdone & ~1u) | (fl == (unsigned)(s + 1))), rw, sto, eb + EX_MST);
        } else tdone = ck.now() + M_TAIL;
        if (budget == 0) set_tmo(p);
        FT(5); FDUMP(32 + mi, s, w);
    }
}

constexpr int RING = 4, VPRE = 4;
constexpr int a_slot(int ks, int tile) { return ks * 16384 + tile * 2048; }
constexpr int a_xblk(int i) { return a_slot(i & 1 ? 9 : 4, i & 2 ? 5 : 2); }
constexpr int A_P = a_slot(4, 7), A_P2 = A_P + 1024, A_CNT = A_P2 + 512;
template <int W> struct AP {
    static constexpr int ROT = 3 * W, NT = W == 0 ? 0 : (W == 3 ? 2 : 3), T0 = W == 0 ? 0 : 3 * (W - 1);
    static constexpr int NL = W == 0 ? 0 : (W == 3 ? 9 : 14);
    static constexpr int QA = W == 0 || W == 3 ? 20 : 15;
    static constexpr bool k_lds(int t, int m) { return W == 0 ? false : (W == 3 ? (t == 1 && m >= 1) : (t == 1 || (m > 0 && (m & 1) == 0))); }
    static constexpr bool k_acc(int t, int m) { return W == 0 && m < 5; }
    static constexpr bool v_lds(int t, int ks) { return W == 3 ? ks < 4 : (ks < 4 || (ks == 4 && t == 0)); }
    static constexpr int k_idx(int t, int m) { int n = 0; for (int i = 0; i < 2 * m + 1 - t; ++i) n += k_lds(1 - (i & 1), i >> 1) ? 1 : 0; return n; }
    static constexpr int k_slot(int i) { return a_slot(5 + i / (NT ? NT : 1), T0 + i % (NT ? NT : 1)); }
    static constexpr int v_slot(int i) { return a_slot(i / (NT ? NT : 1), T0 + i % (NT ? NT : 1)); }
    static constexpr int stream(int i) { return i < NL ? k_slot(i) : v_slot(i - NL); }
    static constexpr int m_of(int ks) { return (ks + 10 - ROT % 10) % 10; }
};
DI void a_dma(const uchar* wsrc, LAS uchar* lds, int h, int w, int half) {
    const int lane = fresh_lane();
    const rsrc_t r = mk_rsrc(wsrc, 640 * 8192);
    const unsigned v = (lane >> 4) * 8192 + (((lane & 15) ^ (2 * (((lane >> 4) & 1) | (w << 1)))) << 4);
    const unsigned lb = (unsigned)(size_t)lds;
#pragma unroll
    for (int i = 20 * half; i < 20 * half + 20; ++i) dma16(r, v, (4 * (w + 4 * i)) * 8192 + h * 256, lb + (w + 4 * i) * 1024);
}
struct ATake {
    unsigned row[4], f[4];
    DI ATake() {
        const int lane = fresh_lane(), c = lane & 15, g = lane >> 4, q = c >> 2, pp = c & 3;
#pragma unroll
        for (int m = 0; m < 4; ++m) {
            const int k0 = m < 2 ? 8 * g + 4 * m : 32 + 8 * g + 4 * (m - 2), ga = k0 >> 4, qq = (k0 >> 2) & 3;
            const int r = 16 * ga + 2 * qq + (q & 1) + 8 * (q >> 1);
            row[m] = r * 256 + pp * 8; f[m] = 32 * ((r & 1) | ((r >> 1) & 6));
        }
    }
    DI u32x8 get(LAS uchar* lds, int ks, int tile) const {
        LAS uchar* a = lds + ks * 16384;
        const u32x2 t0 = trd(a + row[0] + (f[0] ^ (32 * tile))), t1 = trd(a + row[1] + (f[1] ^ (32 * tile))), t2 = trd(a + row[2] + (f[2] ^ (32 * tile))), t3 = trd(a + row[3] + (f[3] ^ (32 * tile)));
        return (u32x8){t0.x, t0.y, t1.x, t1.y, t2.x, t2.y, t3.x, t3.y};
    }
};
DI u32x8 ldsB(LAS uchar* a) { const u32x4 lo = *(LAS u32x4*)a, hi = *(LAS u32x4*)(a + 1024); return (u32x8){lo.x, lo.y, lo.z, lo.w, hi.x, hi.y, hi.z, hi.w}; }
DI void ldsB_put(LAS uchar* a, u32x8 v) { *(LAS u32x4*)a = (u32x4){v[0], v[1], v[2], v[3]}; *(LAS u32x4*)(a + 1024) = (u32x4){v[4], v[5], v[6], v[7]}; }
DI void pin(u32x8& v, bool acc) { if (acc) asm volatile("" : "+a"(v)); else asm volatile("" : "+v"(v)); }

#define NEXT_KV() do { SB(); const int sn_ = min(s + 1, NSEG - 1); kvn = ld4(rw, 0, kvw * 4); \
    _Pragma("unroll") for (int i = 0; i < NPF; ++i) pf[i] = ld16(rw, lo16, pfb + sn_ * 32 * 8192 + i * 1024); SB(); } while (0)
#define KM_OUT() do { unsigned n_ = 0; if (lane == 0) n_ = __hip_atomic_fetch_add((LAS unsigned*)(lds + A_CNT), 1u, __ATOMIC_RELAXED, __HIP_MEMORY_SCOPE_WORKGROUP); \
    if ((__builtin_amdgcn_readfirstlane(n_) & 3) == 3) st4(s + 1, rw, lane == 0 ? (C_KM + h * 32 + 1) * 4 : OOB, 0); } while (0)
template <int W>
DI void a_loop(const Params& p, LAS uchar* lds, int h, const u32x8 (&wq)[2][10], const u32x8 (&wk)[2][10], const u32x8 (&wv)[3][10]) {
    typedef AP<W> P;
    const int lane = fresh_lane(), c = lane & 15, g = lane >> 4;
    const rsrc_t rw = mk_rsrc(p.ws, WS_USED);
    constexpr int ROT = P::ROT, NT = P::NT, T0 = P::T0, NPF = W == 0 ? 8 : NT, NL = P::NL;
    const int idx = (c & 8) ? 0xEEEE : 0x4444;
    const unsigned lo16 = lane * 16, mo16 = (g * 16 + ((c >> 2) & 1) * 8 + (c >> 3) * 4 + (c & 3)) * 16;
    const unsigned oaddr = W == 0 ? (c >= 8 ? (g * 8 + (c & 7)) * 16 : OOB) : (c < 8 ? (c >> 2) * 1024 + (g >> 1) * 64 + (c & 3) * 16 + (g & 1) * 8 : OOB);
    const unsigned oaddr2 = c < 8 ? (c >> 2) * 1024 + (g & 1) * 128 + (g >> 1) * 64 + (c & 3) * 16 : OOB;
    const unsigned xw = a_xblk(W) + c * 40 + g * 8;
    const unsigned asto = lane == 0 ? h * 4 : OOB;
    const unsigned pfb = W == 0 ? OFF_KF + h * 8192 : OFF_VF + h * 8192 + T0 * 1024;
    u32x4 pf[NPF];
    unsigned long long alive = ~0ull;
    unsigned budget = SPIN_CAP;
    const unsigned exo = ex_off(p), pol = read_pol(p);
    Clock ck; ck.start();
    wait_ctr(p, rw, C_PZ, 56);
    wait_ctr(p, rw, C_KVH + h * 16, 4);
#pragma unroll
    for (int i = 0; i < NPF; ++i) pf[i] = ld16(rw, lo16, pfb + i * 1024);
    Sched sc = {ck.now(), 0u, DUR_A0, DUR_M0, PAD0, 0u, 0u};
    const float ninf = g < 2 ? -__builtin_inff() : 0.f;
    unsigned xra[4], xqa[4];
    if (W == 0) {
#pragma unroll
        for (int ks = 0; ks < 4; ++ks) {
            xra[ks] = a_xblk(ks) + (4 * g + (c >> 2)) * 40 + (c & 3) * 8; xqa[ks] = a_xblk(ks) + (4 * g + (c >> 2)) * 40 + (c & 1) * 8;
            asm volatile("" : "+v"(xra[ks]), "+v"(xqa[ks]));
        }
    }
    u32x4 one4 = {0x3F803F80u, 0x3F803F80u, 0x3F803F80u, 0x3F803F80u};
    u32x8 pw = {0u, 0u, 0u, 0u, 0u, 0u, 0u, 0u};
    unsigned pa[3] = {A_P + lo16, A_P2 + lane * 8u, A_P2 + (lane ^ 32) * 8u};
    if (W) asm volatile("" : "+a"(one4), "+v"(pw), "+v"(pa[0]), "+v"(pa[1]), "+v"(pa[2]));
    unsigned lb[3] = {lo16, lo16 + 65536, lo16 + 131072};
    asm volatile("" : "+v"(lb[0]), "+v"(lb[1]), "+v"(lb[2]));
    auto sget = [&](int i) -> u32x8 { const int a = P::stream(i); return ldsB(lds + lb[a >> 16] + (a & 65535)); };
    for (int s = 0; s < NSEG; ++s) {
        const unsigned eb = ex_step(exo, pol, s);
        unsigned mb[10], ob = eb + (h >> 2) * 16384 + (h & 3) * 2048 + T0 * 128;
#pragma unroll
        for (int m = 0; m < 10; ++m) { mb[m] = eb + mf_off((m + ROT) % 10); asm volatile("" : "+s"(mb[m])); }
        f32x4 aq[2], ak[2], va[3];
        u32x8 ring[RING];
#pragma unroll
        for (int t = 0; t < 2; ++t) { aq[t] = (f32x4){0.f, 0.f, 0.f, 0.f}; ak[t] = aq[t]; asm volatile("" : "+v"(aq[t]), "+v"(ak[t])); }
#pragma unroll
        for (int t = 0; t < 3; ++t) {
            const float z = W == 0 && t == 2 ? ninf : 0.f;
            va[t] = (f32x4){z, z, z, z}; asm volatile("" : "+v"(va[t]));
        }
        if (W) {
#pragma unroll
            for (int i = 0; i < RING; ++i) ring[i] = sget(i);
        }
        const unsigned tgo = sc.tgo;
        unsigned tw = ck.cycles(tgo);
        asm volatile("" : "+s"(ob), "+s"(tw));
        SB();
        unsigned mis = waitmt(tw);
        FT(0);
        u32x4 mf[10];
#pragma unroll
        for (int m = 0; m < 10; ++m) {
            mf[m] = ld16(rw, mo16, mb[m]);
            SB();
            if (m < 9) pace();
            SB();
        }
        auto project = [&]() {
            static_for<10>([&](auto mc) {
                constexpr int m = mc.value;
                auto qprod = [&](auto tc) { constexpr int t = tc.value; smf_x(aq[t], mf[m], wq[t][m], idx, 2 * m + t < P::QA - (W == 0 ? 10 : 0)); };
                auto kprod = [&](auto tc) {
                    constexpr int t = tc.value;
                    if constexpr (P::k_lds(t, m)) {
                        constexpr int i = P::k_idx(t, m);
                        smf_v(ak[t], mf[m], ring[i % RING], idx);
                        ring[i % RING] = sget(i + RING);
                    } else smf_x(ak[t], mf[m], wk[t][m], idx, W == 0);
                };
                kprod(IC<1>{});
                qprod(IC<m == 9 ? 1 : 0>{});
                if constexpr (m == 9) qprod(IC<0>{});
                kprod(IC<0>{});
                if constexpr (m < 9) qprod(IC<1>{});
                if (m == 0) FT(1);
                if (m == 9) FT(2);
                SB();
            });
            float x[4], y[4];
            asm volatile("s_nop 2" : "+v"(aq[1]), "+v"(ak[1]));
#pragma unroll
            for (int r = 0; r < 4; ++r) y[r] = swap32_add(aq[1][r], ak[1][r]);
            *(LAS u32x2*)(lds + xw + 640) = (u32x2){pk2(y[0], y[1]), pk2(y[2], y[3])};
            SB();
            asm volatile("" : "+v"(aq[0]), "+v"(ak[0]));
#pragma unroll
            for (int r = 0; r < 4; ++r) x[r] = swap32_add(aq[0][r], ak[0][r]);
            *(LAS u32x2*)(lds + xw) = (u32x2){pk2(x[0], x[1]), pk2(x[2], x[3])};
            SB();
            return nan_lanes((f32x4){x[0], x[1], x[2], x[3]});
        };
        if (__builtin_expect(project() != 0, 0)) {
            if (refetch<10>(mf, rw, mo16, mb, budget)) {
                mis = tw + 100000; TRY();
#pragma unroll
                for (int t = 0; t < 2; ++t) { aq[t] = (f32x4){0.f, 0.f, 0.f, 0.f}; ak[t] = aq[t]; asm volatile("" : "+v"(aq[t]), "+v"(ak[t])); }
                if (W) {
#pragma unroll
                    for (int i = 0; i < RING; ++i) ring[i] = sget(i);
                }
                asm volatile("s_nop 1" ::: "memory");
                project();
            }
        }
        SB();
        if (W) {
#pragma unroll
            for (int i = 0; i < VPRE; ++i) { const int r = 10 * NT - 1 - i; smf_a(va[r % NT], mf[P::m_of(r / NT)], wv[r % NT][r / NT], idx); }
        }
        bar();
        SB();
        FT(3);
        const unsigned kb = OFF_KM + (s * 32 + h) * 4096;
        const int kvw = s < 2 ? C_KVH + h * 16 : C_KV + ((min(s + 2, NSEG - 1) >> 3) * 32 + h) * 16;
        unsigned tpub = 0, kvn;
        if (W == 0) {
            u32x4 xf[4], xd[4];
#pragma unroll
            for (int ks = 0; ks < 4; ++ks) {
                const u32x2 a = trd(lds + xqa[ks]), b = trd(lds + xqa[ks] + 640), lo = trd(lds + xra[ks]), hi = trd(lds + xra[ks] + 640);
                xd[ks] = (u32x4){a.x, a.y, b.x, b.y}; xf[ks] = (u32x4){lo.x, lo.y, hi.x, hi.y};
            }
            SB();
            if (__builtin_expect(s == NSEG - 1, 0)) {
#pragma unroll
                for (int ks = 0; ks < 4; ++ks) st16((u32x4){nosent(xf[ks].x), nosent(xf[ks].y), nosent(xf[ks].z), nosent(xf[ks].w)}, rw, oaddr, kb + ks * 512);
                KM_OUT();
            }
#pragma unroll
            for (int ks = 0; ks < 4; ++ks) { mfma_v(va[0], pf[ks], xd[ks]); mfma_v(va[1], pf[(4 + ks) % NPF], xd[ks]); mfma_v(va[2], xf[ks], xd[ks]); }
            FT(4);
            float a0, a1, a2, a3, a4, a5, tm, tn;
            unsigned e0, e1, e2;
            asm volatile("s_nop 3\n\t"
                         "v_cndmask_b32_e64 %0, %15, %11, %23\n\tv_cndmask_b32_e64 %1, %16, %12, %23\n\tv_cndmask_b32_e64 %2, %17, %13, %23\n\t"
                         "v_cndmask_b32_e64 %3, %18, %14, %23\n\tv_cndmask_b32_e64 %4, %21, %19, %23\n\tv_cndmask_b32_e64 %5, %22, %20, %23\n\t"
                         "v_max3_f32 %6, %0, %1, %2\n\tv_max3_f32 %7, %3, %4, %5\n\tv_max_f32 %6, %6, %7\n\ts_nop 1\n\t"
                         "v_max_f32_dpp %6, %6, %6 row_ror:8 row_mask:0xf bank_mask:0xf\n\t"
                         "v_mov_b32 %7, %6\n\ts_nop 1\n\tv_permlane32_swap_b32 %6, %7\n\tv_max_f32 %6, %6, %7\n\t"
                         "v_mov_b32 %7, %6\n\ts_nop 1\n\tv_permlane16_swap_b32 %6, %7\n\tv_max_f32 %6, %6, %7\n\t"
                         "v_mul_f32 %6, %24, %6\n\t"
                         "v_fma_f32 %0, %0, %24, -%6\n\tv_fma_f32 %1, %1, %24, -%6\n\tv_fma_f32 %2, %2, %24, -%6\n\t"
                         "v_fma_f32 %3, %3, %24, -%6\n\tv_fma_f32 %4, %4, %24, -%6\n\tv_fma_f32 %5, %5, %24, -%6\n\t"
                         "v_exp_f32 %0, %0\n\tv_exp_f32 %1, %1\n\tv_exp_f32 %2, %2\n\tv_exp_f32 %3, %3\n\tv_exp_f32 %4, %4\n\tv_exp_f32 %5, %5\n\t"
                         "v_cvt_pk_bf16_f32 %8, %0, %1\n\tv_cvt_pk_bf16_f32 %9, %2, %3\n\tv_cvt_pk_bf16_f32 %10, %4, %5"
                         : "=&v"(a0), "=&v"(a1), "=&v"(a2), "=&v"(a3), "=&v"(a4), "=&v"(a5), "=&v"(tm), "=&v"(tn), "=&v"(e0), "=&v"(e1), "=&v"(e2)
                         : "v"(va[0][0]), "v"(va[0][1]), "v"(va[0][2]), "v"(va[0][3]), "v"(va[1][0]), "v"(va[1][1]), "v"(va[1][2]), "v"(va[1][3]),
                           "v"(va[2][0]), "v"(va[2][1]), "v"(va[2][2]), "v"(va[2][3]), "s"(0x00FF00FF00FF00FFull), "s"(SCL));
            *(LAS u32x2*)(lds + A_P + (g * 16 + (c & 7)) * 16 + (c >> 3) * 8) = (u32x2){e0, e1};
            *(LAS unsigned*)(lds + A_P2 + (g * 16 + (c & 7)) * 8 + (c >> 3) * 4) = e2;
            FT(5);
            bar();
            FT(6);
            sc.fetch_a(rw, exo, pol, max(s, 1), lane); sc.fetch_m(rw, exo, pol, max(s, 1), lane);
            NEXT_KV();
            if (s < NSEG - 1) waitmt(mt32() + KM_DELAY * 3);
#pragma unroll
            for (int ks = 0; ks < 4; ++ks) if (s < NSEG - 1) st16(xf[ks], rw, oaddr, kb + ks * 512);
        } else {
            if (W == 3) st4(s, rw, asto, C_KM * 4 + h * 124);
#pragma unroll
            for (int i = 0; i < (10 * NT - NL - VPRE > NL ? 10 * NT - NL - VPRE : NL); ++i) {
                if (i < NL) {
                    smf_v(va[i % NT], mf[P::m_of(i / NT)], ring[(NL + i) % RING], idx);
                    if (NL + i + RING < 2 * NL) ring[(NL + i) % RING] = sget(NL + i + RING);
                }
                const int r = NL + i;
                if (r < 10 * NT - VPRE) smf_a(va[r % NT], mf[P::m_of(r / NT)], wv[r % NT][r / NT], idx);
                SB();
            }
            asm volatile("s_nop 7" : "+v"(va[0]), "+v"(va[1]), "+v"(va[2]));
            float sa[4], sb[4];
#pragma unroll
            for (int r = 0; r < 4; ++r) { sa[r] = swap32_add(va[0][r], va[1][r]); if (W != 3) sb[r] = swap32_add(va[2][r], va[2][r]); }
            u32x4 vv = {pk2(sa[0], sa[1]), pk2(sa[2], sa[3]), 0u, 0u};
            if (W != 3) { vv.z = pk2(sb[0], sb[1]); vv.w = pk2(sb[2], sb[3]); }
            asm volatile("" : "+v"(vv));
            if (__builtin_expect(s == NSEG - 1, 0)) {
                st8((u32x2){nosent(vv.x), nosent(vv.y)}, rw, lane * 8, kb + 2048 + T0 * 256);
                if (W != 3) st8((u32x2){nosent(vv.z), nosent(vv.w)}, rw, g < 2 ? lane * 8 : OOB, kb + 2048 + T0 * 256 + 512);
                KM_OUT();
            }
            SB();
            FT(5);
            bar();
            FT(6);
            SB();
            if (s < NSEG - 1) {
                const u32x4 p01 = *(LAS u32x4*)(lds + pa[0]);
                const u32x2 q2 = *(LAS u32x2*)(lds + pa[1]), q2s = *(LAS u32x2*)(lds + pa[2]);
                pw[4] = q2.x; pw[5] = q2.y; pw[0] = q2s.x; pw[1] = q2s.y;
                const u32x4 p2s = __builtin_shufflevector(pw, pw, 0, 1, 2, 3), p2h = __builtin_shufflevector(pw, pw, 2, 3, 4, 5), p2 = __builtin_shufflevector(pw, pw, 4, 5, 6, 7);
                SB();
                f32x4 l, o[3];
                asm volatile("v_mfma_f32_16x16x32_bf16 %0, %1, %2, 0" : "=&v"(l) : "a"(one4), "v"(p01));
                mfma_0(o[0], pf[0], p01);
                mfma_av(l, one4, p2);
                mfma_0(o[1], pf[1 % NPF], p01);
                mfma_v(o[0], vv, p2s);
                mfma_v(o[1], vv, p2);
                float inv;
                if (W != 3) {
                    mfma_0(o[2], pf[2 % NPF], p01);
                    asm volatile("v_rcp_f32 %0, %1" : "=v"(inv) : "v"(l[0]));
                    mfma_v(o[2], vv, p2h);
                    asm volatile("s_nop 3" : "+v"(o[0]), "+v"(o[1]), "+v"(inv));
                } else {
                    asm volatile("s_nop 1\n\tv_rcp_f32 %0, %3\n\ts_nop 5" : "=&v"(inv), "+v"(o[0]), "+v"(o[1]) : "v"(l[0]));
                }
                const f32x4 n0 = o[0] * inv, n1 = o[1] * inv;
                SB();
                const unsigned a0 = pk2(n0[0], n0[1]), b0 = pk2(n1[0], n1[1]), a1 = pk2(n0[2], n0[3]), b1 = pk2(n1[2], n1[3]);
                SB();
                auto r0 = __builtin_amdgcn_permlane16_swap(a0, b0, false, false);
                auto r1 = __builtin_amdgcn_permlane16_swap(a1, b1, false, false);
                st16((u32x4){nosent(r0[0]), nosent(r1[0]), nosent(r0[1]), nosent(r1[1])}, rw, oaddr2, ob);
                if (W != 3) {
                    asm volatile("" : "+v"(o[2]));
                    const f32x4 n2 = o[2] * inv;
                    st8((u32x2){nosent(pk2(n2[0], n2[1])), nosent(pk2(n2[2], n2[3]))}, rw, oaddr, ob + 256);
                }
            }
            tpub = ck.now();
            sc.fetch_a(rw, exo, pol, max(s, 1), lane); sc.fetch_m(rw, exo, pol, max(s, 1), lane);
            NEXT_KV();
            if (W == 1) st4(mis - tw <= 120u ? tpub - tgo : 0u, rw, asto, eb + EX_AST);
            if (s < NSEG - 1) st8((u32x2){vv.x, vv.y}, rw, lane * 8, kb + 2048 + T0 * 256);
            if (W != 3 && s < NSEG - 1) st8((u32x2){vv.z, vv.w}, rw, g < 2 ? lane * 8 : OOB, kb + 2048 + T0 * 256 + 512);
        }
        if (budget == 0) set_tmo(p);
        FT(7); FDUMP(h, s, W);
        STAT(h, s, W, ck.at(mis), tpub, tgo, tries);
        if (s == NSEG - 1) break;
        ck.sync();
        if (s > 0) { sc.take_a(); sc.next(p, rw, exo, pol, s, lane, alive); }
        if ((unsigned)__builtin_amdgcn_readfirstlane(kvn) < 4u) wait_ctr(p, rw, kvw, 4);
        if (s == 2) wait_ctr(p, rw, C_PZ + 32, 56);
    }
    XST(W == 0, h);
    __builtin_amdgcn_endpgm();
}

template <int W>
DI void a_wave(const Params& p, LAS uchar* lds, int h) {
    typedef AP<W> P;
    u32x8 wq[2][10], wk[2][10], wv[3][10];
    a_dma(p.wq, lds, h, W, 0);
    a_dma(p.wq, lds, h, W, 1);
    static_for<6>([&](auto sc) {
        constexpr int mat = sc.value >> 1, half = sc.value & 1;
        if (sc.value < 5) wait_vm<20>(); else wait_vm<0>();
        bar();
        AST_(sc.value);
        const ATake tk;
#pragma unroll
        for (int ks = 5 * half; ks < 5 * half + 5; ++ks) {
            const int m = P::m_of(ks);
#pragma unroll
            for (int t = 0; t < (mat < 2 ? 2 : P::NT); ++t) {
                if (mat == 0) { wq[t][m] = tk.get(lds, ks, 2 * W + t); pin(wq[t][m], 2 * m + t < P::QA - (W == 0 ? 10 : 0)); }
                if (mat == 1) { wk[t][m] = tk.get(lds, ks, 2 * W + t); pin(wk[t][m], W == 0); }
                if (mat == 2) { wv[t][ks] = tk.get(lds, ks, P::T0 + t); pin(wv[t][ks], true); }
            }
            SB();
        }
        bar(); SB();
        if (mat < 2) a_dma(mat == 0 ? p.wk : p.wv, lds, h, W, half);
        else {
            const int lane = fresh_lane();
            if (half == 0) {
#pragma unroll
                for (int i = 0; i < P::NL; ++i) ldsB_put(lds + P::v_slot(i) + lane * 16, wv[i % (P::NT ? P::NT : 1)][i / (P::NT ? P::NT : 1)]);
            } else {
#pragma unroll
                for (int i = 0; i < 20; ++i) if (P::k_lds(i & 1, i >> 1)) ldsB_put(lds + P::k_slot(P::k_idx(i & 1, i >> 1)) + lane * 16, wk[i & 1][i >> 1]);
            }
        }
        SB();
    });
    if (W == 0) *(LAS unsigned*)(lds + A_CNT) = 0;
    bar();
    SB();
    a_loop<W>(p, lds, h, wq, wk, wv);
}
DI void role_A(const Params& p, LAS uchar* lds, int h) {
    const int w = __builtin_amdgcn_readfirstlane(threadIdx.x >> 6);
    if (w == 0) a_wave<0>(p, lds, h);
    else if (w == 1) a_wave<1>(p, lds, h);
    else if (w == 2) a_wave<2>(p, lds, h);
    else a_wave<3>(p, lds, h);
}
DI int take_slot(const Params& p, LAS uchar* lds) {
    if (threadIdx.x == 0) {
        unsigned x = xcc_id(), t;
        for (int i = 0; i < 8; ++i) {
            t = __hip_atomic_fetch_add((unsigned*)p.ws + C_POOL + x * 32, 1u, __ATOMIC_RELAXED, __HIP_MEMORY_SCOPE_AGENT);
            if (t < 32) break;
            x = (x + 1) & 7;
        }
        *(LAS unsigned*)lds = x * 32 + t;
    }
    bar();
    const int slot = __builtin_amdgcn_readfirstlane(*(LAS unsigned*)lds);
    bar();
    return slot;
}
DI int take_ticket(const Params& p, LAS uchar* lds, int word) {
    if (fresh_tid() == 0) *(LAS unsigned*)(lds + LDS_BYTES - 16) = __hip_atomic_fetch_add((unsigned*)p.ws + word, 1u, __ATOMIC_RELAXED, __HIP_MEMORY_SCOPE_AGENT);
    bar();
    const int t = __builtin_amdgcn_readfirstlane(*(LAS unsigned*)(lds + LDS_BYTES - 16));
    bar();
    return t;
}
#define TSTAT(i, a, b)
#define SMALL_DELAY 300
DI void warm_l2(const Params& p, int x, int j) {
    const int lane = fresh_lane(), id = j * 4 + __builtin_amdgcn_readfirstlane(threadIdx.x >> 6);
    u32x4 sink[46];
#pragma unroll
    for (int i = 0; i < 46; ++i) {
        const int n = min(id + 56 * i, 2559), row = n >> 1;
        const rsrc_t r = mk_rsrc(row < 640 ? p.wk : p.wv, 640 * 8192);
        asm volatile("buffer_load_dwordx4 %0, %1, %2, %3 offen" : "=v"(sink[i]) : "v"(lane * 16), "s"(r), "s"((row < 640 ? row : row - 640) * 8192 + x * 2048 + (n & 1) * 1024) : "memory");
    }
    wait_vm<0>();
#pragma unroll
    for (int i = 0; i < 46; ++i) asm volatile("" :: "v"(sink[i]));
}
DI void background(const Params& p, LAS uchar* lds, int x, int j) {
    const bool big = x >= 4;
    if (!big) {
        const int id = j * 4 + x;
        ln_tile(p, id);
        poison(p, id, 0, 80, C_PZ);
        warm_l2(p, x, j);
        ln_tile(p, id + 56);
        if (id < 16) ln_tile(p, id + 112);
        poison(p, id, 80, 1376, C_PZ + 32);
    } else {
        if (j & 2) kv0_task<true>(p, lds, 32 * (x - 4) + j); else kv0_task<false>(p, lds, 32 * (x - 4) + j);
        TSTAT((x - 4) * 128 + j, t0, 0);
    }
    if (!big) return;
    const int y = x - 4;
    for (;;) {
        int t = take_ticket(p, lds, C_Q1 + x * 32), kind = 0, chunk = 0, u0 = 0, nu = 8;
        const int wu = 192 + 8 * y, wn = y < 3 ? 4 : 2;
        if (t < 16) { chunk = 1; u0 = 32 * y + 2 * t; nu = 2; }
        else if ((t -= 16) < 8) { chunk = t >> 1; u0 = wu + wn * (t & 1); nu = y < 3 ? 4 : 2 - (t & 1); }
        else if ((t -= 8) < 8) { chunk = 2; u0 = 32 * y + 4 * t; nu = 4; }
        else if ((t -= 8) < 8) { chunk = t >> 2; u0 = 128 + 16 * y + 4 * (t & 3); nu = 4; }
        else if ((t -= 8) < 8) { chunk = 3; u0 = 32 * y + 4 * t; nu = 4; }
        else if ((t -= 8) < 8) { chunk = 4 + (t >> 2); u0 = 32 * y + 8 * (t & 3); }
        else if ((t -= 8) < 5) { kind = 1; chunk = y; u0 = t; }
        else if ((t -= 5) < 4) { chunk = 4 + t; u0 = wu; nu = y < 3 ? 8 : 3; }
        else if ((t -= 4) < 6) { kind = 3; u0 = 6 * y + t; }
        else if ((t -= 6) < 4) { chunk = 2 + (t >> 1); u0 = 128 + 16 * y + 8 * (t & 1); }
        else if ((t -= 4) < 8) { chunk = 6 + (t >> 2); u0 = 32 * y + 8 * (t & 3); }
        else if ((t -= 8) < 8) { chunk = 4 + (t >> 1); u0 = 128 + 16 * y + 8 * (t & 1); }
        else if ((t -= 8) < 5) { kind = 1; chunk = 4 + y; u0 = t; }
        else if ((t -= 5) < 5) { kind = 2; chunk = y; u0 = t; }
        else if ((t -= 5) < 5 * N_WG) { kind = 4; u0 = t; }
        else return;
        if (kind == 2) waitrt(rt32() + 1000);
        if (kind == 0) p1_task(p, lds, chunk, u0, nu);
        else if (kind == 1) gemm_task<false>(p, lds, chunk, u0);
        else if (kind == 2) gemm_task<true>(p, lds, chunk, u0);
        else {
            wait_ctr(p, mk_rsrc(p.ws, WS_USED), C_PZ + 32, 56);
            if (kind == 3) {
                const int w = __builtin_amdgcn_readfirstlane(threadIdx.x >> 6);
                for (int s = u0 >> 3; s < NSEG; s += N_T / 8) attn_task(p, s, (u0 & 7) * 4 + w, (s & 7) < N_T / 8);
            } else { const int rank = (u0 * 52) >> 8; w_role(p, lds, 5 * y + u0 - 5 * rank, rank); }
            TSTAT(x * 128 + tk0, t0, kind + 2);
            return;
        }
        TSTAT(x * 128 + tk0, t0, kind == 0 ? (u0 < 128 ? 1 : (u0 < 192 ? 2 : 3)) : (kind == 1 ? 4 : 7));
    }
}
__global__ void __launch_bounds__(256, 1) k_main(Params p) {
    extern __shared__ __attribute__((aligned(16))) uchar smem[];
    const int slot = take_slot(p, (LAS uchar*)smem), x = slot >> 5, t = slot & 31;
    if (x < 4 && t < 8) role_A(p, (LAS uchar*)smem, x * 8 + t);
    else if (x < 4 && t < 18) role_M(p, (LAS uchar*)smem, x * 10 + t - 8);
    else background(p, (LAS uchar*)smem, x, x < 4 ? t - 18 : t);
}

extern "C" void kernel_launch(void* const* d_in, const int* in_sizes, int n_in, void* d_out, int out_size, void* d_ws, size_t ws_size, hipStream_t stream) {
    Params p;
    p.x = (const uchar*)d_in[0]; p.mem0 = (const uchar*)d_in[1]; p.wq = (const uchar*)d_in[2]; p.wk = (const uchar*)d_in[3];
    p.wv = (const uchar*)d_in[4]; p.wo = (const uchar*)d_in[5]; p.w1 = (const uchar*)d_in[8]; p.b1 = (const uchar*)d_in[9];
    p.w2 = (const uchar*)d_in[10]; p.out = (uchar*)d_out; p.ws = (uchar*)d_ws;
    (void)hipFuncSetAttribute((const void*)k_main, hipFuncAttributeMaxDynamicSharedMemorySize, LDS_BYTES);
    k_init<<<64, 256, 0, stream>>>(p);
    k_main<<<256, 256, LDS_BYTES, stream>>>(p);
}
```

```cpp
#include <hip/hip_runtime.h>
#include <stdint.h>

typedef __bf16 bf16x2 __attribute__((ext_vector_type(2)));
typedef __bf16 bf16x4 __attribute__((ext_vector_type(4)));
typedef __bf16 bf16x8 __attribute__((ext_vector_type(8)));
typedef __bf16 bf16x16 __attribute__((ext_vector_type(16)));
typedef short s16x4 __attribute__((ext_vector_type(4)));
typedef float f32x2 __attribute__((ext_vector_type(2)));
typedef float f32x4 __attribute__((ext_vector_type(4)));
typedef unsigned u32x2 __attribute__((ext_vector_type(2)));
typedef unsigned u32x4 __attribute__((ext_vector_type(4)));
typedef unsigned u32x8 __attribute__((ext_vector_type(8)));
typedef unsigned char uchar;
typedef __amdgpu_buffer_rsrc_t rsrc_t;
#define LAS __attribute__((address_space(3)))
#define DI __device__ __forceinline__

constexpr int S_ = 2048, D_ = 640, H_ = 32, NSEG = 64, F_ = 1728;
constexpr int LDS_BYTES = 163840;
constexpr int OFF_CTRL = 0;
constexpr int OFF_XF = 65536;
constexpr int OFF_KF = OFF_XF + 128 * 20 * 1024;
constexpr int OFF_QF = OFF_KF + 64 * 32 * 8192;
constexpr int OFF_VF = OFF_QF + 64 * 32 * 8192;
constexpr int OFF_ACT = OFF_VF + 64 * 32 * 8192;
constexpr int OFF_GF = OFF_ACT + 128 * 54 * 1024;
constexpr int OFF_OT = OFF_GF + 128 * 20 * 1024;
constexpr int OFF_KM = OFF_OT + 128 * 32 * 4096;
constexpr int OFF_EX = OFF_KM + 64 * 32 * 4096;
constexpr int EX_STEP = 163840, EX_MF = 8 * 16384, EX_AST = 9 * 16384 + 2048, EX_MST = 9 * 16384 + 2304, EX_PROBE = 9 * 16384 + 7168;
constexpr int WS_USED = OFF_EX + 7 * 2097152;
constexpr int C_TMO = 0, C_POOL = 32  , C_KV = 512  , C_Q = C_KV + 4096, C_ACT = C_Q + 4096  , C_G = C_ACT + 256  ,
              C_OT = C_G + 1280  , C_KM = C_OT + 2048  , C_Q1 = C_KM + 1024  , C_KVH = C_Q1 + 256  , C_X = C_KVH + 1024  , C_PZ = C_X + 512  , C_WT = C_PZ + 64  , C_OF = C_WT + 640  ,
              C_POL = 16320  ;
static_assert(C_OF + 128 <= C_POL, "");
constexpr unsigned SENT = 0xFFFFFFFFu;
constexpr int SC1 = 16;
constexpr unsigned OOB = 0x7FFFFF00u;
constexpr unsigned SPIN_CAP = 400000;

struct Params {
    const uchar* x; const uchar* mem0; const uchar* wq; const uchar* wk; const uchar* wv; const uchar* wo;
    const uchar* w1; const uchar* b1; const uchar* w2; uchar* out; uchar* ws;
};

DI rsrc_t mk_rsrc(const void* p, unsigned bytes) { return __builtin_amdgcn_make_buffer_rsrc((void*)p, 0, bytes, 0x00020000); }
DI u32x4 ld16(rsrc_t r, unsigned v, unsigned s) { return __builtin_amdgcn_raw_buffer_load_b128(r, v, s, SC1); }
DI u32x2 ld8(rsrc_t r, unsigned v, unsigned s) { return __builtin_amdgcn_raw_buffer_load_b64(r, v, s, SC1); }
DI unsigned ld4(rsrc_t r, unsigned v, unsigned s) { return __builtin_amdgcn_raw_buffer_load_b32(r, v, s, SC1); }
DI void st16(u32x4 d, rsrc_t r, unsigned v, unsigned s) { __builtin_amdgcn_raw_buffer_store_b128(d, r, v, s, SC1); asm volatile("s_nop 1" :: "v"(d)); }
DI void st8(u32x2 d, rsrc_t r, unsigned v, unsigned s) { __builtin_amdgcn_raw_buffer_store_b64(d, r, v, s, SC1); }
DI void st4(unsigned d, rsrc_t r, unsigned v, unsigned s) { __builtin_amdgcn_raw_buffer_store_b32(d, r, v, s, SC1); }
DI unsigned pk2(float a, float b) { f32x2 v = {a, b}; return __builtin_bit_cast(unsigned, __builtin_convertvector(v, bf16x2)); }
DI unsigned nosent(unsigned v) { return min(v, 0xFFFFFFFEu); }
DI float bf_lo(unsigned v) { return __builtin_bit_cast(float, v << 16); }
DI float bf_hi(unsigned v) { return __builtin_bit_cast(float, v & 0xFFFF0000u); }
DI float rbf(float a) { return bf_lo(pk2(a, a)); }
DI bf16x8 as8(u32x4 v) { return __builtin_bit_cast(bf16x8, v); }
DI f32x4 mfma32(u32x4 a, u32x4 b, f32x4 c) { return __builtin_amdgcn_mfma_f32_16x16x32_bf16(as8(a), as8(b), c, 0, 0, 0); }
DI f32x4 mfma16(u32x2 a, u32x2 b, f32x4 c) { return mfma32((u32x4){a.x, a.y, 0u, 0u}, (u32x4){b.x, b.y, 0u, 0u}, c); }
DI f32x4 smf(u32x4 a, u32x8 b, f32x4 c, int idx) { return __builtin_amdgcn_smfmac_f32_16x16x64_bf16(as8(a), __builtin_bit_cast(bf16x16, b), c, idx, 0, 0); }
DI void smf_a(f32x4& c, u32x4 a, u32x8 b, int idx) { asm volatile("v_smfmac_f32_16x16x64_bf16 %0, %1, %2, %3" : "+v"(c) : "v"(a), "a"(b), "v"(idx)); }
DI void smf_v(f32x4& c, u32x4 a, u32x8 b, int idx) { asm volatile("v_smfmac_f32_16x16x64_bf16 %0, %1, %2, %3" : "+v"(c) : "v"(a), "v"(b), "v"(idx)); }
DI void mfma_v(f32x4& c, u32x4 a, u32x4 b) { asm volatile("v_mfma_f32_16x16x32_bf16 %0, %1, %2, %0" : "+v"(c) : "v"(a), "v"(b)); }
DI void mfma_0(f32x4& c, u32x4 a, u32x4 b) { asm volatile("v_mfma_f32_16x16x32_bf16 %0, %1, %2, 0" : "=&v"(c) : "v"(a), "v"(b)); }
DI float fmax2(float a, float b) { return __builtin_amdgcn_fmed3f(a, b, __builtin_inff()); }
DI void mfma_pad(f32x4& a, f32x4& b, f32x4& c, f32x4& d) { asm volatile("s_nop 7" : "+v"(a), "+v"(b), "+v"(c), "+v"(d)); }
DI u32x2 trd(LAS uchar* p) { return __builtin_bit_cast(u32x2, __builtin_amdgcn_ds_read_tr16_b64_v4i16((LAS s16x4*)p)); }
DI unsigned mx4(unsigned m, u32x4 v) { return max(max(max(m, v.x), max(v.y, v.z)), v.w); }
DI void mx4v(unsigned& m, u32x4 v) { asm volatile("v_max3_u32 %0, %0, %1, %2\n\tv_max3_u32 %0, %0, %3, %4" : "+v"(m) : "v"(v.x), "v"(v.y), "v"(v.z), "v"(v.w)); }
DI unsigned long long nan_lanes(const f32x4& a) { return __builtin_amdgcn_ballot_w64(__builtin_isunordered(a[0], a[1]) | __builtin_isunordered(a[2], a[3])); }
template <int N> DI bool refetch(u32x4 (&f)[N], rsrc_t r, unsigned v, const unsigned (&so)[N], unsigned& budget) {
    bool redo = false;
#pragma nounroll
    for (;;) {
        unsigned m = 0;
#pragma unroll
        for (int i = 0; i < N; ++i) m = max(max(max(m, f[i].x), max(f[i].y, f[i].z)), f[i].w);
        if (__builtin_amdgcn_ballot_w64(m == SENT) == 0 || budget == 0) break;
        --budget; redo = true;
        asm volatile("" ::: "memory");
#pragma unroll
        for (int i = 0; i < N; ++i) f[i] = __builtin_amdgcn_raw_buffer_load_b128(r, v, so[i], SC1);
    }
    return redo;
}
DI unsigned rt32() { unsigned long long t; asm volatile("s_memrealtime %0\n\ts_waitcnt lgkmcnt(0)" : "=s"(t) :: "memory"); return (unsigned)t; }
DI unsigned waitrt(unsigned t) { unsigned n; do n = rt32(); while ((int)(n - t) < 0); return n; }
DI void bar() { asm volatile("s_waitcnt lgkmcnt(0)" ::: "memory"); __builtin_amdgcn_s_barrier(); asm volatile("" ::: "memory"); }
template <int N> DI void wait_vm() { asm volatile("s_waitcnt vmcnt(%0)" :: "n"(N) : "memory"); }
DI void dma16(rsrc_t r, unsigned voff, unsigned soff, unsigned ldsaddr) {
    asm volatile("s_mov_b32 m0, %0\n\ts_nop 0\n\tbuffer_load_dwordx4 %1, %2, %3 offen lds" :: "s"(ldsaddr), "v"(voff), "s"(r), "s"(soff) : "memory");
}
DI float swap32_add(float a, float b) { auto r = __builtin_amdgcn_permlane32_swap(__builtin_bit_cast(unsigned, a), __builtin_bit_cast(unsigned, b), false, false);
    return __builtin_bit_cast(float, (unsigned)r[0]) + __builtin_bit_cast(float, (unsigned)r[1]); }
DI float swap16_add(float a, float b) { auto r = __builtin_amdgcn_permlane16_swap(__builtin_bit_cast(unsigned, a), __builtin_bit_cast(unsigned, b), false, false);
    return __builtin_bit_cast(float, (unsigned)r[0]) + __builtin_bit_cast(float, (unsigned)r[1]); }
DI float grp_max(float m) {
    auto r = __builtin_amdgcn_permlane32_swap(__builtin_bit_cast(unsigned, m), __builtin_bit_cast(unsigned, m), false, false);
    m = fmax2(__builtin_bit_cast(float, (unsigned)r[0]), __builtin_bit_cast(float, (unsigned)r[1]));
    auto q = __builtin_amdgcn_permlane16_swap(__builtin_bit_cast(unsigned, m), __builtin_bit_cast(unsigned, m), false, false);
    return fmax2(__builtin_bit_cast(float, (unsigned)q[0]), __builtin_bit_cast(float, (unsigned)q[1]));
}
DI int fresh_lane() { int l = __builtin_amdgcn_mbcnt_hi(~0u, __builtin_amdgcn_mbcnt_lo(~0u, 0u)); asm volatile("" : "+v"(l)); return l & 63; }
DI unsigned fresh_tid() { unsigned t = threadIdx.x; asm volatile("" : "+v"(t)); return t; }
DI unsigned ex_off(const Params& p) { return OFF_EX + ((0u - (unsigned)(size_t)(p.ws + OFF_EX)) & 0x1FFFFFu); }
DI unsigned ex_step(unsigned exo, unsigned pol, int s) { const int r = (s * 43) >> 9; return exo + r * 2097152 + (s - 12 * r) * EX_STEP + ((pol >> r) & 1) * 8192; }
DI constexpr unsigned mf_off(int ks) { return EX_MF + (ks >> 3) * 16384 + (ks & 7) * 1024; }
DI unsigned xcc_id() { return (unsigned)__builtin_amdgcn_s_getreg(20 | (3 << 11)) & 7u; }
DI unsigned mt32() { unsigned long long t; asm volatile("s_memtime %0\n\ts_waitcnt lgkmcnt(0)" : "=s"(t) :: "memory"); return (unsigned)t; }
DI unsigned read_pol(const Params& p) {
    const int lane = threadIdx.x & 63;
    const unsigned v = ((const unsigned*)p.ws)[C_POL + min(lane, 47)];
    const unsigned long long m1 = __builtin_amdgcn_ballot_w64(v == 2), m0 = __builtin_amdgcn_ballot_w64(v == 1);
    unsigned pol = 0;
#pragma unroll
    for (int r = 0; r < 6; ++r) pol |= (__builtin_popcountll((m1 >> (8 * r)) & 255) > __builtin_popcountll((m0 >> (8 * r)) & 255) ? 1u : 0u) << r;
    return pol;
}
DI void set_tmo(const Params& p) { __hip_atomic_store((unsigned*)p.ws + C_TMO, 1u, __ATOMIC_RELAXED, __HIP_MEMORY_SCOPE_AGENT); }

DI float row16_sum(float v) {
    v += __builtin_bit_cast(float, __builtin_amdgcn_update_dpp(0, __builtin_bit_cast(int, v), 0x121, 0xf, 0xf, true));
    v += __builtin_bit_cast(float, __builtin_amdgcn_update_dpp(0, __builtin_bit_cast(int, v), 0x122, 0xf, 0xf, true));
    v += __builtin_bit_cast(float, __builtin_amdgcn_update_dpp(0, __builtin_bit_cast(int, v), 0x124, 0xf, 0xf, true));
    v += __builtin_bit_cast(float, __builtin_amdgcn_update_dpp(0, __builtin_bit_cast(int, v), 0x128, 0xf, 0xf, true));
    return v;
}
DI void add_ctr(const Params& p, int word, unsigned v) { (void)__hip_atomic_fetch_add((unsigned*)p.ws + word, v, __ATOMIC_RELAXED, __HIP_MEMORY_SCOPE_AGENT); }
DI void wait_ctr(const Params& p, rsrc_t rw, int word, unsigned want) {
    unsigned n = SPIN_CAP;
#pragma nounroll
    for (;;) {
        asm volatile("" ::: "memory");
        if ((unsigned)__builtin_amdgcn_readfirstlane(ld4(rw, 0, word * 4)) >= want) break;
        if (--n == 0) { set_tmo(p); break; }
        if ((n & 63) == 0 && __builtin_amdgcn_readfirstlane(ld4(rw, 0, C_TMO * 4))) break;
        __builtin_amdgcn_s_sleep(1);
    }
    asm volatile("" ::: "memory");
}

template <int V> struct IC { static constexpr int value = V; };
template <int N, int I = 0, class F> DI void static_for(F&& f) { if constexpr (I < N) { f(IC<I>{}); static_for<N, I + 1>(f); } }
__global__ void __launch_bounds__(256) k_init(Params p) {
    const int tid = threadIdx.x, lane = tid & 63, w = tid >> 6, b = blockIdx.x;
    if (b < 48) {
        __shared__ unsigned lat[4];
        unsigned* a = (unsigned*)(p.ws + ex_off(p) + (b >> 3) * 2097152 + EX_PROBE + (w >> 1) * 8192 + (w & 1) * EX_STEP + (b & 7) * 128);
        unsigned best = ~0u;
        for (int i = 0; i < 3; ++i) {
            const unsigned t0 = mt32();
            unsigned v = 0;
            if (lane == 0) v = __hip_atomic_fetch_add(a, 1u, __ATOMIC_RELAXED, __HIP_MEMORY_SCOPE_AGENT);
            asm volatile("s_waitcnt vmcnt(0)" :: "v"(v) : "memory");
            best = min(best, mt32() - t0);
        }
        if (lane == 0) lat[w] = best;
        __syncthreads();
        const int d = (int)min(lat[0], lat[1]) - (int)min(lat[2], lat[3]);
        if (tid == 0) ((unsigned*)p.ws)[C_POL + b] = abs(d) < 120 ? 0 : 1 + ((d > 0) ^ (xcc_id() >= 4));
    } else {
        const int i = (b - 48) * 256 + tid;
        if (i < C_POL / 4) ((u32x4*)(p.ws + OFF_CTRL))[i] = (u32x4){0u, 0u, 0u, 0u};
    }
}

DI void ln_tile(const Params& p, int b) {
    const int lane = fresh_lane(), w = __builtin_amdgcn_readfirstlane(threadIdx.x >> 6);
    const rsrc_t rw = mk_rsrc(p.ws, WS_USED);
    const int c = w * 4 + (lane >> 4), sub = lane & 15, row = b * 16 + c;
    u32x4 v[5]; float sum = 0.f;
#pragma unroll
    for (int k = 0; k < 5; ++k) v[k] = *(const u32x4*)(p.x + (size_t)row * 1280 + (k * 16 + sub) * 16);
#pragma unroll
    for (int k = 0; k < 5; ++k)
#pragma unroll
        for (int j = 0; j < 4; ++j) sum += bf_lo(v[k][j]) + bf_hi(v[k][j]);
    const float mean = row16_sum(sum) * (1.f / 640.f);
    float sq = 0.f;
#pragma unroll
    for (int k = 0; k < 5; ++k)
#pragma unroll
        for (int j = 0; j < 4; ++j) { const float a = bf_lo(v[k][j]) - mean, bb = bf_hi(v[k][j]) - mean; sq += a * a + bb * bb; }
    const float mb = rbf(mean), sd = rbf(sqrtf(rbf(rbf(row16_sum(sq) * (1.f / 640.f)) + 1e-5f))), ri = 1.f / sd;
#pragma unroll
    for (int k = 0; k < 5; ++k) {
        u32x4 o;
#pragma unroll
        for (int j = 0; j < 4; ++j) { const unsigned d = pk2(bf_lo(v[k][j]) - mb, bf_hi(v[k][j]) - mb); o[j] = pk2(bf_lo(d) * ri, bf_hi(d) * ri); }
        st16(o, rw, (k * 16 + sub) * 256 + c * 16, OFF_XF + b * 20480);
    }
    wait_vm<0>();
    bar();
    if (fresh_tid() == 0) add_ctr(p, C_X + (b >> 3) * 32, 1u);
}
DI void poison(const Params& p, int j, int n0, int n1, int word) {
    const int tid = fresh_tid();
    const rsrc_t rw = mk_rsrc(p.ws, WS_USED);
    const unsigned exo = ex_off(p), pol = read_pol(p);
    const u32x4 sv = {SENT, SENT, SENT, SENT};
    for (int n = n0 + j; n < n1; n += 56) {
        const int s = min((n * 3277) >> 16, 63), i = n - 20 * s;
        u32x4 v = sv;
        if (s == 0 && i >= 16 && (i < 18 || (i == 18 && tid < 128))) {
            const int f = (i - 16) * 256 + tid, ks = f >> 6, l = f & 63, r = (l & 3) | ((l >> 1) & 4), half = (l >> 2) & 1, g = l >> 4;
            v = *(const u32x4*)(p.mem0 + (r * 640 + 64 * ks + 16 * g + 8 * half) * 2);
            v.x = nosent(v.x); v.y = nosent(v.y); v.z = nosent(v.z); v.w = nosent(v.w);
        }
        st16(v, rw, tid * 16, n < 1280 ? ex_step(exo, pol, s) + (i >> 1) * 16384 + (i & 1) * 4096
                                       : (n < 1312 ? OFF_KM + 63 * 131072 + (n - 1280) * 4096 : OFF_OT + 126 * 131072 + (n - 1312) * 4096));
    }
    wait_vm<0>();
    bar();
    if (fresh_tid() == 0) add_ctr(p, word, 1u);
}

#define SB() __builtin_amdgcn_sched_barrier(0)
DI void mfma_va(f32x4& c, u32x4 a, u32x4 b) { asm volatile("v_mfma_f32_16x16x32_bf16 %0, %1, %2, %0" : "+v"(c) : "v"(a), "a"(b)); }
DI void mfma_av(f32x4& c, u32x4 a, u32x4 b) { asm volatile("v_mfma_f32_16x16x32_bf16 %0, %1, %2, %0" : "+v"(c) : "a"(a), "v"(b)); }
DI void mfma_cacc(f32x4& c, u32x4 a, u32x4 b) { asm volatile("v_mfma_f32_16x16x32_bf16 %0, %1, %2, %0" : "+a"(c) : "v"(a), "v"(b)); }
struct P1Src {
    rsrc_t r; unsigned v0, v1, so, step;
    DI void set(const Params& p, int u, int kh, int w, int lane) {
        const uchar* base; unsigned stride, col, bytes;
        if (u < 128) { base = (u & 2) ? p.wv : p.wk; stride = 8192; col = ((u >> 2) * 128 + (u & 1) * 64) * 2; bytes = 640 * 8192; }
        else if (u < 192) { base = p.wq; stride = 8192; col = (u - 128) * 128; bytes = 640 * 8192; }
        else { base = p.w1; stride = 3456; col = (u - 192) * 128; bytes = u < 219 ? 640 * 3456 : 0; }
        r = mk_rsrc(base, bytes);
        const int rl = lane >> 3, f0 = ((rl >> 1) & 1) | (((rl >> 2) & 1) << 1);
        v0 = rl * stride + (((lane & 7) ^ (2 * f0)) << 4); v1 = v0 ^ 64;
        so = (kh * 320 + 80 * w) * stride + col; step = 8 * stride;
    }
    DI void piece(int i, unsigned ldsw) const { dma16(r, (i & 1) ? v1 : v0, so + i * step, ldsw + i * 1024); }
};
DI float mish(float a, float bias) {
    const float h = a + bias;
    const float e = __builtin_amdgcn_exp2f(fminf(h, 30.f) * 1.44269504f);
    const float n = e * (e + 2.f);
    return h * n * __builtin_amdgcn_rcpf(n + 2.f);
}
#define PT(i)
constexpr int XA_ACC = 15;
template <bool ISV, int KH>
DI void p1_slab(LAS uchar* sb, const unsigned (&tb)[4], const u32x4 (&xa)[4][20], f32x4 (&acc)[4][4], const P1Src& src, unsigned ldsn) {
    u32x4 wf[2][4];
    auto rd = [&](int kk, int nt) { const u32x2 lo = trd(sb + tb[nt] + kk * 4096), hi = trd(sb + tb[nt ^ 2] + kk * 4096 + 512); return (u32x4){lo.x, lo.y, hi.x, hi.y}; };
#pragma unroll
    for (int nt = 0; nt < 4; ++nt) wf[0][nt] = rd(0, nt);
    SB();
#pragma unroll
    for (int kk = 0; kk < 10; ++kk) {
#pragma unroll
        for (int nt = 0; nt < 4; ++nt) {
            if (kk + 1 < 10) wf[(kk + 1) & 1][nt] = rd(kk + 1, nt);
            if (kk < 5 && (nt & 1)) src.piece(2 * kk + (nt >> 1), ldsn);
#pragma unroll
            for (int t = 0; t < 4; ++t) {
                const int ks = KH * 10 + kk;
                if (ISV) { if (ks < XA_ACC) mfma_av(acc[t][nt], xa[t][ks], wf[kk & 1][nt]); else mfma_v(acc[t][nt], xa[t][ks], wf[kk & 1][nt]); }
                else { if (ks < XA_ACC) mfma_va(acc[t][nt], wf[kk & 1][nt], xa[t][ks]); else mfma_v(acc[t][nt], wf[kk & 1][nt], xa[t][ks]); }
            }
            SB();
        }
    }
    asm volatile("s_nop 7" : "+v"(acc[3][3]), "+v"(acc[2][3]), "+v"(acc[1][3]), "+v"(acc[0][3]), "+v"(acc[3][2]), "+v"(acc[2][2]), "+v"(acc[1][2]), "+v"(acc[0][2]));
#pragma unroll
    for (int t = 0; t < 4; ++t) asm volatile("" : "+v"(acc[t][0]), "+v"(acc[t][1]));
}
template <bool ISV>
DI void p1_group(const Params& p, LAS uchar* lds, rsrc_t rw, unsigned lbase, const unsigned (&tb)[4], const u32x4 (&xa)[4][20], P1Src& src, int buf,
                 int chunk, int u, int un, bool first, int w, int lane, unsigned (&pt)[5]) {
    const int g = lane >> 4;
    f32x4 acc[4][4];
#pragma unroll
    for (int t = 0; t < 4; ++t)
#pragma unroll
        for (int nt = 0; nt < 4; ++nt) { acc[t][nt] = (f32x4){0.f, 0.f, 0.f, 0.f}; asm volatile("" : "+v"(acc[t][nt])); }
    PT(0);
    if (first) wait_vm<50>(); else wait_vm<10>();
    PT(1);
    bar();
    PT(2);
    src.set(p, un, 0, w, lane);
    {
        const unsigned ln = lbase + (buf == 0 ? 2 : buf - 1) * 40960;
        p1_slab<ISV, 0>(lds + buf * 40960, tb, xa, acc, src, ln);
    }
    buf = buf == 2 ? 0 : buf + 1;
    PT(3);
    wait_vm<10>();
    PT(1);
    bar();
    PT(2);
    src.set(p, un, 1, w, lane);
    {
        const unsigned ln = lbase + (buf == 0 ? 2 : buf - 1) * 40960;
        p1_slab<ISV, 1>(lds + buf * 40960, tb, xa, acc, src, ln);
    }
    PT(3);
    const int tt0 = chunk * 16 + w * 4;
    if (ISV) {
        const int h = u >> 2;
#pragma unroll
        for (int sp = 0; sp < 2; ++sp)
#pragma unroll
            for (int nt = 0; nt < 4; ++nt) {
                const f32x4 a = acc[2 * sp][nt], b = acc[2 * sp + 1][nt];
                const u32x4 o = {pk2(a[0], a[1]), pk2(a[2], a[3]), pk2(b[0], b[1]), pk2(b[2], b[3])};
                st16(o, rw, lane * 16, OFF_VF + (((tt0 >> 1) + sp) * 32 + h) * 8192 + ((u & 1) * 4 + nt) * 1024);
            }
    } else if (u < 192) {
        const int h = u < 128 ? u >> 2 : (u - 128) >> 1;
        const int ob = u < 128 ? OFF_KF : OFF_QF;
#pragma unroll
        for (int t = 0; t < 4; ++t)
#pragma unroll
            for (int m = 0; m < 2; ++m) {
                const f32x4 a = acc[t][2 * m], b = acc[t][2 * m + 1];
                const u32x4 o = {pk2(a[0], a[1]), pk2(a[2], a[3]), pk2(b[0], b[1]), pk2(b[2], b[3])};
                st16(o, rw, lane * 16, ob + (((tt0 + t) >> 1) * 32 + h) * 8192 + ((t & 1) * 4 + (u & 1) * 2 + m) * 1024);
            }
    } else {
        const int fg = u - 192;
        u32x2 bv[4];
#pragma unroll
        for (int nt = 0; nt < 4; ++nt) bv[nt] = *(const u32x2*)(p.b1 + (fg * 64 + nt * 16 + 4 * g) * 2);
#pragma unroll
        for (int t = 0; t < 4; ++t)
#pragma unroll
            for (int m = 0; m < 2; ++m) {
                const f32x4 a = acc[t][2 * m], b = acc[t][2 * m + 1];
                const u32x2 ba = bv[2 * m], bb = bv[2 * m + 1];
                const u32x4 o = {pk2(mish(a[0], bf_lo(ba.x)), mish(a[1], bf_hi(ba.x))), pk2(mish(a[2], bf_lo(ba.y)), mish(a[3], bf_hi(ba.y))),
                                 pk2(mish(b[0], bf_lo(bb.x)), mish(b[1], bf_hi(bb.x))), pk2(mish(b[2], bf_lo(bb.y)), mish(b[3], bf_hi(bb.y)))};
                st16(o, rw, lane * 16, OFF_ACT + ((tt0 + t) * 54 + fg * 2 + m) * 1024);
                SB();
            }
    }
}
DI void ld16_a(u32x4& d, rsrc_t r, unsigned v, unsigned s) { asm volatile("buffer_load_dwordx4 %0, %1, %2, %3 offen sc1" : "=a"(d) : "v"(v), "s"(r), "s"(s) : "memory"); }
DI void ld16_v(u32x4& d, rsrc_t r, unsigned v, unsigned s) { asm volatile("buffer_load_dwordx4 %0, %1, %2, %3 offen sc1" : "=v"(d) : "v"(v), "s"(r), "s"(s) : "memory"); }
DI void ld16_s(u32x4& d, rsrc_t r, unsigned v, unsigned s) { asm volatile("buffer_load_dwordx4 %0, %1, %2, %3 offen sc1" : "=v"(d) : "v"(v), "s"(r), "s"(s) : "memory"); }
DI void p1_task(const Params& p, LAS uchar* lds, int chunk, int u0, int nu) {
    const int lane = fresh_lane(), w = __builtin_amdgcn_readfirstlane(threadIdx.x >> 6), c = lane & 15, g = lane >> 4;
    const rsrc_t rw = mk_rsrc(p.ws, WS_USED);
    const unsigned lbase = (unsigned)(size_t)lds + w * 10240;
    P1Src src;
    u32x4 xa[4][20];
    src.set(p, u0, 0, w, lane);
    if (w == 0) { wait_ctr(p, rw, C_X + chunk * 64, 8); wait_ctr(p, rw, C_X + chunk * 64 + 32, 8); }
    bar();
#pragma unroll
    for (int i = 0; i < 10; ++i) src.piece(i, lbase);
#pragma unroll
    for (int ks = 0; ks < 20; ++ks) {
        if (ks == 10) {
            src.set(p, u0, 1, w, lane);
#pragma unroll
            for (int i = 0; i < 10; ++i) src.piece(i, lbase + 40960);
        }
#pragma unroll
        for (int t = 0; t < 4; ++t) {
            const unsigned so = OFF_XF + ((chunk * 16 + w * 4 + t) * 20 + ks) * 1024;
            if (ks < XA_ACC) ld16_a(xa[t][ks], rw, lane * 16, so); else ld16_v(xa[t][ks], rw, lane * 16, so);
        }
        SB();
    }
    const int q = c >> 2, pp = c & 3, sw = (q >> 1) | ((g & 1) << 1);
    unsigned tb[4];
#pragma unroll
    for (int nt = 0; nt < 4; ++nt) tb[nt] = (8 * g + q) * 128 + 32 * (nt ^ sw) + 16 * (pp >> 1) + 8 * (pp & 1);
    int buf = 0;
    unsigned pt[5] = {0, 0, 0, 0, mt32()};
    for (int gi = 0; gi < nu; ++gi) {
        const int u = u0 + gi, un = gi + 1 < nu ? u + 1 : 219;
        if (u < 128 && (u & 2)) p1_group<true>(p, lds, rw, lbase, tb, xa, src, buf, chunk, u, un, gi == 0, w, lane, pt);
        else p1_group<false>(p, lds, rw, lbase, tb, xa, src, buf, chunk, u, un, gi == 0, w, lane, pt);
        buf = buf == 0 ? 2 : buf - 1;
    }
    wait_vm<0>();
    bar();
    const unsigned tid = fresh_tid();
    if (tid < (unsigned)nu) {
        const int u = u0 + tid;
        add_ctr(p, u < 128 ? C_KV + (chunk * 32 + (u >> 2)) * 16 : (u < 192 ? C_Q + (chunk * 32 + ((u - 128) >> 1)) * 16 : C_ACT + chunk * 32), 1u);
    }
}

#define KST(i)
#define AST_(i)
template <bool ISV>
DI void kv0_task(const Params& p, LAS uchar* lds, int u) {
    const int lane = fresh_lane(), w = __builtin_amdgcn_readfirstlane(threadIdx.x >> 6), c = lane & 15, g = lane >> 4;
    const rsrc_t rw = mk_rsrc(p.ws, WS_USED);
    const unsigned lbase = (unsigned)(size_t)lds + w * 10240;
    P1Src src;
    u32x4 xa[2][2][20];
    auto fetch = [&](int part, int i) {
        const unsigned so = OFF_XF + ((part * 8 + w * 2 + (i & 1)) * 20 + (i >> 1)) * 1024;
        if (part & 1) ld16_a(xa[1][i & 1][i >> 1], rw, lane * 16, so); else ld16_v(xa[0][i & 1][i >> 1], rw, lane * 16, so);
    };
#pragma unroll
    for (int kh = 0; kh < 2; ++kh) {
        src.set(p, u, kh, w, lane);
#pragma unroll
        for (int i = 0; i < 10; ++i) src.piece(i, lbase + kh * 40960);
    }
    KST(8);
    KST(9);
    if (w == 0) wait_ctr(p, rw, C_X, 8);
    bar();
    KST(10);
#pragma unroll
    for (int i = 0; i < 40; ++i) fetch(0, i);
    if (w == 0) { wait_ctr(p, rw, C_X + 32, 8); wait_ctr(p, rw, C_X + 64, 8); wait_ctr(p, rw, C_X + 96, 8); }
    const int q = c >> 2, pp = c & 3, sw = (q >> 1) | ((g & 1) << 1), h = u >> 2;
    unsigned tb[4];
#pragma unroll
    for (int nt = 0; nt < 4; ++nt) tb[nt] = (8 * g + q) * 128 + 32 * (nt ^ sw) + 16 * (pp >> 1) + 8 * (pp & 1);
    auto rd = [&](int i) {
        const int ks = i >> 2, nt = i & 3; LAS uchar* sb = lds + (ks / 10) * 40960 + (ks % 10) * 4096;
        const u32x2 lo = trd(sb + tb[nt]), hi = trd(sb + tb[nt ^ 2] + 512); return (u32x4){lo.x, lo.y, hi.x, hi.y};
    };
    static_for<4>([&](auto pc) {
        constexpr int part = pc.value, b = part & 1;
        f32x4 acc[2][4];
#pragma unroll
        for (int i = 0; i < 8; ++i) { acc[i >> 2][i & 3] = (f32x4){0.f, 0.f, 0.f, 0.f}; asm volatile("" : "+v"(acc[i >> 2][i & 3])); }
        if (part < 2) KST(part * 4 + 0);
        wait_vm<0>();
#pragma unroll
        for (int ks = 0; ks < 20; ++ks) { if (b) asm volatile("" : "+a"(xa[b][0][ks]), "+a"(xa[b][1][ks])); else asm volatile("" : "+v"(xa[b][0][ks]), "+v"(xa[b][1][ks])); }
        bar();
        if (part > 0 && fresh_tid() == 0) add_ctr(p, (b ? C_KVH : C_KV) + (((part - 1) >> 1) * 32 + h) * 16, 1u);
        if (part < 2) KST(part * 4 + 1);
        u32x4 wf[8];
#pragma unroll
        for (int i = 0; i < 8; ++i) wf[i] = rd(i);
        SB();
#pragma unroll
        for (int i = 0; i < 80; ++i) {
#pragma unroll
            for (int t = 0; t < 2; ++t) {
                if (ISV) { if (b) mfma_av(acc[t][i & 3], xa[b][t][i >> 2], wf[i & 7]); else mfma_v(acc[t][i & 3], xa[b][t][i >> 2], wf[i & 7]); }
                else { if (b) mfma_va(acc[t][i & 3], wf[i & 7], xa[b][t][i >> 2]); else mfma_v(acc[t][i & 3], wf[i & 7], xa[b][t][i >> 2]); }
            }
            if (i + 8 < 80) wf[i & 7] = rd(i + 8);
            if (part < 3 && i < 40) fetch(part + 1, i);
            SB();
        }
        asm volatile("s_nop 7" : "+v"(acc[0][0]), "+v"(acc[0][1]), "+v"(acc[0][2]), "+v"(acc[0][3]), "+v"(acc[1][0]), "+v"(acc[1][1]), "+v"(acc[1][2]), "+v"(acc[1][3]));
        if (part < 2) KST(part * 4 + 2);
        const unsigned ob = (ISV ? OFF_VF : OFF_KF) + ((part * 4 + w) * 32 + h) * 8192;
#pragma unroll
        for (int i = 0; i < 4; ++i) {
            const f32x4 a = ISV ? acc[0][i] : acc[i >> 1][2 * (i & 1)], bb = ISV ? acc[1][i] : acc[i >> 1][2 * (i & 1) + 1];
            const u32x4 o = {pk2(a[0], a[1]), pk2(a[2], a[3]), pk2(bb[0], bb[1]), pk2(bb[2], bb[3])};
            st16(o, rw, lane * 16, ob + (ISV ? (u & 1) * 4 + i : (i >> 1) * 4 + (u & 1) * 2 + (i & 1)) * 1024);
        }
        if (part < 2) KST(part * 4 + 3);
        SB();
    });
    wait_vm<0>();
    bar();
    if (fresh_tid() == 0) add_ctr(p, C_KV + (32 + h) * 16, 1u);
}

template <bool WO>
DI void gemm_task(const Params& p, LAS uchar* lds, int chunk, int cg) {
    constexpr int KS = WO ? 4 : 3, NS = WO ? 32 : 18, NP = 2 * KS, IMG = KS * 4096, NKF = KS * NS, XOFF = WO ? OFF_OT : OFF_ACT;
    const int lane = fresh_lane(), w = __builtin_amdgcn_readfirstlane(threadIdx.x >> 6), c = lane & 15, g = lane >> 4;
    const rsrc_t rw = mk_rsrc(p.ws, WS_USED), r2 = mk_rsrc(WO ? p.wo : p.w2, NKF * 32 * 1280);
    const unsigned lbase = (unsigned)(size_t)lds + (w >> 1) * IMG + (w & 1) * NP * 1024;
    const int rl = lane >> 3, f0 = ((rl >> 1) & 1) | (((rl >> 2) & 1) << 1);
    const unsigned v0 = rl * 1280 + (((lane & 7) ^ (2 * f0)) << 4), v1 = v0 ^ 64;
    const unsigned wbase = (w & 1) * NP * 8 * 1280 + cg * 256 + (w >> 1) * 128;
    auto piece = [&](int sl, int buf, int i) { dma16(r2, (i & 1) ? v1 : v0, wbase + (sl * KS * 32 + 8 * i) * 1280, lbase + buf * 2 * IMG + i * 1024); };
    const int tt0 = chunk * 16 + w * 4;
    u32x4 xb[2][KS][4];
    auto frag = [&](int par, int sl, int i) { ld16_s(xb[par][i >> 2][i & 3], rw, lane * 16, XOFF + ((tt0 + (i & 3)) * NKF + sl * KS + (i >> 2)) * 1024); };
#pragma unroll
    for (int i = 0; i < NP; ++i) piece(0, 0, i);
    if (w == 0) {
        if (WO) {
            wait_ctr(p, rw, C_G + (chunk * 5 + cg) * 32, 1);
            for (int i = 0; i < 8; ++i) wait_ctr(p, rw, C_OT + (chunk * 8 + i) * 32, 32);
        } else wait_ctr(p, rw, C_ACT + chunk * 32, 27);
    }
    bar();
#pragma unroll
    for (int i = 0; i < 4 * KS; ++i) frag(0, 0, i);
#pragma unroll
    for (int i = 0; i < NP; ++i) piece(1, 1, i);
    const int q = c >> 2, pp = c & 3, sw = (q >> 1) | (((g & 1) ^ (g >> 1)) << 1);
    unsigned tb[4];
#pragma unroll
    for (int nt = 0; nt < 4; ++nt) tb[nt] = (4 * g + q) * 128 + 32 * (nt ^ sw) + 16 * (pp >> 1) + 8 * (pp & 1);
    f32x4 acc[4][8];
#pragma unroll
    for (int t = 0; t < 4; ++t)
#pragma unroll
        for (int nt = 0; nt < 8; ++nt) { acc[t][nt] = (f32x4){0.f, 0.f, 0.f, 0.f}; asm volatile("" : "+a"(acc[t][nt])); }
    int buf = 0;
    for (int s2 = 0; s2 < NS / 2; ++s2) {
#pragma unroll
        for (int par = 0; par < 2; ++par) {
            const int sl = 2 * s2 + par;
            wait_vm<NP>();
#pragma unroll
            for (int kk = 0; kk < KS; ++kk) asm volatile("" : "+v"(xb[par][kk][0]), "+v"(xb[par][kk][1]), "+v"(xb[par][kk][2]), "+v"(xb[par][kk][3]));
            bar();
            const int sn = min(sl + 1, NS - 1), sp = min(sl + 2, NS - 1), bn = buf == 0 ? 2 : buf - 1;
            LAS uchar* sb = lds + buf * 2 * IMG;
            auto rd = [&](int i) { LAS uchar* a = sb + ((i >> 2) & 1) * IMG + tb[i & 3] + (i >> 3) * 4096; const u32x2 lo = trd(a), hi = trd(a + 2048); return (u32x4){lo.x, lo.y, hi.x, hi.y}; };
            u32x4 wf[4];
#pragma unroll
            for (int i = 0; i < 4; ++i) wf[i] = rd(i);
            SB();
#pragma unroll
            for (int i = 0; i < 8 * KS; ++i) {
                const u32x4 cur = wf[i & 3];
                mfma_cacc(acc[0][i & 7], cur, xb[par][i >> 3][0]);
                if (i < 4 * KS) frag(par ^ 1, sn, i); else if (i < 4 * KS + NP) piece(sp, bn, i - 4 * KS);
#pragma unroll
                for (int t = 1; t < 4; ++t) mfma_cacc(acc[t][i & 7], cur, xb[par][i >> 3][t]);
                if (i + 4 < 8 * KS) wf[i & 3] = rd(i + 4);
                SB();
            }
            if (par) asm volatile("s_nop 7");
            SB();
            buf = buf == 2 ? 0 : buf + 1;
        }
    }
    wait_vm<0>();
#pragma unroll
    for (int kk = 0; kk < KS; ++kk) asm volatile("" :: "v"(xb[0][kk][0]), "v"(xb[0][kk][1]), "v"(xb[0][kk][2]), "v"(xb[0][kk][3]));
    SB();
#pragma unroll
    for (int t = 0; t < 4; ++t) {
        if (WO) {
            const size_t xo = (size_t)((tt0 + t) * 16 + c) * 1280 + cg * 256 + g * 8;
            u32x4 gv[4]; u32x2 xv[8];
#pragma unroll
            for (int m = 0; m < 4; ++m) gv[m] = ld16(rw, lane * 16, OFF_GF + ((tt0 + t) * 20 + cg * 4 + m) * 1024);
#pragma unroll
            for (int nt = 0; nt < 8; ++nt) xv[nt] = *(const u32x2*)(p.x + xo + nt * 32);
#pragma unroll
            for (int nt = 0; nt < 8; ++nt) {
                const f32x4 a = acc[t][nt];
                const unsigned g0 = gv[nt >> 1][2 * (nt & 1)], g1 = gv[nt >> 1][2 * (nt & 1) + 1], s0 = pk2(a[0], a[1]), s1 = pk2(a[2], a[3]);
                const unsigned m0 = pk2(bf_lo(s0) * bf_lo(g0), bf_hi(s0) * bf_hi(g0)), m1 = pk2(bf_lo(s1) * bf_lo(g1), bf_hi(s1) * bf_hi(g1));
                *(u32x2*)(p.out + xo + nt * 32) = (u32x2){pk2(bf_lo(xv[nt].x) + bf_lo(m0), bf_hi(xv[nt].x) + bf_hi(m0)), pk2(bf_lo(xv[nt].y) + bf_lo(m1), bf_hi(xv[nt].y) + bf_hi(m1))};
            }
        } else {
#pragma unroll
            for (int m = 0; m < 4; ++m) {
                float y[8];
#pragma unroll
                for (int r = 0; r < 8; ++r) { const float a = rbf(acc[t][2 * m + (r >> 2)][r & 3]); y[r] = a * __builtin_amdgcn_rcpf(1.f + __builtin_amdgcn_exp2f(-a * 1.44269504f)); }
                const u32x4 o = {pk2(y[0], y[1]), pk2(y[2], y[3]), pk2(y[4], y[5]), pk2(y[6], y[7])};
                st16(o, rw, lane * 16, OFF_GF + ((tt0 + t) * 20 + cg * 4 + m) * 1024);
            }
        }
        SB();
    }
    wait_vm<0>();
    bar();
    if (!WO && fresh_tid() == 0) add_ctr(p, C_G + (chunk * 5 + cg) * 32, 1u);
}

#define XST(cond, idx)
constexpr float SCL = 0.08838834764831845f * 1.4426950408889634f;
DI void attn_task(const Params& p, int s, int h, bool new_chunk) {
    const int lane = fresh_lane(), c = lane & 15, g = lane >> 4;
    const rsrc_t rw = mk_rsrc(p.ws, WS_USED);
    const int sh = (s * 32 + h);
    u32x4 qf[2][4], kf[2][4], vf[8], km[4]; u32x2 vm[8];
    if (new_chunk) {
        wait_ctr(p, rw, C_Q + ((s >> 3) * 32 + h) * 16, 2);
        wait_ctr(p, rw, C_KV + ((s >> 3) * 32 + h) * 16, 4);
    }
#pragma unroll
    for (int i = 0; i < 8; ++i) qf[i >> 2][i & 3] = ld16(rw, lane * 16, OFF_QF + sh * 8192 + i * 1024);
#pragma unroll
    for (int i = 0; i < 8; ++i) kf[i >> 2][i & 3] = ld16(rw, lane * 16, OFF_KF + sh * 8192 + i * 1024);
#pragma unroll
    for (int i = 0; i < 8; ++i) vf[i] = ld16(rw, lane * 16, OFF_VF + sh * 8192 + i * 1024);
    const float NINF = -__builtin_inff();
    const f32x4 zero = {0.f, 0.f, 0.f, 0.f};
    float t[2][12], m[2];
    {
        f32x4 s0[2] = {zero, zero}, s1 = zero;
#pragma unroll
        for (int ks = 0; ks < 4; ++ks) { s0[0] = mfma32(kf[0][ks], qf[0][ks], s0[0]); s0[1] = mfma32(kf[0][ks], qf[1][ks], s0[1]); s1 = mfma32(kf[1][ks], qf[1][ks], s1); }
#pragma unroll
        for (int r = 0; r < 4; ++r) {
            const bool diag = 4 * g + r <= c;
            t[0][r] = diag ? s0[0][r] * SCL : NINF; t[1][r] = s0[1][r] * SCL; t[1][4 + r] = diag ? s1[r] * SCL : NINF;
        }
        m[0] = fmaxf(fmaxf(t[0][0], t[0][1]), fmaxf(t[0][2], t[0][3]));
        m[1] = fmaxf(fmaxf(fmaxf(t[1][0], t[1][1]), fmaxf(t[1][2], t[1][3])), fmaxf(fmaxf(t[1][4], t[1][5]), fmaxf(t[1][6], t[1][7])));
#pragma unroll
        for (int r = 0; r < 4; ++r) asm volatile("" : "+v"(t[0][r]), "+v"(t[1][r]), "+v"(t[1][4 + r]));
        asm volatile("" : "+v"(m[0]), "+v"(m[1]));
#pragma unroll
        for (int i = 0; i < 8; ++i) asm volatile("" : "+v"(vf[i]));
    }
    u32x4 one4 = {0x3F803F80u, 0x3F803F80u, 0x3F803F80u, 0x3F803F80u};
    const float z2 = g >= 2 ? 0.f : NINF;
    f32x4 s2[2] = {{z2, z2, z2, z2}, {z2, z2, z2, z2}};
    asm volatile("" : "+v"(s2[0]), "+v"(s2[1]));
    XST(s == 63, 64 + h * 4);
    wait_ctr(p, rw, C_KM + h * 32 + (s == NSEG - 1), s + 1);
    XST(s == 63, 64 + h * 4 + 1);
    for (unsigned n = SPIN_CAP;;) {
        asm volatile("" ::: "memory");
#pragma unroll
        for (int i = 0; i < 4; ++i) km[i] = ld16(rw, (g * 8 + (c & 7)) * 16, OFF_KM + sh * 4096 + i * 512);
#pragma unroll
        for (int i = 0; i < 8; ++i) vm[i] = ld8(rw, (lane & 31) * 8, OFF_KM + sh * 4096 + 2048 + i * 256);
        if (s < NSEG - 1) break;
        unsigned mx = 0;
#pragma unroll
        for (int i = 0; i < 4; ++i) mx = mx4(mx, km[i]);
#pragma unroll
        for (int i = 0; i < 8; ++i) mx = max(mx, max(vm[i].x, vm[i].y));
        if (__builtin_amdgcn_ballot_w64(mx == SENT) == 0) break;
        if (--n == 0) { set_tmo(p); break; }
    }
    SB();
    SB();
#pragma unroll
    for (int ks = 0; ks < 4; ++ks)
#pragma unroll
        for (int qt = 0; qt < 2; ++qt) mfma_v(s2[qt], km[ks], qf[qt][ks]);
    asm volatile("s_nop 7" : "+v"(s2[0]), "+v"(s2[1]));
    u32x4 p01[2], p2[2];
#pragma unroll
    for (int qt = 0; qt < 2; ++qt) {
#pragma unroll
        for (int r = 0; r < 4; ++r) t[qt][8 + r] = s2[qt][r] * SCL;
        m[qt] = fmaxf(fmaxf(m[qt], fmaxf(t[qt][8], t[qt][9])), fmaxf(t[qt][10], t[qt][11]));
    }
#pragma unroll
    for (int qt = 0; qt < 2; ++qt) m[qt] = grp_max(m[qt]);
#pragma unroll
    for (int qt = 0; qt < 2; ++qt) {
#pragma unroll
        for (int r = 0; r < 12; ++r) if (qt == 1 || r < 4 || r >= 8) t[qt][r] = __builtin_amdgcn_exp2f(t[qt][r] - m[qt]);
        p01[qt] = (u32x4){pk2(t[qt][0], t[qt][1]), pk2(t[qt][2], t[qt][3]), qt ? pk2(t[qt][4], t[qt][5]) : 0u, qt ? pk2(t[qt][6], t[qt][7]) : 0u};
        p2[qt] = (u32x4){pk2(t[qt][8], t[qt][9]), pk2(t[qt][10], t[qt][11]), 0u, 0u};
    }
    u32x4 vq[4];
#pragma unroll
    for (int i = 0; i < 4; ++i) vq[i] = (u32x4){vm[2 * i].x, vm[2 * i].y, vm[2 * i + 1].x, vm[2 * i + 1].y};
    u32x4 p2h[2] = {{0u, 0u, p2[0].x, p2[0].y}, {0u, 0u, p2[1].x, p2[1].y}};
    asm volatile("s_nop 1" : "+v"(p01[0]), "+v"(p01[1]), "+v"(p2[0]), "+v"(p2[1]), "+v"(p2h[0]), "+v"(p2h[1]), "+v"(vq[0]), "+v"(vq[1]), "+v"(vq[2]), "+v"(vq[3]), "+v"(one4));
    f32x4 l[2], o[2][8];
#pragma unroll
    for (int qt = 0; qt < 2; ++qt) mfma_0(l[qt], one4, p01[qt]);
#pragma unroll
    for (int qt = 0; qt < 2; ++qt) mfma_v(l[qt], one4, p2[qt]);
#pragma unroll
    for (int et = 0; et < 8; ++et)
#pragma unroll
        for (int qt = 0; qt < 2; ++qt) { mfma_0(o[qt][et], vf[et], p01[qt]); mfma_v(o[qt][et], vq[et >> 1], et & 1 ? p2h[qt] : p2[qt]); }
    asm volatile("s_nop 3" : "+v"(l[0]), "+v"(l[1]), "+v"(o[0][0]), "+v"(o[0][1]), "+v"(o[1][0]), "+v"(o[1][1]));
    const float inv[2] = {__builtin_amdgcn_rcpf(l[0][0]), __builtin_amdgcn_rcpf(l[1][0])};
#pragma unroll
    for (int tp = 0; tp < 4; ++tp) {
        asm volatile("" : "+v"(o[0][2 * tp]), "+v"(o[0][2 * tp + 1]), "+v"(o[1][2 * tp]), "+v"(o[1][2 * tp + 1]));
#pragma unroll
        for (int qt = 0; qt < 2; ++qt) {
            const f32x4 a = o[qt][2 * tp] * inv[qt], b = o[qt][2 * tp + 1] * inv[qt];
            const u32x4 ov = {nosent(pk2(a[0], a[1])), nosent(pk2(a[2], a[3])), nosent(pk2(b[0], b[1])), nosent(pk2(b[2], b[3]))};
            st16(ov, rw, lane * 16, OFF_OT + ((2 * s + qt) * 32 + h) * 4096 + tp * 1024);
        }
        SB();
    }
    if (s == NSEG - 1) st4(1u, rw, lane == 0 ? (C_OF + (h >> 3) * 32 + (h & 7)) * 4 : OOB, 0);
    XST(s == 63, 64 + h * 4 + 2);
    wait_vm<0>();
    XST(s == 63, 64 + h * 4 + 3);
    if (lane == 0) add_ctr(p, C_OT + s * 32, 1u);
}

constexpr int N_T = 24, N_WG = 4, W_FIRST = 64;
DI void wo_stage(const Params& p, unsigned ldsw, int h0, int cs, int lane) {
    const rsrc_t ro = mk_rsrc(p.wo, 4096 * 1280);
    const unsigned v = (lane >> 2) * 1280 + (lane & 3) * 16;
#pragma unroll
    for (int i = 0; i < 16; ++i) dma16(ro, v, (h0 * 128 + 16 * i) * 1280 + cs * 64, ldsw + i * 1024);
}
DI void w_role(const Params& p, LAS uchar* lds, int cs, int rank) {
    const int lane = fresh_lane(), w = __builtin_amdgcn_readfirstlane(threadIdx.x >> 6), c = lane & 15, g = lane >> 4;
    const rsrc_t rw = mk_rsrc(p.ws, WS_USED);
    u32x4 wf[8][4][2];
    {
        LAS uchar* lw = lds + w * 32768;
        const int q = c >> 2, pp = c & 3;
        wo_stage(p, (unsigned)(size_t)lw, 8 * w, cs, lane);
        wo_stage(p, (unsigned)(size_t)lw + 16384, 8 * w + 2, cs, lane);
#pragma unroll
        for (int rd = 0; rd < 4; ++rd) {
            if (rd < 3) wait_vm<16>(); else wait_vm<0>();
#pragma unroll
            for (int i = 0; i < 2; ++i)
#pragma unroll
                for (int tp = 0; tp < 4; ++tp)
#pragma unroll
                    for (int nt = 0; nt < 2; ++nt) {
                        LAS uchar* a = lw + (rd & 1) * 16384 + i * 8192 + (32 * tp + 4 * g + q) * 64 + nt * 32 + pp * 8;
                        const u32x2 lo = trd(a), hi = trd(a + 1024);
                        wf[2 * rd + i][tp][nt] = (u32x4){lo.x, lo.y, hi.x, hi.y};
                        if (2 * rd + i < 7) asm volatile("" : "+a"(wf[2 * rd + i][tp][nt])); else asm volatile("" : "+v"(wf[2 * rd + i][tp][nt]));
                    }
            asm volatile("s_waitcnt lgkmcnt(0)" ::: "memory");
            SB();
            if (rd < 2) wo_stage(p, (unsigned)(size_t)lw + (rd & 1) * 16384, 8 * w + 2 * rd + 4, cs, lane);
        }
    }
    bar();
    LAS f32x2* red = (LAS f32x2*)lds;
    volatile LAS int* nxt = (volatile LAS int*)(lds + 16384);
    const rsrc_t rx = mk_rsrc(p.x, S_ * 1280), ro = mk_rsrc(p.out, S_ * 1280);
    unsigned tk = 0;
    const unsigned wto = lane == 0 ? (C_WT + cs * 32) * 4 : OOB;
    auto draw = [&]() { tk = 1; asm volatile("buffer_atomic_add %0, %1, %2, 0 offen sc0" : "+v"(tk) : "v"(wto), "s"(rw) : "memory"); };
    auto tile_of = [&](int ticket, int cur) { return ticket + W_FIRST < 120 ? ticket + W_FIRST : (cur < 120 ? 120 + rank : (cur < 124 ? (rank < 2 ? 126 : 122) + rank : 128)); };
    u32x4 ob[8][4];
    int tt = 0;
    bool have = false, careful = false;
    if (w == 0) { draw(); asm volatile("s_waitcnt vmcnt(0)" : "+v"(tk)); *nxt = tile_of(__builtin_amdgcn_readfirstlane(tk), 0); draw(); }
    bar();
    tt = __builtin_amdgcn_readfirstlane(*nxt);
    while (tt < 2 * NSEG) {
        if (!have) {
            if (w == 0) { wait_ctr(p, rw, C_G + ((tt >> 4) * 5 + (cs >> 2)) * 32, 1); if (tt < 126 || careful) wait_ctr(p, rw, C_OT + (tt >> 1) * 32, 32); }
            bar();
            if (tt >= 126 && !careful) {
                for (unsigned n = SPIN_CAP;;) {
                    asm volatile("" ::: "memory");
                    if (__builtin_amdgcn_ballot_w64(ld4(rw, (lane & 7) * 4, (C_OF + w * 32) * 4) == 0) == 0) break;
                    if (--n == 0) { set_tmo(p); break; }
                }
            }
            XST(w == 0 && tt >= 126, 256 + ((tt - 126) * 20 + cs) * 4);
#pragma unroll
            for (int i = 0; i < 32; ++i) ld16_s(ob[i >> 2][i & 3], rw, lane * 16, OFF_OT + (tt * 32 + 8 * w) * 4096 + i * 1024);
        }
        f32x4 acc[2][2];
#pragma unroll
        for (int i = 0; i < 4; ++i) { acc[i >> 1][i & 1] = (f32x4){0.f, 0.f, 0.f, 0.f}; asm volatile("" : "+v"(acc[i >> 1][i & 1])); }
        const unsigned xo = c * 1280 + g * 8, xs = tt * 16 * 1280 + cs * 64 + (w >> 1) * 32 + (w & 1) * 4;
        unsigned xv, gv;
        asm volatile("buffer_load_dword %0, %1, %2, %3 offen" : "=v"(xv) : "v"(xo), "s"(rx), "s"(xs) : "memory");
        asm volatile("buffer_load_dword %0, %1, %2, %3 offen sc1" : "=v"(gv) : "v"(lane * 16), "s"(rw), "s"(OFF_GF + (tt * 20 + cs) * 1024 + w * 4) : "memory");
        if (w == 0) {
            asm volatile("s_waitcnt vmcnt(32)" : "+v"(tk));
            const int tn = tile_of(__builtin_amdgcn_readfirstlane(tk), tt);
            int rd = 0;
            if (tn < 126) {
                const unsigned f = ld4(rw, (lane == 0 ? C_G + ((tn >> 4) * 5 + (cs >> 2)) * 32 : C_OT + (tn >> 1) * 32) * 4, 0);
                rd = __builtin_amdgcn_ballot_w64(f < (lane == 0 ? 1u : 32u)) == 0 ? 0x10000 : 0;
            }
            nxt[1] = tn | rd;
        }
        bar();
        XST(w == 0 && tt >= 126, 256 + ((tt - 126) * 20 + cs) * 4 + 1);
        const int nx = __builtin_amdgcn_readfirstlane(nxt[1]);
        have = (nx >> 16) != 0;
        const rsrc_t rn = mk_rsrc(p.ws, have ? WS_USED : 0);
        const unsigned nb = OFF_OT + ((nx & 127) * 32 + 8 * w) * 4096;
        if (w == 0) draw();
#pragma unroll
        for (int i = 0; i < 8; ++i) {
            asm volatile("s_waitcnt vmcnt(28)" : "+v"(ob[i][0]), "+v"(ob[i][1]), "+v"(ob[i][2]), "+v"(ob[i][3]));
#pragma unroll
            for (int tp = 0; tp < 4; ++tp)
#pragma unroll
                for (int n = 0; n < 2; ++n) { if (i < 7) mfma_av(acc[tp & 1][n], wf[i][tp][n], ob[i][tp]); else mfma_v(acc[tp & 1][n], wf[i][tp][n], ob[i][tp]); }
#pragma unroll
            for (int tp = 0; tp < 4; ++tp) ld16_s(ob[i][tp], rn, lane * 16, nb + (i * 4 + tp) * 1024);
            SB();
        }
        mfma_pad(acc[0][0], acc[0][1], acc[1][0], acc[1][1]);
#pragma unroll
        for (int n = 0; n < 2; ++n) {
            const f32x4 a = acc[0][n] + acc[1][n];
            red[((2 * n) * 4 + w) * 64 + lane] = (f32x2){a[0], a[1]};
            red[((2 * n + 1) * 4 + w) * 64 + lane] = (f32x2){a[2], a[3]};
        }
        bar();
        const f32x2 r = (red[(w * 4 + 0) * 64 + lane] + red[(w * 4 + 1) * 64 + lane]) + (red[(w * 4 + 2) * 64 + lane] + red[(w * 4 + 3) * 64 + lane]);
        asm volatile("s_waitcnt vmcnt(32)" : "+v"(xv), "+v"(gv));
        const bool redo = tt >= 126 && !careful && __builtin_amdgcn_ballot_w64(r[0] != r[0]) != 0;
        const unsigned sv = pk2(r[0], r[1]), m = pk2(bf_lo(sv) * bf_lo(gv), bf_hi(sv) * bf_hi(gv));
        __builtin_amdgcn_raw_buffer_store_b32(pk2(bf_lo(xv) + bf_lo(m), bf_hi(xv) + bf_hi(m)), ro, xo, xs, 0);
        XST(w == 0 && tt >= 126, 256 + ((tt - 126) * 20 + cs) * 4 + 2);
        if (!have) {
            wait_vm<0>();
#pragma unroll
            for (int i = 0; i < 8; ++i) asm volatile("" : "+v"(ob[i][0]), "+v"(ob[i][1]), "+v"(ob[i][2]), "+v"(ob[i][3]));
        }
        careful = redo;
        if (!redo) tt = nx & 0xFFFF;
    }
}

DI void smf_x(f32x4& c, u32x4 a, u32x8 b, int idx, bool acc) { if (acc) smf_a(c, a, b, idx); else smf_v(c, a, b, idx); }
#define TRY()
#define V_A 80
#define V_M 52
#define KM_DELAY 240
#define PAD0 0
#define PAD_UP 0
#define PAD_MAX 48
#define RISE 64
#define DECAY 8
#ifndef M_DELAY
#define M_DELAY 100
#endif
#define L_SMALL 96
#define F_SMALL 280
#ifndef M_TAIL
#define M_TAIL 40
#endif
#define D_MST 300
#define F_LATE 1000
#define V_LATE 8
#define DUR_A0 980
#define DUR_M0 700
#define FT(i)
#define FDUMP(blk, s, w)
#define STAT(blk, s, w, a, b, c, d)
DI int wave_max(int d) {
    d = max(d, __builtin_amdgcn_update_dpp(0, d, 0x121, 0xf, 0xf, true));
    d = max(d, __builtin_amdgcn_update_dpp(0, d, 0x122, 0xf, 0xf, true));
    d = max(d, __builtin_amdgcn_update_dpp(0, d, 0x124, 0xf, 0xf, true));
    d = max(d, __builtin_amdgcn_update_dpp(0, d, 0x128, 0xf, 0xf, true));
    auto r = __builtin_amdgcn_permlane32_swap((unsigned)d, (unsigned)d, false, false); d = max((int)r[0], (int)r[1]);
    auto q = __builtin_amdgcn_permlane16_swap((unsigned)d, (unsigned)d, false, false); d = max((int)q[0], (int)q[1]);
    return __builtin_amdgcn_readfirstlane(d);
}
struct Clock {
    unsigned r0, m0, cq, qc;
    DI void edge(unsigned& r, unsigned& m) {
        unsigned long long a, b;
        const unsigned first = rt32();
        do asm volatile("s_memrealtime %0\n\ts_memtime %1\n\ts_waitcnt lgkmcnt(0)" : "=s"(a), "=s"(b) :: "memory"); while ((unsigned)a == first);
        r = (unsigned)a << 3; m = (unsigned)b - 24;
    }
    DI void start() { edge(r0, m0); cq = 3000; qc = 22370; }
    DI void sync() {
        unsigned r, m; edge(r, m);
        const float f = (float)(m - m0) * __builtin_amdgcn_rcpf((float)(r - r0));
        asm volatile("" : "+s"(r), "+s"(m));
        cq = __builtin_amdgcn_readfirstlane((unsigned)(f * 1024.f));
        qc = __builtin_amdgcn_readfirstlane((unsigned)(65536.f * __builtin_amdgcn_rcpf(f)));
        r0 = r; m0 = m;
    }
    DI unsigned at(unsigned m) const { return r0 + (unsigned)(((unsigned long long)(m - m0) * qc) >> 16); }
    DI static void sample(unsigned long long& a, unsigned long long& b) { asm volatile("s_memrealtime %0\n\ts_memtime %1\n\ts_waitcnt lgkmcnt(0)" : "=s"(a), "=s"(b) :: "memory"); }
    DI unsigned value(unsigned long long a, unsigned long long b) const {
        const unsigned lo = (unsigned)a << 3;
        return lo + __builtin_amdgcn_readfirstlane(min(max((int)(at((unsigned)b) - lo), 0), 31));
    }
    DI unsigned now() const { unsigned long long a, b; sample(a, b); return value(a, b); }
    DI unsigned cycles(unsigned t) const { const int d = __builtin_amdgcn_readfirstlane(min(max((int)(t - r0), 0), 1 << 20)); return m0 + (((unsigned)d * cq) >> 10) - 26; }
};
DI unsigned waitmt(unsigned t) { unsigned n; do n = mt32(); while ((int)(n - t) < 0); return n; }
#define MST_SH 2
DI unsigned mst_off(int m) { return (m >> MST_SH) * 128 + (m & ((1 << MST_SH) - 1)) * 4; }
struct Sched {
    unsigned tgo, tau, durA, durM, pad;
    unsigned a, v;
    DI void fetch_a(rsrc_t rw, unsigned exo, unsigned pol, int s, int lane) { a = ld4(rw, s >= 2 ? (lane & 31) * 4 : OOB, ex_step(exo, pol, max(s - 2, 0)) + EX_AST); }
    DI void fetch_m(rsrc_t rw, unsigned exo, unsigned pol, int s, int lane) { v = ld4(rw, mst_off(min(lane, 39)), ex_step(exo, pol, s - 1) + EX_MST); }
    DI void take_a() { const int m = wave_max((int)a); durA += __builtin_amdgcn_ballot_w64(a - 1u >= 0xFFFFu) == 0 ? (min(m - (int)durA, RISE) + 2) >> 2 : 0; }
    DI void next(const Params& p, rsrc_t rw, unsigned exo, unsigned pol, int s, int lane, unsigned long long& alive) {
        if ((__builtin_amdgcn_ballot_w64(v == SENT) & alive) != 0) {
            const unsigned mo = ex_step(exo, pol, s - 1) + EX_MST;
            unsigned n = SPIN_CAP; bool bad;
#pragma nounroll
            do {
                asm volatile("" ::: "memory");
                v = ld4(rw, mst_off(min(lane, 39)), mo);
                bad = __builtin_amdgcn_ballot_w64(v == SENT) != 0;
            } while (bad && --n);
            if (bad) { alive = 0; set_tmo(p); }
        }
        const unsigned ref = __builtin_amdgcn_readfirstlane(v);
        const unsigned pm = ref + (unsigned)wave_max((int)(v - ref));
        if (s >= 2 && __builtin_amdgcn_ballot_w64(v & 1) == 0) durM += (min((int)(pm - tau - durM), RISE) + 2) >> 2;
        const bool late = s == 1 || (int)(pm + V_LATE - tgo) > 0;
        pad = late || __builtin_amdgcn_ballot_w64(v & 1) != 0 ? min(pad + PAD_UP, (unsigned)PAD_MAX) : (pad > 4 ? pad - 4 : 0);
        tau = (late ? pm + F_LATE : tgo) + durA + pad;
        tgo = tau + durM + V_A + pad;
    }
};

#define PACE 8
DI void pace() { asm volatile("s_nop %0" :: "n"(PACE)); }
DI void role_M(const Params& p, LAS uchar* lds, int mi) {
    const int tid = threadIdx.x, lane = tid & 63, w = __builtin_amdgcn_readfirstlane(tid >> 6), c = lane & 15, g = lane >> 4;
    const int qh = mi & 1, cs = mi >> 1;
    const rsrc_t rw = mk_rsrc(p.ws, WS_USED);
    u32x8 wf[8][2][2];
    waitrt(rt32() + M_DELAY);
    {
        LAS uchar* lw = lds + w * 32768;
        const int q = c >> 2, pp = c & 3;
        unsigned ra[4];
#pragma unroll
        for (int m = 0; m < 4; ++m) {
            const int k0 = m < 2 ? 8 * g + 4 * m : 32 + 8 * g + 4 * (m - 2), ga = k0 >> 4, qq = (k0 >> 2) & 3;
            const int e = 32 * ga + 2 * qq + (q & 1) + 8 * (q >> 1);
            ra[m] = e * 64 + pp * 8;
        }
        wo_stage(p, (unsigned)(size_t)lw, 8 * w, cs, lane);
        wo_stage(p, (unsigned)(size_t)lw + 16384, 8 * w + 2, cs, lane);
#pragma unroll
        for (int rd = 0; rd < 4; ++rd) {
            if (rd < 3) wait_vm<16>(); else wait_vm<0>();
#pragma unroll
            for (int i = 0; i < 2; ++i)
#pragma unroll
                for (int sel = 0; sel < 2; ++sel)
#pragma unroll
                    for (int nt = 0; nt < 2; ++nt) {
                        LAS uchar* a = lw + (rd & 1) * 16384 + i * 8192 + sel * 1024 + nt * 32;
                        const u32x2 t0 = trd(a + ra[0]), t1 = trd(a + ra[1]), t2 = trd(a + ra[2]), t3 = trd(a + ra[3]);
                        wf[2 * rd + i][sel][nt] = (u32x8){t0.x, t0.y, t1.x, t1.y, t2.x, t2.y, t3.x, t3.y};
                        if (2 * rd + i < 6) asm volatile("" : "+a"(wf[2 * rd + i][sel][nt])); else asm volatile("" : "+v"(wf[2 * rd + i][sel][nt]));
                    }
            asm volatile("s_waitcnt lgkmcnt(0)" ::: "memory");
            SB();
            if (rd < 2) wo_stage(p, (unsigned)(size_t)lw + (rd & 1) * 16384, 8 * w + 2 * rd + 4, cs, lane);
        }
    }
    bar();
    if (tid < 2) *(LAS unsigned*)(lds + 4096 + tid * 4) = 0;
    bar();
    const int idx = (c & 4) ? 0xEEEE : 0x4444;
    const int dl = 32 * cs + 2 * c;
    const unsigned so = mf_off(dl >> 6) + (((dl >> 4) & 3) * 16 + qh * 8 + ((dl >> 3) & 1) * 4 + g) * 16 + (dl & 7) * 2;
    const unsigned rwo = (w * 128 + (2 * (g & 1)) * 32 + 16 * (g >> 1) + c) * 4, rro = (g * 32 + 2 * c) * 4;
    const unsigned lo16 = lane * 16, sto = lane == 0 ? mst_off(mi) : OOB;
    unsigned long long alive = ~0ull;
    unsigned budget = SPIN_CAP;
    const unsigned exo = ex_off(p), pol = read_pol(p);
    wait_ctr(p, rw, C_PZ, 56);
    Clock ck; ck.start();
    unsigned tdone = ck.now();
    Sched sc = {0u, tdone - V_M, DUR_A0, DUR_M0, PAD0, 0u, 0u};
    for (int s = 0; s < NSEG - 1; ++s) {
        const unsigned eb = ex_step(exo, pol, s);
        const unsigned ob = eb + w * 32768 + qh * 1024;
        if (s == 3) wait_ctr(p, rw, C_PZ + 32, 56);
        if (s > 0) {
            sc.fetch_a(rw, exo, pol, s, lane);
            ck.sync();
            sc.take_a();
            SB();
            waitmt(ck.cycles(tdone + D_MST));
            sc.fetch_m(rw, exo, pol, s, lane);
            sc.next(p, rw, exo, pol, s, lane, alive);
        }
        const unsigned tau = sc.tau;
        unsigned obs[8], mb = ex_step(exo, pol, s + 1);
#pragma unroll
        for (int i = 0; i < 8; ++i) { obs[i] = ob + (i >> 2) * 16384 + (i & 3) * 2048; asm volatile("" : "+s"(obs[i])); }
        unsigned rwa = rwo + (s & 1) * 2048, rra = rro + (s & 1) * 2048;
        f32x4 acc[2][2];
#pragma unroll
        for (int i = 0; i < 4; ++i) { acc[i >> 1][i & 1] = (f32x4){0.f, 0.f, 0.f, 0.f}; asm volatile("" : "+v"(acc[i >> 1][i & 1])); }
        unsigned tw = ck.cycles(tau + V_M);
        asm volatile("" : "+s"(mb), "+v"(rwa), "+v"(rra), "+s"(tw));
        waitmt(tw);
        FT(0);
        u32x4 of[8];
#pragma unroll
        for (int i = 0; i < 8; ++i) { of[i] = ld16(rw, lo16, obs[i]); SB(); if (i < 7) pace(); SB(); }
        auto mix = [&]() {
#pragma unroll
            for (int i = 0; i < 8; ++i) {
                smf_x(acc[0][1], of[i], wf[i][0][1], idx, i < 6);
                smf_x(acc[1][0], of[i], wf[i][1][0], idx, i < 6);
                smf_x(acc[0][0], of[i], wf[i][0][0], idx, i < 6);
                smf_x(acc[1][1], of[i], wf[i][1][1], idx, i < 6);
                if (i == 0) FT(1);
                if (i == 7) FT(2);
                SB();
            }
            asm volatile("s_nop 2" : "+v"(acc[0][1]), "+v"(acc[1][0]));
            float u[4], v[4], y[4];
#pragma unroll
            for (int r = 0; r < 4; ++r) {
                const float fa = acc[1][0][r], fb = acc[0][1][r];
                auto q = __builtin_amdgcn_permlane32_swap(__builtin_bit_cast(unsigned, fa), __builtin_bit_cast(unsigned, fb), false, false);
                u[r] = __builtin_bit_cast(float, (unsigned)q[0]); v[r] = __builtin_bit_cast(float, (unsigned)q[1]);
            }
            SB();
            asm volatile("s_nop 0" : "+v"(acc[0][0]), "+v"(acc[1][1]));
#pragma unroll
            for (int r = 0; r < 4; ++r) { float lo = acc[0][0][r] + v[r], hi = acc[1][1][r] + u[r]; asm("" : "+v"(lo), "+v"(hi)); y[r] = g < 2 ? lo : hi; }
            const float z0 = swap16_add(y[0], y[2]), z1 = swap16_add(y[1], y[3]);
            *(LAS float*)(lds + rwa) = z0;
            *(LAS float*)(lds + rwa + 128) = z1;
            SB();
            return __builtin_amdgcn_ballot_w64(__builtin_isunordered(z0, z1));
        };
        if (__builtin_expect(mix() != 0, 0)) {
            if (refetch<8>(of, rw, lo16, obs, budget)) {
                TRY();
                *(LAS unsigned*)(lds + 4096 + (s & 1) * 4) = s + 1;
#pragma unroll
                for (int i = 0; i < 4; ++i) { acc[i >> 1][i & 1] = (f32x4){0.f, 0.f, 0.f, 0.f}; asm volatile("" : "+v"(acc[i >> 1][i & 1])); }
                asm volatile("s_nop 1" ::: "memory");
                mix();
            }
        }
        FT(3);
        bar();
        FT(4);
        if (__builtin_expect(w == 0, 1)) {
            LAS f32x2* r2 = (LAS f32x2*)(lds + rra);
            const f32x2 a0 = r2[0], a1 = r2[64], a2 = r2[128], a3 = r2[192];
            const unsigned fl = *(LAS unsigned*)(lds + 4096 + (s & 1) * 4);
            SB();
            unsigned long long ca, cb;
            Clock::sample(ca, cb);
            SB();
            const f32x2 v = (a0 + a1) + (a2 + a3);
            st4(nosent(pk2(v[0], v[1])), rw, so, mb);
            SB();
            tdone = ck.value(ca, cb) + M_TAIL;
            st4(nosent((tdone & ~1u) | (fl == (unsigned)(s + 1))), rw, sto, eb + EX_MST);
        } else tdone = ck.now() + M_TAIL;
        if (budget == 0) set_tmo(p);
        FT(5); FDUMP(32 + mi, s, w);
    }
}

constexpr int RING = 4, VPRE = 4;
constexpr int a_slot(int ks, int tile) { return ks * 16384 + tile * 2048; }
constexpr int a_xblk(int i) { return a_slot(i & 1 ? 9 : 4, i & 2 ? 5 : 2); }
constexpr int A_P = a_slot(4, 7), A_P2 = A_P + 1024, A_CNT = A_P2 + 512;
template <int W> struct AP {
    static constexpr int ROT = 3 * W, NT = W == 0 ? 0 : (W == 3 ? 2 : 3), T0 = W == 0 ? 0 : 3 * (W - 1);
    static constexpr int NL = W == 0 ? 0 : (W == 3 ? 9 : 14);
    static constexpr int QA = W == 0 || W == 3 ? 20 : 15;
    static constexpr bool k_lds(int t, int m) { return W == 0 ? false : (W == 3 ? (t == 1 && m >= 1) : (t == 1 || (m > 0 && (m & 1) == 0))); }
    static constexpr bool k_acc(int t, int m) { return W == 0 && m < 5; }
    static constexpr bool v_lds(int t, int ks) { return W == 3 ? ks < 4 : (ks < 4 || (ks == 4 && t == 0)); }
    static constexpr int k_idx(int t, int m) { int n = 0; for (int i = 0; i < 2 * m + 1 - t; ++i) n += k_lds(1 - (i & 1), i >> 1) ? 1 : 0; return n; }
    static constexpr int k_slot(int i) { return a_slot(5 + i / (NT ? NT : 1), T0 + i % (NT ? NT : 1)); }
    static constexpr int v_slot(int i) { return a_slot(i / (NT ? NT : 1), T0 + i % (NT ? NT : 1)); }
    static constexpr int stream(int i) { return i < NL ? k_slot(i) : v_slot(i - NL); }
    static constexpr int m_of(int ks) { return (ks + 10 - ROT % 10) % 10; }
};
DI void a_dma(const uchar* wsrc, LAS uchar* lds, int h, int w, int half) {
    const int lane = fresh_lane();
    const rsrc_t r = mk_rsrc(wsrc, 640 * 8192);
    const unsigned v = (lane >> 4) * 8192 + (((lane & 15) ^ (2 * (((lane >> 4) & 1) | (w << 1)))) << 4);
    const unsigned lb = (unsigned)(size_t)lds;
#pragma unroll
    for (int i = 20 * half; i < 20 * half + 20; ++i) dma16(r, v, (4 * (w + 4 * i)) * 8192 + h * 256, lb + (w + 4 * i) * 1024);
}
struct ATake {
    unsigned row[4], f[4];
    DI ATake() {
        const int lane = fresh_lane(), c = lane & 15, g = lane >> 4, q = c >> 2, pp = c & 3;
#pragma unroll
        for (int m = 0; m < 4; ++m) {
            const int k0 = m < 2 ? 8 * g + 4 * m : 32 + 8 * g + 4 * (m - 2), ga = k0 >> 4, qq = (k0 >> 2) & 3;
            const int r = 16 * ga + 2 * qq + (q & 1) + 8 * (q >> 1);
            row[m] = r * 256 + pp * 8; f[m] = 32 * ((r & 1) | ((r >> 1) & 6));
        }
    }
    DI u32x8 get(LAS uchar* lds, int ks, int tile) const {
        LAS uchar* a = lds + ks * 16384;
        const u32x2 t0 = trd(a + row[0] + (f[0] ^ (32 * tile))), t1 = trd(a + row[1] + (f[1] ^ (32 * tile))), t2 = trd(a + row[2] + (f[2] ^ (32 * tile))), t3 = trd(a + row[3] + (f[3] ^ (32 * tile)));
        return (u32x8){t0.x, t0.y, t1.x, t1.y, t2.x, t2.y, t3.x, t3.y};
    }
};
DI u32x8 ldsB(LAS uchar* a) { const u32x4 lo = *(LAS u32x4*)a, hi = *(LAS u32x4*)(a + 1024); return (u32x8){lo.x, lo.y, lo.z, lo.w, hi.x, hi.y, hi.z, hi.w}; }
DI void ldsB_put(LAS uchar* a, u32x8 v) { *(LAS u32x4*)a = (u32x4){v[0], v[1], v[2], v[3]}; *(LAS u32x4*)(a + 1024) = (u32x4){v[4], v[5], v[6], v[7]}; }
DI void pin(u32x8& v, bool acc) { if (acc) asm volatile("" : "+a"(v)); else asm volatile("" : "+v"(v)); }

#define NEXT_KV() do { SB(); const int sn_ = min(s + 1, NSEG - 1); kvn = ld4(rw, 0, kvw * 4); \
    _Pragma("unroll") for (int i = 0; i < NPF; ++i) pf[i] = ld16(rw, lo16, pfb + sn_ * 32 * 8192 + i * 1024); SB(); } while (0)
#define KM_OUT() do { unsigned n_ = 0; if (lane == 0) n_ = __hip_atomic_fetch_add((LAS unsigned*)(lds + A_CNT), 1u, __ATOMIC_RELAXED, __HIP_MEMORY_SCOPE_WORKGROUP); \
    if ((__builtin_amdgcn_readfirstlane(n_) & 3) == 3) st4(s + 1, rw, lane == 0 ? (C_KM + h * 32 + 1) * 4 : OOB, 0); } while (0)
template <int W>
DI void a_loop(const Params& p, LAS uchar* lds, int h, const u32x8 (&wq)[2][10], const u32x8 (&wk)[2][10], const u32x8 (&wv)[3][10]) {
    typedef AP<W> P;
    const int lane = fresh_lane(), c = lane & 15, g = lane >> 4;
    const rsrc_t rw = mk_rsrc(p.ws, WS_USED);
    constexpr int ROT = P::ROT, NT = P::NT, T0 = P::T0, NPF = W == 0 ? 8 : NT, NL = P::NL;
    const int idx = (c & 8) ? 0xEEEE : 0x4444;
    const unsigned lo16 = lane * 16, mo16 = (g * 16 + ((c >> 2) & 1) * 8 + (c >> 3) * 4 + (c & 3)) * 16;
    const unsigned oaddr = W == 0 ? (c >= 8 ? (g * 8 + (c & 7)) * 16 : OOB) : (c < 8 ? (c >> 2) * 1024 + (g >> 1) * 64 + (c & 3) * 16 + (g & 1) * 8 : OOB);
    const unsigned oaddr2 = c < 8 ? (c >> 2) * 1024 + (g & 1) * 128 + (g >> 1) * 64 + (c & 3) * 16 : OOB;
    const unsigned xw = a_xblk(W) + c * 40 + g * 8;
    const unsigned asto = lane == 0 ? h * 4 : OOB;
    const unsigned pfb = W == 0 ? OFF_KF + h * 8192 : OFF_VF + h * 8192 + T0 * 1024;
    u32x4 pf[NPF];
    unsigned long long alive = ~0ull;
    unsigned budget = SPIN_CAP;
    const unsigned exo = ex_off(p), pol = read_pol(p);
    Clock ck; ck.start();
    wait_ctr(p, rw, C_PZ, 56);
    wait_ctr(p, rw, C_KVH + h * 16, 4);
#pragma unroll
    for (int i = 0; i < NPF; ++i) pf[i] = ld16(rw, lo16, pfb + i * 1024);
    Sched sc = {ck.now(), 0u, DUR_A0, DUR_M0, PAD0, 0u, 0u};
    const float ninf = g < 2 ? -__builtin_inff() : 0.f;
    unsigned xra[4], xqa[4];
    if (W == 0) {
#pragma unroll
        for (int ks = 0; ks < 4; ++ks) {
            xra[ks] = a_xblk(ks) + (4 * g + (c >> 2)) * 40 + (c & 3) * 8; xqa[ks] = a_xblk(ks) + (4 * g + (c >> 2)) * 40 + (c & 1) * 8;
            asm volatile("" : "+v"(xra[ks]), "+v"(xqa[ks]));
        }
    }
    u32x4 one4 = {0x3F803F80u, 0x3F803F80u, 0x3F803F80u, 0x3F803F80u};
    u32x8 pw = {0u, 0u, 0u, 0u, 0u, 0u, 0u, 0u};
    unsigned pa[3] = {A_P + lo16, A_P2 + lane * 8u, A_P2 + (lane ^ 32) * 8u};
    if (W) asm volatile("" : "+a"(one4), "+v"(pw), "+v"(pa[0]), "+v"(pa[1]), "+v"(pa[2]));
    unsigned lb[3] = {lo16, lo16 + 65536, lo16 + 131072};
    asm volatile("" : "+v"(lb[0]), "+v"(lb[1]), "+v"(lb[2]));
    auto sget = [&](int i) -> u32x8 { const int a = P::stream(i); return ldsB(lds + lb[a >> 16] + (a & 65535)); };
    for (int s = 0; s < NSEG; ++s) {
        const unsigned eb = ex_step(exo, pol, s);
        unsigned mb[10], ob = eb + (h >> 2) * 16384 + (h & 3) * 2048 + T0 * 128;
#pragma unroll
        for (int m = 0; m < 10; ++m) { mb[m] = eb + mf_off((m + ROT) % 10); asm volatile("" : "+s"(mb[m])); }
        f32x4 aq[2], ak[2], va[3];
        u32x8 ring[RING];
#pragma unroll
        for (int t = 0; t < 2; ++t) { aq[t] = (f32x4){0.f, 0.f, 0.f, 0.f}; ak[t] = aq[t]; asm volatile("" : "+v"(aq[t]), "+v"(ak[t])); }
#pragma unroll
        for (int t = 0; t < 3; ++t) {
            const float z = W == 0 && t == 2 ? ninf : 0.f;
            va[t] = (f32x4){z, z, z, z}; asm volatile("" : "+v"(va[t]));
        }
        if (W) {
#pragma unroll
            for (int i = 0; i < RING; ++i) ring[i] = sget(i);
        }
        const unsigned tgo = sc.tgo;
        unsigned tw = ck.cycles(tgo);
        asm volatile("" : "+s"(ob), "+s"(tw));
        SB();
        unsigned mis = waitmt(tw);
        FT(0);
        u32x4 mf[10];
#pragma unroll
        for (int m = 0; m < 10; ++m) {
            mf[m] = ld16(rw, mo16, mb[m]);
            SB();
            if (m < 9) pace();
            SB();
        }
        auto project = [&]() {
            static_for<10>([&](auto mc) {
                constexpr int m = mc.value;
                auto qprod = [&](auto tc) { constexpr int t = tc.value; smf_x(aq[t], mf[m], wq[t][m], idx, 2 * m + t < P::QA - (W == 0 ? 10 : 0)); };
                auto kprod = [&](auto tc) {
                    constexpr int t = tc.value;
                    if constexpr (P::k_lds(t, m)) {
                        constexpr int i = P::k_idx(t, m);
                        smf_v(ak[t], mf[m], ring[i % RING], idx);
                        ring[i % RING] = sget(i + RING);
                    } else smf_x(ak[t], mf[m], wk[t][m], idx, W == 0);
                };
                kprod(IC<1>{});
                qprod(IC<m == 9 ? 1 : 0>{});
                if constexpr (m == 9) qprod(IC<0>{});
                kprod(IC<0>{});
                if constexpr (m < 9) qprod(IC<1>{});
                if (m == 0) FT(1);
                if (m == 9) FT(2);
                SB();
            });
            float x[4], y[4];
            asm volatile("s_nop 2" : "+v"(aq[1]), "+v"(ak[1]));
#pragma unroll
            for (int r = 0; r < 4; ++r) y[r] = swap32_add(aq[1][r], ak[1][r]);
            *(LAS u32x2*)(lds + xw + 640) = (u32x2){pk2(y[0], y[1]), pk2(y[2], y[3])};
            SB();
            asm volatile("" : "+v"(aq[0]), "+v"(ak[0]));
#pragma unroll
            for (int r = 0; r < 4; ++r) x[r] = swap32_add(aq[0][r], ak[0][r]);
            *(LAS u32x2*)(lds + xw) = (u32x2){pk2(x[0], x[1]), pk2(x[2], x[3])};
            SB();
            return nan_lanes((f32x4){x[0], x[1], x[2], x[3]});
        };
        if (__builtin_expect(project() != 0, 0)) {
            if (refetch<10>(mf, rw, mo16, mb, budget)) {
                mis = tw + 100000; TRY();
#pragma unroll
                for (int t = 0; t < 2; ++t) { aq[t] = (f32x4){0.f, 0.f, 0.f, 0.f}; ak[t] = aq[t]; asm volatile("" : "+v"(aq[t]), "+v"(ak[t])); }
                if (W) {
#pragma unroll
                    for (int i = 0; i < RING; ++i) ring[i] = sget(i);
                }
                asm volatile("s_nop 1" ::: "memory");
                project();
            }
        }
        SB();
        if (W) {
#pragma unroll
            for (int i = 0; i < VPRE; ++i) { const int r = 10 * NT - 1 - i; smf_a(va[r % NT], mf[P::m_of(r / NT)], wv[r % NT][r / NT], idx); }
        }
        bar();
        SB();
        FT(3);
        const unsigned kb = OFF_KM + (s * 32 + h) * 4096;
        const int kvs = min(s + 2, NSEG - 1), kvw = (kvs < 16 && (kvs & 4) == 0 ? C_KVH : C_KV) + ((kvs >> 3) * 32 + h) * 16;
        unsigned tpub = 0, kvn;
        if (W == 0) {
            u32x4 xf[4], xd[4];
#pragma unroll
            for (int ks = 0; ks < 4; ++ks) {
                const u32x2 a = trd(lds + xqa[ks]), b = trd(lds + xqa[ks] + 640), lo = trd(lds + xra[ks]), hi = trd(lds + xra[ks] + 640);
                xd[ks] = (u32x4){a.x, a.y, b.x, b.y}; xf[ks] = (u32x4){lo.x, lo.y, hi.x, hi.y};
            }
            SB();
            if (__builtin_expect(s == NSEG - 1, 0)) {
#pragma unroll
                for (int ks = 0; ks < 4; ++ks) st16((u32x4){nosent(xf[ks].x), nosent(xf[ks].y), nosent(xf[ks].z), nosent(xf[ks].w)}, rw, oaddr, kb + ks * 512);
                KM_OUT();
            }
#pragma unroll
            for (int ks = 0; ks < 4; ++ks) { mfma_v(va[0], pf[ks], xd[ks]); mfma_v(va[1], pf[(4 + ks) % NPF], xd[ks]); mfma_v(va[2], xf[ks], xd[ks]); }
            FT(4);
            float a0, a1, a2, a3, a4, a5, tm, tn;
            unsigned e0, e1, e2;
            asm volatile("s_nop 3\n\t"
                         "v_cndmask_b32_e64 %0, %15, %11, %23\n\tv_cndmask_b32_e64 %1, %16, %12, %23\n\tv_cndmask_b32_e64 %2, %17, %13, %23\n\t"
                         "v_cndmask_b32_e64 %3, %18, %14, %23\n\tv_cndmask_b32_e64 %4, %21, %19, %23\n\tv_cndmask_b32_e64 %5, %22, %20, %23\n\t"
                         "v_max3_f32 %6, %0, %1, %2\n\tv_max3_f32 %7, %3, %4, %5\n\tv_max_f32 %6, %6, %7\n\ts_nop 1\n\t"
                         "v_max_f32_dpp %6, %6, %6 row_ror:8 row_mask:0xf bank_mask:0xf\n\t"
                         "v_mov_b32 %7, %6\n\ts_nop 1\n\tv_permlane32_swap_b32 %6, %7\n\tv_max_f32 %6, %6, %7\n\t"
                         "v_mov_b32 %7, %6\n\ts_nop 1\n\tv_permlane16_swap_b32 %6, %7\n\tv_max_f32 %6, %6, %7\n\t"
                         "v_mul_f32 %6, %24, %6\n\t"
                         "v_fma_f32 %0, %0, %24, -%6\n\tv_fma_f32 %1, %1, %24, -%6\n\tv_fma_f32 %2, %2, %24, -%6\n\t"
                         "v_fma_f32 %3, %3, %24, -%6\n\tv_fma_f32 %4, %4, %24, -%6\n\tv_fma_f32 %5, %5, %24, -%6\n\t"
                         "v_exp_f32 %0, %0\n\tv_exp_f32 %1, %1\n\tv_exp_f32 %2, %2\n\tv_exp_f32 %3, %3\n\tv_exp_f32 %4, %4\n\tv_exp_f32 %5, %5\n\t"
                         "v_cvt_pk_bf16_f32 %8, %0, %1\n\tv_cvt_pk_bf16_f32 %9, %2, %3\n\tv_cvt_pk_bf16_f32 %10, %4, %5"
                         : "=&v"(a0), "=&v"(a1), "=&v"(a2), "=&v"(a3), "=&v"(a4), "=&v"(a5), "=&v"(tm), "=&v"(tn), "=&v"(e0), "=&v"(e1), "=&v"(e2)
                         : "v"(va[0][0]), "v"(va[0][1]), "v"(va[0][2]), "v"(va[0][3]), "v"(va[1][0]), "v"(va[1][1]), "v"(va[1][2]), "v"(va[1][3]),
                           "v"(va[2][0]), "v"(va[2][1]), "v"(va[2][2]), "v"(va[2][3]), "s"(0x00FF00FF00FF00FFull), "s"(SCL));
            *(LAS u32x2*)(lds + A_P + (g * 16 + (c & 7)) * 16 + (c >> 3) * 8) = (u32x2){e0, e1};
            *(LAS unsigned*)(lds + A_P2 + (g * 16 + (c & 7)) * 8 + (c >> 3) * 4) = e2;
            FT(5);
            bar();
            FT(6);
            sc.fetch_a(rw, exo, pol, max(s, 1), lane); sc.fetch_m(rw, exo, pol, max(s, 1), lane);
            NEXT_KV();
            if (s < NSEG - 1) waitmt(mt32() + KM_DELAY * 3);
#pragma unroll
            for (int ks = 0; ks < 4; ++ks) if (s < NSEG - 1) st16(xf[ks], rw, oaddr, kb + ks * 512);
        } else {
            if (W == 3) st4(s, rw, asto, C_KM * 4 + h * 124);
#pragma unroll
            for (int i = 0; i < (10 * NT - NL - VPRE > NL ? 10 * NT - NL - VPRE : NL); ++i) {
                if (i < NL) {
                    smf_v(va[i % NT], mf[P::m_of(i / NT)], ring[(NL + i) % RING], idx);
                    if (NL + i + RING < 2 * NL) ring[(NL + i) % RING] = sget(NL + i + RING);
                }
                const int r = NL + i;
                if (r < 10 * NT - VPRE) smf_a(va[r % NT], mf[P::m_of(r / NT)], wv[r % NT][r / NT], idx);
                SB();
            }
            asm volatile("s_nop 7" : "+v"(va[0]), "+v"(va[1]), "+v"(va[2]));
            float sa[4], sb[4];
#pragma unroll
            for (int r = 0; r < 4; ++r) { sa[r] = swap32_add(va[0][r], va[1][r]); if (W != 3) sb[r] = swap32_add(va[2][r], va[2][r]); }
            u32x4 vv = {pk2(sa[0], sa[1]), pk2(sa[2], sa[3]), 0u, 0u};
            if (W != 3) { vv.z = pk2(sb[0], sb[1]); vv.w = pk2(sb[2], sb[3]); }
            asm volatile("" : "+v"(vv));
            if (__builtin_expect(s == NSEG - 1, 0)) {
                st8((u32x2){nosent(vv.x), nosent(vv.y)}, rw, lane * 8, kb + 2048 + T0 * 256);
                if (W != 3) st8((u32x2){nosent(vv.z), nosent(vv.w)}, rw, g < 2 ? lane * 8 : OOB, kb + 2048 + T0 * 256 + 512);
                KM_OUT();
            }
            SB();
            FT(5);
            bar();
            FT(6);
            SB();
            if (s < NSEG - 1) {
                const u32x4 p01 = *(LAS u32x4*)(lds + pa[0]);
                const u32x2 q2 = *(LAS u32x2*)(lds + pa[1]), q2s = *(LAS u32x2*)(lds + pa[2]);
                pw[4] = q2.x; pw[5] = q2.y; pw[0] = q2s.x; pw[1] = q2s.y;
                const u32x4 p2s = __builtin_shufflevector(pw, pw, 0, 1, 2, 3), p2h = __builtin_shufflevector(pw, pw, 2, 3, 4, 5), p2 = __builtin_shufflevector(pw, pw, 4, 5, 6, 7);
                SB();
                f32x4 l, o[3];
                asm volatile("v_mfma_f32_16x16x32_bf16 %0, %1, %2, 0" : "=&v"(l) : "a"(one4), "v"(p01));
                mfma_0(o[0], pf[0], p01);
                mfma_av(l, one4, p2);
                mfma_0(o[1], pf[1 % NPF], p01);
                mfma_v(o[0], vv, p2s);
                mfma_v(o[1], vv, p2);
                float inv;
                if (W != 3) {
                    mfma_0(o[2], pf[2 % NPF], p01);
                    asm volatile("v_rcp_f32 %0, %1" : "=v"(inv) : "v"(l[0]));
                    mfma_v(o[2], vv, p2h);
                    asm volatile("s_nop 3" : "+v"(o[0]), "+v"(o[1]), "+v"(inv));
                } else {
                    asm volatile("s_nop 1\n\tv_rcp_f32 %0, %3\n\ts_nop 5" : "=&v"(inv), "+v"(o[0]), "+v"(o[1]) : "v"(l[0]));
                }
                const f32x4 n0 = o[0] * inv, n1 = o[1] * inv;
                SB();
                const unsigned a0 = pk2(n0[0], n0[1]), b0 = pk2(n1[0], n1[1]), a1 = pk2(n0[2], n0[3]), b1 = pk2(n1[2], n1[3]);
                SB();
                auto r0 = __builtin_amdgcn_permlane16_swap(a0, b0, false, false);
                auto r1 = __builtin_amdgcn_permlane16_swap(a1, b1, false, false);
                st16((u32x4){nosent(r0[0]), nosent(r1[0]), nosent(r0[1]), nosent(r1[1])}, rw, oaddr2, ob);
                if (W != 3) {
                    asm volatile("" : "+v"(o[2]));
                    const f32x4 n2 = o[2] * inv;
                    st8((u32x2){nosent(pk2(n2[0], n2[1])), nosent(pk2(n2[2], n2[3]))}, rw, oaddr, ob + 256);
                }
            }
            tpub = ck.now();
            sc.fetch_a(rw, exo, pol, max(s, 1), lane); sc.fetch_m(rw, exo, pol, max(s, 1), lane);
            NEXT_KV();
            if (W == 1) st4(mis - tw <= 120u ? tpub - tgo : 0u, rw, asto, eb + EX_AST);
            if (s < NSEG - 1) st8((u32x2){vv.x, vv.y}, rw, lane * 8, kb + 2048 + T0 * 256);
            if (W != 3 && s < NSEG - 1) st8((u32x2){vv.z, vv.w}, rw, g < 2 ? lane * 8 : OOB, kb + 2048 + T0 * 256 + 512);
        }
        if (budget == 0) set_tmo(p);
        FT(7); FDUMP(h, s, W);
        STAT(h, s, W, ck.at(mis), tpub, tgo, tries);
        if (s == NSEG - 1) break;
        ck.sync();
        if (s > 0) { sc.take_a(); sc.next(p, rw, exo, pol, s, lane, alive); }
        if ((unsigned)__builtin_amdgcn_readfirstlane(kvn) < 4u) wait_ctr(p, rw, kvw, 4);
        if (s == 2) wait_ctr(p, rw, C_PZ + 32, 56);
    }
    XST(W == 0, h);
    __builtin_amdgcn_endpgm();
}

template <int W>
DI void a_wave(const Params& p, LAS uchar* lds, int h) {
    typedef AP<W> P;
    u32x8 wq[2][10], wk[2][10], wv[3][10];
    a_dma(p.wq, lds, h, W, 0);
    a_dma(p.wq, lds, h, W, 1);
    static_for<6>([&](auto sc) {
        constexpr int mat = sc.value >> 1, half = sc.value & 1;
        if (sc.value < 5) wait_vm<20>(); else wait_vm<0>();
        bar();
        AST_(sc.value);
        const ATake tk;
#pragma unroll
        for (int ks = 5 * half; ks < 5 * half + 5; ++ks) {
            const int m = P::m_of(ks);
#pragma unroll
            for (int t = 0; t < (mat < 2 ? 2 : P::NT); ++t) {
                if (mat == 0) { wq[t][m] = tk.get(lds, ks, 2 * W + t); pin(wq[t][m], 2 * m + t < P::QA - (W == 0 ? 10 : 0)); }
                if (mat == 1) { wk[t][m] = tk.get(lds, ks, 2 * W + t); pin(wk[t][m], W == 0); }
                if (mat == 2) { wv[t][ks] = tk.get(lds, ks, P::T0 + t); pin(wv[t][ks], true); }
            }
            SB();
        }
        bar(); SB();
        if (mat < 2) a_dma(mat == 0 ? p.wk : p.wv, lds, h, W, half);
        else {
            const int lane = fresh_lane();
            if (half == 0) {
#pragma unroll
                for (int i = 0; i < P::NL; ++i) ldsB_put(lds + P::v_slot(i) + lane * 16, wv[i % (P::NT ? P::NT : 1)][i / (P::NT ? P::NT : 1)]);
            } else {
#pragma unroll
                for (int i = 0; i < 20; ++i) if (P::k_lds(i & 1, i >> 1)) ldsB_put(lds + P::k_slot(P::k_idx(i & 1, i >> 1)) + lane * 16, wk[i & 1][i >> 1]);
            }
        }
        SB();
    });
    if (W == 0) *(LAS unsigned*)(lds + A_CNT) = 0;
    bar();
    SB();
    a_loop<W>(p, lds, h, wq, wk, wv);
}
DI void role_A(const Params& p, LAS uchar* lds, int h) {
    const int w = __builtin_amdgcn_readfirstlane(threadIdx.x >> 6);
    if (w == 0) a_wave<0>(p, lds, h);
    else if (w == 1) a_wave<1>(p, lds, h);
    else if (w == 2) a_wave<2>(p, lds, h);
    else a_wave<3>(p, lds, h);
}
DI int take_slot(const Params& p, LAS uchar* lds) {
    if (threadIdx.x == 0) {
        unsigned x = xcc_id(), t;
        for (int i = 0; i < 8; ++i) {
            t = __hip_atomic_fetch_add((unsigned*)p.ws + C_POOL + x * 32, 1u, __ATOMIC_RELAXED, __HIP_MEMORY_SCOPE_AGENT);
            if (t < 32) break;
            x = (x + 1) & 7;
        }
        *(LAS unsigned*)lds = x * 32 + t;
    }
    bar();
    const int slot = __builtin_amdgcn_readfirstlane(*(LAS unsigned*)lds);
    bar();
    return slot;
}
DI int take_ticket(const Params& p, LAS uchar* lds, int word) {
    if (fresh_tid() == 0) *(LAS unsigned*)(lds + LDS_BYTES - 16) = __hip_atomic_fetch_add((unsigned*)p.ws + word, 1u, __ATOMIC_RELAXED, __HIP_MEMORY_SCOPE_AGENT);
    bar();
    const int t = __builtin_amdgcn_readfirstlane(*(LAS unsigned*)(lds + LDS_BYTES - 16));
    bar();
    return t;
}
#define TSTAT(i, a, b)
#define SMALL_DELAY 300
DI void warm_l2(const Params& p, int x, int j) {
    const int lane = fresh_lane(), id = j * 4 + __builtin_amdgcn_readfirstlane(threadIdx.x >> 6);
    u32x4 sink[46];
#pragma unroll
    for (int i = 0; i < 46; ++i) {
        const int n = min(id + 56 * i, 2559), row = n >> 1;
        const rsrc_t r = mk_rsrc(row < 640 ? p.wk : p.wv, 640 * 8192);
        asm volatile("buffer_load_dwordx4 %0, %1, %2, %3 offen" : "=v"(sink[i]) : "v"(lane * 16), "s"(r), "s"((row < 640 ? row : row - 640) * 8192 + x * 2048 + (n & 1) * 1024) : "memory");
    }
    wait_vm<0>();
#pragma unroll
    for (int i = 0; i < 46; ++i) asm volatile("" :: "v"(sink[i]));
}
DI void background(const Params& p, LAS uchar* lds, int x, int j) {
    const bool big = x >= 4;
    if (!big) {
        const int id = j * 4 + x;
        ln_tile(p, id);
        poison(p, id, 0, 80, C_PZ);
        warm_l2(p, x, j);
        ln_tile(p, id + 56);
        if (id < 16) ln_tile(p, id + 112);
        poison(p, id, 80, 1376, C_PZ + 32);
    } else {
        if (j & 2) kv0_task<true>(p, lds, 32 * (x - 4) + j); else kv0_task<false>(p, lds, 32 * (x - 4) + j);
        TSTAT((x - 4) * 128 + j, t0, 0);
    }
    if (!big) return;
    const int y = x - 4;
    for (;;) {
        int t = take_ticket(p, lds, C_Q1 + x * 32), kind = 0, chunk = 0, u0 = 0, nu = 8;
        const int wu = 192 + 8 * y, wn = y < 3 ? 4 : 2;
        if (t < 8) { chunk = t >> 1; u0 = wu + wn * (t & 1); nu = y < 3 ? 4 : 2 - (t & 1); }
        else if ((t -= 8) < 8) { chunk = 2; u0 = 32 * y + 4 * t; nu = 4; }
        else if ((t -= 8) < 8) { chunk = t >> 2; u0 = 128 + 16 * y + 4 * (t & 3); nu = 4; }
        else if ((t -= 8) < 8) { chunk = 3; u0 = 32 * y + 4 * t; nu = 4; }
        else if ((t -= 8) < 8) { chunk = 4 + (t >> 2); u0 = 32 * y + 8 * (t & 3); }
        else if ((t -= 8) < 5) { kind = 1; chunk = y; u0 = t; }
        else if ((t -= 5) < 4) { chunk = 4 + t; u0 = wu; nu = y < 3 ? 8 : 3; }
        else if ((t -= 4) < 6) { kind = 3; u0 = 6 * y + t; }
        else if ((t -= 6) < 4) { chunk = 2 + (t >> 1); u0 = 128 + 16 * y + 8 * (t & 1); }
        else if ((t -= 4) < 8) { chunk = 6 + (t >> 2); u0 = 32 * y + 8 * (t & 3); }
        else if ((t -= 8) < 8) { chunk = 4 + (t >> 1); u0 = 128 + 16 * y + 8 * (t & 1); }
        else if ((t -= 8) < 5) { kind = 1; chunk = 4 + y; u0 = t; }
        else if ((t -= 5) < 5) { kind = 2; chunk = y; u0 = t; }
        else if ((t -= 5) < 5 * N_WG) { kind = 4; u0 = t; }
        else return;
        if (kind == 0) p1_task(p, lds, chunk, u0, nu);
        else if (kind == 1) gemm_task<false>(p, lds, chunk, u0);
        else if (kind == 2) gemm_task<true>(p, lds, chunk, u0);
        else {
            wait_ctr(p, mk_rsrc(p.ws, WS_USED), C_PZ + 32, 56);
            if (kind == 3) {
                const int w = __builtin_amdgcn_readfirstlane(threadIdx.x >> 6);
                for (int s = u0 >> 3; s < NSEG; s += N_T / 8) attn_task(p, s, (u0 & 7) * 4 + w, (s & 7) < N_T / 8);
            } else { const int rank = (u0 * 52) >> 8; w_role(p, lds, 5 * y + u0 - 5 * rank, rank); }
            TSTAT(x * 128 + tk0, t0, kind + 2);
            return;
        }
        TSTAT(x * 128 + tk0, t0, kind == 0 ? (u0 < 128 ? 1 : (u0 < 192 ? 2 : 3)) : (kind == 1 ? 4 : 7));
    }
}
__global__ void __launch_bounds__(256, 1) k_main(Params p) {
    extern __shared__ __attribute__((aligned(16))) uchar smem[];
    const int slot = take_slot(p, (LAS uchar*)smem), x = slot >> 5, t = slot & 31;
    if (x < 4 && t < 8) role_A(p, (LAS uchar*)smem, x * 8 + t);
    else if (x < 4 && t < 18) role_M(p, (LAS uchar*)smem, x * 10 + t - 8);
    else background(p, (LAS uchar*)smem, x, x < 4 ? t - 18 : t);
}

extern "C" void kernel_launch(void* const* d_in, const int* in_sizes, int n_in, void* d_out, int out_size, void* d_ws, size_t ws_size, hipStream_t stream) {
    Params p;
    p.x = (const uchar*)d_in[0]; p.mem0 = (const uchar*)d_in[1]; p.wq = (const uchar*)d_in[2]; p.wk = (const uchar*)d_in[3];
    p.wv = (const uchar*)d_in[4]; p.wo = (const uchar*)d_in[5]; p.w1 = (const uchar*)d_in[8]; p.b1 = (const uchar*)d_in[9];
    p.w2 = (const uchar*)d_in[10]; p.out = (uchar*)d_out; p.ws = (uchar*)d_ws;
    (void)hipFuncSetAttribute((const void*)k_main, hipFuncAttributeMaxDynamicSharedMemorySize, LDS_BYTES);
    k_init<<<64, 256, 0, stream>>>(p);
    k_main<<<256, 256, LDS_BYTES, stream>>>(p);
}
```

```cpp
#include <hip/hip_runtime.h>
#include <stdint.h>

typedef __bf16 bf16x2 __attribute__((ext_vector_type(2)));
typedef __bf16 bf16x4 __attribute__((ext_vector_type(4)));
typedef __bf16 bf16x8 __attribute__((ext_vector_type(8)));
typedef __bf16 bf16x16 __attribute__((ext_vector_type(16)));
typedef short s16x4 __attribute__((ext_vector_type(4)));
typedef float f32x2 __attribute__((ext_vector_type(2)));
typedef float f32x4 __attribute__((ext_vector_type(4)));
typedef unsigned u32x2 __attribute__((ext_vector_type(2)));
typedef unsigned u32x4 __attribute__((ext_vector_type(4)));
typedef unsigned u32x8 __attribute__((ext_vector_type(8)));
typedef unsigned char uchar;
typedef __amdgpu_buffer_rsrc_t rsrc_t;
#define LAS __attribute__((address_space(3)))
#define DI __device__ __forceinline__

constexpr int S_ = 2048, D_ = 640, H_ = 32, NSEG = 64, F_ = 1728;
constexpr int LDS_BYTES = 163840;
constexpr int OFF_CTRL = 0;
constexpr int OFF_XF = 65536;
constexpr int OFF_KF = OFF_XF + 128 * 20 * 1024;
constexpr int OFF_QF = OFF_KF + 64 * 32 * 8192;
constexpr int OFF_VF = OFF_QF + 64 * 32 * 8192;
constexpr int OFF_ACT = OFF_VF + 64 * 32 * 8192;
constexpr int OFF_GF = OFF_ACT + 128 * 54 * 1024;
constexpr int OFF_OT = OFF_GF + 128 * 20 * 1024;
constexpr int OFF_KM = OFF_OT + 128 * 32 * 4096;
constexpr int OFF_EX = OFF_KM + 64 * 32 * 4096;
constexpr int EX_STEP = 163840, EX_MF = 8 * 16384, EX_AST = 9 * 16384 + 2048, EX_MST = 9 * 16384 + 2304, EX_PROBE = 9 * 16384 + 7168;
constexpr int WS_USED = OFF_EX + 7 * 2097152;
constexpr int C_TMO = 0, C_ACK = 32, C_KV = 512  , C_Q = C_KV + 4096, C_ACT = C_Q + 4096  , C_G = C_ACT + 256  ,
              C_OT = C_G + 1280  , C_KM = C_OT + 2048  , C_Q1 = C_KM + 1024  , C_KVH = C_Q1 + 256  , C_X = C_KVH + 1024  , C_PZ = C_X + 512  , C_WT = C_PZ + 64  , C_OF = C_WT + 640  ,
              C_ZEND = 56 * 288;
constexpr int B_INIT = C_ZEND  , B_VOTE = B_INIT + 112  , B_EPOCH = 16352;
static_assert(C_OF + 128 <= C_ZEND && B_VOTE + 96 <= B_EPOCH && B_EPOCH + 2 <= 16384, "");
constexpr unsigned SENT = 0xFFFFFFFFu;
constexpr int SC1 = 16;
constexpr unsigned OOB = 0x7FFFFF00u;
constexpr unsigned SPIN_CAP = 400000;

struct Params {
    const uchar* x; const uchar* mem0; const uchar* wq; const uchar* wk; const uchar* wv; const uchar* wo;
    const uchar* w1; const uchar* b1; const uchar* w2; uchar* out; uchar* ws;
};

DI rsrc_t mk_rsrc(const void* p, unsigned bytes) { return __builtin_amdgcn_make_buffer_rsrc((void*)p, 0, bytes, 0x00020000); }
DI u32x4 ld16(rsrc_t r, unsigned v, unsigned s) { return __builtin_amdgcn_raw_buffer_load_b128(r, v, s, SC1); }
DI u32x2 ld8(rsrc_t r, unsigned v, unsigned s) { return __builtin_amdgcn_raw_buffer_load_b64(r, v, s, SC1); }
DI unsigned ld4(rsrc_t r, unsigned v, unsigned s) { return __builtin_amdgcn_raw_buffer_load_b32(r, v, s, SC1); }
DI void st16(u32x4 d, rsrc_t r, unsigned v, unsigned s) { __builtin_amdgcn_raw_buffer_store_b128(d, r, v, s, SC1); asm volatile("s_nop 1" :: "v"(d)); }
DI void st8(u32x2 d, rsrc_t r, unsigned v, unsigned s) { __builtin_amdgcn_raw_buffer_store_b64(d, r, v, s, SC1); }
DI void st4(unsigned d, rsrc_t r, unsigned v, unsigned s) { __builtin_amdgcn_raw_buffer_store_b32(d, r, v, s, SC1); }
DI unsigned pk2(float a, float b) { f32x2 v = {a, b}; return __builtin_bit_cast(unsigned, __builtin_convertvector(v, bf16x2)); }
DI unsigned nosent(unsigned v) { return min(v, 0xFFFFFFFEu); }
DI float bf_lo(unsigned v) { return __builtin_bit_cast(float, v << 16); }
DI float bf_hi(unsigned v) { return __builtin_bit_cast(float, v & 0xFFFF0000u); }
DI float rbf(float a) { return bf_lo(pk2(a, a)); }
DI bf16x8 as8(u32x4 v) { return __builtin_bit_cast(bf16x8, v); }
DI f32x4 mfma32(u32x4 a, u32x4 b, f32x4 c) { return __builtin_amdgcn_mfma_f32_16x16x32_bf16(as8(a), as8(b), c, 0, 0, 0); }
DI f32x4 mfma16(u32x2 a, u32x2 b, f32x4 c) { return mfma32((u32x4){a.x, a.y, 0u, 0u}, (u32x4){b.x, b.y, 0u, 0u}, c); }
DI f32x4 smf(u32x4 a, u32x8 b, f32x4 c, int idx) { return __builtin_amdgcn_smfmac_f32_16x16x64_bf16(as8(a), __builtin_bit_cast(bf16x16, b), c, idx, 0, 0); }
DI void smf_a(f32x4& c, u32x4 a, u32x8 b, int idx) { asm volatile("v_smfmac_f32_16x16x64_bf16 %0, %1, %2, %3" : "+v"(c) : "v"(a), "a"(b), "v"(idx)); }
DI void smf_v(f32x4& c, u32x4 a, u32x8 b, int idx) { asm volatile("v_smfmac_f32_16x16x64_bf16 %0, %1, %2, %3" : "+v"(c) : "v"(a), "v"(b), "v"(idx)); }
DI void mfma_v(f32x4& c, u32x4 a, u32x4 b) { asm volatile("v_mfma_f32_16x16x32_bf16 %0, %1, %2, %0" : "+v"(c) : "v"(a), "v"(b)); }
DI void mfma_0(f32x4& c, u32x4 a, u32x4 b) { asm volatile("v_mfma_f32_16x16x32_bf16 %0, %1, %2, 0" : "=&v"(c) : "v"(a), "v"(b)); }
DI float fmax2(float a, float b) { return __builtin_amdgcn_fmed3f(a, b, __builtin_inff()); }
DI void mfma_pad(f32x4& a, f32x4& b, f32x4& c, f32x4& d) { asm volatile("s_nop 7" : "+v"(a), "+v"(b), "+v"(c), "+v"(d)); }
DI u32x2 trd(LAS uchar* p) { return __builtin_bit_cast(u32x2, __builtin_amdgcn_ds_read_tr16_b64_v4i16((LAS s16x4*)p)); }
DI unsigned mx4(unsigned m, u32x4 v) { return max(max(max(m, v.x), max(v.y, v.z)), v.w); }
DI void mx4v(unsigned& m, u32x4 v) { asm volatile("v_max3_u32 %0, %0, %1, %2\n\tv_max3_u32 %0, %0, %3, %4" : "+v"(m) : "v"(v.x), "v"(v.y), "v"(v.z), "v"(v.w)); }
DI unsigned long long nan_lanes(const f32x4& a) { return __builtin_amdgcn_ballot_w64(__builtin_isunordered(a[0], a[1]) | __builtin_isunordered(a[2], a[3])); }
template <int N> DI bool refetch(u32x4 (&f)[N], rsrc_t r, unsigned v, const unsigned (&so)[N], unsigned& budget) {
    bool redo = false;
#pragma nounroll
    for (;;) {
        unsigned m = 0;
#pragma unroll
        for (int i = 0; i < N; ++i) m = max(max(max(m, f[i].x), max(f[i].y, f[i].z)), f[i].w);
        if (__builtin_amdgcn_ballot_w64(m == SENT) == 0 || budget == 0) break;
        --budget; redo = true;
        asm volatile("" ::: "memory");
#pragma unroll
        for (int i = 0; i < N; ++i) f[i] = __builtin_amdgcn_raw_buffer_load_b128(r, v, so[i], SC1);
    }
    return redo;
}
DI unsigned rt32() { unsigned long long t; asm volatile("s_memrealtime %0\n\ts_waitcnt lgkmcnt(0)" : "=s"(t) :: "memory"); return (unsigned)t; }
DI unsigned waitrt(unsigned t) { unsigned n; do n = rt32(); while ((int)(n - t) < 0); return n; }
DI void bar() { asm volatile("s_waitcnt lgkmcnt(0)" ::: "memory"); __builtin_amdgcn_s_barrier(); asm volatile("" ::: "memory"); }
template <int N> DI void wait_vm() { asm volatile("s_waitcnt vmcnt(%0)" :: "n"(N) : "memory"); }
DI void dma16(rsrc_t r, unsigned voff, unsigned soff, unsigned ldsaddr) {
    asm volatile("s_mov_b32 m0, %0\n\ts_nop 0\n\tbuffer_load_dwordx4 %1, %2, %3 offen lds" :: "s"(ldsaddr), "v"(voff), "s"(r), "s"(soff) : "memory");
}
DI float swap32_add(float a, float b) { auto r = __builtin_amdgcn_permlane32_swap(__builtin_bit_cast(unsigned, a), __builtin_bit_cast(unsigned, b), false, false);
    return __builtin_bit_cast(float, (unsigned)r[0]) + __builtin_bit_cast(float, (unsigned)r[1]); }
DI float swap16_add(float a, float b) { auto r = __builtin_amdgcn_permlane16_swap(__builtin_bit_cast(unsigned, a), __builtin_bit_cast(unsigned, b), false, false);
    return __builtin_bit_cast(float, (unsigned)r[0]) + __builtin_bit_cast(float, (unsigned)r[1]); }
DI float grp_max(float m) {
    auto r = __builtin_amdgcn_permlane32_swap(__builtin_bit_cast(unsigned, m), __builtin_bit_cast(unsigned, m), false, false);
    m = fmax2(__builtin_bit_cast(float, (unsigned)r[0]), __builtin_bit_cast(float, (unsigned)r[1]));
    auto q = __builtin_amdgcn_permlane16_swap(__builtin_bit_cast(unsigned, m), __builtin_bit_cast(unsigned, m), false, false);
    return fmax2(__builtin_bit_cast(float, (unsigned)q[0]), __builtin_bit_cast(float, (unsigned)q[1]));
}
DI int fresh_lane() { int l = __builtin_amdgcn_mbcnt_hi(~0u, __builtin_amdgcn_mbcnt_lo(~0u, 0u)); asm volatile("" : "+v"(l)); return l & 63; }
DI unsigned fresh_tid() { unsigned t = threadIdx.x; asm volatile("" : "+v"(t)); return t; }
DI unsigned uni(unsigned v) { asm volatile("" : "+v"(v)); return __builtin_amdgcn_readfirstlane(v); }
DI unsigned ex_off(const Params& p) { return OFF_EX + ((0u - uni((unsigned)(size_t)p.ws) - OFF_EX) & 0x1FFFFFu); }
DI unsigned ex_step(unsigned exo, unsigned pol, int s) { const int r = (s * 43) >> 9; return exo + r * 2097152 + (s - 12 * r) * EX_STEP + ((pol >> r) & 1) * 8192; }
DI constexpr unsigned mf_off(int ks) { return EX_MF + (ks >> 3) * 16384 + (ks & 7) * 1024; }
DI unsigned mt32() { unsigned long long t; asm volatile("s_memtime %0\n\ts_waitcnt lgkmcnt(0)" : "=s"(t) :: "memory"); return (unsigned)t; }
DI void set_tmo(const Params& p) { __hip_atomic_store((unsigned*)p.ws + C_TMO, 1u, __ATOMIC_RELAXED, __HIP_MEMORY_SCOPE_AGENT); }

DI float row16_sum(float v) {
    v += __builtin_bit_cast(float, __builtin_amdgcn_update_dpp(0, __builtin_bit_cast(int, v), 0x121, 0xf, 0xf, true));
    v += __builtin_bit_cast(float, __builtin_amdgcn_update_dpp(0, __builtin_bit_cast(int, v), 0x122, 0xf, 0xf, true));
    v += __builtin_bit_cast(float, __builtin_amdgcn_update_dpp(0, __builtin_bit_cast(int, v), 0x124, 0xf, 0xf, true));
    v += __builtin_bit_cast(float, __builtin_amdgcn_update_dpp(0, __builtin_bit_cast(int, v), 0x128, 0xf, 0xf, true));
    return v;
}
DI void add_ctr(const Params& p, int word, unsigned v) { (void)__hip_atomic_fetch_add((unsigned*)p.ws + word, v, __ATOMIC_RELAXED, __HIP_MEMORY_SCOPE_AGENT); }
DI void wait_ctr(const Params& p, rsrc_t rw, int word, unsigned want) {
    unsigned n = SPIN_CAP;
#pragma nounroll
    for (;;) {
        asm volatile("" ::: "memory");
        if ((unsigned)__builtin_amdgcn_readfirstlane(ld4(rw, 0, word * 4)) >= want) break;
        if (--n == 0) { set_tmo(p); break; }
        if ((n & 63) == 0 && __builtin_amdgcn_readfirstlane(ld4(rw, 0, C_TMO * 4))) break;
        __builtin_amdgcn_s_sleep(1);
    }
    asm volatile("" ::: "memory");
}

template <int V> struct IC { static constexpr int value = V; };
template <int N, int I = 0, class F> DI void static_for(F&& f) { if constexpr (I < N) { f(IC<I>{}); static_for<N, I + 1>(f); } }
DI u32x2 launch_tag(rsrc_t rw) {
    const u32x2 e = ld8(rw, 0, B_EPOCH * 4);
    const unsigned lo = __builtin_amdgcn_readfirstlane(e.x) + 1u;
    return (u32x2){lo, (unsigned)__builtin_amdgcn_readfirstlane(e.y) + (lo == 0u ? 1u : 0u)};
}
DI void wait_boot(rsrc_t rw, u32x2 tag) {
    const int lane = fresh_lane();
    for (unsigned n = SPIN_CAP; n; --n) {
        asm volatile("" ::: "memory");
        const u32x2 v = ld8(rw, min(lane, 55) * 8, B_INIT * 4);
        if (__builtin_amdgcn_ballot_w64(v.x != tag.x || v.y != tag.y) == 0) break;
        __builtin_amdgcn_s_sleep(1);
    }
    asm volatile("" ::: "memory");
}
DI void probe(const Params& p, LAS uchar* lds, int v) {
    const int lane = fresh_lane(), w = __builtin_amdgcn_readfirstlane(threadIdx.x >> 6);
    const rsrc_t rw = mk_rsrc(p.ws, WS_USED);
    unsigned* a = (unsigned*)(p.ws + ex_off(p) + (v >> 3) * 2097152 + EX_PROBE + (w >> 1) * 8192 + (w & 1) * EX_STEP + (v & 7) * 128);
    unsigned best = ~0u;
    for (int i = 0; i < 4; ++i) {
        const unsigned t0 = mt32();
        unsigned r = 0;
        if (lane == 0) r = __hip_atomic_fetch_add(a, 1u, __ATOMIC_RELAXED, __HIP_MEMORY_SCOPE_AGENT);
        asm volatile("s_waitcnt vmcnt(0)" :: "v"(r) : "memory");
        best = min(best, mt32() - t0);
    }
    ((LAS unsigned*)lds)[w] = best;
    const u32x2 tag = launch_tag(rw);
    bar();
    const LAS unsigned* lat = (const LAS unsigned*)lds;
    const int d = (int)min(lat[0], lat[1]) - (int)min(lat[2], lat[3]);
    st8((u32x2){tag.x, tag.y ^ ((abs(d) < 120 ? 0u : (d > 0 ? 1u : 2u)) << 30)}, rw, fresh_tid() == 0 ? v * 8 : OOB, B_VOTE * 4);
    bar();
}
DI unsigned read_pol(rsrc_t rw, u32x2 tag) {
    const int lane = fresh_lane();
    u32x2 v = {0u, 0u};
    for (unsigned n = SPIN_CAP; n; --n) {
        asm volatile("" ::: "memory");
        v = ld8(rw, min(lane, 47) * 8, B_VOTE * 4);
        if (__builtin_amdgcn_ballot_w64(v.x != tag.x || ((v.y ^ tag.y) << 2) != 0) == 0) break;
        __builtin_amdgcn_s_sleep(1);
    }
    const unsigned c = (v.y ^ tag.y) >> 30;
    const unsigned long long m1 = __builtin_amdgcn_ballot_w64(c == 2), m0 = __builtin_amdgcn_ballot_w64(c == 1);
    unsigned pol = 0;
#pragma unroll
    for (int r = 0; r < 6; ++r) pol |= (__builtin_popcountll((m1 >> (8 * r)) & 255) > __builtin_popcountll((m0 >> (8 * r)) & 255) ? 1u : 0u) << r;
    return uni(pol);
}

template <bool FIRST>
DI void ln_tile(const Params& p, int b, u32x2& tag) {
    const int lane = fresh_lane(), w = __builtin_amdgcn_readfirstlane(threadIdx.x >> 6);
    const rsrc_t rw = mk_rsrc(p.ws, WS_USED);
    const int c = w * 4 + (lane >> 4), sub = lane & 15, row = b * 16 + c;
    u32x4 v[5]; float sum = 0.f;
#pragma unroll
    for (int k = 0; k < 5; ++k) v[k] = *(const u32x4*)(p.x + (size_t)row * 1280 + (k * 16 + sub) * 16);
    if (FIRST) {
        const unsigned tid = fresh_tid();
        st16((u32x4){0u, 0u, 0u, 0u}, rw, tid < 72 ? tid * 16 : OOB, b * 1152);
        tag = launch_tag(rw);
        wait_vm<0>();
        bar();
        st8(tag, rw, tid == 0 ? b * 8 : OOB, B_INIT * 4);
    }
#pragma unroll
    for (int k = 0; k < 5; ++k)
#pragma unroll
        for (int j = 0; j < 4; ++j) sum += bf_lo(v[k][j]) + bf_hi(v[k][j]);
    const float mean = row16_sum(sum) * (1.f / 640.f);
    float sq = 0.f;
#pragma unroll
    for (int k = 0; k < 5; ++k)
#pragma unroll
        for (int j = 0; j < 4; ++j) { const float a = bf_lo(v[k][j]) - mean, bb = bf_hi(v[k][j]) - mean; sq += a * a + bb * bb; }
    const float mb = rbf(mean), sd = rbf(sqrtf(rbf(rbf(row16_sum(sq) * (1.f / 640.f)) + 1e-5f))), ri = 1.f / sd;
#pragma unroll
    for (int k = 0; k < 5; ++k) {
        u32x4 o;
#pragma unroll
        for (int j = 0; j < 4; ++j) { const unsigned d = pk2(bf_lo(v[k][j]) - mb, bf_hi(v[k][j]) - mb); o[j] = pk2(bf_lo(d) * ri, bf_hi(d) * ri); }
        st16(o, rw, (k * 16 + sub) * 256 + c * 16, OFF_XF + b * 20480);
    }
    if (FIRST && w == 0) wait_boot(rw, tag);
    wait_vm<0>();
    bar();
    if (fresh_tid() == 0) add_ctr(p, C_X + (b >> 3) * 32, 1u);
}
DI void poison(const Params& p, unsigned pol, int j, int n0, int n1, int word) {
    const int tid = fresh_tid();
    const rsrc_t rw = mk_rsrc(p.ws, WS_USED);
    const unsigned exo = ex_off(p);
    const u32x4 sv = {SENT, SENT, SENT, SENT};
    for (int n = n0 + j; n < n1; n += 56) {
        const int s = min((n * 3277) >> 16, 63), i = n - 20 * s;
        u32x4 v = sv;
        if (s == 0 && i >= 16 && (i < 18 || (i == 18 && tid < 128))) {
            const int f = (i - 16) * 256 + tid, ks = f >> 6, l = f & 63, r = (l & 3) | ((l >> 1) & 4), half = (l >> 2) & 1, g = l >> 4;
            v = *(const u32x4*)(p.mem0 + (r * 640 + 64 * ks + 16 * g + 8 * half) * 2);
            v.x = nosent(v.x); v.y = nosent(v.y); v.z = nosent(v.z); v.w = nosent(v.w);
        }
        st16(v, rw, tid * 16, n < 1280 ? ex_step(exo, pol, s) + (i >> 1) * 16384 + (i & 1) * 4096
                                       : (n < 1312 ? OFF_KM + 63 * 131072 + (n - 1280) * 4096 : OFF_OT + 126 * 131072 + (n - 1312) * 4096));
    }
    wait_vm<0>();
    bar();
    if (fresh_tid() == 0) add_ctr(p, word, 1u);
}

#define SB() __builtin_amdgcn_sched_barrier(0)
DI void mfma_va(f32x4& c, u32x4 a, u32x4 b) { asm volatile("v_mfma_f32_16x16x32_bf16 %0, %1, %2, %0" : "+v"(c) : "v"(a), "a"(b)); }
DI void mfma_av(f32x4& c, u32x4 a, u32x4 b) { asm volatile("v_mfma_f32_16x16x32_bf16 %0, %1, %2, %0" : "+v"(c) : "a"(a), "v"(b)); }
DI void mfma_cacc(f32x4& c, u32x4 a, u32x4 b) { asm volatile("v_mfma_f32_16x16x32_bf16 %0, %1, %2, %0" : "+a"(c) : "v"(a), "v"(b)); }
struct P1Src {
    rsrc_t r; unsigned v0, v1, so, step;
    DI void set(const Params& p, int u, int kh, int w, int lane) {
        const uchar* base; unsigned stride, col, bytes;
        if (u < 128) { base = (u & 2) ? p.wv : p.wk; stride = 8192; col = ((u >> 2) * 128 + (u & 1) * 64) * 2; bytes = 640 * 8192; }
        else if (u < 192) { base = p.wq; stride = 8192; col = (u - 128) * 128; bytes = 640 * 8192; }
        else { base = p.w1; stride = 3456; col = (u - 192) * 128; bytes = u < 219 ? 640 * 3456 : 0; }
        r = mk_rsrc(base, bytes);
        const int rl = lane >> 3, f0 = ((rl >> 1) & 1) | (((rl >> 2) & 1) << 1);
        v0 = rl * stride + (((lane & 7) ^ (2 * f0)) << 4); v1 = v0 ^ 64;
        so = (kh * 320 + 80 * w) * stride + col; step = 8 * stride;
    }
    DI void piece(int i, unsigned ldsw) const { dma16(r, (i & 1) ? v1 : v0, so + i * step, ldsw + i * 1024); }
};
DI float mish(float a, float bias) {
    const float h = a + bias;
    const float e = __builtin_amdgcn_exp2f(fminf(h, 30.f) * 1.44269504f);
    const float n = e * (e + 2.f);
    return h * n * __builtin_amdgcn_rcpf(n + 2.f);
}
#define PT(i)
constexpr int XA_ACC = 15;
template <bool ISV, int KH>
DI void p1_slab(LAS uchar* sb, const unsigned (&tb)[4], const u32x4 (&xa)[4][20], f32x4 (&acc)[4][4], const P1Src& src, unsigned ldsn) {
    u32x4 wf[2][4];
    auto rd = [&](int kk, int nt) { const u32x2 lo = trd(sb + tb[nt] + kk * 4096), hi = trd(sb + tb[nt ^ 2] + kk * 4096 + 512); return (u32x4){lo.x, lo.y, hi.x, hi.y}; };
#pragma unroll
    for (int nt = 0; nt < 4; ++nt) wf[0][nt] = rd(0, nt);
    SB();
#pragma unroll
    for (int kk = 0; kk < 10; ++kk) {
#pragma unroll
        for (int nt = 0; nt < 4; ++nt) {
            if (kk + 1 < 10) wf[(kk + 1) & 1][nt] = rd(kk + 1, nt);
            if (kk < 5 && (nt & 1)) src.piece(2 * kk + (nt >> 1), ldsn);
#pragma unroll
            for (int t = 0; t < 4; ++t) {
                const int ks = KH * 10 + kk;
                if (ISV) { if (ks < XA_ACC) mfma_av(acc[t][nt], xa[t][ks], wf[kk & 1][nt]); else mfma_v(acc[t][nt], xa[t][ks], wf[kk & 1][nt]); }
                else { if (ks < XA_ACC) mfma_va(acc[t][nt], wf[kk & 1][nt], xa[t][ks]); else mfma_v(acc[t][nt], wf[kk & 1][nt], xa[t][ks]); }
            }
            SB();
        }
    }
    asm volatile("s_nop 7" : "+v"(acc[3][3]), "+v"(acc[2][3]), "+v"(acc[1][3]), "+v"(acc[0][3]), "+v"(acc[3][2]), "+v"(acc[2][2]), "+v"(acc[1][2]), "+v"(acc[0][2]));
#pragma unroll
    for (int t = 0; t < 4; ++t) asm volatile("" : "+v"(acc[t][0]), "+v"(acc[t][1]));
}
template <bool ISV>
DI void p1_group(const Params& p, LAS uchar* lds, rsrc_t rw, unsigned lbase, const unsigned (&tb)[4], const u32x4 (&xa)[4][20], P1Src& src, int buf,
                 int chunk, int u, int un, bool first, int w, int lane, unsigned (&pt)[5]) {
    const int g = lane >> 4;
    f32x4 acc[4][4];
#pragma unroll
    for (int t = 0; t < 4; ++t)
#pragma unroll
        for (int nt = 0; nt < 4; ++nt) { acc[t][nt] = (f32x4){0.f, 0.f, 0.f, 0.f}; asm volatile("" : "+v"(acc[t][nt])); }
    PT(0);
    if (first) wait_vm<50>(); else wait_vm<10>();
    PT(1);
    bar();
    PT(2);
    src.set(p, un, 0, w, lane);
    {
        const unsigned ln = lbase + (buf == 0 ? 2 : buf - 1) * 40960;
        p1_slab<ISV, 0>(lds + buf * 40960, tb, xa, acc, src, ln);
    }
    buf = buf == 2 ? 0 : buf + 1;
    PT(3);
    wait_vm<10>();
    PT(1);
    bar();
    PT(2);
    src.set(p, un, 1, w, lane);
    {
        const unsigned ln = lbase + (buf == 0 ? 2 : buf - 1) * 40960;
        p1_slab<ISV, 1>(lds + buf * 40960, tb, xa, acc, src, ln);
    }
    PT(3);
    const int tt0 = chunk * 16 + w * 4;
    if (ISV) {
        const int h = u >> 2;
#pragma unroll
        for (int sp = 0; sp < 2; ++sp)
#pragma unroll
            for (int nt = 0; nt < 4; ++nt) {
                const f32x4 a = acc[2 * sp][nt], b = acc[2 * sp + 1][nt];
                const u32x4 o = {pk2(a[0], a[1]), pk2(a[2], a[3]), pk2(b[0], b[1]), pk2(b[2], b[3])};
                st16(o, rw, lane * 16, OFF_VF + (((tt0 >> 1) + sp) * 32 + h) * 8192 + ((u & 1) * 4 + nt) * 1024);
            }
    } else if (u < 192) {
        const int h = u < 128 ? u >> 2 : (u - 128) >> 1;
        const int ob = u < 128 ? OFF_KF : OFF_QF;
#pragma unroll
        for (int t = 0; t < 4; ++t)
#pragma unroll
            for (int m = 0; m < 2; ++m) {
                const f32x4 a = acc[t][2 * m], b = acc[t][2 * m + 1];
                const u32x4 o = {pk2(a[0], a[1]), pk2(a[2], a[3]), pk2(b[0], b[1]), pk2(b[2], b[3])};
                st16(o, rw, lane * 16, ob + (((tt0 + t) >> 1) * 32 + h) * 8192 + ((t & 1) * 4 + (u & 1) * 2 + m) * 1024);
            }
    } else {
        const int fg = u - 192;
        u32x2 bv[4];
#pragma unroll
        for (int nt = 0; nt < 4; ++nt) bv[nt] = *(const u32x2*)(p.b1 + (fg * 64 + nt * 16 + 4 * g) * 2);
#pragma unroll
        for (int t = 0; t < 4; ++t)
#pragma unroll
            for (int m = 0; m < 2; ++m) {
                const f32x4 a = acc[t][2 * m], b = acc[t][2 * m + 1];
                const u32x2 ba = bv[2 * m], bb = bv[2 * m + 1];
                const u32x4 o = {pk2(mish(a[0], bf_lo(ba.x)), mish(a[1], bf_hi(ba.x))), pk2(mish(a[2], bf_lo(ba.y)), mish(a[3], bf_hi(ba.y))),
                                 pk2(mish(b[0], bf_lo(bb.x)), mish(b[1], bf_hi(bb.x))), pk2(mish(b[2], bf_lo(bb.y)), mish(b[3], bf_hi(bb.y)))};
                st16(o, rw, lane * 16, OFF_ACT + ((tt0 + t) * 54 + fg * 2 + m) * 1024);
                SB();
            }
    }
}
DI void ld16_a(u32x4& d, rsrc_t r, unsigned v, unsigned s) { asm volatile("buffer_load_dwordx4 %0, %1, %2, %3 offen sc1" : "=a"(d) : "v"(v), "s"(r), "s"(s) : "memory"); }
DI void ld16_v(u32x4& d, rsrc_t r, unsigned v, unsigned s) { asm volatile("buffer_load_dwordx4 %0, %1, %2, %3 offen sc1" : "=v"(d) : "v"(v), "s"(r), "s"(s) : "memory"); }
DI void ld16_s(u32x4& d, rsrc_t r, unsigned v, unsigned s) { asm volatile("buffer_load_dwordx4 %0, %1, %2, %3 offen sc1" : "=v"(d) : "v"(v), "s"(r), "s"(s) : "memory"); }
DI void p1_task(const Params& p, LAS uchar* lds, int chunk, int u0, int nu) {
    const int lane = fresh_lane(), w = __builtin_amdgcn_readfirstlane(threadIdx.x >> 6), c = lane & 15, g = lane >> 4;
    const rsrc_t rw = mk_rsrc(p.ws, WS_USED);
    const unsigned lbase = (unsigned)(size_t)lds + w * 10240;
    P1Src src;
    u32x4 xa[4][20];
    src.set(p, u0, 0, w, lane);
    if (w == 0) { wait_ctr(p, rw, C_X + chunk * 64, 8); wait_ctr(p, rw, C_X + chunk * 64 + 32, 8); }
    bar();
#pragma unroll
    for (int i = 0; i < 10; ++i) src.piece(i, lbase);
#pragma unroll
    for (int ks = 0; ks < 20; ++ks) {
        if (ks == 10) {
            src.set(p, u0, 1, w, lane);
#pragma unroll
            for (int i = 0; i < 10; ++i) src.piece(i, lbase + 40960);
        }
#pragma unroll
        for (int t = 0; t < 4; ++t) {
            const unsigned so = OFF_XF + ((chunk * 16 + w * 4 + t) * 20 + ks) * 1024;
            if (ks < XA_ACC) ld16_a(xa[t][ks], rw, lane * 16, so); else ld16_v(xa[t][ks], rw, lane * 16, so);
        }
        SB();
    }
    const int q = c >> 2, pp = c & 3, sw = (q >> 1) | ((g & 1) << 1);
    unsigned tb[4];
#pragma unroll
    for (int nt = 0; nt < 4; ++nt) tb[nt] = (8 * g + q) * 128 + 32 * (nt ^ sw) + 16 * (pp >> 1) + 8 * (pp & 1);
    int buf = 0;
    unsigned pt[5] = {0, 0, 0, 0, mt32()};
    for (int gi = 0; gi < nu; ++gi) {
        const int u = u0 + gi, un = gi + 1 < nu ? u + 1 : 219;
        if (u < 128 && (u & 2)) p1_group<true>(p, lds, rw, lbase, tb, xa, src, buf, chunk, u, un, gi == 0, w, lane, pt);
        else p1_group<false>(p, lds, rw, lbase, tb, xa, src, buf, chunk, u, un, gi == 0, w, lane, pt);
        buf = buf == 0 ? 2 : buf - 1;
    }
    wait_vm<0>();
    bar();
    const unsigned tid = fresh_tid();
    if (tid < (unsigned)nu) {
        const int u = u0 + tid;
        add_ctr(p, u < 128 ? C_KV + (chunk * 32 + (u >> 2)) * 16 : (u < 192 ? C_Q + (chunk * 32 + ((u - 128) >> 1)) * 16 : C_ACT + chunk * 32), 1u);
    }
}

#define KST(i)
#define AST_(i)
template <bool ISV>
DI void kv0_task(const Params& p, LAS uchar* lds, int u) {
    const int lane = fresh_lane(), w = __builtin_amdgcn_readfirstlane(threadIdx.x >> 6), c = lane & 15, g = lane >> 4;
    const rsrc_t rw = mk_rsrc(p.ws, WS_USED);
    const unsigned lbase = (unsigned)(size_t)lds + w * 10240;
    P1Src src;
    u32x4 xa[2][2][20];
    auto fetch = [&](int part, int i) {
        const unsigned so = OFF_XF + ((part * 8 + w * 2 + (i & 1)) * 20 + (i >> 1)) * 1024;
        if (part & 1) ld16_a(xa[1][i & 1][i >> 1], rw, lane * 16, so); else ld16_v(xa[0][i & 1][i >> 1], rw, lane * 16, so);
    };
#pragma unroll
    for (int kh = 0; kh < 2; ++kh) {
        src.set(p, u, kh, w, lane);
#pragma unroll
        for (int i = 0; i < 10; ++i) src.piece(i, lbase + kh * 40960);
    }
    KST(8);
    KST(9);
    if (w == 0) {
        wait_boot(rw, launch_tag(rw));
        if (fresh_tid() == 0) add_ctr(p, C_ACK, 1u);
        wait_ctr(p, rw, C_X, 8);
    }
    bar();
    KST(10);
#pragma unroll
    for (int i = 0; i < 40; ++i) fetch(0, i);
    if (w == 0) { wait_ctr(p, rw, C_X + 32, 8); wait_ctr(p, rw, C_X + 64, 8); wait_ctr(p, rw, C_X + 96, 8); }
    const int q = c >> 2, pp = c & 3, sw = (q >> 1) | ((g & 1) << 1), h = u >> 2;
    unsigned tb[4];
#pragma unroll
    for (int nt = 0; nt < 4; ++nt) tb[nt] = (8 * g + q) * 128 + 32 * (nt ^ sw) + 16 * (pp >> 1) + 8 * (pp & 1);
    auto rd = [&](int i) {
        const int ks = i >> 2, nt = i & 3; LAS uchar* sb = lds + (ks / 10) * 40960 + (ks % 10) * 4096;
        const u32x2 lo = trd(sb + tb[nt]), hi = trd(sb + tb[nt ^ 2] + 512); return (u32x4){lo.x, lo.y, hi.x, hi.y};
    };
    static_for<4>([&](auto pc) {
        constexpr int part = pc.value, b = part & 1;
        f32x4 acc[2][4];
#pragma unroll
        for (int i = 0; i < 8; ++i) { acc[i >> 2][i & 3] = (f32x4){0.f, 0.f, 0.f, 0.f}; asm volatile("" : "+v"(acc[i >> 2][i & 3])); }
        if (part < 2) KST(part * 4 + 0);
        wait_vm<0>();
#pragma unroll
        for (int ks = 0; ks < 20; ++ks) { if (b) asm volatile("" : "+a"(xa[b][0][ks]), "+a"(xa[b][1][ks])); else asm volatile("" : "+v"(xa[b][0][ks]), "+v"(xa[b][1][ks])); }
        bar();
        if (part > 0 && fresh_tid() == 0) add_ctr(p, (b ? C_KVH : C_KV) + (((part - 1) >> 1) * 32 + h) * 16, 1u);
        if (part < 2) KST(part * 4 + 1);
        u32x4 wf[8];
#pragma unroll
        for (int i = 0; i < 8; ++i) wf[i] = rd(i);
        SB();
#pragma unroll
        for (int i = 0; i < 80; ++i) {
#pragma unroll
            for (int t = 0; t < 2; ++t) {
                if (ISV) { if (b) mfma_av(acc[t][i & 3], xa[b][t][i >> 2], wf[i & 7]); else mfma_v(acc[t][i & 3], xa[b][t][i >> 2], wf[i & 7]); }
                else { if (b) mfma_va(acc[t][i & 3], wf[i & 7], xa[b][t][i >> 2]); else mfma_v(acc[t][i & 3], wf[i & 7], xa[b][t][i >> 2]); }
            }
            if (i + 8 < 80) wf[i & 7] = rd(i + 8);
            if (part < 3 && i < 40) fetch(part + 1, i);
            SB();
        }
        asm volatile("s_nop 7" : "+v"(acc[0][0]), "+v"(acc[0][1]), "+v"(acc[0][2]), "+v"(acc[0][3]), "+v"(acc[1][0]), "+v"(acc[1][1]), "+v"(acc[1][2]), "+v"(acc[1][3]));
        if (part < 2) KST(part * 4 + 2);
        const unsigned ob = (ISV ? OFF_VF : OFF_KF) + ((part * 4 + w) * 32 + h) * 8192;
#pragma unroll
        for (int i = 0; i < 4; ++i) {
            const f32x4 a = ISV ? acc[0][i] : acc[i >> 1][2 * (i & 1)], bb = ISV ? acc[1][i] : acc[i >> 1][2 * (i & 1) + 1];
            const u32x4 o = {pk2(a[0], a[1]), pk2(a[2], a[3]), pk2(bb[0], bb[1]), pk2(bb[2], bb[3])};
            st16(o, rw, lane * 16, ob + (ISV ? (u & 1) * 4 + i : (i >> 1) * 4 + (u & 1) * 2 + (i & 1)) * 1024);
        }
        if (part < 2) KST(part * 4 + 3);
        SB();
    });
    wait_vm<0>();
    bar();
    if (fresh_tid() == 0) add_ctr(p, C_KV + (32 + h) * 16, 1u);
}

template <bool WO>
DI void gemm_task(const Params& p, LAS uchar* lds, int chunk, int cg) {
    constexpr int KS = WO ? 4 : 3, NS = WO ? 32 : 18, NP = 2 * KS, IMG = KS * 4096, NKF = KS * NS, XOFF = WO ? OFF_OT : OFF_ACT;
    const int lane = fresh_lane(), w = __builtin_amdgcn_readfirstlane(threadIdx.x >> 6), c = lane & 15, g = lane >> 4;
    const rsrc_t rw = mk_rsrc(p.ws, WS_USED), r2 = mk_rsrc(WO ? p.wo : p.w2, NKF * 32 * 1280);
    const unsigned lbase = (unsigned)(size_t)lds + (w >> 1) * IMG + (w & 1) * NP * 1024;
    const int rl = lane >> 3, f0 = ((rl >> 1) & 1) | (((rl >> 2) & 1) << 1);
    const unsigned v0 = rl * 1280 + (((lane & 7) ^ (2 * f0)) << 4), v1 = v0 ^ 64;
    const unsigned wbase = (w & 1) * NP * 8 * 1280 + cg * 256 + (w >> 1) * 128;
    auto piece = [&](int sl, int buf, int i) { dma16(r2, (i & 1) ? v1 : v0, wbase + (sl * KS * 32 + 8 * i) * 1280, lbase + buf * 2 * IMG + i * 1024); };
    const int tt0 = chunk * 16 + w * 4;
    u32x4 xb[2][KS][4];
    auto frag = [&](int par, int sl, int i) { ld16_s(xb[par][i >> 2][i & 3], rw, lane * 16, XOFF + ((tt0 + (i & 3)) * NKF + sl * KS + (i >> 2)) * 1024); };
#pragma unroll
    for (int i = 0; i < NP; ++i) piece(0, 0, i);
    if (w == 0) {
        if (WO) {
            wait_ctr(p, rw, C_G + (chunk * 5 + cg) * 32, 1);
            for (int i = 0; i < 8; ++i) wait_ctr(p, rw, C_OT + (chunk * 8 + i) * 32, 32);
        } else wait_ctr(p, rw, C_ACT + chunk * 32, 27);
    }
    bar();
#pragma unroll
    for (int i = 0; i < 4 * KS; ++i) frag(0, 0, i);
#pragma unroll
    for (int i = 0; i < NP; ++i) piece(1, 1, i);
    const int q = c >> 2, pp = c & 3, sw = (q >> 1) | (((g & 1) ^ (g >> 1)) << 1);
    unsigned tb[4];
#pragma unroll
    for (int nt = 0; nt < 4; ++nt) tb[nt] = (4 * g + q) * 128 + 32 * (nt ^ sw) + 16 * (pp >> 1) + 8 * (pp & 1);
    f32x4 acc[4][8];
#pragma unroll
    for (int t = 0; t < 4; ++t)
#pragma unroll
        for (int nt = 0; nt < 8; ++nt) { acc[t][nt] = (f32x4){0.f, 0.f, 0.f, 0.f}; asm volatile("" : "+a"(acc[t][nt])); }
    int buf = 0;
    for (int s2 = 0; s2 < NS / 2; ++s2) {
#pragma unroll
        for (int par = 0; par < 2; ++par) {
            const int sl = 2 * s2 + par;
            wait_vm<NP>();
#pragma unroll
            for (int kk = 0; kk < KS; ++kk) asm volatile("" : "+v"(xb[par][kk][0]), "+v"(xb[par][kk][1]), "+v"(xb[par][kk][2]), "+v"(xb[par][kk][3]));
            bar();
            const int sn = min(sl + 1, NS - 1), sp = min(sl + 2, NS - 1), bn = buf == 0 ? 2 : buf - 1;
            LAS uchar* sb = lds + buf * 2 * IMG;
            auto rd = [&](int i) { LAS uchar* a = sb + ((i >> 2) & 1) * IMG + tb[i & 3] + (i >> 3) * 4096; const u32x2 lo = trd(a), hi = trd(a + 2048); return (u32x4){lo.x, lo.y, hi.x, hi.y}; };
            u32x4 wf[4];
#pragma unroll
            for (int i = 0; i < 4; ++i) wf[i] = rd(i);
            SB();
#pragma unroll
            for (int i = 0; i < 8 * KS; ++i) {
                const u32x4 cur = wf[i & 3];
                mfma_cacc(acc[0][i & 7], cur, xb[par][i >> 3][0]);
                if (i < 4 * KS) frag(par ^ 1, sn, i); else if (i < 4 * KS + NP) piece(sp, bn, i - 4 * KS);
#pragma unroll
                for (int t = 1; t < 4; ++t) mfma_cacc(acc[t][i & 7], cur, xb[par][i >> 3][t]);
                if (i + 4 < 8 * KS) wf[i & 3] = rd(i + 4);
                SB();
            }
            if (par) asm volatile("s_nop 7");
            SB();
            buf = buf == 2 ? 0 : buf + 1;
        }
    }
    wait_vm<0>();
#pragma unroll
    for (int kk = 0; kk < KS; ++kk) asm volatile("" :: "v"(xb[0][kk][0]), "v"(xb[0][kk][1]), "v"(xb[0][kk][2]), "v"(xb[0][kk][3]));
    SB();
#pragma unroll
    for (int t = 0; t < 4; ++t) {
        if (WO) {
            const size_t xo = (size_t)((tt0 + t) * 16 + c) * 1280 + cg * 256 + g * 8;
            u32x4 gv[4]; u32x2 xv[8];
#pragma unroll
            for (int m = 0; m < 4; ++m) gv[m] = ld16(rw, lane * 16, OFF_GF + ((tt0 + t) * 20 + cg * 4 + m) * 1024);
#pragma unroll
            for (int nt = 0; nt < 8; ++nt) xv[nt] = *(const u32x2*)(p.x + xo + nt * 32);
#pragma unroll
            for (int nt = 0; nt < 8; ++nt) {
                const f32x4 a = acc[t][nt];
                const unsigned g0 = gv[nt >> 1][2 * (nt & 1)], g1 = gv[nt >> 1][2 * (nt & 1) + 1], s0 = pk2(a[0], a[1]), s1 = pk2(a[2], a[3]);
                const unsigned m0 = pk2(bf_lo(s0) * bf_lo(g0), bf_hi(s0) * bf_hi(g0)), m1 = pk2(bf_lo(s1) * bf_lo(g1), bf_hi(s1) * bf_hi(g1));
                *(u32x2*)(p.out + xo + nt * 32) = (u32x2){pk2(bf_lo(xv[nt].x) + bf_lo(m0), bf_hi(xv[nt].x) + bf_hi(m0)), pk2(bf_lo(xv[nt].y) + bf_lo(m1), bf_hi(xv[nt].y) + bf_hi(m1))};
            }
        } else {
#pragma unroll
            for (int m = 0; m < 4; ++m) {
                float y[8];
#pragma unroll
                for (int r = 0; r < 8; ++r) { const float a = rbf(acc[t][2 * m + (r >> 2)][r & 3]); y[r] = a * __builtin_amdgcn_rcpf(1.f + __builtin_amdgcn_exp2f(-a * 1.44269504f)); }
                const u32x4 o = {pk2(y[0], y[1]), pk2(y[2], y[3]), pk2(y[4], y[5]), pk2(y[6], y[7])};
                st16(o, rw, lane * 16, OFF_GF + ((tt0 + t) * 20 + cg * 4 + m) * 1024);
            }
        }
        SB();
    }
    wait_vm<0>();
    bar();
    if (!WO && fresh_tid() == 0) add_ctr(p, C_G + (chunk * 5 + cg) * 32, 1u);
}

#define XST(cond, idx)
constexpr float SCL = 0.08838834764831845f * 1.4426950408889634f;
DI void attn_task(const Params& p, int s, int h, bool new_chunk) {
    const int lane = fresh_lane(), c = lane & 15, g = lane >> 4;
    const rsrc_t rw = mk_rsrc(p.ws, WS_USED);
    const int sh = (s * 32 + h);
    u32x4 qf[2][4], kf[2][4], vf[8], km[4]; u32x2 vm[8];
    if (new_chunk) {
        wait_ctr(p, rw, C_Q + ((s >> 3) * 32 + h) * 16, 2);
        wait_ctr(p, rw, C_KV + ((s >> 3) * 32 + h) * 16, 4);
    }
#pragma unroll
    for (int i = 0; i < 8; ++i) qf[i >> 2][i & 3] = ld16(rw, lane * 16, OFF_QF + sh * 8192 + i * 1024);
#pragma unroll
    for (int i = 0; i < 8; ++i) kf[i >> 2][i & 3] = ld16(rw, lane * 16, OFF_KF + sh * 8192 + i * 1024);
#pragma unroll
    for (int i = 0; i < 8; ++i) vf[i] = ld16(rw, lane * 16, OFF_VF + sh * 8192 + i * 1024);
    const float NINF = -__builtin_inff();
    const f32x4 zero = {0.f, 0.f, 0.f, 0.f};
    float t[2][12], m[2];
    {
        f32x4 s0[2] = {zero, zero}, s1 = zero;
#pragma unroll
        for (int ks = 0; ks < 4; ++ks) { s0[0] = mfma32(kf[0][ks], qf[0][ks], s0[0]); s0[1] = mfma32(kf[0][ks], qf[1][ks], s0[1]); s1 = mfma32(kf[1][ks], qf[1][ks], s1); }
#pragma unroll
        for (int r = 0; r < 4; ++r) {
            const bool diag = 4 * g + r <= c;
            t[0][r] = diag ? s0[0][r] * SCL : NINF; t[1][r] = s0[1][r] * SCL; t[1][4 + r] = diag ? s1[r] * SCL : NINF;
        }
        m[0] = fmaxf(fmaxf(t[0][0], t[0][1]), fmaxf(t[0][2], t[0][3]));
        m[1] = fmaxf(fmaxf(fmaxf(t[1][0], t[1][1]), fmaxf(t[1][2], t[1][3])), fmaxf(fmaxf(t[1][4], t[1][5]), fmaxf(t[1][6], t[1][7])));
#pragma unroll
        for (int r = 0; r < 4; ++r) asm volatile("" : "+v"(t[0][r]), "+v"(t[1][r]), "+v"(t[1][4 + r]));
        asm volatile("" : "+v"(m[0]), "+v"(m[1]));
#pragma unroll
        for (int i = 0; i < 8; ++i) asm volatile("" : "+v"(vf[i]));
    }
    u32x4 one4 = {0x3F803F80u, 0x3F803F80u, 0x3F803F80u, 0x3F803F80u};
    const float z2 = g >= 2 ? 0.f : NINF;
    f32x4 s2[2] = {{z2, z2, z2, z2}, {z2, z2, z2, z2}};
    asm volatile("" : "+v"(s2[0]), "+v"(s2[1]));
    XST(s == 63, 64 + h * 4);
    wait_ctr(p, rw, C_KM + h * 32 + (s == NSEG - 1), s + 1);
    XST(s == 63, 64 + h * 4 + 1);
    for (unsigned n = SPIN_CAP;;) {
        asm volatile("" ::: "memory");
#pragma unroll
        for (int i = 0; i < 4; ++i) km[i] = ld16(rw, (g * 8 + (c & 7)) * 16, OFF_KM + sh * 4096 + i * 512);
#pragma unroll
        for (int i = 0; i < 8; ++i) vm[i] = ld8(rw, (lane & 31) * 8, OFF_KM + sh * 4096 + 2048 + i * 256);
        if (s < NSEG - 1) break;
        unsigned mx = 0;
#pragma unroll
        for (int i = 0; i < 4; ++i) mx = mx4(mx, km[i]);
#pragma unroll
        for (int i = 0; i < 8; ++i) mx = max(mx, max(vm[i].x, vm[i].y));
        if (__builtin_amdgcn_ballot_w64(mx == SENT) == 0) break;
        if (--n == 0) { set_tmo(p); break; }
    }
    SB();
    SB();
#pragma unroll
    for (int ks = 0; ks < 4; ++ks)
#pragma unroll
        for (int qt = 0; qt < 2; ++qt) mfma_v(s2[qt], km[ks], qf[qt][ks]);
    asm volatile("s_nop 7" : "+v"(s2[0]), "+v"(s2[1]));
    u32x4 p01[2], p2[2];
#pragma unroll
    for (int qt = 0; qt < 2; ++qt) {
#pragma unroll
        for (int r = 0; r < 4; ++r) t[qt][8 + r] = s2[qt][r] * SCL;
        m[qt] = fmaxf(fmaxf(m[qt], fmaxf(t[qt][8], t[qt][9])), fmaxf(t[qt][10], t[qt][11]));
    }
#pragma unroll
    for (int qt = 0; qt < 2; ++qt) m[qt] = grp_max(m[qt]);
#pragma unroll
    for (int qt = 0; qt < 2; ++qt) {
#pragma unroll
        for (int r = 0; r < 12; ++r) if (qt == 1 || r < 4 || r >= 8) t[qt][r] = __builtin_amdgcn_exp2f(t[qt][r] - m[qt]);
        p01[qt] = (u32x4){pk2(t[qt][0], t[qt][1]), pk2(t[qt][2], t[qt][3]), qt ? pk2(t[qt][4], t[qt][5]) : 0u, qt ? pk2(t[qt][6], t[qt][7]) : 0u};
        p2[qt] = (u32x4){pk2(t[qt][8], t[qt][9]), pk2(t[qt][10], t[qt][11]), 0u, 0u};
    }
    u32x4 vq[4];
#pragma unroll
    for (int i = 0; i < 4; ++i) vq[i] = (u32x4){vm[2 * i].x, vm[2 * i].y, vm[2 * i + 1].x, vm[2 * i + 1].y};
    u32x4 p2h[2] = {{0u, 0u, p2[0].x, p2[0].y}, {0u, 0u, p2[1].x, p2[1].y}};
    asm volatile("s_nop 1" : "+v"(p01[0]), "+v"(p01[1]), "+v"(p2[0]), "+v"(p2[1]), "+v"(p2h[0]), "+v"(p2h[1]), "+v"(vq[0]), "+v"(vq[1]), "+v"(vq[2]), "+v"(vq[3]), "+v"(one4));
    f32x4 l[2], o[2][8];
#pragma unroll
    for (int qt = 0; qt < 2; ++qt) mfma_0(l[qt], one4, p01[qt]);
#pragma unroll
    for (int qt = 0; qt < 2; ++qt) mfma_v(l[qt], one4, p2[qt]);
#pragma unroll
    for (int et = 0; et < 8; ++et)
#pragma unroll
        for (int qt = 0; qt < 2; ++qt) { mfma_0(o[qt][et], vf[et], p01[qt]); mfma_v(o[qt][et], vq[et >> 1], et & 1 ? p2h[qt] : p2[qt]); }
    asm volatile("s_nop 3" : "+v"(l[0]), "+v"(l[1]), "+v"(o[0][0]), "+v"(o[0][1]), "+v"(o[1][0]), "+v"(o[1][1]));
    const float inv[2] = {__builtin_amdgcn_rcpf(l[0][0]), __builtin_amdgcn_rcpf(l[1][0])};
#pragma unroll
    for (int tp = 0; tp < 4; ++tp) {
        asm volatile("" : "+v"(o[0][2 * tp]), "+v"(o[0][2 * tp + 1]), "+v"(o[1][2 * tp]), "+v"(o[1][2 * tp + 1]));
#pragma unroll
        for (int qt = 0; qt < 2; ++qt) {
            const f32x4 a = o[qt][2 * tp] * inv[qt], b = o[qt][2 * tp + 1] * inv[qt];
            const u32x4 ov = {nosent(pk2(a[0], a[1])), nosent(pk2(a[2], a[3])), nosent(pk2(b[0], b[1])), nosent(pk2(b[2], b[3]))};
            st16(ov, rw, lane * 16, OFF_OT + ((2 * s + qt) * 32 + h) * 4096 + tp * 1024);
        }
        SB();
    }
    if (s == NSEG - 1) st4(1u, rw, lane == 0 ? (C_OF + (h >> 3) * 32 + (h & 7)) * 4 : OOB, 0);
    XST(s == 63, 64 + h * 4 + 2);
    wait_vm<0>();
    XST(s == 63, 64 + h * 4 + 3);
    if (lane == 0) add_ctr(p, C_OT + s * 32, 1u);
}

constexpr int N_T = 24, N_WG = 4, W_FIRST = 64;
DI void wo_stage(const Params& p, unsigned ldsw, int h0, int cs, int lane) {
    const rsrc_t ro = mk_rsrc(p.wo, 4096 * 1280);
    const unsigned v = (lane >> 2) * 1280 + (lane & 3) * 16;
#pragma unroll
    for (int i = 0; i < 16; ++i) dma16(ro, v, (h0 * 128 + 16 * i) * 1280 + cs * 64, ldsw + i * 1024);
}
DI void w_role(const Params& p, LAS uchar* lds, int cs, int rank) {
    const int lane = fresh_lane(), w = __builtin_amdgcn_readfirstlane(threadIdx.x >> 6), c = lane & 15, g = lane >> 4;
    const rsrc_t rw = mk_rsrc(p.ws, WS_USED);
    u32x4 wf[8][4][2];
    {
        LAS uchar* lw = lds + w * 32768;
        const int q = c >> 2, pp = c & 3;
        wo_stage(p, (unsigned)(size_t)lw, 8 * w, cs, lane);
        wo_stage(p, (unsigned)(size_t)lw + 16384, 8 * w + 2, cs, lane);
#pragma unroll
        for (int rd = 0; rd < 4; ++rd) {
            if (rd < 3) wait_vm<16>(); else wait_vm<0>();
#pragma unroll
            for (int i = 0; i < 2; ++i)
#pragma unroll
                for (int tp = 0; tp < 4; ++tp)
#pragma unroll
                    for (int nt = 0; nt < 2; ++nt) {
                        LAS uchar* a = lw + (rd & 1) * 16384 + i * 8192 + (32 * tp + 4 * g + q) * 64 + nt * 32 + pp * 8;
                        const u32x2 lo = trd(a), hi = trd(a + 1024);
                        wf[2 * rd + i][tp][nt] = (u32x4){lo.x, lo.y, hi.x, hi.y};
                        if (2 * rd + i < 7) asm volatile("" : "+a"(wf[2 * rd + i][tp][nt])); else asm volatile("" : "+v"(wf[2 * rd + i][tp][nt]));
                    }
            asm volatile("s_waitcnt lgkmcnt(0)" ::: "memory");
            SB();
            if (rd < 2) wo_stage(p, (unsigned)(size_t)lw + (rd & 1) * 16384, 8 * w + 2 * rd + 4, cs, lane);
        }
    }
    bar();
    LAS f32x2* red = (LAS f32x2*)lds;
    volatile LAS int* nxt = (volatile LAS int*)(lds + 16384);
    const rsrc_t rx = mk_rsrc(p.x, S_ * 1280), ro = mk_rsrc(p.out, S_ * 1280);
    unsigned tk = 0;
    const unsigned wto = lane == 0 ? (C_WT + cs * 32) * 4 : OOB;
    auto draw = [&]() { tk = 1; asm volatile("buffer_atomic_add %0, %1, %2, 0 offen sc0" : "+v"(tk) : "v"(wto), "s"(rw) : "memory"); };
    auto tile_of = [&](int ticket, int cur) { return ticket + W_FIRST < 120 ? ticket + W_FIRST : (cur < 120 ? 120 + rank : (cur < 124 ? (rank < 2 ? 126 : 122) + rank : 128)); };
    u32x4 ob[8][4];
    int tt = 0;
    bool have = false, careful = false;
    if (w == 0) { draw(); asm volatile("s_waitcnt vmcnt(0)" : "+v"(tk)); *nxt = tile_of(__builtin_amdgcn_readfirstlane(tk), 0); draw(); }
    bar();
    tt = __builtin_amdgcn_readfirstlane(*nxt);
    while (tt < 2 * NSEG) {
        if (!have) {
            if (w == 0) { wait_ctr(p, rw, C_G + ((tt >> 4) * 5 + (cs >> 2)) * 32, 1); if (tt < 126 || careful) wait_ctr(p, rw, C_OT + (tt >> 1) * 32, 32); }
            bar();
            if (tt >= 126 && !careful) {
                for (unsigned n = SPIN_CAP;;) {
                    asm volatile("" ::: "memory");
                    if (__builtin_amdgcn_ballot_w64(ld4(rw, (lane & 7) * 4, (C_OF + w * 32) * 4) == 0) == 0) break;
                    if (--n == 0) { set_tmo(p); break; }
                }
            }
            XST(w == 0 && tt >= 126, 256 + ((tt - 126) * 20 + cs) * 4);
#pragma unroll
            for (int i = 0; i < 32; ++i) ld16_s(ob[i >> 2][i & 3], rw, lane * 16, OFF_OT + (tt * 32 + 8 * w) * 4096 + i * 1024);
        }
        f32x4 acc[2][2];
#pragma unroll
        for (int i = 0; i < 4; ++i) { acc[i >> 1][i & 1] = (f32x4){0.f, 0.f, 0.f, 0.f}; asm volatile("" : "+v"(acc[i >> 1][i & 1])); }
        const unsigned xo = c * 1280 + g * 8, xs = tt * 16 * 1280 + cs * 64 + (w >> 1) * 32 + (w & 1) * 4;
        unsigned xv, gv;
        asm volatile("buffer_load_dword %0, %1, %2, %3 offen" : "=v"(xv) : "v"(xo), "s"(rx), "s"(xs) : "memory");
        asm volatile("buffer_load_dword %0, %1, %2, %3 offen sc1" : "=v"(gv) : "v"(lane * 16), "s"(rw), "s"(OFF_GF + (tt * 20 + cs) * 1024 + w * 4) : "memory");
        if (w == 0) {
            asm volatile("s_waitcnt vmcnt(32)" : "+v"(tk));
            const int tn = tile_of(__builtin_amdgcn_readfirstlane(tk), tt);
            int rd = 0;
            if (tn < 126) {
                const unsigned f = ld4(rw, (lane == 0 ? C_G + ((tn >> 4) * 5 + (cs >> 2)) * 32 : C_OT + (tn >> 1) * 32) * 4, 0);
                rd = __builtin_amdgcn_ballot_w64(f < (lane == 0 ? 1u : 32u)) == 0 ? 0x10000 : 0;
            }
            nxt[1] = tn | rd;
        }
        bar();
        XST(w == 0 && tt >= 126, 256 + ((tt - 126) * 20 + cs) * 4 + 1);
        const int nx = __builtin_amdgcn_readfirstlane(nxt[1]);
        have = (nx >> 16) != 0;
        const rsrc_t rn = mk_rsrc(p.ws, have ? WS_USED : 0);
        const unsigned nb = OFF_OT + ((nx & 127) * 32 + 8 * w) * 4096;
        if (w == 0) draw();
#pragma unroll
        for (int i = 0; i < 8; ++i) {
            asm volatile("s_waitcnt vmcnt(28)" : "+v"(ob[i][0]), "+v"(ob[i][1]), "+v"(ob[i][2]), "+v"(ob[i][3]));
#pragma unroll
            for (int tp = 0; tp < 4; ++tp)
#pragma unroll
                for (int n = 0; n < 2; ++n) { if (i < 7) mfma_av(acc[tp & 1][n], wf[i][tp][n], ob[i][tp]); else mfma_v(acc[tp & 1][n], wf[i][tp][n], ob[i][tp]); }
#pragma unroll
            for (int tp = 0; tp < 4; ++tp) ld16_s(ob[i][tp], rn, lane * 16, nb + (i * 4 + tp) * 1024);
            SB();
        }
        mfma_pad(acc[0][0], acc[0][1], acc[1][0], acc[1][1]);
#pragma unroll
        for (int n = 0; n < 2; ++n) {
            const f32x4 a = acc[0][n] + acc[1][n];
            red[((2 * n) * 4 + w) * 64 + lane] = (f32x2){a[0], a[1]};
            red[((2 * n + 1) * 4 + w) * 64 + lane] = (f32x2){a[2], a[3]};
        }
        bar();
        const f32x2 r = (red[(w * 4 + 0) * 64 + lane] + red[(w * 4 + 1) * 64 + lane]) + (red[(w * 4 + 2) * 64 + lane] + red[(w * 4 + 3) * 64 + lane]);
        asm volatile("s_waitcnt vmcnt(32)" : "+v"(xv), "+v"(gv));
        const bool redo = tt >= 126 && !careful && __builtin_amdgcn_ballot_w64(r[0] != r[0]) != 0;
        const unsigned sv = pk2(r[0], r[1]), m = pk2(bf_lo(sv) * bf_lo(gv), bf_hi(sv) * bf_hi(gv));
        __builtin_amdgcn_raw_buffer_store_b32(pk2(bf_lo(xv) + bf_lo(m), bf_hi(xv) + bf_hi(m)), ro, xo, xs, 0);
        XST(w == 0 && tt >= 126, 256 + ((tt - 126) * 20 + cs) * 4 + 2);
        if (!have) {
            wait_vm<0>();
#pragma unroll
            for (int i = 0; i < 8; ++i) asm volatile("" : "+v"(ob[i][0]), "+v"(ob[i][1]), "+v"(ob[i][2]), "+v"(ob[i][3]));
        }
        careful = redo;
        if (!redo) tt = nx & 0xFFFF;
    }
}

DI void smf_x(f32x4& c, u32x4 a, u32x8 b, int idx, bool acc) { if (acc) smf_a(c, a, b, idx); else smf_v(c, a, b, idx); }
#define TRY()
#define V_A 80
#define V_M 52
#define KM_DELAY 240
#define PAD0 0
#define PAD_UP 0
#define PAD_MAX 48
#define RISE 64
#define DECAY 8
#ifndef M_DELAY
#define M_DELAY 100
#endif
#define L_SMALL 96
#define F_SMALL 280
#ifndef M_TAIL
#define M_TAIL 40
#endif
#define D_MST 300
#define F_LATE 1000
#define V_LATE 8
#define DUR_A0 980
#define DUR_M0 700
#define FT(i)
#define FDUMP(blk, s, w)
#define STAT(blk, s, w, a, b, c, d)
DI int wave_max(int d) {
    d = max(d, __builtin_amdgcn_update_dpp(0, d, 0x121, 0xf, 0xf, true));
    d = max(d, __builtin_amdgcn_update_dpp(0, d, 0x122, 0xf, 0xf, true));
    d = max(d, __builtin_amdgcn_update_dpp(0, d, 0x124, 0xf, 0xf, true));
    d = max(d, __builtin_amdgcn_update_dpp(0, d, 0x128, 0xf, 0xf, true));
    auto r = __builtin_amdgcn_permlane32_swap((unsigned)d, (unsigned)d, false, false); d = max((int)r[0], (int)r[1]);
    auto q = __builtin_amdgcn_permlane16_swap((unsigned)d, (unsigned)d, false, false); d = max((int)q[0], (int)q[1]);
    return __builtin_amdgcn_readfirstlane(d);
}
struct Clock {
    unsigned r0, m0, cq, qc;
    DI void edge(unsigned& r, unsigned& m) {
        unsigned long long a, b;
        const unsigned first = rt32();
        do asm volatile("s_memrealtime %0\n\ts_memtime %1\n\ts_waitcnt lgkmcnt(0)" : "=s"(a), "=s"(b) :: "memory"); while ((unsigned)a == first);
        r = (unsigned)a << 3; m = (unsigned)b - 24;
    }
    DI void start() { edge(r0, m0); cq = 3000; qc = 22370; }
    DI void sync() {
        unsigned r, m; edge(r, m);
        const float f = (float)(m - m0) * __builtin_amdgcn_rcpf((float)(r - r0));
        asm volatile("" : "+s"(r), "+s"(m));
        cq = __builtin_amdgcn_readfirstlane((unsigned)(f * 1024.f));
        qc = __builtin_amdgcn_readfirstlane((unsigned)(65536.f * __builtin_amdgcn_rcpf(f)));
        r0 = r; m0 = m;
    }
    DI unsigned at(unsigned m) const { return r0 + (unsigned)(((unsigned long long)(m - m0) * qc) >> 16); }
    DI static void sample(unsigned long long& a, unsigned long long& b) { asm volatile("s_memrealtime %0\n\ts_memtime %1\n\ts_waitcnt lgkmcnt(0)" : "=s"(a), "=s"(b) :: "memory"); }
    DI unsigned value(unsigned long long a, unsigned long long b) const {
        const unsigned lo = (unsigned)a << 3;
        return lo + __builtin_amdgcn_readfirstlane(min(max((int)(at((unsigned)b) - lo), 0), 31));
    }
    DI unsigned now() const { unsigned long long a, b; sample(a, b); return value(a, b); }
    DI unsigned cycles(unsigned t) const { const int d = __builtin_amdgcn_readfirstlane(min(max((int)(t - r0), 0), 1 << 20)); return m0 + (((unsigned)d * cq) >> 10) - 26; }
};
DI unsigned waitmt(unsigned t) { unsigned n; do n = mt32(); while ((int)(n - t) < 0); return n; }
#define MST_SH 2
DI unsigned mst_off(int m) { return (m >> MST_SH) * 128 + (m & ((1 << MST_SH) - 1)) * 4; }
struct Sched {
    unsigned tgo, tau, durA, durM, pad;
    unsigned a, v;
    DI void fetch_a(rsrc_t rw, unsigned exo, unsigned pol, int s, int lane) { a = ld4(rw, s >= 2 ? (lane & 31) * 4 : OOB, ex_step(exo, pol, max(s - 2, 0)) + EX_AST); }
    DI void fetch_m(rsrc_t rw, unsigned exo, unsigned pol, int s, int lane) { v = ld4(rw, mst_off(min(lane, 39)), ex_step(exo, pol, s - 1) + EX_MST); }
    DI void take_a() { const int m = wave_max((int)a); durA += __builtin_amdgcn_ballot_w64(a - 1u >= 0xFFFFu) == 0 ? (min(m - (int)durA, RISE) + 2) >> 2 : 0; }
    DI void next(const Params& p, rsrc_t rw, unsigned exo, unsigned pol, int s, int lane, unsigned long long& alive) {
        if ((__builtin_amdgcn_ballot_w64(v == SENT) & alive) != 0) {
            const unsigned mo = ex_step(exo, pol, s - 1) + EX_MST;
            unsigned n = SPIN_CAP; bool bad;
#pragma nounroll
            do {
                asm volatile("" ::: "memory");
                v = ld4(rw, mst_off(min(lane, 39)), mo);
                bad = __builtin_amdgcn_ballot_w64(v == SENT) != 0;
            } while (bad && --n);
            if (bad) { alive = 0; set_tmo(p); }
        }
        const unsigned ref = __builtin_amdgcn_readfirstlane(v);
        const unsigned pm = ref + (unsigned)wave_max((int)(v - ref));
        if (s >= 2 && __builtin_amdgcn_ballot_w64(v & 1) == 0) durM += (min((int)(pm - tau - durM), RISE) + 2) >> 2;
        const bool late = s == 1 || (int)(pm + V_LATE - tgo) > 0;
        pad = late || __builtin_amdgcn_ballot_w64(v & 1) != 0 ? min(pad + PAD_UP, (unsigned)PAD_MAX) : (pad > 4 ? pad - 4 : 0);
        tau = (late ? pm + F_LATE : tgo) + durA + pad;
        tgo = tau + durM + V_A + pad;
    }
};

#define PACE 8
DI void pace() { asm volatile("s_nop %0" :: "n"(PACE)); }
DI void role_M(const Params& p, LAS uchar* lds, int mi) {
    const int tid = threadIdx.x, lane = tid & 63, w = __builtin_amdgcn_readfirstlane(tid >> 6), c = lane & 15, g = lane >> 4;
    const int qh = mi & 1, cs = mi >> 1;
    const rsrc_t rw = mk_rsrc(p.ws, WS_USED);
    u32x8 wf[8][2][2];
    waitrt(rt32() + M_DELAY);
    {
        LAS uchar* lw = lds + w * 32768;
        const int q = c >> 2, pp = c & 3;
        unsigned ra[4];
#pragma unroll
        for (int m = 0; m < 4; ++m) {
            const int k0 = m < 2 ? 8 * g + 4 * m : 32 + 8 * g + 4 * (m - 2), ga = k0 >> 4, qq = (k0 >> 2) & 3;
            const int e = 32 * ga + 2 * qq + (q & 1) + 8 * (q >> 1);
            ra[m] = e * 64 + pp * 8;
        }
        wo_stage(p, (unsigned)(size_t)lw, 8 * w, cs, lane);
        wo_stage(p, (unsigned)(size_t)lw + 16384, 8 * w + 2, cs, lane);
#pragma unroll
        for (int rd = 0; rd < 4; ++rd) {
            if (rd < 3) wait_vm<16>(); else wait_vm<0>();
#pragma unroll
            for (int i = 0; i < 2; ++i)
#pragma unroll
                for (int sel = 0; sel < 2; ++sel)
#pragma unroll
                    for (int nt = 0; nt < 2; ++nt) {
                        LAS uchar* a = lw + (rd & 1) * 16384 + i * 8192 + sel * 1024 + nt * 32;
                        const u32x2 t0 = trd(a + ra[0]), t1 = trd(a + ra[1]), t2 = trd(a + ra[2]), t3 = trd(a + ra[3]);
                        wf[2 * rd + i][sel][nt] = (u32x8){t0.x, t0.y, t1.x, t1.y, t2.x, t2.y, t3.x, t3.y};
                        if (2 * rd + i < 6) asm volatile("" : "+a"(wf[2 * rd + i][sel][nt])); else asm volatile("" : "+v"(wf[2 * rd + i][sel][nt]));
                    }
            asm volatile("s_waitcnt lgkmcnt(0)" ::: "memory");
            SB();
            if (rd < 2) wo_stage(p, (unsigned)(size_t)lw + (rd & 1) * 16384, 8 * w + 2 * rd + 4, cs, lane);
        }
    }
    bar();
    if (tid < 2) *(LAS unsigned*)(lds + 4096 + tid * 4) = 0;
    bar();
    const int idx = (c & 4) ? 0xEEEE : 0x4444;
    const int dl = 32 * cs + 2 * c;
    const unsigned so = mf_off(dl >> 6) + (((dl >> 4) & 3) * 16 + qh * 8 + ((dl >> 3) & 1) * 4 + g) * 16 + (dl & 7) * 2;
    const unsigned rwo = (w * 128 + (2 * (g & 1)) * 32 + 16 * (g >> 1) + c) * 4, rro = (g * 32 + 2 * c) * 4;
    const unsigned lo16 = lane * 16, sto = lane == 0 ? mst_off(mi) : OOB;
    unsigned long long alive = ~0ull;
    unsigned budget = SPIN_CAP;
    const u32x2 tag = launch_tag(rw);
    wait_boot(rw, tag);
    if (fresh_tid() == 0) add_ctr(p, C_ACK, 1u);
    const unsigned exo = ex_off(p), pol = read_pol(rw, tag);
    wait_ctr(p, rw, C_PZ, 56);
    const unsigned obo = uni(w * 32768 + qh * 1024);
    Clock ck; ck.start();
    unsigned tdone = ck.now();
    Sched sc = {0u, tdone - V_M, DUR_A0, DUR_M0, PAD0, 0u, 0u};
    for (int s = 0; s < NSEG - 1; ++s) {
        const unsigned eb = ex_step(exo, pol, s);
        const unsigned ob = eb + obo;
        if (s == 3) wait_ctr(p, rw, C_PZ + 32, 56);
        if (s > 0) {
            sc.fetch_a(rw, exo, pol, s, lane);
            ck.sync();
            sc.take_a();
            SB();
            waitmt(ck.cycles(tdone + D_MST));
            sc.fetch_m(rw, exo, pol, s, lane);
            sc.next(p, rw, exo, pol, s, lane, alive);
        }
        const unsigned tau = sc.tau;
        unsigned obs[8], mb = ex_step(exo, pol, s + 1);
#pragma unroll
        for (int i = 0; i < 8; ++i) { obs[i] = ob + (i >> 2) * 16384 + (i & 3) * 2048; asm volatile("" : "+s"(obs[i])); }
        unsigned rwa = rwo + (s & 1) * 2048, rra = rro + (s & 1) * 2048;
        f32x4 acc[2][2];
#pragma unroll
        for (int i = 0; i < 4; ++i) { acc[i >> 1][i & 1] = (f32x4){0.f, 0.f, 0.f, 0.f}; asm volatile("" : "+v"(acc[i >> 1][i & 1])); }
        unsigned tw = ck.cycles(tau + V_M);
        asm volatile("" : "+s"(mb), "+v"(rwa), "+v"(rra), "+s"(tw));
        waitmt(tw);
        FT(0);
        u32x4 of[8];
#pragma unroll
        for (int i = 0; i < 8; ++i) { of[i] = ld16(rw, lo16, obs[i]); SB(); if (i < 7) pace(); SB(); }
        auto mix = [&]() {
#pragma unroll
            for (int i = 0; i < 8; ++i) {
                smf_x(acc[0][1], of[i], wf[i][0][1], idx, i < 6);
                smf_x(acc[1][0], of[i], wf[i][1][0], idx, i < 6);
                smf_x(acc[0][0], of[i], wf[i][0][0], idx, i < 6);
                smf_x(acc[1][1], of[i], wf[i][1][1], idx, i < 6);
                if (i == 0) FT(1);
                if (i == 7) FT(2);
                SB();
            }
            asm volatile("s_nop 2" : "+v"(acc[0][1]), "+v"(acc[1][0]));
            float u[4], v[4], y[4];
#pragma unroll
            for (int r = 0; r < 4; ++r) {
                const float fa = acc[1][0][r], fb = acc[0][1][r];
                auto q = __builtin_amdgcn_permlane32_swap(__builtin_bit_cast(unsigned, fa), __builtin_bit_cast(unsigned, fb), false, false);
                u[r] = __builtin_bit_cast(float, (unsigned)q[0]); v[r] = __builtin_bit_cast(float, (unsigned)q[1]);
            }
            SB();
            asm volatile("s_nop 0" : "+v"(acc[0][0]), "+v"(acc[1][1]));
#pragma unroll
            for (int r = 0; r < 4; ++r) { float lo = acc[0][0][r] + v[r], hi = acc[1][1][r] + u[r]; asm("" : "+v"(lo), "+v"(hi)); y[r] = g < 2 ? lo : hi; }
            const float z0 = swap16_add(y[0], y[2]), z1 = swap16_add(y[1], y[3]);
            *(LAS float*)(lds + rwa) = z0;
            *(LAS float*)(lds + rwa + 128) = z1;
            SB();
            return __builtin_amdgcn_ballot_w64(__builtin_isunordered(z0, z1));
        };
        if (__builtin_expect(mix() != 0, 0)) {
            if (refetch<8>(of, rw, lo16, obs, budget)) {
                TRY();
                *(LAS unsigned*)(lds + 4096 + (s & 1) * 4) = s + 1;
#pragma unroll
                for (int i = 0; i < 4; ++i) { acc[i >> 1][i & 1] = (f32x4){0.f, 0.f, 0.f, 0.f}; asm volatile("" : "+v"(acc[i >> 1][i & 1])); }
                asm volatile("s_nop 1" ::: "memory");
                mix();
            }
        }
        FT(3);
        bar();
        FT(4);
        if (__builtin_expect(w == 0, 1)) {
            LAS f32x2* r2 = (LAS f32x2*)(lds + rra);
            const f32x2 a0 = r2[0], a1 = r2[64], a2 = r2[128], a3 = r2[192];
            const unsigned fl = *(LAS unsigned*)(lds + 4096 + (s & 1) * 4);
            SB();
            unsigned long long ca, cb;
            Clock::sample(ca, cb);
            SB();
            const f32x2 v = (a0 + a1) + (a2 + a3);
            st4(nosent(pk2(v[0], v[1])), rw, so, mb);
            SB();
            tdone = ck.value(ca, cb) + M_TAIL;
            st4(nosent((tdone & ~1u) | (fl == (unsigned)(s + 1))), rw, sto, eb + EX_MST);
        } else tdone = ck.now() + M_TAIL;
        if (budget == 0) set_tmo(p);
        FT(5); FDUMP(32 + mi, s, w);
    }
}

constexpr int RING = 4, VPRE = 4;
constexpr int a_slot(int ks, int tile) { return ks * 16384 + tile * 2048; }
constexpr int a_xblk(int i) { return a_slot(i & 1 ? 9 : 4, i & 2 ? 5 : 2); }
constexpr int A_P = a_slot(4, 7), A_P2 = A_P + 1024, A_CNT = A_P2 + 512;
template <int W> struct AP {
    static constexpr int ROT = 3 * W, NT = W == 0 ? 0 : (W == 3 ? 2 : 3), T0 = W == 0 ? 0 : 3 * (W - 1);
    static constexpr int NL = W == 0 ? 0 : (W == 3 ? 9 : 14);
    static constexpr int QA = W == 0 || W == 3 ? 20 : 15;
    static constexpr bool k_lds(int t, int m) { return W == 0 ? false : (W == 3 ? (t == 1 && m >= 1) : (t == 1 || (m > 0 && (m & 1) == 0))); }
    static constexpr bool k_acc(int t, int m) { return W == 0 && m < 5; }
    static constexpr bool v_lds(int t, int ks) { return W == 3 ? ks < 4 : (ks < 4 || (ks == 4 && t == 0)); }
    static constexpr int k_idx(int t, int m) { int n = 0; for (int i = 0; i < 2 * m + 1 - t; ++i) n += k_lds(1 - (i & 1), i >> 1) ? 1 : 0; return n; }
    static constexpr int k_slot(int i) { return a_slot(5 + i / (NT ? NT : 1), T0 + i % (NT ? NT : 1)); }
    static constexpr int v_slot(int i) { return a_slot(i / (NT ? NT : 1), T0 + i % (NT ? NT : 1)); }
    static constexpr int stream(int i) { return i < NL ? k_slot(i) : v_slot(i - NL); }
    static constexpr int m_of(int ks) { return (ks + 10 - ROT % 10) % 10; }
};
DI void a_dma(const uchar* wsrc, LAS uchar* lds, int h, int w, int half) {
    const int lane = fresh_lane();
    const rsrc_t r = mk_rsrc(wsrc, 640 * 8192);
    const unsigned v = (lane >> 4) * 8192 + (((lane & 15) ^ (2 * (((lane >> 4) & 1) | (w << 1)))) << 4);
    const unsigned lb = (unsigned)(size_t)lds;
#pragma unroll
    for (int i = 20 * half; i < 20 * half + 20; ++i) dma16(r, v, (4 * (w + 4 * i)) * 8192 + h * 256, lb + (w + 4 * i) * 1024);
}
struct ATake {
    unsigned row[4], f[4];
    DI ATake() {
        const int lane = fresh_lane(), c = lane & 15, g = lane >> 4, q = c >> 2, pp = c & 3;
#pragma unroll
        for (int m = 0; m < 4; ++m) {
            const int k0 = m < 2 ? 8 * g + 4 * m : 32 + 8 * g + 4 * (m - 2), ga = k0 >> 4, qq = (k0 >> 2) & 3;
            const int r = 16 * ga + 2 * qq + (q & 1) + 8 * (q >> 1);
            row[m] = r * 256 + pp * 8; f[m] = 32 * ((r & 1) | ((r >> 1) & 6));
        }
    }
    DI u32x8 get(LAS uchar* lds, int ks, int tile) const {
        LAS uchar* a = lds + ks * 16384;
        const u32x2 t0 = trd(a + row[0] + (f[0] ^ (32 * tile))), t1 = trd(a + row[1] + (f[1] ^ (32 * tile))), t2 = trd(a + row[2] + (f[2] ^ (32 * tile))), t3 = trd(a + row[3] + (f[3] ^ (32 * tile)));
        return (u32x8){t0.x, t0.y, t1.x, t1.y, t2.x, t2.y, t3.x, t3.y};
    }
};
DI u32x8 ldsB(LAS uchar* a) { const u32x4 lo = *(LAS u32x4*)a, hi = *(LAS u32x4*)(a + 1024); return (u32x8){lo.x, lo.y, lo.z, lo.w, hi.x, hi.y, hi.z, hi.w}; }
DI void ldsB_put(LAS uchar* a, u32x8 v) { *(LAS u32x4*)a = (u32x4){v[0], v[1], v[2], v[3]}; *(LAS u32x4*)(a + 1024) = (u32x4){v[4], v[5], v[6], v[7]}; }
DI void pin(u32x8& v, bool acc) { if (acc) asm volatile("" : "+a"(v)); else asm volatile("" : "+v"(v)); }

#define NEXT_KV() do { SB(); const int sn_ = min(s + 1, NSEG - 1); kvn = ld4(rw, 0, kvw * 4); \
    _Pragma("unroll") for (int i = 0; i < NPF; ++i) pf[i] = ld16(rw, lo16, pfb + sn_ * 32 * 8192 + i * 1024); SB(); } while (0)
#define KM_OUT() do { unsigned n_ = 0; if (lane == 0) n_ = __hip_atomic_fetch_add((LAS unsigned*)(lds + A_CNT), 1u, __ATOMIC_RELAXED, __HIP_MEMORY_SCOPE_WORKGROUP); \
    if ((__builtin_amdgcn_readfirstlane(n_) & 3) == 3) st4(s + 1, rw, lane == 0 ? (C_KM + h * 32 + 1) * 4 : OOB, 0); } while (0)
template <int W>
DI void a_loop(const Params& p, LAS uchar* lds, int h, const u32x8 (&wq)[2][10], const u32x8 (&wk)[2][10], const u32x8 (&wv)[3][10]) {
    typedef AP<W> P;
    const int lane = fresh_lane(), c = lane & 15, g = lane >> 4;
    const rsrc_t rw = mk_rsrc(p.ws, WS_USED);
    constexpr int ROT = P::ROT, NT = P::NT, T0 = P::T0, NPF = W == 0 ? 8 : NT, NL = P::NL;
    const int idx = (c & 8) ? 0xEEEE : 0x4444;
    const unsigned lo16 = lane * 16, mo16 = (g * 16 + ((c >> 2) & 1) * 8 + (c >> 3) * 4 + (c & 3)) * 16;
    const unsigned oaddr = W == 0 ? (c >= 8 ? (g * 8 + (c & 7)) * 16 : OOB) : (c < 8 ? (c >> 2) * 1024 + (g >> 1) * 64 + (c & 3) * 16 + (g & 1) * 8 : OOB);
    const unsigned oaddr2 = c < 8 ? (c >> 2) * 1024 + (g & 1) * 128 + (g >> 1) * 64 + (c & 3) * 16 : OOB;
    const unsigned xw = a_xblk(W) + c * 40 + g * 8;
    const unsigned asto = lane == 0 ? h * 4 : OOB;
    const unsigned pfb = W == 0 ? OFF_KF + h * 8192 : OFF_VF + h * 8192 + T0 * 1024;
    u32x4 pf[NPF];
    unsigned long long alive = ~0ull;
    unsigned budget = SPIN_CAP;
    const u32x2 tag = launch_tag(rw);
    wait_boot(rw, tag);
    if (W == 0 && fresh_tid() == 0) add_ctr(p, C_ACK, 1u);
    const unsigned exo = ex_off(p), pol = read_pol(rw, tag);
    const unsigned obo = uni((h >> 2) * 16384 + (h & 3) * 2048 + T0 * 128);
    Clock ck; ck.start();
    wait_ctr(p, rw, C_PZ, 56);
    wait_ctr(p, rw, C_KVH + h * 16, 4);
#pragma unroll
    for (int i = 0; i < NPF; ++i) pf[i] = ld16(rw, lo16, pfb + i * 1024);
    Sched sc = {ck.now(), 0u, DUR_A0, DUR_M0, PAD0, 0u, 0u};
    const float ninf = g < 2 ? -__builtin_inff() : 0.f;
    unsigned xra[4], xqa[4];
    if (W == 0) {
#pragma unroll
        for (int ks = 0; ks < 4; ++ks) {
            xra[ks] = a_xblk(ks) + (4 * g + (c >> 2)) * 40 + (c & 3) * 8; xqa[ks] = a_xblk(ks) + (4 * g + (c >> 2)) * 40 + (c & 1) * 8;
            asm volatile("" : "+v"(xra[ks]), "+v"(xqa[ks]));
        }
    }
    u32x4 one4 = {0x3F803F80u, 0x3F803F80u, 0x3F803F80u, 0x3F803F80u};
    u32x8 pw = {0u, 0u, 0u, 0u, 0u, 0u, 0u, 0u};
    unsigned pa[3] = {A_P + lo16, A_P2 + lane * 8u, A_P2 + (lane ^ 32) * 8u};
    if (W) asm volatile("" : "+a"(one4), "+v"(pw), "+v"(pa[0]), "+v"(pa[1]), "+v"(pa[2]));
    unsigned lb[3] = {lo16, lo16 + 65536, lo16 + 131072};
    asm volatile("" : "+v"(lb[0]), "+v"(lb[1]), "+v"(lb[2]));
    auto sget = [&](int i) -> u32x8 { const int a = P::stream(i); return ldsB(lds + lb[a >> 16] + (a & 65535)); };
    for (int s = 0; s < NSEG; ++s) {
        const unsigned eb = ex_step(exo, pol, s);
        unsigned mb[10], ob = eb + obo;
#pragma unroll
        for (int m = 0; m < 10; ++m) { mb[m] = eb + mf_off((m + ROT) % 10); asm volatile("" : "+s"(mb[m])); }
        f32x4 aq[2], ak[2], va[3];
        u32x8 ring[RING];
#pragma unroll
        for (int t = 0; t < 2; ++t) { aq[t] = (f32x4){0.f, 0.f, 0.f, 0.f}; ak[t] = aq[t]; asm volatile("" : "+v"(aq[t]), "+v"(ak[t])); }
#pragma unroll
        for (int t = 0; t < 3; ++t) {
            const float z = W == 0 && t == 2 ? ninf : 0.f;
            va[t] = (f32x4){z, z, z, z}; asm volatile("" : "+v"(va[t]));
        }
        if (W) {
#pragma unroll
            for (int i = 0; i < RING; ++i) ring[i] = sget(i);
        }
        const unsigned tgo = sc.tgo;
        unsigned tw = ck.cycles(tgo);
        asm volatile("" : "+s"(ob), "+s"(tw));
        SB();
        unsigned mis = waitmt(tw);
        FT(0);
        u32x4 mf[10];
#pragma unroll
        for (int m = 0; m < 10; ++m) {
            mf[m] = ld16(rw, mo16, mb[m]);
            SB();
            if (m < 9) pace();
            SB();
        }
        auto project = [&]() {
            static_for<10>([&](auto mc) {
                constexpr int m = mc.value;
                auto qprod = [&](auto tc) { constexpr int t = tc.value; smf_x(aq[t], mf[m], wq[t][m], idx, 2 * m + t < P::QA - (W == 0 ? 10 : 0)); };
                auto kprod = [&](auto tc) {
                    constexpr int t = tc.value;
                    if constexpr (P::k_lds(t, m)) {
                        constexpr int i = P::k_idx(t, m);
                        smf_v(ak[t], mf[m], ring[i % RING], idx);
                        ring[i % RING] = sget(i + RING);
                    } else smf_x(ak[t], mf[m], wk[t][m], idx, W == 0);
                };
                kprod(IC<1>{});
                qprod(IC<m == 9 ? 1 : 0>{});
                if constexpr (m == 9) qprod(IC<0>{});
                kprod(IC<0>{});
                if constexpr (m < 9) qprod(IC<1>{});
                if (m == 0) FT(1);
                if (m == 9) FT(2);
                SB();
            });
            float x[4], y[4];
            asm volatile("s_nop 2" : "+v"(aq[1]), "+v"(ak[1]));
#pragma unroll
            for (int r = 0; r < 4; ++r) y[r] = swap32_add(aq[1][r], ak[1][r]);
            *(LAS u32x2*)(lds + xw + 640) = (u32x2){pk2(y[0], y[1]), pk2(y[2], y[3])};
            SB();
            asm volatile("" : "+v"(aq[0]), "+v"(ak[0]));
#pragma unroll
            for (int r = 0; r < 4; ++r) x[r] = swap32_add(aq[0][r], ak[0][r]);
            *(LAS u32x2*)(lds + xw) = (u32x2){pk2(x[0], x[1]), pk2(x[2], x[3])};
            SB();
            return nan_lanes((f32x4){x[0], x[1], x[2], x[3]});
        };
        if (__builtin_expect(project() != 0, 0)) {
            if (refetch<10>(mf, rw, mo16, mb, budget)) {
                mis = tw + 100000; TRY();
#pragma unroll
                for (int t = 0; t < 2; ++t) { aq[t] = (f32x4){0.f, 0.f, 0.f, 0.f}; ak[t] = aq[t]; asm volatile("" : "+v"(aq[t]), "+v"(ak[t])); }
                if (W) {
#pragma unroll
                    for (int i = 0; i < RING; ++i) ring[i] = sget(i);
                }
                asm volatile("s_nop 1" ::: "memory");
                project();
            }
        }
        SB();
        if (W) {
#pragma unroll
            for (int i = 0; i < VPRE; ++i) { const int r = 10 * NT - 1 - i; smf_a(va[r % NT], mf[P::m_of(r / NT)], wv[r % NT][r / NT], idx); }
        }
        bar();
        SB();
        FT(3);
        const unsigned kb = OFF_KM + (s * 32 + h) * 4096;
        const int kvs = min(s + 2, NSEG - 1), kvw = (kvs < 16 && (kvs & 4) == 0 ? C_KVH : C_KV) + ((kvs >> 3) * 32 + h) * 16;
        unsigned tpub = 0, kvn;
        if (W == 0) {
            u32x4 xf[4], xd[4];
#pragma unroll
            for (int ks = 0; ks < 4; ++ks) {
                const u32x2 a = trd(lds + xqa[ks]), b = trd(lds + xqa[ks] + 640), lo = trd(lds + xra[ks]), hi = trd(lds + xra[ks] + 640);
                xd[ks] = (u32x4){a.x, a.y, b.x, b.y}; xf[ks] = (u32x4){lo.x, lo.y, hi.x, hi.y};
            }
            SB();
            if (__builtin_expect(s == NSEG - 1, 0)) {
#pragma unroll
                for (int ks = 0; ks < 4; ++ks) st16((u32x4){nosent(xf[ks].x), nosent(xf[ks].y), nosent(xf[ks].z), nosent(xf[ks].w)}, rw, oaddr, kb + ks * 512);
                KM_OUT();
            }
#pragma unroll
            for (int ks = 0; ks < 4; ++ks) { mfma_v(va[0], pf[ks], xd[ks]); mfma_v(va[1], pf[(4 + ks) % NPF], xd[ks]); mfma_v(va[2], xf[ks], xd[ks]); }
            FT(4);
            float a0, a1, a2, a3, a4, a5, tm, tn;
            unsigned e0, e1, e2;
            asm volatile("s_nop 3\n\t"
                         "v_cndmask_b32_e64 %0, %15, %11, %23\n\tv_cndmask_b32_e64 %1, %16, %12, %23\n\tv_cndmask_b32_e64 %2, %17, %13, %23\n\t"
                         "v_cndmask_b32_e64 %3, %18, %14, %23\n\tv_cndmask_b32_e64 %4, %21, %19, %23\n\tv_cndmask_b32_e64 %5, %22, %20, %23\n\t"
                         "v_max3_f32 %6, %0, %1, %2\n\tv_max3_f32 %7, %3, %4, %5\n\tv_max_f32 %6, %6, %7\n\ts_nop 1\n\t"
                         "v_max_f32_dpp %6, %6, %6 row_ror:8 row_mask:0xf bank_mask:0xf\n\t"
                         "v_mov_b32 %7, %6\n\ts_nop 1\n\tv_permlane32_swap_b32 %6, %7\n\tv_max_f32 %6, %6, %7\n\t"
                         "v_mov_b32 %7, %6\n\ts_nop 1\n\tv_permlane16_swap_b32 %6, %7\n\tv_max_f32 %6, %6, %7\n\t"
                         "v_mul_f32 %6, %24, %6\n\t"
                         "v_fma_f32 %0, %0, %24, -%6\n\tv_fma_f32 %1, %1, %24, -%6\n\tv_fma_f32 %2, %2, %24, -%6\n\t"
                         "v_fma_f32 %3, %3, %24, -%6\n\tv_fma_f32 %4, %4, %24, -%6\n\tv_fma_f32 %5, %5, %24, -%6\n\t"
                         "v_exp_f32 %0, %0\n\tv_exp_f32 %1, %1\n\tv_exp_f32 %2, %2\n\tv_exp_f32 %3, %3\n\tv_exp_f32 %4, %4\n\tv_exp_f32 %5, %5\n\t"
                         "v_cvt_pk_bf16_f32 %8, %0, %1\n\tv_cvt_pk_bf16_f32 %9, %2, %3\n\tv_cvt_pk_bf16_f32 %10, %4, %5"
                         : "=&v"(a0), "=&v"(a1), "=&v"(a2), "=&v"(a3), "=&v"(a4), "=&v"(a5), "=&v"(tm), "=&v"(tn), "=&v"(e0), "=&v"(e1), "=&v"(e2)
                         : "v"(va[0][0]), "v"(va[0][1]), "v"(va[0][2]), "v"(va[0][3]), "v"(va[1][0]), "v"(va[1][1]), "v"(va[1][2]), "v"(va[1][3]),
                           "v"(va[2][0]), "v"(va[2][1]), "v"(va[2][2]), "v"(va[2][3]), "s"(0x00FF00FF00FF00FFull), "s"(SCL));
            *(LAS u32x2*)(lds + A_P + (g * 16 + (c & 7)) * 16 + (c >> 3) * 8) = (u32x2){e0, e1};
            *(LAS unsigned*)(lds + A_P2 + (g * 16 + (c & 7)) * 8 + (c >> 3) * 4) = e2;
            FT(5);
            bar();
            FT(6);
            sc.fetch_a(rw, exo, pol, max(s, 1), lane); sc.fetch_m(rw, exo, pol, max(s, 1), lane);
            NEXT_KV();
            if (s < NSEG - 1) waitmt(mt32() + KM_DELAY * 3);
#pragma unroll
            for (int ks = 0; ks < 4; ++ks) if (s < NSEG - 1) st16(xf[ks], rw, oaddr, kb + ks * 512);
        } else {
            if (W == 3) st4(s, rw, asto, C_KM * 4 + h * 124);
#pragma unroll
            for (int i = 0; i < (10 * NT - NL - VPRE > NL ? 10 * NT - NL - VPRE : NL); ++i) {
                if (i < NL) {
                    smf_v(va[i % NT], mf[P::m_of(i / NT)], ring[(NL + i) % RING], idx);
                    if (NL + i + RING < 2 * NL) ring[(NL + i) % RING] = sget(NL + i + RING);
                }
                const int r = NL + i;
                if (r < 10 * NT - VPRE) smf_a(va[r % NT], mf[P::m_of(r / NT)], wv[r % NT][r / NT], idx);
                SB();
            }
            asm volatile("s_nop 7" : "+v"(va[0]), "+v"(va[1]), "+v"(va[2]));
            float sa[4], sb[4];
#pragma unroll
            for (int r = 0; r < 4; ++r) { sa[r] = swap32_add(va[0][r], va[1][r]); if (W != 3) sb[r] = swap32_add(va[2][r], va[2][r]); }
            u32x4 vv = {pk2(sa[0], sa[1]), pk2(sa[2], sa[3]), 0u, 0u};
            if (W != 3) { vv.z = pk2(sb[0], sb[1]); vv.w = pk2(sb[2], sb[3]); }
            asm volatile("" : "+v"(vv));
            if (__builtin_expect(s == NSEG - 1, 0)) {
                st8((u32x2){nosent(vv.x), nosent(vv.y)}, rw, lane * 8, kb + 2048 + T0 * 256);
                if (W != 3) st8((u32x2){nosent(vv.z), nosent(vv.w)}, rw, g < 2 ? lane * 8 : OOB, kb + 2048 + T0 * 256 + 512);
                KM_OUT();
            }
            SB();
            FT(5);
            bar();
            FT(6);
            SB();
            if (s < NSEG - 1) {
                const u32x4 p01 = *(LAS u32x4*)(lds + pa[0]);
                const u32x2 q2 = *(LAS u32x2*)(lds + pa[1]), q2s = *(LAS u32x2*)(lds + pa[2]);
                pw[4] = q2.x; pw[5] = q2.y; pw[0] = q2s.x; pw[1] = q2s.y;
                const u32x4 p2s = __builtin_shufflevector(pw, pw, 0, 1, 2, 3), p2h = __builtin_shufflevector(pw, pw, 2, 3, 4, 5), p2 = __builtin_shufflevector(pw, pw, 4, 5, 6, 7);
                SB();
                f32x4 l, o[3];
                asm volatile("v_mfma_f32_16x16x32_bf16 %0, %1, %2, 0" : "=&v"(l) : "a"(one4), "v"(p01));
                mfma_0(o[0], pf[0], p01);
                mfma_av(l, one4, p2);
                mfma_0(o[1], pf[1 % NPF], p01);
                mfma_v(o[0], vv, p2s);
                mfma_v(o[1], vv, p2);
                float inv;
                if (W != 3) {
                    mfma_0(o[2], pf[2 % NPF], p01);
                    asm volatile("v_rcp_f32 %0, %1" : "=v"(inv) : "v"(l[0]));
                    mfma_v(o[2], vv, p2h);
                    asm volatile("s_nop 3" : "+v"(o[0]), "+v"(o[1]), "+v"(inv));
                } else {
                    asm volatile("s_nop 1\n\tv_rcp_f32 %0, %3\n\ts_nop 5" : "=&v"(inv), "+v"(o[0]), "+v"(o[1]) : "v"(l[0]));
                }
                const f32x4 n0 = o[0] * inv, n1 = o[1] * inv;
                SB();
                const unsigned a0 = pk2(n0[0], n0[1]), b0 = pk2(n1[0], n1[1]), a1 = pk2(n0[2], n0[3]), b1 = pk2(n1[2], n1[3]);
                SB();
                auto r0 = __builtin_amdgcn_permlane16_swap(a0, b0, false, false);
                auto r1 = __builtin_amdgcn_permlane16_swap(a1, b1, false, false);
                st16((u32x4){nosent(r0[0]), nosent(r1[0]), nosent(r0[1]), nosent(r1[1])}, rw, oaddr2, ob);
                if (W != 3) {
                    asm volatile("" : "+v"(o[2]));
                    const f32x4 n2 = o[2] * inv;
                    st8((u32x2){nosent(pk2(n2[0], n2[1])), nosent(pk2(n2[2], n2[3]))}, rw, oaddr, ob + 256);
                }
            }
            tpub = ck.now();
            sc.fetch_a(rw, exo, pol, max(s, 1), lane); sc.fetch_m(rw, exo, pol, max(s, 1), lane);
            NEXT_KV();
            if (W == 1) st4(mis - tw <= 120u ? tpub - tgo : 0u, rw, asto, eb + EX_AST);
            if (s < NSEG - 1) st8((u32x2){vv.x, vv.y}, rw, lane * 8, kb + 2048 + T0 * 256);
            if (W != 3 && s < NSEG - 1) st8((u32x2){vv.z, vv.w}, rw, g < 2 ? lane * 8 : OOB, kb + 2048 + T0 * 256 + 512);
        }
        if (budget == 0) set_tmo(p);
        FT(7); FDUMP(h, s, W);
        STAT(h, s, W, ck.at(mis), tpub, tgo, tries);
        if (s == NSEG - 1) break;
        ck.sync();
        if (s > 0) { sc.take_a(); sc.next(p, rw, exo, pol, s, lane, alive); }
        if ((unsigned)__builtin_amdgcn_readfirstlane(kvn) < 4u) wait_ctr(p, rw, kvw, 4);
        if (s == 2) wait_ctr(p, rw, C_PZ + 32, 56);
    }
    XST(W == 0, h);
    if (W == 0 && h == 0) {
        wait_ctr(p, rw, C_ACK, 256);
        st8(tag, rw, lane == 0 ? 0 : OOB, B_EPOCH * 4);
    }
    __builtin_amdgcn_endpgm();
}

template <int W>
DI void a_wave(const Params& p, LAS uchar* lds, int h) {
    typedef AP<W> P;
    u32x8 wq[2][10], wk[2][10], wv[3][10];
    a_dma(p.wq, lds, h, W, 0);
    a_dma(p.wq, lds, h, W, 1);
    static_for<6>([&](auto sc) {
        constexpr int mat = sc.value >> 1, half = sc.value & 1;
        if (sc.value < 5) wait_vm<20>(); else wait_vm<0>();
        bar();
        AST_(sc.value);
        const ATake tk;
#pragma unroll
        for (int ks = 5 * half; ks < 5 * half + 5; ++ks) {
            const int m = P::m_of(ks);
#pragma unroll
            for (int t = 0; t < (mat < 2 ? 2 : P::NT); ++t) {
                if (mat == 0) { wq[t][m] = tk.get(lds, ks, 2 * W + t); pin(wq[t][m], 2 * m + t < P::QA - (W == 0 ? 10 : 0)); }
                if (mat == 1) { wk[t][m] = tk.get(lds, ks, 2 * W + t); pin(wk[t][m], W == 0); }
                if (mat == 2) { wv[t][ks] = tk.get(lds, ks, P::T0 + t); pin(wv[t][ks], true); }
            }
            SB();
        }
        bar(); SB();
        if (mat < 2) a_dma(mat == 0 ? p.wk : p.wv, lds, h, W, half);
        else {
            const int lane = fresh_lane();
            if (half == 0) {
#pragma unroll
                for (int i = 0; i < P::NL; ++i) ldsB_put(lds + P::v_slot(i) + lane * 16, wv[i % (P::NT ? P::NT : 1)][i / (P::NT ? P::NT : 1)]);
            } else {
#pragma unroll
                for (int i = 0; i < 20; ++i) if (P::k_lds(i & 1, i >> 1)) ldsB_put(lds + P::k_slot(P::k_idx(i & 1, i >> 1)) + lane * 16, wk[i & 1][i >> 1]);
            }
        }
        SB();
    });
    if (W == 0) *(LAS unsigned*)(lds + A_CNT) = 0;
    bar();
    SB();
    a_loop<W>(p, lds, h, wq, wk, wv);
}
DI void role_A(const Params& p, LAS uchar* lds, int h) {
    const int w = __builtin_amdgcn_readfirstlane(threadIdx.x >> 6);
    if (w == 0) a_wave<0>(p, lds, h);
    else if (w == 1) a_wave<1>(p, lds, h);
    else if (w == 2) a_wave<2>(p, lds, h);
    else a_wave<3>(p, lds, h);
}
DI int take_ticket(const Params& p, LAS uchar* lds, int word) {
    if (fresh_tid() == 0) *(LAS unsigned*)(lds + LDS_BYTES - 16) = __hip_atomic_fetch_add((unsigned*)p.ws + word, 1u, __ATOMIC_RELAXED, __HIP_MEMORY_SCOPE_AGENT);
    bar();
    const int t = __builtin_amdgcn_readfirstlane(*(LAS unsigned*)(lds + LDS_BYTES - 16));
    bar();
    return t;
}
#define TSTAT(i, a, b)
#define SMALL_DELAY 300
DI void warm_l2(const Params& p, int x, int j) {
    const int lane = fresh_lane(), id = j * 4 + __builtin_amdgcn_readfirstlane(threadIdx.x >> 6);
    u32x4 sink[46];
#pragma unroll
    for (int i = 0; i < 46; ++i) {
        const int n = min(id + 56 * i, 2559), row = n >> 1;
        const rsrc_t r = mk_rsrc(row < 640 ? p.wk : p.wv, 640 * 8192);
        asm volatile("buffer_load_dwordx4 %0, %1, %2, %3 offen" : "=v"(sink[i]) : "v"(lane * 16), "s"(r), "s"((row < 640 ? row : row - 640) * 8192 + x * 2048 + (n & 1) * 1024) : "memory");
    }
    wait_vm<0>();
#pragma unroll
    for (int i = 0; i < 46; ++i) asm volatile("" :: "v"(sink[i]));
}
DI void background(const Params& p, LAS uchar* lds, int x, int j) {
    const bool big = x >= 4;
    if (!big) {
        const int id = j * 4 + x;
        u32x2 tag;
        ln_tile<true>(p, id, tag);
        if (fresh_tid() == 0) add_ctr(p, C_ACK, 1u);
        const unsigned pol = read_pol(mk_rsrc(p.ws, WS_USED), tag);
        poison(p, pol, id, 0, 80, C_PZ);
        warm_l2(p, x, j);
        ln_tile<false>(p, id + 56, tag);
        if (id < 16) ln_tile<false>(p, id + 112, tag);
        poison(p, pol, id, 80, 1376, C_PZ + 32);
    } else {
        if (j < 12) probe(p, lds, j * 4 + x - 4);
        if (j & 2) kv0_task<true>(p, lds, 32 * (x - 4) + j); else kv0_task<false>(p, lds, 32 * (x - 4) + j);
        TSTAT((x - 4) * 128 + j, t0, 0);
    }
    if (!big) return;
    const int y = x - 4;
    for (;;) {
        int t = take_ticket(p, lds, C_Q1 + x * 32), kind = 0, chunk = 0, u0 = 0, nu = 8;
        const int wu = 192 + 8 * y, wn = y < 3 ? 4 : 2;
        if (t < 8) { chunk = t >> 1; u0 = wu + wn * (t & 1); nu = y < 3 ? 4 : 2 - (t & 1); }
        else if ((t -= 8) < 8) { chunk = 2; u0 = 32 * y + 4 * t; nu = 4; }
        else if ((t -= 8) < 8) { chunk = t >> 2; u0 = 128 + 16 * y + 4 * (t & 3); nu = 4; }
        else if ((t -= 8) < 8) { chunk = 3; u0 = 32 * y + 4 * t; nu = 4; }
        else if ((t -= 8) < 8) { chunk = 4 + (t >> 2); u0 = 32 * y + 8 * (t & 3); }
        else if ((t -= 8) < 5) { kind = 1; chunk = y; u0 = t; }
        else if ((t -= 5) < 4) { chunk = 4 + t; u0 = wu; nu = y < 3 ? 8 : 3; }
        else if ((t -= 4) < 6) { kind = 3; u0 = 6 * y + t; }
        else if ((t -= 6) < 4) { chunk = 2 + (t >> 1); u0 = 128 + 16 * y + 8 * (t & 1); }
        else if ((t -= 4) < 8) { chunk = 6 + (t >> 2); u0 = 32 * y + 8 * (t & 3); }
        else if ((t -= 8) < 8) { chunk = 4 + (t >> 1); u0 = 128 + 16 * y + 8 * (t & 1); }
        else if ((t -= 8) < 5) { kind = 1; chunk = 4 + y; u0 = t; }
        else if ((t -= 5) < 5) { kind = 2; chunk = y; u0 = t; }
        else if ((t -= 5) < 5 * N_WG) { kind = 4; u0 = t; }
        else return;
        if (kind == 0) p1_task(p, lds, chunk, u0, nu);
        else if (kind == 1) gemm_task<false>(p, lds, chunk, u0);
        else if (kind == 2) gemm_task<true>(p, lds, chunk, u0);
        else {
            wait_ctr(p, mk_rsrc(p.ws, WS_USED), C_PZ + 32, 56);
            if (kind == 3) {
                const int w = __builtin_amdgcn_readfirstlane(threadIdx.x >> 6);
                for (int s = u0 >> 3; s < NSEG; s += N_T / 8) attn_task(p, s, (u0 & 7) * 4 + w, (s & 7) < N_T / 8);
            } else { const int rank = (u0 * 52) >> 8; w_role(p, lds, 5 * y + u0 - 5 * rank, rank); }
            TSTAT(x * 128 + tk0, t0, kind + 2);
            return;
        }
        TSTAT(x * 128 + tk0, t0, kind == 0 ? (u0 < 128 ? 1 : (u0 < 192 ? 2 : 3)) : (kind == 1 ? 4 : 7));
    }
}
__global__ void __launch_bounds__(256, 1) k_main(Params p) {
    extern __shared__ __attribute__((aligned(16))) uchar smem[];
    const int x = blockIdx.x & 7, t = blockIdx.x >> 3;
    if (x < 4 && t < 8) role_A(p, (LAS uchar*)smem, x * 8 + t);
    else if (x < 4 && t < 18) role_M(p, (LAS uchar*)smem, x * 10 + t - 8);
    else background(p, (LAS uchar*)smem, x, x < 4 ? t - 18 : t);
}

extern "C" void kernel_launch(void* const* d_in, const int* in_sizes, int n_in, void* d_out, int out_size, void* d_ws, size_t ws_size, hipStream_t stream) {
    Params p;
    p.x = (const uchar*)d_in[0]; p.mem0 = (const uchar*)d_in[1]; p.wq = (const uchar*)d_in[2]; p.wk = (const uchar*)d_in[3];
    p.wv = (const uchar*)d_in[4]; p.wo = (const uchar*)d_in[5]; p.w1 = (const uchar*)d_in[8]; p.b1 = (const uchar*)d_in[9];
    p.w2 = (const uchar*)d_in[10]; p.out = (uchar*)d_out; p.ws = (uchar*)d_ws;
    (void)hipFuncSetAttribute((const void*)k_main, hipFuncAttributeMaxDynamicSharedMemorySize, LDS_BYTES);
    k_main<<<256, 256, LDS_BYTES, stream>>>(p);
}
```

```cpp
#include <hip/hip_runtime.h>
#include <stdint.h>

typedef __bf16 bf16x2 __attribute__((ext_vector_type(2)));
typedef __bf16 bf16x4 __attribute__((ext_vector_type(4)));
typedef __bf16 bf16x8 __attribute__((ext_vector_type(8)));
typedef __bf16 bf16x16 __attribute__((ext_vector_type(16)));
typedef short s16x4 __attribute__((ext_vector_type(4)));
typedef float f32x2 __attribute__((ext_vector_type(2)));
typedef float f32x4 __attribute__((ext_vector_type(4)));
typedef unsigned u32x2 __attribute__((ext_vector_type(2)));
typedef unsigned u32x4 __attribute__((ext_vector_type(4)));
typedef unsigned u32x8 __attribute__((ext_vector_type(8)));
typedef unsigned char uchar;
typedef __amdgpu_buffer_rsrc_t rsrc_t;
#define LAS __attribute__((address_space(3)))
#define DI __device__ __forceinline__

constexpr int S_ = 2048, D_ = 640, H_ = 32, NSEG = 64, F_ = 1728;
constexpr int LDS_BYTES = 163840;
constexpr int OFF_CTRL = 0;
constexpr int OFF_XF = 65536;
constexpr int OFF_KF = OFF_XF + 128 * 20 * 1024;
constexpr int OFF_QF = OFF_KF + 64 * 32 * 8192;
constexpr int OFF_VF = OFF_QF + 64 * 32 * 8192;
constexpr int OFF_ACT = OFF_VF + 64 * 32 * 8192;
constexpr int OFF_GF = OFF_ACT + 128 * 54 * 1024;
constexpr int OFF_OT = OFF_GF + 128 * 20 * 1024;
constexpr int OFF_KM = OFF_OT + 128 * 32 * 4096;
constexpr int OFF_EX = OFF_KM + 64 * 32 * 4096;
constexpr int EX_STEP = 163840, EX_MF = 8 * 16384, EX_AST = 9 * 16384 + 2048, EX_MST = 9 * 16384 + 2304, EX_PROBE = 9 * 16384 + 7168;
constexpr int WS_USED = OFF_EX + 7 * 2097152;
constexpr int C_TMO = 0, C_ACK = 32, C_KV = 512  , C_Q = C_KV + 4096, C_ACT = C_Q + 4096  , C_G = C_ACT + 256  ,
              C_OT = C_G + 1280  , C_KM = C_OT + 2048  , C_Q1 = C_KM + 1024  , C_KVH = C_Q1 + 256  , C_X = C_KVH + 1024  , C_PZ = C_X + 512  , C_WT = C_PZ + 64  , C_OF = C_WT + 640  ,
              C_ZEND = 16 * 1008;
constexpr int B_INIT = C_ZEND  , B_VOTE = B_INIT + 112  , B_EPOCH = 16352;
static_assert(C_OF + 128 <= C_ZEND && B_VOTE + 96 <= B_EPOCH && B_EPOCH + 2 <= 16384, "");
constexpr unsigned SENT = 0xFFFFFFFFu;
constexpr int SC1 = 16;
constexpr unsigned OOB = 0x7FFFFF00u;
constexpr unsigned SPIN_CAP = 400000;

struct Params {
    const uchar* x; const uchar* mem0; const uchar* wq; const uchar* wk; const uchar* wv; const uchar* wo;
    const uchar* w1; const uchar* b1; const uchar* w2; uchar* out; uchar* ws;
};

DI rsrc_t mk_rsrc(const void* p, unsigned bytes) { return __builtin_amdgcn_make_buffer_rsrc((void*)p, 0, bytes, 0x00020000); }
DI u32x4 ld16(rsrc_t r, unsigned v, unsigned s) { return __builtin_amdgcn_raw_buffer_load_b128(r, v, s, SC1); }
DI u32x2 ld8(rsrc_t r, unsigned v, unsigned s) { return __builtin_amdgcn_raw_buffer_load_b64(r, v, s, SC1); }
DI unsigned ld4(rsrc_t r, unsigned v, unsigned s) { return __builtin_amdgcn_raw_buffer_load_b32(r, v, s, SC1); }
DI void st16(u32x4 d, rsrc_t r, unsigned v, unsigned s) { __builtin_amdgcn_raw_buffer_store_b128(d, r, v, s, SC1); asm volatile("s_nop 1" :: "v"(d)); }
DI void st8(u32x2 d, rsrc_t r, unsigned v, unsigned s) { __builtin_amdgcn_raw_buffer_store_b64(d, r, v, s, SC1); }
DI void st4(unsigned d, rsrc_t r, unsigned v, unsigned s) { __builtin_amdgcn_raw_buffer_store_b32(d, r, v, s, SC1); }
DI unsigned pk2(float a, float b) { f32x2 v = {a, b}; return __builtin_bit_cast(unsigned, __builtin_convertvector(v, bf16x2)); }
DI unsigned nosent(unsigned v) { return min(v, 0xFFFFFFFEu); }
DI float bf_lo(unsigned v) { return __builtin_bit_cast(float, v << 16); }
DI float bf_hi(unsigned v) { return __builtin_bit_cast(float, v & 0xFFFF0000u); }
DI float rbf(float a) { return bf_lo(pk2(a, a)); }
DI bf16x8 as8(u32x4 v) { return __builtin_bit_cast(bf16x8, v); }
DI f32x4 mfma32(u32x4 a, u32x4 b, f32x4 c) { return __builtin_amdgcn_mfma_f32_16x16x32_bf16(as8(a), as8(b), c, 0, 0, 0); }
DI f32x4 mfma16(u32x2 a, u32x2 b, f32x4 c) { return mfma32((u32x4){a.x, a.y, 0u, 0u}, (u32x4){b.x, b.y, 0u, 0u}, c); }
DI f32x4 smf(u32x4 a, u32x8 b, f32x4 c, int idx) { return __builtin_amdgcn_smfmac_f32_16x16x64_bf16(as8(a), __builtin_bit_cast(bf16x16, b), c, idx, 0, 0); }
DI void smf_a(f32x4& c, u32x4 a, u32x8 b, int idx) { asm volatile("v_smfmac_f32_16x16x64_bf16 %0, %1, %2, %3" : "+v"(c) : "v"(a), "a"(b), "v"(idx)); }
DI void smf_v(f32x4& c, u32x4 a, u32x8 b, int idx) { asm volatile("v_smfmac_f32_16x16x64_bf16 %0, %1, %2, %3" : "+v"(c) : "v"(a), "v"(b), "v"(idx)); }
DI void mfma_v(f32x4& c, u32x4 a, u32x4 b) { asm volatile("v_mfma_f32_16x16x32_bf16 %0, %1, %2, %0" : "+v"(c) : "v"(a), "v"(b)); }
DI void mfma_0(f32x4& c, u32x4 a, u32x4 b) { asm volatile("v_mfma_f32_16x16x32_bf16 %0, %1, %2, 0" : "=&v"(c) : "v"(a), "v"(b)); }
DI float fmax2(float a, float b) { return __builtin_amdgcn_fmed3f(a, b, __builtin_inff()); }
DI void mfma_pad(f32x4& a, f32x4& b, f32x4& c, f32x4& d) { asm volatile("s_nop 7" : "+v"(a), "+v"(b), "+v"(c), "+v"(d)); }
DI u32x2 trd(LAS uchar* p) { return __builtin_bit_cast(u32x2, __builtin_amdgcn_ds_read_tr16_b64_v4i16((LAS s16x4*)p)); }
DI unsigned mx4(unsigned m, u32x4 v) { return max(max(max(m, v.x), max(v.y, v.z)), v.w); }
DI void mx4v(unsigned& m, u32x4 v) { asm volatile("v_max3_u32 %0, %0, %1, %2\n\tv_max3_u32 %0, %0, %3, %4" : "+v"(m) : "v"(v.x), "v"(v.y), "v"(v.z), "v"(v.w)); }
DI unsigned long long nan_lanes(const f32x4& a) { return __builtin_amdgcn_ballot_w64(__builtin_isunordered(a[0], a[1]) | __builtin_isunordered(a[2], a[3])); }
template <int N> DI bool refetch(u32x4 (&f)[N], rsrc_t r, unsigned v, const unsigned (&so)[N], unsigned& budget) {
    bool redo = false;
#pragma nounroll
    for (;;) {
        unsigned m = 0;
#pragma unroll
        for (int i = 0; i < N; ++i) m = max(max(max(m, f[i].x), max(f[i].y, f[i].z)), f[i].w);
        if (__builtin_amdgcn_ballot_w64(m == SENT) == 0 || budget == 0) break;
        --budget; redo = true;
        asm volatile("" ::: "memory");
#pragma unroll
        for (int i = 0; i < N; ++i) f[i] = __builtin_amdgcn_raw_buffer_load_b128(r, v, so[i], SC1);
    }
    return redo;
}
DI unsigned rt32() { unsigned long long t; asm volatile("s_memrealtime %0\n\ts_waitcnt lgkmcnt(0)" : "=s"(t) :: "memory"); return (unsigned)t; }
DI unsigned waitrt(unsigned t) { unsigned n; do n = rt32(); while ((int)(n - t) < 0); return n; }
DI void bar() { asm volatile("s_waitcnt lgkmcnt(0)" ::: "memory"); __builtin_amdgcn_s_barrier(); asm volatile("" ::: "memory"); }
template <int N> DI void wait_vm() { asm volatile("s_waitcnt vmcnt(%0)" :: "n"(N) : "memory"); }
DI void dma16(rsrc_t r, unsigned voff, unsigned soff, unsigned ldsaddr) {
    asm volatile("s_mov_b32 m0, %0\n\ts_nop 0\n\tbuffer_load_dwordx4 %1, %2, %3 offen lds" :: "s"(ldsaddr), "v"(voff), "s"(r), "s"(soff) : "memory");
}
DI float swap32_add(float a, float b) { auto r = __builtin_amdgcn_permlane32_swap(__builtin_bit_cast(unsigned, a), __builtin_bit_cast(unsigned, b), false, false);
    return __builtin_bit_cast(float, (unsigned)r[0]) + __builtin_bit_cast(float, (unsigned)r[1]); }
DI float swap16_add(float a, float b) { auto r = __builtin_amdgcn_permlane16_swap(__builtin_bit_cast(unsigned, a), __builtin_bit_cast(unsigned, b), false, false);
    return __builtin_bit_cast(float, (unsigned)r[0]) + __builtin_bit_cast(float, (unsigned)r[1]); }
DI float grp_max(float m) {
    auto r = __builtin_amdgcn_permlane32_swap(__builtin_bit_cast(unsigned, m), __builtin_bit_cast(unsigned, m), false, false);
    m = fmax2(__builtin_bit_cast(float, (unsigned)r[0]), __builtin_bit_cast(float, (unsigned)r[1]));
    auto q = __builtin_amdgcn_permlane16_swap(__builtin_bit_cast(unsigned, m), __builtin_bit_cast(unsigned, m), false, false);
    return fmax2(__builtin_bit_cast(float, (unsigned)q[0]), __builtin_bit_cast(float, (unsigned)q[1]));
}
DI int fresh_lane() { int l = __builtin_amdgcn_mbcnt_hi(~0u, __builtin_amdgcn_mbcnt_lo(~0u, 0u)); asm volatile("" : "+v"(l)); return l & 63; }
DI unsigned fresh_tid() { unsigned t = threadIdx.x; asm volatile("" : "+v"(t)); return t; }
DI unsigned uni(unsigned v) { asm volatile("" : "+v"(v)); return __builtin_amdgcn_readfirstlane(v); }
DI unsigned ex_off(const Params& p) { return OFF_EX + ((0u - uni((unsigned)(size_t)p.ws) - OFF_EX) & 0x1FFFFFu); }
DI unsigned ex_step(unsigned exo, unsigned pol, int s) { const int r = (s * 43) >> 9; return exo + r * 2097152 + (s - 12 * r) * EX_STEP + ((pol >> r) & 1) * 8192; }
DI constexpr unsigned mf_off(int ks) { return EX_MF + (ks >> 3) * 16384 + (ks & 7) * 1024; }
DI unsigned xcc_id() { return (unsigned)__builtin_amdgcn_s_getreg(20 | (3 << 11)) & 7u; }
DI unsigned mt32() { unsigned long long t; asm volatile("s_memtime %0\n\ts_waitcnt lgkmcnt(0)" : "=s"(t) :: "memory"); return (unsigned)t; }
DI void set_tmo(const Params& p) { __hip_atomic_store((unsigned*)p.ws + C_TMO, 1u, __ATOMIC_RELAXED, __HIP_MEMORY_SCOPE_AGENT); }

DI float row16_sum(float v) {
    v += __builtin_bit_cast(float, __builtin_amdgcn_update_dpp(0, __builtin_bit_cast(int, v), 0x121, 0xf, 0xf, true));
    v += __builtin_bit_cast(float, __builtin_amdgcn_update_dpp(0, __builtin_bit_cast(int, v), 0x122, 0xf, 0xf, true));
    v += __builtin_bit_cast(float, __builtin_amdgcn_update_dpp(0, __builtin_bit_cast(int, v), 0x124, 0xf, 0xf, true));
    v += __builtin_bit_cast(float, __builtin_amdgcn_update_dpp(0, __builtin_bit_cast(int, v), 0x128, 0xf, 0xf, true));
    return v;
}
DI void add_ctr(const Params& p, int word, unsigned v) { (void)__hip_atomic_fetch_add((unsigned*)p.ws + word, v, __ATOMIC_RELAXED, __HIP_MEMORY_SCOPE_AGENT); }
DI void wait_ctr(const Params& p, rsrc_t rw, int word, unsigned want) {
    unsigned n = SPIN_CAP;
#pragma nounroll
    for (;;) {
        asm volatile("" ::: "memory");
        if ((unsigned)__builtin_amdgcn_readfirstlane(ld4(rw, 0, word * 4)) >= want) break;
        if (--n == 0) { set_tmo(p); break; }
        if ((n & 63) == 0 && __builtin_amdgcn_readfirstlane(ld4(rw, 0, C_TMO * 4))) break;
        __builtin_amdgcn_s_sleep(1);
    }
    asm volatile("" ::: "memory");
}

template <int V> struct IC { static constexpr int value = V; };
template <int N, int I = 0, class F> DI void static_for(F&& f) { if constexpr (I < N) { f(IC<I>{}); static_for<N, I + 1>(f); } }
DI u32x2 launch_tag(rsrc_t rw) {
    const u32x2 e = ld8(rw, 0, B_EPOCH * 4);
    const unsigned lo = __builtin_amdgcn_readfirstlane(e.x) + 1u;
    return (u32x2){lo, (unsigned)__builtin_amdgcn_readfirstlane(e.y) + (lo == 0u ? 1u : 0u)};
}
DI void wait_boot(rsrc_t rw, u32x2 tag) {
    const int lane = fresh_lane();
    for (unsigned n = SPIN_CAP; n; --n) {
        asm volatile("" ::: "memory");
        const u32x2 v = ld8(rw, (lane & 15) * 8, B_INIT * 4);
        if (__builtin_amdgcn_ballot_w64(v.x != tag.x || v.y != tag.y) == 0) break;
        __builtin_amdgcn_s_sleep(1);
    }
    asm volatile("" ::: "memory");
}
DI void probe(const Params& p, LAS uchar* lds, int v) {
    const int lane = fresh_lane(), w = __builtin_amdgcn_readfirstlane(threadIdx.x >> 6);
    const rsrc_t rw = mk_rsrc(p.ws, WS_USED);
    unsigned* a = (unsigned*)(p.ws + ex_off(p) + (v >> 3) * 2097152 + EX_PROBE + (w >> 1) * 8192 + (w & 1) * EX_STEP + (v & 7) * 128);
    unsigned best = ~0u;
    for (int i = 0; i < 4; ++i) {
        const unsigned t0 = mt32();
        unsigned r = 0;
        if (lane == 0) r = __hip_atomic_fetch_add(a, 1u, __ATOMIC_RELAXED, __HIP_MEMORY_SCOPE_AGENT);
        asm volatile("s_waitcnt vmcnt(0)" :: "v"(r) : "memory");
        best = min(best, mt32() - t0);
    }
    ((LAS unsigned*)lds)[w] = best;
    const u32x2 tag = launch_tag(rw);
    bar();
    const LAS unsigned* lat = (const LAS unsigned*)lds;
    const int d = (int)min(lat[0], lat[1]) - (int)min(lat[2], lat[3]);
    st8((u32x2){tag.x, tag.y ^ ((abs(d) < 120 ? 0u : ((d > 0) == (xcc_id() >= 4) ? 1u : 2u)) << 30)}, rw, fresh_tid() == 0 ? v * 8 : OOB, B_VOTE * 4);
    bar();
}
DI unsigned read_pol(rsrc_t rw, u32x2 tag) {
    const int lane = fresh_lane();
    u32x2 v = {0u, 0u};
    for (unsigned n = SPIN_CAP; n; --n) {
        asm volatile("" ::: "memory");
        v = ld8(rw, min(lane, 47) * 8, B_VOTE * 4);
        if (__builtin_amdgcn_ballot_w64(v.x != tag.x || ((v.y ^ tag.y) << 2) != 0) == 0) break;
        __builtin_amdgcn_s_sleep(1);
    }
    const unsigned c = (v.y ^ tag.y) >> 30;
    const unsigned long long m1 = __builtin_amdgcn_ballot_w64(c == 2), m0 = __builtin_amdgcn_ballot_w64(c == 1);
    unsigned pol = 0;
#pragma unroll
    for (int r = 0; r < 6; ++r) pol |= (__builtin_popcountll((m1 >> (8 * r)) & 255) > __builtin_popcountll((m0 >> (8 * r)) & 255) ? 1u : 0u) << r;
    return uni(pol);
}

template <bool FIRST>
DI void ln_tile(const Params& p, int b, u32x2& tag) {
    const int lane = fresh_lane(), w = __builtin_amdgcn_readfirstlane(threadIdx.x >> 6);
    const rsrc_t rw = mk_rsrc(p.ws, WS_USED);
    const int c = w * 4 + (lane >> 4), sub = lane & 15, row = b * 16 + c;
    u32x4 v[5]; float sum = 0.f;
#pragma unroll
    for (int k = 0; k < 5; ++k) v[k] = *(const u32x4*)(p.x + (size_t)row * 1280 + (k * 16 + sub) * 16);
    if (FIRST) {
        const unsigned tid = fresh_tid();
        st16((u32x4){0u, 0u, 0u, 0u}, rw, tid < 252 ? tid * 16 : OOB, b * 4032);
        tag = launch_tag(rw);
        wait_vm<0>();
        bar();
        st8(tag, rw, tid == 0 ? b * 8 : OOB, B_INIT * 4);
    }
#pragma unroll
    for (int k = 0; k < 5; ++k)
#pragma unroll
        for (int j = 0; j < 4; ++j) sum += bf_lo(v[k][j]) + bf_hi(v[k][j]);
    const float mean = row16_sum(sum) * (1.f / 640.f);
    float sq = 0.f;
#pragma unroll
    for (int k = 0; k < 5; ++k)
#pragma unroll
        for (int j = 0; j < 4; ++j) { const float a = bf_lo(v[k][j]) - mean, bb = bf_hi(v[k][j]) - mean; sq += a * a + bb * bb; }
    const float mb = rbf(mean), sd = rbf(sqrtf(rbf(rbf(row16_sum(sq) * (1.f / 640.f)) + 1e-5f))), ri = 1.f / sd;
#pragma unroll
    for (int k = 0; k < 5; ++k) {
        u32x4 o;
#pragma unroll
        for (int j = 0; j < 4; ++j) { const unsigned d = pk2(bf_lo(v[k][j]) - mb, bf_hi(v[k][j]) - mb); o[j] = pk2(bf_lo(d) * ri, bf_hi(d) * ri); }
        st16(o, rw, (k * 16 + sub) * 256 + c * 16, OFF_XF + b * 20480);
    }
    if (FIRST && w == 0) wait_boot(rw, tag);
    wait_vm<0>();
    bar();
    if (fresh_tid() == 0) add_ctr(p, C_X + (b >> 3) * 32, 1u);
}
DI void poison(const Params& p, unsigned pol, int j, int nj, int n0, int n1, int word) {
    const int tid = fresh_tid();
    const rsrc_t rw = mk_rsrc(p.ws, WS_USED);
    const unsigned exo = ex_off(p);
    const u32x4 sv = {SENT, SENT, SENT, SENT};
    for (int n = n0 + j; n < n1; n += nj) {
        const int s = min((n * 3277) >> 16, 63), i = n - 20 * s;
        u32x4 v = sv;
        if (s == 0 && i >= 16 && (i < 18 || (i == 18 && tid < 128))) {
            const int f = (i - 16) * 256 + tid, ks = f >> 6, l = f & 63, r = (l & 3) | ((l >> 1) & 4), half = (l >> 2) & 1, g = l >> 4;
            v = *(const u32x4*)(p.mem0 + (r * 640 + 64 * ks + 16 * g + 8 * half) * 2);
            v.x = nosent(v.x); v.y = nosent(v.y); v.z = nosent(v.z); v.w = nosent(v.w);
        }
        st16(v, rw, tid * 16, n < 1280 ? ex_step(exo, pol, s) + (i >> 1) * 16384 + (i & 1) * 4096
                                       : (n < 1312 ? OFF_KM + 63 * 131072 + (n - 1280) * 4096 : OFF_OT + 126 * 131072 + (n - 1312) * 4096));
    }
    wait_vm<0>();
    bar();
    if (fresh_tid() == 0) add_ctr(p, word, 1u);
}

#define SB() __builtin_amdgcn_sched_barrier(0)
DI void mfma_va(f32x4& c, u32x4 a, u32x4 b) { asm volatile("v_mfma_f32_16x16x32_bf16 %0, %1, %2, %0" : "+v"(c) : "v"(a), "a"(b)); }
DI void mfma_av(f32x4& c, u32x4 a, u32x4 b) { asm volatile("v_mfma_f32_16x16x32_bf16 %0, %1, %2, %0" : "+v"(c) : "a"(a), "v"(b)); }
DI void mfma_cacc(f32x4& c, u32x4 a, u32x4 b) { asm volatile("v_mfma_f32_16x16x32_bf16 %0, %1, %2, %0" : "+a"(c) : "v"(a), "v"(b)); }
struct P1Src {
    rsrc_t r; unsigned v0, v1, so, step;
    DI void set(const Params& p, int u, int kh, int w, int lane) {
        const uchar* base; unsigned stride, col, bytes;
        if (u < 128) { base = (u & 2) ? p.wv : p.wk; stride = 8192; col = ((u >> 2) * 128 + (u & 1) * 64) * 2; bytes = 640 * 8192; }
        else if (u < 192) { base = p.wq; stride = 8192; col = (u - 128) * 128; bytes = 640 * 8192; }
        else { base = p.w1; stride = 3456; col = (u - 192) * 128; bytes = u < 219 ? 640 * 3456 : 0; }
        r = mk_rsrc(base, bytes);
        const int rl = lane >> 3, f0 = ((rl >> 1) & 1) | (((rl >> 2) & 1) << 1);
        v0 = rl * stride + (((lane & 7) ^ (2 * f0)) << 4); v1 = v0 ^ 64;
        so = (kh * 320 + 80 * w) * stride + col; step = 8 * stride;
    }
    DI void piece(int i, unsigned ldsw) const { dma16(r, (i & 1) ? v1 : v0, so + i * step, ldsw + i * 1024); }
};
DI float mish(float a, float bias) {
    const float h = a + bias;
    const float e = __builtin_amdgcn_exp2f(fminf(h, 30.f) * 1.44269504f);
    const float n = e * (e + 2.f);
    return h * n * __builtin_amdgcn_rcpf(n + 2.f);
}
#define PT(i)
constexpr int XA_ACC = 15;
template <bool ISV, int KH>
DI void p1_slab(LAS uchar* sb, const unsigned (&tb)[4], const u32x4 (&xa)[4][20], f32x4 (&acc)[4][4], const P1Src& src, unsigned ldsn) {
    u32x4 wf[2][4];
    auto rd = [&](int kk, int nt) { const u32x2 lo = trd(sb + tb[nt] + kk * 4096), hi = trd(sb + tb[nt ^ 2] + kk * 4096 + 512); return (u32x4){lo.x, lo.y, hi.x, hi.y}; };
#pragma unroll
    for (int nt = 0; nt < 4; ++nt) wf[0][nt] = rd(0, nt);
    SB();
#pragma unroll
    for (int kk = 0; kk < 10; ++kk) {
#pragma unroll
        for (int nt = 0; nt < 4; ++nt) {
            if (kk + 1 < 10) wf[(kk + 1) & 1][nt] = rd(kk + 1, nt);
            if (kk < 5 && (nt & 1)) src.piece(2 * kk + (nt >> 1), ldsn);
#pragma unroll
            for (int t = 0; t < 4; ++t) {
                const int ks = KH * 10 + kk;
                if (ISV) { if (ks < XA_ACC) mfma_av(acc[t][nt], xa[t][ks], wf[kk & 1][nt]); else mfma_v(acc[t][nt], xa[t][ks], wf[kk & 1][nt]); }
                else { if (ks < XA_ACC) mfma_va(acc[t][nt], wf[kk & 1][nt], xa[t][ks]); else mfma_v(acc[t][nt], wf[kk & 1][nt], xa[t][ks]); }
            }
            SB();
        }
    }
    asm volatile("s_nop 7" : "+v"(acc[3][3]), "+v"(acc[2][3]), "+v"(acc[1][3]), "+v"(acc[0][3]), "+v"(acc[3][2]), "+v"(acc[2][2]), "+v"(acc[1][2]), "+v"(acc[0][2]));
#pragma unroll
    for (int t = 0; t < 4; ++t) asm volatile("" : "+v"(acc[t][0]), "+v"(acc[t][1]));
}
template <bool ISV>
DI void p1_group(const Params& p, LAS uchar* lds, rsrc_t rw, unsigned lbase, const unsigned (&tb)[4], const u32x4 (&xa)[4][20], P1Src& src, int buf,
                 int chunk, int u, int un, bool first, int w, int lane, unsigned (&pt)[5]) {
    const int g = lane >> 4;
    f32x4 acc[4][4];
#pragma unroll
    for (int t = 0; t < 4; ++t)
#pragma unroll
        for (int nt = 0; nt < 4; ++nt) { acc[t][nt] = (f32x4){0.f, 0.f, 0.f, 0.f}; asm volatile("" : "+v"(acc[t][nt])); }
    PT(0);
    if (first) wait_vm<50>(); else wait_vm<10>();
    PT(1);
    bar();
    PT(2);
    src.set(p, un, 0, w, lane);
    {
        const unsigned ln = lbase + (buf == 0 ? 2 : buf - 1) * 40960;
        p1_slab<ISV, 0>(lds + buf * 40960, tb, xa, acc, src, ln);
    }
    buf = buf == 2 ? 0 : buf + 1;
    PT(3);
    wait_vm<10>();
    PT(1);
    bar();
    PT(2);
    src.set(p, un, 1, w, lane);
    {
        const unsigned ln = lbase + (buf == 0 ? 2 : buf - 1) * 40960;
        p1_slab<ISV, 1>(lds + buf * 40960, tb, xa, acc, src, ln);
    }
    PT(3);
    const int tt0 = chunk * 16 + w * 4;
    if (ISV) {
        const int h = u >> 2;
#pragma unroll
        for (int sp = 0; sp < 2; ++sp)
#pragma unroll
            for (int nt = 0; nt < 4; ++nt) {
                const f32x4 a = acc[2 * sp][nt], b = acc[2 * sp + 1][nt];
                const u32x4 o = {pk2(a[0], a[1]), pk2(a[2], a[3]), pk2(b[0], b[1]), pk2(b[2], b[3])};
                st16(o, rw, lane * 16, OFF_VF + (((tt0 >> 1) + sp) * 32 + h) * 8192 + ((u & 1) * 4 + nt) * 1024);
            }
    } else if (u < 192) {
        const int h = u < 128 ? u >> 2 : (u - 128) >> 1;
        const int ob = u < 128 ? OFF_KF : OFF_QF;
#pragma unroll
        for (int t = 0; t < 4; ++t)
#pragma unroll
            for (int m = 0; m < 2; ++m) {
                const f32x4 a = acc[t][2 * m], b = acc[t][2 * m + 1];
                const u32x4 o = {pk2(a[0], a[1]), pk2(a[2], a[3]), pk2(b[0], b[1]), pk2(b[2], b[3])};
                st16(o, rw, lane * 16, ob + (((tt0 + t) >> 1) * 32 + h) * 8192 + ((t & 1) * 4 + (u & 1) * 2 + m) * 1024);
            }
    } else {
        const int fg = u - 192;
        u32x2 bv[4];
#pragma unroll
        for (int nt = 0; nt < 4; ++nt) bv[nt] = *(const u32x2*)(p.b1 + (fg * 64 + nt * 16 + 4 * g) * 2);
#pragma unroll
        for (int t = 0; t < 4; ++t)
#pragma unroll
            for (int m = 0; m < 2; ++m) {
                const f32x4 a = acc[t][2 * m], b = acc[t][2 * m + 1];
                const u32x2 ba = bv[2 * m], bb = bv[2 * m + 1];
                const u32x4 o = {pk2(mish(a[0], bf_lo(ba.x)), mish(a[1], bf_hi(ba.x))), pk2(mish(a[2], bf_lo(ba.y)), mish(a[3], bf_hi(ba.y))),
                                 pk2(mish(b[0], bf_lo(bb.x)), mish(b[1], bf_hi(bb.x))), pk2(mish(b[2], bf_lo(bb.y)), mish(b[3], bf_hi(bb.y)))};
                st16(o, rw, lane * 16, OFF_ACT + ((tt0 + t) * 54 + fg * 2 + m) * 1024);
                SB();
            }
    }
}
DI void ld16_a(u32x4& d, rsrc_t r, unsigned v, unsigned s) { asm volatile("buffer_load_dwordx4 %0, %1, %2, %3 offen sc1" : "=a"(d) : "v"(v), "s"(r), "s"(s) : "memory"); }
DI void ld16_v(u32x4& d, rsrc_t r, unsigned v, unsigned s) { asm volatile("buffer_load_dwordx4 %0, %1, %2, %3 offen sc1" : "=v"(d) : "v"(v), "s"(r), "s"(s) : "memory"); }
DI void ld16_s(u32x4& d, rsrc_t r, unsigned v, unsigned s) { asm volatile("buffer_load_dwordx4 %0, %1, %2, %3 offen sc1" : "=v"(d) : "v"(v), "s"(r), "s"(s) : "memory"); }
DI void p1_task(const Params& p, LAS uchar* lds, int chunk, int u0, int nu) {
    const int lane = fresh_lane(), w = __builtin_amdgcn_readfirstlane(threadIdx.x >> 6), c = lane & 15, g = lane >> 4;
    const rsrc_t rw = mk_rsrc(p.ws, WS_USED);
    const unsigned lbase = (unsigned)(size_t)lds + w * 10240;
    P1Src src;
    u32x4 xa[4][20];
    src.set(p, u0, 0, w, lane);
    if (w == 0) { wait_ctr(p, rw, C_X + chunk * 64, 8); wait_ctr(p, rw, C_X + chunk * 64 + 32, 8); }
    bar();
#pragma unroll
    for (int i = 0; i < 10; ++i) src.piece(i, lbase);
#pragma unroll
    for (int ks = 0; ks < 20; ++ks) {
        if (ks == 10) {
            src.set(p, u0, 1, w, lane);
#pragma unroll
            for (int i = 0; i < 10; ++i) src.piece(i, lbase + 40960);
        }
#pragma unroll
        for (int t = 0; t < 4; ++t) {
            const unsigned so = OFF_XF + ((chunk * 16 + w * 4 + t) * 20 + ks) * 1024;
            if (ks < XA_ACC) ld16_a(xa[t][ks], rw, lane * 16, so); else ld16_v(xa[t][ks], rw, lane * 16, so);
        }
        SB();
    }
    const int q = c >> 2, pp = c & 3, sw = (q >> 1) | ((g & 1) << 1);
    unsigned tb[4];
#pragma unroll
    for (int nt = 0; nt < 4; ++nt) tb[nt] = (8 * g + q) * 128 + 32 * (nt ^ sw) + 16 * (pp >> 1) + 8 * (pp & 1);
    int buf = 0;
    unsigned pt[5] = {0, 0, 0, 0, mt32()};
    for (int gi = 0; gi < nu; ++gi) {
        const int u = u0 + gi, un = gi + 1 < nu ? u + 1 : 219;
        if (u < 128 && (u & 2)) p1_group<true>(p, lds, rw, lbase, tb, xa, src, buf, chunk, u, un, gi == 0, w, lane, pt);
        else p1_group<false>(p, lds, rw, lbase, tb, xa, src, buf, chunk, u, un, gi == 0, w, lane, pt);
        buf = buf == 0 ? 2 : buf - 1;
    }
    wait_vm<0>();
    bar();
    const unsigned tid = fresh_tid();
    if (tid < (unsigned)nu) {
        const int u = u0 + tid;
        add_ctr(p, u < 128 ? C_KV + (chunk * 32 + (u >> 2)) * 16 : (u < 192 ? C_Q + (chunk * 32 + ((u - 128) >> 1)) * 16 : C_ACT + chunk * 32), 1u);
    }
}

#define KST(i)
#define AST_(i)
template <bool ISV>
DI void kv0_task(const Params& p, LAS uchar* lds, int u) {
    const int lane = fresh_lane(), w = __builtin_amdgcn_readfirstlane(threadIdx.x >> 6), c = lane & 15, g = lane >> 4;
    const rsrc_t rw = mk_rsrc(p.ws, WS_USED);
    const unsigned lbase = (unsigned)(size_t)lds + w * 10240;
    P1Src src;
    u32x4 xa[2][2][20];
    auto fetch = [&](int part, int i) {
        const unsigned so = OFF_XF + ((part * 8 + w * 2 + (i & 1)) * 20 + (i >> 1)) * 1024;
        if (part & 1) ld16_a(xa[1][i & 1][i >> 1], rw, lane * 16, so); else ld16_v(xa[0][i & 1][i >> 1], rw, lane * 16, so);
    };
#pragma unroll
    for (int kh = 0; kh < 2; ++kh) {
        src.set(p, u, kh, w, lane);
#pragma unroll
        for (int i = 0; i < 10; ++i) src.piece(i, lbase + kh * 40960);
    }
    KST(8);
    KST(9);
    if (w == 0) {
        wait_boot(rw, launch_tag(rw));
        if (fresh_tid() == 0) add_ctr(p, C_ACK, 1u);
        wait_ctr(p, rw, C_X, 8);
    }
    bar();
    KST(10);
#pragma unroll
    for (int i = 0; i < 40; ++i) fetch(0, i);
    const int q = c >> 2, pp = c & 3, sw = (q >> 1) | ((g & 1) << 1), h = u >> 2;
    unsigned tb[4];
#pragma unroll
    for (int nt = 0; nt < 4; ++nt) tb[nt] = (8 * g + q) * 128 + 32 * (nt ^ sw) + 16 * (pp >> 1) + 8 * (pp & 1);
    auto rd = [&](int i) {
        const int ks = i >> 2, nt = i & 3; LAS uchar* sb = lds + (ks / 10) * 40960 + (ks % 10) * 4096;
        const u32x2 lo = trd(sb + tb[nt]), hi = trd(sb + tb[nt ^ 2] + 512); return (u32x4){lo.x, lo.y, hi.x, hi.y};
    };
    static_for<4>([&](auto pc) {
        constexpr int part = pc.value, b = part & 1;
        f32x4 acc[2][4];
#pragma unroll
        for (int i = 0; i < 8; ++i) { acc[i >> 2][i & 3] = (f32x4){0.f, 0.f, 0.f, 0.f}; asm volatile("" : "+v"(acc[i >> 2][i & 3])); }
        if (part < 2) KST(part * 4 + 0);
        if (part < 3 && w == 0) wait_ctr(p, rw, C_X + 32 * (part + 1), 8);
        wait_vm<0>();
#pragma unroll
        for (int ks = 0; ks < 20; ++ks) { if (b) asm volatile("" : "+a"(xa[b][0][ks]), "+a"(xa[b][1][ks])); else asm volatile("" : "+v"(xa[b][0][ks]), "+v"(xa[b][1][ks])); }
        bar();
        if (part > 0 && fresh_tid() == 0) add_ctr(p, (b ? C_KVH : C_KV) + (((part - 1) >> 1) * 32 + h) * 16, 1u);
        if (part < 2) KST(part * 4 + 1);
        u32x4 wf[8];
#pragma unroll
        for (int i = 0; i < 8; ++i) wf[i] = rd(i);
        SB();
#pragma unroll
        for (int i = 0; i < 80; ++i) {
#pragma unroll
            for (int t = 0; t < 2; ++t) {
                if (ISV) { if (b) mfma_av(acc[t][i & 3], xa[b][t][i >> 2], wf[i & 7]); else mfma_v(acc[t][i & 3], xa[b][t][i >> 2], wf[i & 7]); }
                else { if (b) mfma_va(acc[t][i & 3], wf[i & 7], xa[b][t][i >> 2]); else mfma_v(acc[t][i & 3], wf[i & 7], xa[b][t][i >> 2]); }
            }
            if (i + 8 < 80) wf[i & 7] = rd(i + 8);
            if (part < 3 && i < 40) fetch(part + 1, i);
            SB();
        }
        asm volatile("s_nop 7" : "+v"(acc[0][0]), "+v"(acc[0][1]), "+v"(acc[0][2]), "+v"(acc[0][3]), "+v"(acc[1][0]), "+v"(acc[1][1]), "+v"(acc[1][2]), "+v"(acc[1][3]));
        if (part < 2) KST(part * 4 + 2);
        const unsigned ob = (ISV ? OFF_VF : OFF_KF) + ((part * 4 + w) * 32 + h) * 8192;
#pragma unroll
        for (int i = 0; i < 4; ++i) {
            const f32x4 a = ISV ? acc[0][i] : acc[i >> 1][2 * (i & 1)], bb = ISV ? acc[1][i] : acc[i >> 1][2 * (i & 1) + 1];
            const u32x4 o = {pk2(a[0], a[1]), pk2(a[2], a[3]), pk2(bb[0], bb[1]), pk2(bb[2], bb[3])};
            st16(o, rw, lane * 16, ob + (ISV ? (u & 1) * 4 + i : (i >> 1) * 4 + (u & 1) * 2 + (i & 1)) * 1024);
        }
        if (part < 2) KST(part * 4 + 3);
        SB();
    });
    wait_vm<0>();
    bar();
    if (fresh_tid() == 0) add_ctr(p, C_KV + (32 + h) * 16, 1u);
}

template <bool WO>
DI void gemm_task(const Params& p, LAS uchar* lds, int chunk, int cg) {
    constexpr int KS = WO ? 4 : 3, NS = WO ? 32 : 18, NP = 2 * KS, IMG = KS * 4096, NKF = KS * NS, XOFF = WO ? OFF_OT : OFF_ACT;
    const int lane = fresh_lane(), w = __builtin_amdgcn_readfirstlane(threadIdx.x >> 6), c = lane & 15, g = lane >> 4;
    const rsrc_t rw = mk_rsrc(p.ws, WS_USED), r2 = mk_rsrc(WO ? p.wo : p.w2, NKF * 32 * 1280);
    const unsigned lbase = (unsigned)(size_t)lds + (w >> 1) * IMG + (w & 1) * NP * 1024;
    const int rl = lane >> 3, f0 = ((rl >> 1) & 1) | (((rl >> 2) & 1) << 1);
    const unsigned v0 = rl * 1280 + (((lane & 7) ^ (2 * f0)) << 4), v1 = v0 ^ 64;
    const unsigned wbase = (w & 1) * NP * 8 * 1280 + cg * 256 + (w >> 1) * 128;
    auto piece = [&](int sl, int buf, int i) { dma16(r2, (i & 1) ? v1 : v0, wbase + (sl * KS * 32 + 8 * i) * 1280, lbase + buf * 2 * IMG + i * 1024); };
    const int tt0 = chunk * 16 + w * 4;
    u32x4 xb[2][KS][4];
    auto frag = [&](int par, int sl, int i) { ld16_s(xb[par][i >> 2][i & 3], rw, lane * 16, XOFF + ((tt0 + (i & 3)) * NKF + sl * KS + (i >> 2)) * 1024); };
#pragma unroll
    for (int i = 0; i < NP; ++i) piece(0, 0, i);
    if (w == 0) {
        if (WO) {
            wait_ctr(p, rw, C_G + (chunk * 5 + cg) * 32, 1);
            for (int i = 0; i < 8; ++i) wait_ctr(p, rw, C_OT + (chunk * 8 + i) * 32, 32);
        } else wait_ctr(p, rw, C_ACT + chunk * 32, 27);
    }
    bar();
#pragma unroll
    for (int i = 0; i < 4 * KS; ++i) frag(0, 0, i);
#pragma unroll
    for (int i = 0; i < NP; ++i) piece(1, 1, i);
    const int q = c >> 2, pp = c & 3, sw = (q >> 1) | (((g & 1) ^ (g >> 1)) << 1);
    unsigned tb[4];
#pragma unroll
    for (int nt = 0; nt < 4; ++nt) tb[nt] = (4 * g + q) * 128 + 32 * (nt ^ sw) + 16 * (pp >> 1) + 8 * (pp & 1);
    f32x4 acc[4][8];
#pragma unroll
    for (int t = 0; t < 4; ++t)
#pragma unroll
        for (int nt = 0; nt < 8; ++nt) { acc[t][nt] = (f32x4){0.f, 0.f, 0.f, 0.f}; asm volatile("" : "+a"(acc[t][nt])); }
    int buf = 0;
    for (int s2 = 0; s2 < NS / 2; ++s2) {
#pragma unroll
        for (int par = 0; par < 2; ++par) {
            const int sl = 2 * s2 + par;
            wait_vm<NP>();
#pragma unroll
            for (int kk = 0; kk < KS; ++kk) asm volatile("" : "+v"(xb[par][kk][0]), "+v"(xb[par][kk][1]), "+v"(xb[par][kk][2]), "+v"(xb[par][kk][3]));
            bar();
            const int sn = min(sl + 1, NS - 1), sp = min(sl + 2, NS - 1), bn = buf == 0 ? 2 : buf - 1;
            LAS uchar* sb = lds + buf * 2 * IMG;
            auto rd = [&](int i) { LAS uchar* a = sb + ((i >> 2) & 1) * IMG + tb[i & 3] + (i >> 3) * 4096; const u32x2 lo = trd(a), hi = trd(a + 2048); return (u32x4){lo.x, lo.y, hi.x, hi.y}; };
            u32x4 wf[4];
#pragma unroll
            for (int i = 0; i < 4; ++i) wf[i] = rd(i);
            SB();
#pragma unroll
            for (int i = 0; i < 8 * KS; ++i) {
                const u32x4 cur = wf[i & 3];
                mfma_cacc(acc[0][i & 7], cur, xb[par][i >> 3][0]);
                if (i < 4 * KS) frag(par ^ 1, sn, i); else if (i < 4 * KS + NP) piece(sp, bn, i - 4 * KS);
#pragma unroll
                for (int t = 1; t < 4; ++t) mfma_cacc(acc[t][i & 7], cur, xb[par][i >> 3][t]);
                if (i + 4 < 8 * KS) wf[i & 3] = rd(i + 4);
                SB();
            }
            if (par) asm volatile("s_nop 7");
            SB();
            buf = buf == 2 ? 0 : buf + 1;
        }
    }
    wait_vm<0>();
#pragma unroll
    for (int kk = 0; kk < KS; ++kk) asm volatile("" :: "v"(xb[0][kk][0]), "v"(xb[0][kk][1]), "v"(xb[0][kk][2]), "v"(xb[0][kk][3]));
    SB();
#pragma unroll
    for (int t = 0; t < 4; ++t) {
        if (WO) {
            const size_t xo = (size_t)((tt0 + t) * 16 + c) * 1280 + cg * 256 + g * 8;
            u32x4 gv[4]; u32x2 xv[8];
#pragma unroll
            for (int m = 0; m < 4; ++m) gv[m] = ld16(rw, lane * 16, OFF_GF + ((tt0 + t) * 20 + cg * 4 + m) * 1024);
#pragma unroll
            for (int nt = 0; nt < 8; ++nt) xv[nt] = *(const u32x2*)(p.x + xo + nt * 32);
#pragma unroll
            for (int nt = 0; nt < 8; ++nt) {
                const f32x4 a = acc[t][nt];
                const unsigned g0 = gv[nt >> 1][2 * (nt & 1)], g1 = gv[nt >> 1][2 * (nt & 1) + 1], s0 = pk2(a[0], a[1]), s1 = pk2(a[2], a[3]);
                const unsigned m0 = pk2(bf_lo(s0) * bf_lo(g0), bf_hi(s0) * bf_hi(g0)), m1 = pk2(bf_lo(s1) * bf_lo(g1), bf_hi(s1) * bf_hi(g1));
                *(u32x2*)(p.out + xo + nt * 32) = (u32x2){pk2(bf_lo(xv[nt].x) + bf_lo(m0), bf_hi(xv[nt].x) + bf_hi(m0)), pk2(bf_lo(xv[nt].y) + bf_lo(m1), bf_hi(xv[nt].y) + bf_hi(m1))};
            }
        } else {
#pragma unroll
            for (int m = 0; m < 4; ++m) {
                float y[8];
#pragma unroll
                for (int r = 0; r < 8; ++r) { const float a = rbf(acc[t][2 * m + (r >> 2)][r & 3]); y[r] = a * __builtin_amdgcn_rcpf(1.f + __builtin_amdgcn_exp2f(-a * 1.44269504f)); }
                const u32x4 o = {pk2(y[0], y[1]), pk2(y[2], y[3]), pk2(y[4], y[5]), pk2(y[6], y[7])};
                st16(o, rw, lane * 16, OFF_GF + ((tt0 + t) * 20 + cg * 4 + m) * 1024);
            }
        }
        SB();
    }
    wait_vm<0>();
    bar();
    if (!WO && fresh_tid() == 0) add_ctr(p, C_G + (chunk * 5 + cg) * 32, 1u);
}

#define XST(cond, idx)
constexpr float SCL = 0.08838834764831845f * 1.4426950408889634f;
DI void attn_task(const Params& p, int s, int h, bool new_chunk) {
    const int lane = fresh_lane(), c = lane & 15, g = lane >> 4;
    const rsrc_t rw = mk_rsrc(p.ws, WS_USED);
    const int sh = (s * 32 + h);
    u32x4 qf[2][4], kf[2][4], vf[8], km[4]; u32x2 vm[8];
    if (new_chunk) {
        wait_ctr(p, rw, C_Q + ((s >> 3) * 32 + h) * 16, 2);
        wait_ctr(p, rw, C_KV + ((s >> 3) * 32 + h) * 16, 4);
    }
#pragma unroll
    for (int i = 0; i < 8; ++i) qf[i >> 2][i & 3] = ld16(rw, lane * 16, OFF_QF + sh * 8192 + i * 1024);
#pragma unroll
    for (int i = 0; i < 8; ++i) kf[i >> 2][i & 3] = ld16(rw, lane * 16, OFF_KF + sh * 8192 + i * 1024);
#pragma unroll
    for (int i = 0; i < 8; ++i) vf[i] = ld16(rw, lane * 16, OFF_VF + sh * 8192 + i * 1024);
    const float NINF = -__builtin_inff();
    const f32x4 zero = {0.f, 0.f, 0.f, 0.f};
    float t[2][12], m[2];
    {
        f32x4 s0[2] = {zero, zero}, s1 = zero;
#pragma unroll
        for (int ks = 0; ks < 4; ++ks) { s0[0] = mfma32(kf[0][ks], qf[0][ks], s0[0]); s0[1] = mfma32(kf[0][ks], qf[1][ks], s0[1]); s1 = mfma32(kf[1][ks], qf[1][ks], s1); }
#pragma unroll
        for (int r = 0; r < 4; ++r) {
            const bool diag = 4 * g + r <= c;
            t[0][r] = diag ? s0[0][r] * SCL : NINF; t[1][r] = s0[1][r] * SCL; t[1][4 + r] = diag ? s1[r] * SCL : NINF;
        }
        m[0] = fmaxf(fmaxf(t[0][0], t[0][1]), fmaxf(t[0][2], t[0][3]));
        m[1] = fmaxf(fmaxf(fmaxf(t[1][0], t[1][1]), fmaxf(t[1][2], t[1][3])), fmaxf(fmaxf(t[1][4], t[1][5]), fmaxf(t[1][6], t[1][7])));
#pragma unroll
        for (int r = 0; r < 4; ++r) asm volatile("" : "+v"(t[0][r]), "+v"(t[1][r]), "+v"(t[1][4 + r]));
        asm volatile("" : "+v"(m[0]), "+v"(m[1]));
#pragma unroll
        for (int i = 0; i < 8; ++i) asm volatile("" : "+v"(vf[i]));
    }
    u32x4 one4 = {0x3F803F80u, 0x3F803F80u, 0x3F803F80u, 0x3F803F80u};
    const float z2 = g >= 2 ? 0.f : NINF;
    f32x4 s2[2] = {{z2, z2, z2, z2}, {z2, z2, z2, z2}};
    asm volatile("" : "+v"(s2[0]), "+v"(s2[1]));
    XST(s == 63, 64 + h * 4);
    wait_ctr(p, rw, C_KM + h * 32 + (s == NSEG - 1), s + 1);
    XST(s == 63, 64 + h * 4 + 1);
    for (unsigned n = SPIN_CAP;;) {
        asm volatile("" ::: "memory");
#pragma unroll
        for (int i = 0; i < 4; ++i) km[i] = ld16(rw, (g * 8 + (c & 7)) * 16, OFF_KM + sh * 4096 + i * 512);
#pragma unroll
        for (int i = 0; i < 8; ++i) vm[i] = ld8(rw, (lane & 31) * 8, OFF_KM + sh * 4096 + 2048 + i * 256);
        if (s < NSEG - 1) break;
        unsigned mx = 0;
#pragma unroll
        for (int i = 0; i < 4; ++i) mx = mx4(mx, km[i]);
#pragma unroll
        for (int i = 0; i < 8; ++i) mx = max(mx, max(vm[i].x, vm[i].y));
        if (__builtin_amdgcn_ballot_w64(mx == SENT) == 0) break;
        if (--n == 0) { set_tmo(p); break; }
    }
    SB();
    SB();
#pragma unroll
    for (int ks = 0; ks < 4; ++ks)
#pragma unroll
        for (int qt = 0; qt < 2; ++qt) mfma_v(s2[qt], km[ks], qf[qt][ks]);
    asm volatile("s_nop 7" : "+v"(s2[0]), "+v"(s2[1]));
    u32x4 p01[2], p2[2];
#pragma unroll
    for (int qt = 0; qt < 2; ++qt) {
#pragma unroll
        for (int r = 0; r < 4; ++r) t[qt][8 + r] = s2[qt][r] * SCL;
        m[qt] = fmaxf(fmaxf(m[qt], fmaxf(t[qt][8], t[qt][9])), fmaxf(t[qt][10], t[qt][11]));
    }
#pragma unroll
    for (int qt = 0; qt < 2; ++qt) m[qt] = grp_max(m[qt]);
#pragma unroll
    for (int qt = 0; qt < 2; ++qt) {
#pragma unroll
        for (int r = 0; r < 12; ++r) if (qt == 1 || r < 4 || r >= 8) t[qt][r] = __builtin_amdgcn_exp2f(t[qt][r] - m[qt]);
        p01[qt] = (u32x4){pk2(t[qt][0], t[qt][1]), pk2(t[qt][2], t[qt][3]), qt ? pk2(t[qt][4], t[qt][5]) : 0u, qt ? pk2(t[qt][6], t[qt][7]) : 0u};
        p2[qt] = (u32x4){pk2(t[qt][8], t[qt][9]), pk2(t[qt][10], t[qt][11]), 0u, 0u};
    }
    u32x4 vq[4];
#pragma unroll
    for (int i = 0; i < 4; ++i) vq[i] = (u32x4){vm[2 * i].x, vm[2 * i].y, vm[2 * i + 1].x, vm[2 * i + 1].y};
    u32x4 p2h[2] = {{0u, 0u, p2[0].x, p2[0].y}, {0u, 0u, p2[1].x, p2[1].y}};
    asm volatile("s_nop 1" : "+v"(p01[0]), "+v"(p01[1]), "+v"(p2[0]), "+v"(p2[1]), "+v"(p2h[0]), "+v"(p2h[1]), "+v"(vq[0]), "+v"(vq[1]), "+v"(vq[2]), "+v"(vq[3]), "+v"(one4));
    f32x4 l[2], o[2][8];
#pragma unroll
    for (int qt = 0; qt < 2; ++qt) mfma_0(l[qt], one4, p01[qt]);
#pragma unroll
    for (int qt = 0; qt < 2; ++qt) mfma_v(l[qt], one4, p2[qt]);
#pragma unroll
    for (int et = 0; et < 8; ++et)
#pragma unroll
        for (int qt = 0; qt < 2; ++qt) { mfma_0(o[qt][et], vf[et], p01[qt]); mfma_v(o[qt][et], vq[et >> 1], et & 1 ? p2h[qt] : p2[qt]); }
    asm volatile("s_nop 3" : "+v"(l[0]), "+v"(l[1]), "+v"(o[0][0]), "+v"(o[0][1]), "+v"(o[1][0]), "+v"(o[1][1]));
    const float inv[2] = {__builtin_amdgcn_rcpf(l[0][0]), __builtin_amdgcn_rcpf(l[1][0])};
#pragma unroll
    for (int tp = 0; tp < 4; ++tp) {
        asm volatile("" : "+v"(o[0][2 * tp]), "+v"(o[0][2 * tp + 1]), "+v"(o[1][2 * tp]), "+v"(o[1][2 * tp + 1]));
#pragma unroll
        for (int qt = 0; qt < 2; ++qt) {
            const f32x4 a = o[qt][2 * tp] * inv[qt], b = o[qt][2 * tp + 1] * inv[qt];
            const u32x4 ov = {nosent(pk2(a[0], a[1])), nosent(pk2(a[2], a[3])), nosent(pk2(b[0], b[1])), nosent(pk2(b[2], b[3]))};
            st16(ov, rw, lane * 16, OFF_OT + ((2 * s + qt) * 32 + h) * 4096 + tp * 1024);
        }
        SB();
    }
    if (s == NSEG - 1) st4(1u, rw, lane == 0 ? (C_OF + (h >> 3) * 32 + (h & 7)) * 4 : OOB, 0);
    XST(s == 63, 64 + h * 4 + 2);
    wait_vm<0>();
    XST(s == 63, 64 + h * 4 + 3);
    if (lane == 0) add_ctr(p, C_OT + s * 32, 1u);
}

constexpr int N_T = 24, N_WG = 4, W_FIRST = 64;
DI void wo_stage(const Params& p, unsigned ldsw, int h0, int cs, int lane) {
    const rsrc_t ro = mk_rsrc(p.wo, 4096 * 1280);
    const unsigned v = (lane >> 2) * 1280 + (lane & 3) * 16;
    const unsigned so = uni(h0 * 128 * 1280 + cs * 64);
    ldsw = uni(ldsw);
#pragma unroll
    for (int i = 0; i < 16; ++i) dma16(ro, v, so + 16 * i * 1280, ldsw + i * 1024);
}
DI void w_role(const Params& p, LAS uchar* lds, int cs, int rank) {
    const int lane = fresh_lane(), w = __builtin_amdgcn_readfirstlane(threadIdx.x >> 6), c = lane & 15, g = lane >> 4;
    const rsrc_t rw = mk_rsrc(p.ws, WS_USED);
    u32x4 wf[8][4][2];
    {
        LAS uchar* lw = lds + w * 32768;
        const int q = c >> 2, pp = c & 3;
        wo_stage(p, (unsigned)(size_t)lw, 8 * w, cs, lane);
        wo_stage(p, (unsigned)(size_t)lw + 16384, 8 * w + 2, cs, lane);
#pragma unroll
        for (int rd = 0; rd < 4; ++rd) {
            if (rd < 3) wait_vm<16>(); else wait_vm<0>();
#pragma unroll
            for (int i = 0; i < 2; ++i)
#pragma unroll
                for (int tp = 0; tp < 4; ++tp)
#pragma unroll
                    for (int nt = 0; nt < 2; ++nt) {
                        LAS uchar* a = lw + (rd & 1) * 16384 + i * 8192 + (32 * tp + 4 * g + q) * 64 + nt * 32 + pp * 8;
                        const u32x2 lo = trd(a), hi = trd(a + 1024);
                        wf[2 * rd + i][tp][nt] = (u32x4){lo.x, lo.y, hi.x, hi.y};
                        if (2 * rd + i < 7) asm volatile("" : "+a"(wf[2 * rd + i][tp][nt])); else asm volatile("" : "+v"(wf[2 * rd + i][tp][nt]));
                    }
            asm volatile("s_waitcnt lgkmcnt(0)" ::: "memory");
            SB();
            if (rd < 2) wo_stage(p, (unsigned)(size_t)lw + (rd & 1) * 16384, 8 * w + 2 * rd + 4, cs, lane);
        }
    }
    bar();
    LAS f32x2* red = (LAS f32x2*)lds;
    volatile LAS int* nxt = (volatile LAS int*)(lds + 16384);
    const rsrc_t rx = mk_rsrc(p.x, S_ * 1280), ro = mk_rsrc(p.out, S_ * 1280);
    unsigned tk = 0;
    const unsigned wto = lane == 0 ? (C_WT + cs * 32) * 4 : OOB;
    auto draw = [&]() { tk = 1; asm volatile("buffer_atomic_add %0, %1, %2, 0 offen sc0" : "+v"(tk) : "v"(wto), "s"(rw) : "memory"); };
    auto tile_of = [&](int ticket, int cur) { return ticket + W_FIRST < 120 ? ticket + W_FIRST : (cur < 120 ? 120 + rank : (cur < 124 ? (rank < 2 ? 126 : 122) + rank : 128)); };
    u32x4 ob[8][4];
    int tt = 0;
    bool have = false, careful = false;
    if (w == 0) { draw(); asm volatile("s_waitcnt vmcnt(0)" : "+v"(tk)); *nxt = tile_of(__builtin_amdgcn_readfirstlane(tk), 0); draw(); }
    bar();
    tt = __builtin_amdgcn_readfirstlane(*nxt);
    while (tt < 2 * NSEG) {
        if (!have) {
            if (w == 0) { wait_ctr(p, rw, C_G + ((tt >> 4) * 5 + (cs >> 2)) * 32, 1); if (tt < 126 || careful) wait_ctr(p, rw, C_OT + (tt >> 1) * 32, 32); }
            bar();
            if (tt >= 126 && !careful) {
                for (unsigned n = SPIN_CAP;;) {
                    asm volatile("" ::: "memory");
                    if (__builtin_amdgcn_ballot_w64(ld4(rw, (lane & 7) * 4, (C_OF + w * 32) * 4) == 0) == 0) break;
                    if (--n == 0) { set_tmo(p); break; }
                }
            }
            XST(w == 0 && tt >= 126, 256 + ((tt - 126) * 20 + cs) * 4);
#pragma unroll
            for (int i = 0; i < 32; ++i) ld16_s(ob[i >> 2][i & 3], rw, lane * 16, OFF_OT + (tt * 32 + 8 * w) * 4096 + i * 1024);
        }
        f32x4 acc[2][2];
#pragma unroll
        for (int i = 0; i < 4; ++i) { acc[i >> 1][i & 1] = (f32x4){0.f, 0.f, 0.f, 0.f}; asm volatile("" : "+v"(acc[i >> 1][i & 1])); }
        const unsigned xo = c * 1280 + g * 8, xs = tt * 16 * 1280 + cs * 64 + (w >> 1) * 32 + (w & 1) * 4;
        unsigned xv, gv;
        asm volatile("buffer_load_dword %0, %1, %2, %3 offen" : "=v"(xv) : "v"(xo), "s"(rx), "s"(xs) : "memory");
        asm volatile("buffer_load_dword %0, %1, %2, %3 offen sc1" : "=v"(gv) : "v"(lane * 16), "s"(rw), "s"(OFF_GF + (tt * 20 + cs) * 1024 + w * 4) : "memory");
        if (w == 0) {
            asm volatile("s_waitcnt vmcnt(32)" : "+v"(tk));
            const int tn = tile_of(__builtin_amdgcn_readfirstlane(tk), tt);
            int rd = 0;
            if (tn < 126) {
                const unsigned f = ld4(rw, (lane == 0 ? C_G + ((tn >> 4) * 5 + (cs >> 2)) * 32 : C_OT + (tn >> 1) * 32) * 4, 0);
                rd = __builtin_amdgcn_ballot_w64(f < (lane == 0 ? 1u : 32u)) == 0 ? 0x10000 : 0;
            }
            nxt[1] = tn | rd;
        }
        bar();
        XST(w == 0 && tt >= 126, 256 + ((tt - 126) * 20 + cs) * 4 + 1);
        const int nx = __builtin_amdgcn_readfirstlane(nxt[1]);
        have = (nx >> 16) != 0;
        const rsrc_t rn = mk_rsrc(p.ws, have ? WS_USED : 0);
        const unsigned nb = OFF_OT + ((nx & 127) * 32 + 8 * w) * 4096;
        if (w == 0) draw();
#pragma unroll
        for (int i = 0; i < 8; ++i) {
            asm volatile("s_waitcnt vmcnt(28)" : "+v"(ob[i][0]), "+v"(ob[i][1]), "+v"(ob[i][2]), "+v"(ob[i][3]));
#pragma unroll
            for (int tp = 0; tp < 4; ++tp)
#pragma unroll
                for (int n = 0; n < 2; ++n) { if (i < 7) mfma_av(acc[tp & 1][n], wf[i][tp][n], ob[i][tp]); else mfma_v(acc[tp & 1][n], wf[i][tp][n], ob[i][tp]); }
#pragma unroll
            for (int tp = 0; tp < 4; ++tp) ld16_s(ob[i][tp], rn, lane * 16, nb + (i * 4 + tp) * 1024);
            SB();
        }
        mfma_pad(acc[0][0], acc[0][1], acc[1][0], acc[1][1]);
#pragma unroll
        for (int n = 0; n < 2; ++n) {
            const f32x4 a = acc[0][n] + acc[1][n];
            red[((2 * n) * 4 + w) * 64 + lane] = (f32x2){a[0], a[1]};
            red[((2 * n + 1) * 4 + w) * 64 + lane] = (f32x2){a[2], a[3]};
        }
        bar();
        const f32x2 r = (red[(w * 4 + 0) * 64 + lane] + red[(w * 4 + 1) * 64 + lane]) + (red[(w * 4 + 2) * 64 + lane] + red[(w * 4 + 3) * 64 + lane]);
        asm volatile("s_waitcnt vmcnt(32)" : "+v"(xv), "+v"(gv));
        const bool redo = tt >= 126 && !careful && __builtin_amdgcn_ballot_w64(r[0] != r[0]) != 0;
        const unsigned sv = pk2(r[0], r[1]), m = pk2(bf_lo(sv) * bf_lo(gv), bf_hi(sv) * bf_hi(gv));
        __builtin_amdgcn_raw_buffer_store_b32(pk2(bf_lo(xv) + bf_lo(m), bf_hi(xv) + bf_hi(m)), ro, xo, xs, 0);
        XST(w == 0 && tt >= 126, 256 + ((tt - 126) * 20 + cs) * 4 + 2);
        if (!have) {
            wait_vm<0>();
#pragma unroll
            for (int i = 0; i < 8; ++i) asm volatile("" : "+v"(ob[i][0]), "+v"(ob[i][1]), "+v"(ob[i][2]), "+v"(ob[i][3]));
        }
        careful = redo;
        if (!redo) tt = nx & 0xFFFF;
    }
}

DI void smf_x(f32x4& c, u32x4 a, u32x8 b, int idx, bool acc) { if (acc) smf_a(c, a, b, idx); else smf_v(c, a, b, idx); }
#define TRY()
#define V_A 80
#define V_M 52
#define KM_DELAY 240
#define PAD0 0
#define PAD_UP 0
#define PAD_MAX 48
#define RISE 64
#define DECAY 8
#ifndef M_DELAY
#define M_DELAY 0
#endif
#define L_SMALL 96
#define F_SMALL 280
#ifndef M_TAIL
#define M_TAIL 40
#endif
#define D_MST 300
#define F_LATE 1000
#define V_LATE 8
#define DUR_A0 980
#define DUR_M0 700
#define FT(i)
#define FDUMP(blk, s, w)
#define STAT(blk, s, w, a, b, c, d)
DI int wave_max(int d) {
    d = max(d, __builtin_amdgcn_update_dpp(0, d, 0x121, 0xf, 0xf, true));
    d = max(d, __builtin_amdgcn_update_dpp(0, d, 0x122, 0xf, 0xf, true));
    d = max(d, __builtin_amdgcn_update_dpp(0, d, 0x124, 0xf, 0xf, true));
    d = max(d, __builtin_amdgcn_update_dpp(0, d, 0x128, 0xf, 0xf, true));
    auto r = __builtin_amdgcn_permlane32_swap((unsigned)d, (unsigned)d, false, false); d = max((int)r[0], (int)r[1]);
    auto q = __builtin_amdgcn_permlane16_swap((unsigned)d, (unsigned)d, false, false); d = max((int)q[0], (int)q[1]);
    return __builtin_amdgcn_readfirstlane(d);
}
struct Clock {
    unsigned r0, m0, cq, qc;
    DI void edge(unsigned& r, unsigned& m) {
        unsigned long long a, b;
        const unsigned first = rt32();
        do asm volatile("s_memrealtime %0\n\ts_memtime %1\n\ts_waitcnt lgkmcnt(0)" : "=s"(a), "=s"(b) :: "memory"); while ((unsigned)a == first);
        r = (unsigned)a << 3; m = (unsigned)b - 24;
    }
    DI void start() { edge(r0, m0); cq = 3000; qc = 22370; }
    DI void sync() {
        unsigned r, m; edge(r, m);
        const float f = (float)(m - m0) * __builtin_amdgcn_rcpf((float)(r - r0));
        asm volatile("" : "+s"(r), "+s"(m));
        cq = __builtin_amdgcn_readfirstlane((unsigned)(f * 1024.f));
        qc = __builtin_amdgcn_readfirstlane((unsigned)(65536.f * __builtin_amdgcn_rcpf(f)));
        r0 = r; m0 = m;
    }
    DI unsigned at(unsigned m) const { return r0 + (unsigned)(((unsigned long long)(m - m0) * qc) >> 16); }
    DI static void sample(unsigned long long& a, unsigned long long& b) { asm volatile("s_memrealtime %0\n\ts_memtime %1\n\ts_waitcnt lgkmcnt(0)" : "=s"(a), "=s"(b) :: "memory"); }
    DI unsigned value(unsigned long long a, unsigned long long b) const {
        const unsigned lo = (unsigned)a << 3;
        return lo + __builtin_amdgcn_readfirstlane(min(max((int)(at((unsigned)b) - lo), 0), 31));
    }
    DI unsigned now() const { unsigned long long a, b; sample(a, b); return value(a, b); }
    DI unsigned cycles(unsigned t) const { const int d = __builtin_amdgcn_readfirstlane(min(max((int)(t - r0), 0), 1 << 20)); return m0 + (((unsigned)d * cq) >> 10) - 26; }
};
DI unsigned waitmt(unsigned t) { unsigned n; do n = mt32(); while ((int)(n - t) < 0); return n; }
#define MST_SH 2
DI unsigned mst_off(int m) { return (m >> MST_SH) * 128 + (m & ((1 << MST_SH) - 1)) * 4; }
struct Sched {
    unsigned tgo, tau, durA, durM, pad;
    unsigned a, v;
    DI void fetch_a(rsrc_t rw, unsigned exo, unsigned pol, int s, int lane) { a = ld4(rw, s >= 2 ? (lane & 31) * 4 : OOB, ex_step(exo, pol, max(s - 2, 0)) + EX_AST); }
    DI void fetch_m(rsrc_t rw, unsigned exo, unsigned pol, int s, int lane) { v = ld4(rw, mst_off(min(lane, 39)), ex_step(exo, pol, s - 1) + EX_MST); }
    DI void take_a() { const int m = wave_max((int)a); durA += __builtin_amdgcn_ballot_w64(a - 1u >= 0xFFFFu) == 0 ? (min(m - (int)durA, RISE) + 2) >> 2 : 0; }
    DI void next(const Params& p, rsrc_t rw, unsigned exo, unsigned pol, int s, int lane, unsigned long long& alive) {
        if ((__builtin_amdgcn_ballot_w64(v == SENT) & alive) != 0) {
            const unsigned mo = ex_step(exo, pol, s - 1) + EX_MST;
            unsigned n = SPIN_CAP; bool bad;
#pragma nounroll
            do {
                asm volatile("" ::: "memory");
                v = ld4(rw, mst_off(min(lane, 39)), mo);
                bad = __builtin_amdgcn_ballot_w64(v == SENT) != 0;
            } while (bad && --n);
            if (bad) { alive = 0; set_tmo(p); }
        }
        const unsigned ref = __builtin_amdgcn_readfirstlane(v);
        const unsigned pm = ref + (unsigned)wave_max((int)(v - ref));
        if (s >= 2 && __builtin_amdgcn_ballot_w64(v & 1) == 0) durM += (min((int)(pm - tau - durM), RISE) + 2) >> 2;
        const bool late = s == 1 || (int)(pm + V_LATE - tgo) > 0;
        pad = late || __builtin_amdgcn_ballot_w64(v & 1) != 0 ? min(pad + PAD_UP, (unsigned)PAD_MAX) : (pad > 4 ? pad - 4 : 0);
        tau = (late ? pm + F_LATE : tgo) + durA + pad;
        tgo = tau + durM + V_A + pad;
    }
};

#define PACE 8
DI void pace() { asm volatile("s_nop %0" :: "n"(PACE)); }
DI void role_M(const Params& p, LAS uchar* lds, int mi, bool started) {
    const int tid = threadIdx.x, lane = tid & 63, w = __builtin_amdgcn_readfirstlane(tid >> 6), c = lane & 15, g = lane >> 4;
    const int qh = mi & 1, cs = mi >> 1;
    const rsrc_t rw = mk_rsrc(p.ws, WS_USED);
    u32x8 wf[8][2][2];
    {
        LAS uchar* lw = lds + w * 32768;
        const int q = c >> 2, pp = c & 3;
        unsigned ra[4];
#pragma unroll
        for (int m = 0; m < 4; ++m) {
            const int k0 = m < 2 ? 8 * g + 4 * m : 32 + 8 * g + 4 * (m - 2), ga = k0 >> 4, qq = (k0 >> 2) & 3;
            const int e = 32 * ga + 2 * qq + (q & 1) + 8 * (q >> 1);
            ra[m] = e * 64 + pp * 8;
        }
        if (w == 0 || !started) {
            wo_stage(p, (unsigned)(size_t)lw, 8 * w, cs, lane);
            wo_stage(p, (unsigned)(size_t)lw + 16384, 8 * w + 2, cs, lane);
        }
#pragma unroll
        for (int rd = 0; rd < 4; ++rd) {
            if (rd < 3) wait_vm<16>(); else wait_vm<0>();
#pragma unroll
            for (int i = 0; i < 2; ++i)
#pragma unroll
                for (int sel = 0; sel < 2; ++sel)
#pragma unroll
                    for (int nt = 0; nt < 2; ++nt) {
                        LAS uchar* a = lw + (rd & 1) * 16384 + i * 8192 + sel * 1024 + nt * 32;
                        const u32x2 t0 = trd(a + ra[0]), t1 = trd(a + ra[1]), t2 = trd(a + ra[2]), t3 = trd(a + ra[3]);
                        wf[2 * rd + i][sel][nt] = (u32x8){t0.x, t0.y, t1.x, t1.y, t2.x, t2.y, t3.x, t3.y};
                        if (2 * rd + i < 6) asm volatile("" : "+a"(wf[2 * rd + i][sel][nt])); else asm volatile("" : "+v"(wf[2 * rd + i][sel][nt]));
                    }
            asm volatile("s_waitcnt lgkmcnt(0)" ::: "memory");
            SB();
            if (rd < 2) wo_stage(p, (unsigned)(size_t)lw + (rd & 1) * 16384, 8 * w + 2 * rd + 4, cs, lane);
        }
    }
    bar();
    if (tid < 2) *(LAS unsigned*)(lds + 4096 + tid * 4) = 0;
    bar();
    const int idx = (c & 4) ? 0xEEEE : 0x4444;
    const int dl = 32 * cs + 2 * c;
    const unsigned so = mf_off(dl >> 6) + (((dl >> 4) & 3) * 16 + qh * 8 + ((dl >> 3) & 1) * 4 + g) * 16 + (dl & 7) * 2;
    const unsigned rwo = (w * 128 + (2 * (g & 1)) * 32 + 16 * (g >> 1) + c) * 4, rro = (g * 32 + 2 * c) * 4;
    const unsigned lo16 = lane * 16, sto = lane == 0 ? mst_off(mi) : OOB;
    unsigned long long alive = ~0ull;
    unsigned budget = SPIN_CAP;
    const u32x2 tag = launch_tag(rw);
    wait_boot(rw, tag);
    if (fresh_tid() == 0) add_ctr(p, C_ACK, 1u);
    const unsigned exo = ex_off(p), pol = read_pol(rw, tag);
    wait_ctr(p, rw, C_PZ, 16);
    const unsigned obo = uni(w * 32768 + qh * 1024);
    Clock ck; ck.start();
    unsigned tdone = ck.now();
    Sched sc = {0u, tdone - V_M, DUR_A0, DUR_M0, PAD0, 0u, 0u};
    for (int s = 0; s < NSEG - 1; ++s) {
        const unsigned eb = ex_step(exo, pol, s);
        const unsigned ob = eb + obo;
        if (s == 3) wait_ctr(p, rw, C_PZ + 32, 56);
        if (s > 0) {
            sc.fetch_a(rw, exo, pol, s, lane);
            ck.sync();
            sc.take_a();
            SB();
            waitmt(ck.cycles(tdone + D_MST));
            sc.fetch_m(rw, exo, pol, s, lane);
            sc.next(p, rw, exo, pol, s, lane, alive);
        }
        const unsigned tau = sc.tau;
        unsigned obs[8], mb = ex_step(exo, pol, s + 1);
#pragma unroll
        for (int i = 0; i < 8; ++i) { obs[i] = ob + (i >> 2) * 16384 + (i & 3) * 2048; asm volatile("" : "+s"(obs[i])); }
        unsigned rwa = rwo + (s & 1) * 2048, rra = rro + (s & 1) * 2048;
        f32x4 acc[2][2];
#pragma unroll
        for (int i = 0; i < 4; ++i) { acc[i >> 1][i & 1] = (f32x4){0.f, 0.f, 0.f, 0.f}; asm volatile("" : "+v"(acc[i >> 1][i & 1])); }
        unsigned tw = ck.cycles(tau + V_M);
        asm volatile("" : "+s"(mb), "+v"(rwa), "+v"(rra), "+s"(tw));
        waitmt(tw);
        FT(0);
        u32x4 of[8];
#pragma unroll
        for (int i = 0; i < 8; ++i) { of[i] = ld16(rw, lo16, obs[i]); SB(); if (i < 7) pace(); SB(); }
        auto mix = [&]() {
#pragma unroll
            for (int i = 0; i < 8; ++i) {
                smf_x(acc[0][1], of[i], wf[i][0][1], idx, i < 6);
                smf_x(acc[1][0], of[i], wf[i][1][0], idx, i < 6);
                smf_x(acc[0][0], of[i], wf[i][0][0], idx, i < 6);
                smf_x(acc[1][1], of[i], wf[i][1][1], idx, i < 6);
                if (i == 0) FT(1);
                if (i == 7) FT(2);
                SB();
            }
            asm volatile("s_nop 2" : "+v"(acc[0][1]), "+v"(acc[1][0]));
            float u[4], v[4], y[4];
#pragma unroll
            for (int r = 0; r < 4; ++r) {
                const float fa = acc[1][0][r], fb = acc[0][1][r];
                auto q = __builtin_amdgcn_permlane32_swap(__builtin_bit_cast(unsigned, fa), __builtin_bit_cast(unsigned, fb), false, false);
                u[r] = __builtin_bit_cast(float, (unsigned)q[0]); v[r] = __builtin_bit_cast(float, (unsigned)q[1]);
            }
            SB();
            asm volatile("s_nop 0" : "+v"(acc[0][0]), "+v"(acc[1][1]));
#pragma unroll
            for (int r = 0; r < 4; ++r) { float lo = acc[0][0][r] + v[r], hi = acc[1][1][r] + u[r]; asm("" : "+v"(lo), "+v"(hi)); y[r] = g < 2 ? lo : hi; }
            const float z0 = swap16_add(y[0], y[2]), z1 = swap16_add(y[1], y[3]);
            *(LAS float*)(lds + rwa) = z0;
            *(LAS float*)(lds + rwa + 128) = z1;
            SB();
            return __builtin_amdgcn_ballot_w64(__builtin_isunordered(z0, z1));
        };
        if (__builtin_expect(mix() != 0, 0)) {
            if (refetch<8>(of, rw, lo16, obs, budget)) {
                TRY();
                *(LAS unsigned*)(lds + 4096 + (s & 1) * 4) = s + 1;
#pragma unroll
                for (int i = 0; i < 4; ++i) { acc[i >> 1][i & 1] = (f32x4){0.f, 0.f, 0.f, 0.f}; asm volatile("" : "+v"(acc[i >> 1][i & 1])); }
                asm volatile("s_nop 1" ::: "memory");
                mix();
            }
        }
        FT(3);
        bar();
        FT(4);
        if (__builtin_expect(w == 0, 1)) {
            LAS f32x2* r2 = (LAS f32x2*)(lds + rra);
            const f32x2 a0 = r2[0], a1 = r2[64], a2 = r2[128], a3 = r2[192];
            const unsigned fl = *(LAS unsigned*)(lds + 4096 + (s & 1) * 4);
            SB();
            unsigned long long ca, cb;
            Clock::sample(ca, cb);
            SB();
            const f32x2 v = (a0 + a1) + (a2 + a3);
            st4(nosent(pk2(v[0], v[1])), rw, so, mb);
            SB();
            tdone = ck.value(ca, cb) + M_TAIL;
            st4(nosent((tdone & ~1u) | (fl == (unsigned)(s + 1))), rw, sto, eb + EX_MST);
        } else tdone = ck.now() + M_TAIL;
        if (budget == 0) set_tmo(p);
        FT(5); FDUMP(32 + mi, s, w);
    }
}

constexpr int RING = 4, VPRE = 4;
constexpr int a_slot(int ks, int tile) { return ks * 16384 + tile * 2048; }
constexpr int a_xblk(int i) { return a_slot(i & 1 ? 9 : 4, i & 2 ? 5 : 2); }
constexpr int A_P = a_slot(4, 7), A_P2 = A_P + 1024, A_CNT = A_P2 + 512;
template <int W> struct AP {
    static constexpr int ROT = 3 * W, NT = W == 0 ? 0 : (W == 3 ? 2 : 3), T0 = W == 0 ? 0 : 3 * (W - 1);
    static constexpr int NL = W == 0 ? 0 : (W == 3 ? 9 : 14);
    static constexpr int QA = W == 0 || W == 3 ? 20 : 15;
    static constexpr bool k_lds(int t, int m) { return W == 0 ? false : (W == 3 ? (t == 1 && m >= 1) : (t == 1 || (m > 0 && (m & 1) == 0))); }
    static constexpr bool k_acc(int t, int m) { return W == 0 && m < 5; }
    static constexpr bool v_lds(int t, int ks) { return W == 3 ? ks < 4 : (ks < 4 || (ks == 4 && t == 0)); }
    static constexpr int k_idx(int t, int m) { int n = 0; for (int i = 0; i < 2 * m + 1 - t; ++i) n += k_lds(1 - (i & 1), i >> 1) ? 1 : 0; return n; }
    static constexpr int k_slot(int i) { return a_slot(5 + i / (NT ? NT : 1), T0 + i % (NT ? NT : 1)); }
    static constexpr int v_slot(int i) { return a_slot(i / (NT ? NT : 1), T0 + i % (NT ? NT : 1)); }
    static constexpr int stream(int i) { return i < NL ? k_slot(i) : v_slot(i - NL); }
    static constexpr int m_of(int ks) { return (ks + 10 - ROT % 10) % 10; }
};
DI void a_dma(const uchar* wsrc, LAS uchar* lds, int h, int w, int half) {
    const int lane = fresh_lane();
    const rsrc_t r = mk_rsrc(wsrc, 640 * 8192);
    const unsigned v = (lane >> 4) * 8192 + (((lane & 15) ^ (2 * (((lane >> 4) & 1) | (w << 1)))) << 4);
    const unsigned lb = (unsigned)(size_t)lds;
#pragma unroll
    for (int i = 20 * half; i < 20 * half + 20; ++i) dma16(r, v, (4 * (w + 4 * i)) * 8192 + h * 256, lb + (w + 4 * i) * 1024);
}
struct ATake {
    unsigned row[4], f[4];
    DI ATake() {
        const int lane = fresh_lane(), c = lane & 15, g = lane >> 4, q = c >> 2, pp = c & 3;
#pragma unroll
        for (int m = 0; m < 4; ++m) {
            const int k0 = m < 2 ? 8 * g + 4 * m : 32 + 8 * g + 4 * (m - 2), ga = k0 >> 4, qq = (k0 >> 2) & 3;
            const int r = 16 * ga + 2 * qq + (q & 1) + 8 * (q >> 1);
            row[m] = r * 256 + pp * 8; f[m] = 32 * ((r & 1) | ((r >> 1) & 6));
        }
    }
    DI u32x8 get(LAS uchar* lds, int ks, int tile) const {
        LAS uchar* a = lds + ks * 16384;
        const u32x2 t0 = trd(a + row[0] + (f[0] ^ (32 * tile))), t1 = trd(a + row[1] + (f[1] ^ (32 * tile))), t2 = trd(a + row[2] + (f[2] ^ (32 * tile))), t3 = trd(a + row[3] + (f[3] ^ (32 * tile)));
        return (u32x8){t0.x, t0.y, t1.x, t1.y, t2.x, t2.y, t3.x, t3.y};
    }
};
DI u32x8 ldsB(LAS uchar* a) { const u32x4 lo = *(LAS u32x4*)a, hi = *(LAS u32x4*)(a + 1024); return (u32x8){lo.x, lo.y, lo.z, lo.w, hi.x, hi.y, hi.z, hi.w}; }
DI void ldsB_put(LAS uchar* a, u32x8 v) { *(LAS u32x4*)a = (u32x4){v[0], v[1], v[2], v[3]}; *(LAS u32x4*)(a + 1024) = (u32x4){v[4], v[5], v[6], v[7]}; }
DI void pin(u32x8& v, bool acc) { if (acc) asm volatile("" : "+a"(v)); else asm volatile("" : "+v"(v)); }

#define NEXT_KV() do { SB(); const int sn_ = min(s + 1, NSEG - 1); kvn = ld4(rw, 0, kvw * 4); \
    _Pragma("unroll") for (int i = 0; i < NPF; ++i) pf[i] = ld16(rw, lo16, pfb + sn_ * 32 * 8192 + i * 1024); SB(); } while (0)
#define KM_OUT() do { unsigned n_ = 0; if (lane == 0) n_ = __hip_atomic_fetch_add((LAS unsigned*)(lds + A_CNT), 1u, __ATOMIC_RELAXED, __HIP_MEMORY_SCOPE_WORKGROUP); \
    if ((__builtin_amdgcn_readfirstlane(n_) & 3) == 3) st4(s + 1, rw, lane == 0 ? (C_KM + h * 32 + 1) * 4 : OOB, 0); } while (0)
template <int W>
DI void a_loop(const Params& p, LAS uchar* lds, int h, const u32x8 (&wq)[2][10], const u32x8 (&wk)[2][10], const u32x8 (&wv)[3][10]) {
    typedef AP<W> P;
    const int lane = fresh_lane(), c = lane & 15, g = lane >> 4;
    const rsrc_t rw = mk_rsrc(p.ws, WS_USED);
    constexpr int ROT = P::ROT, NT = P::NT, T0 = P::T0, NPF = W == 0 ? 8 : NT, NL = P::NL;
    const int idx = (c & 8) ? 0xEEEE : 0x4444;
    const unsigned lo16 = lane * 16, mo16 = (g * 16 + ((c >> 2) & 1) * 8 + (c >> 3) * 4 + (c & 3)) * 16;
    const unsigned oaddr = W == 0 ? (c >= 8 ? (g * 8 + (c & 7)) * 16 : OOB) : (c < 8 ? (c >> 2) * 1024 + (g >> 1) * 64 + (c & 3) * 16 + (g & 1) * 8 : OOB);
    const unsigned oaddr2 = c < 8 ? (c >> 2) * 1024 + (g & 1) * 128 + (g >> 1) * 64 + (c & 3) * 16 : OOB;
    const unsigned xw = a_xblk(W) + c * 40 + g * 8;
    const unsigned asto = lane == 0 ? h * 4 : OOB;
    const unsigned pfb = W == 0 ? OFF_KF + h * 8192 : OFF_VF + h * 8192 + T0 * 1024;
    u32x4 pf[NPF];
    unsigned long long alive = ~0ull;
    unsigned budget = SPIN_CAP;
    const u32x2 tag = launch_tag(rw);
    wait_boot(rw, tag);
    if (W == 0 && fresh_tid() == 0) add_ctr(p, C_ACK, 1u);
    const unsigned exo = ex_off(p), pol = read_pol(rw, tag);
    const unsigned obo = uni((h >> 2) * 16384 + (h & 3) * 2048 + T0 * 128);
    Clock ck; ck.start();
    wait_ctr(p, rw, C_PZ, 16);
    wait_ctr(p, rw, C_KVH + h * 16, 4);
#pragma unroll
    for (int i = 0; i < NPF; ++i) pf[i] = ld16(rw, lo16, pfb + i * 1024);
    Sched sc = {ck.now(), 0u, DUR_A0, DUR_M0, PAD0, 0u, 0u};
    const float ninf = g < 2 ? -__builtin_inff() : 0.f;
    unsigned xra[4], xqa[4];
    if (W == 0) {
#pragma unroll
        for (int ks = 0; ks < 4; ++ks) {
            xra[ks] = a_xblk(ks) + (4 * g + (c >> 2)) * 40 + (c & 3) * 8; xqa[ks] = a_xblk(ks) + (4 * g + (c >> 2)) * 40 + (c & 1) * 8;
            asm volatile("" : "+v"(xra[ks]), "+v"(xqa[ks]));
        }
    }
    u32x4 one4 = {0x3F803F80u, 0x3F803F80u, 0x3F803F80u, 0x3F803F80u};
    u32x8 pw = {0u, 0u, 0u, 0u, 0u, 0u, 0u, 0u};
    unsigned pa[3] = {A_P + lo16, A_P2 + lane * 8u, A_P2 + (lane ^ 32) * 8u};
    if (W) asm volatile("" : "+a"(one4), "+v"(pw), "+v"(pa[0]), "+v"(pa[1]), "+v"(pa[2]));
    unsigned lb[3] = {lo16, lo16 + 65536, lo16 + 131072};
    asm volatile("" : "+v"(lb[0]), "+v"(lb[1]), "+v"(lb[2]));
    auto sget = [&](int i) -> u32x8 { const int a = P::stream(i); return ldsB(lds + lb[a >> 16] + (a & 65535)); };
    for (int s = 0; s < NSEG; ++s) {
        const unsigned eb = ex_step(exo, pol, s);
        unsigned mb[10], ob = eb + obo;
#pragma unroll
        for (int m = 0; m < 10; ++m) { mb[m] = eb + mf_off((m + ROT) % 10); asm volatile("" : "+s"(mb[m])); }
        f32x4 aq[2], ak[2], va[3];
        u32x8 ring[RING];
#pragma unroll
        for (int t = 0; t < 2; ++t) { aq[t] = (f32x4){0.f, 0.f, 0.f, 0.f}; ak[t] = aq[t]; asm volatile("" : "+v"(aq[t]), "+v"(ak[t])); }
#pragma unroll
        for (int t = 0; t < 3; ++t) {
            const float z = W == 0 && t == 2 ? ninf : 0.f;
            va[t] = (f32x4){z, z, z, z}; asm volatile("" : "+v"(va[t]));
        }
        if (W) {
#pragma unroll
            for (int i = 0; i < RING; ++i) ring[i] = sget(i);
        }
        const unsigned tgo = sc.tgo;
        unsigned tw = ck.cycles(tgo);
        asm volatile("" : "+s"(ob), "+s"(tw));
        SB();
        unsigned mis = waitmt(tw);
        FT(0);
        u32x4 mf[10];
#pragma unroll
        for (int m = 0; m < 10; ++m) {
            mf[m] = ld16(rw, mo16, mb[m]);
            SB();
            if (m < 9) pace();
            SB();
        }
        auto project = [&]() {
            static_for<10>([&](auto mc) {
                constexpr int m = mc.value;
                auto qprod = [&](auto tc) { constexpr int t = tc.value; smf_x(aq[t], mf[m], wq[t][m], idx, 2 * m + t < P::QA - (W == 0 ? 10 : 0)); };
                auto kprod = [&](auto tc) {
                    constexpr int t = tc.value;
                    if constexpr (P::k_lds(t, m)) {
                        constexpr int i = P::k_idx(t, m);
                        smf_v(ak[t], mf[m], ring[i % RING], idx);
                        ring[i % RING] = sget(i + RING);
                    } else smf_x(ak[t], mf[m], wk[t][m], idx, W == 0);
                };
                kprod(IC<1>{});
                qprod(IC<m == 9 ? 1 : 0>{});
                if constexpr (m == 9) qprod(IC<0>{});
                kprod(IC<0>{});
                if constexpr (m < 9) qprod(IC<1>{});
                if (m == 0) FT(1);
                if (m == 9) FT(2);
                SB();
            });
            float x[4], y[4];
            asm volatile("s_nop 2" : "+v"(aq[1]), "+v"(ak[1]));
#pragma unroll
            for (int r = 0; r < 4; ++r) y[r] = swap32_add(aq[1][r], ak[1][r]);
            *(LAS u32x2*)(lds + xw + 640) = (u32x2){pk2(y[0], y[1]), pk2(y[2], y[3])};
            SB();
            asm volatile("" : "+v"(aq[0]), "+v"(ak[0]));
#pragma unroll
            for (int r = 0; r < 4; ++r) x[r] = swap32_add(aq[0][r], ak[0][r]);
            *(LAS u32x2*)(lds + xw) = (u32x2){pk2(x[0], x[1]), pk2(x[2], x[3])};
            SB();
            return nan_lanes((f32x4){x[0], x[1], x[2], x[3]});
        };
        if (__builtin_expect(project() != 0, 0)) {
            if (refetch<10>(mf, rw, mo16, mb, budget)) {
                mis = tw + 100000; TRY();
#pragma unroll
                for (int t = 0; t < 2; ++t) { aq[t] = (f32x4){0.f, 0.f, 0.f, 0.f}; ak[t] = aq[t]; asm volatile("" : "+v"(aq[t]), "+v"(ak[t])); }
                if (W) {
#pragma unroll
                    for (int i = 0; i < RING; ++i) ring[i] = sget(i);
                }
                asm volatile("s_nop 1" ::: "memory");
                project();
            }
        }
        SB();
        if (W) {
#pragma unroll
            for (int i = 0; i < VPRE; ++i) { const int r = 10 * NT - 1 - i; smf_a(va[r % NT], mf[P::m_of(r / NT)], wv[r % NT][r / NT], idx); }
        }
        bar();
        SB();
        FT(3);
        const unsigned kb = OFF_KM + (s * 32 + h) * 4096;
        const int kvs = min(s + 2, NSEG - 1), kvw = (kvs < 16 && (kvs & 4) == 0 ? C_KVH : C_KV) + ((kvs >> 3) * 32 + h) * 16;
        unsigned tpub = 0, kvn;
        if (W == 0) {
            u32x4 xf[4], xd[4];
#pragma unroll
            for (int ks = 0; ks < 4; ++ks) {
                const u32x2 a = trd(lds + xqa[ks]), b = trd(lds + xqa[ks] + 640), lo = trd(lds + xra[ks]), hi = trd(lds + xra[ks] + 640);
                xd[ks] = (u32x4){a.x, a.y, b.x, b.y}; xf[ks] = (u32x4){lo.x, lo.y, hi.x, hi.y};
            }
            SB();
            if (__builtin_expect(s == NSEG - 1, 0)) {
#pragma unroll
                for (int ks = 0; ks < 4; ++ks) st16((u32x4){nosent(xf[ks].x), nosent(xf[ks].y), nosent(xf[ks].z), nosent(xf[ks].w)}, rw, oaddr, kb + ks * 512);
                KM_OUT();
            }
#pragma unroll
            for (int ks = 0; ks < 4; ++ks) { mfma_v(va[0], pf[ks], xd[ks]); mfma_v(va[1], pf[(4 + ks) % NPF], xd[ks]); mfma_v(va[2], xf[ks], xd[ks]); }
            FT(4);
            float a0, a1, a2, a3, a4, a5, tm, tn;
            unsigned e0, e1, e2;
            asm volatile("s_nop 3\n\t"
                         "v_cndmask_b32_e64 %0, %15, %11, %23\n\tv_cndmask_b32_e64 %1, %16, %12, %23\n\tv_cndmask_b32_e64 %2, %17, %13, %23\n\t"
                         "v_cndmask_b32_e64 %3, %18, %14, %23\n\tv_cndmask_b32_e64 %4, %21, %19, %23\n\tv_cndmask_b32_e64 %5, %22, %20, %23\n\t"
                         "v_max3_f32 %6, %0, %1, %2\n\tv_max3_f32 %7, %3, %4, %5\n\tv_max_f32 %6, %6, %7\n\ts_nop 1\n\t"
                         "v_max_f32_dpp %6, %6, %6 row_ror:8 row_mask:0xf bank_mask:0xf\n\t"
                         "v_mov_b32 %7, %6\n\ts_nop 1\n\tv_permlane32_swap_b32 %6, %7\n\tv_max_f32 %6, %6, %7\n\t"
                         "v_mov_b32 %7, %6\n\ts_nop 1\n\tv_permlane16_swap_b32 %6, %7\n\tv_max_f32 %6, %6, %7\n\t"
                         "v_mul_f32 %6, %24, %6\n\t"
                         "v_fma_f32 %0, %0, %24, -%6\n\tv_fma_f32 %1, %1, %24, -%6\n\tv_fma_f32 %2, %2, %24, -%6\n\t"
                         "v_fma_f32 %3, %3, %24, -%6\n\tv_fma_f32 %4, %4, %24, -%6\n\tv_fma_f32 %5, %5, %24, -%6\n\t"
                         "v_exp_f32 %0, %0\n\tv_exp_f32 %1, %1\n\tv_exp_f32 %2, %2\n\tv_exp_f32 %3, %3\n\tv_exp_f32 %4, %4\n\tv_exp_f32 %5, %5\n\t"
                         "v_cvt_pk_bf16_f32 %8, %0, %1\n\tv_cvt_pk_bf16_f32 %9, %2, %3\n\tv_cvt_pk_bf16_f32 %10, %4, %5"
                         : "=&v"(a0), "=&v"(a1), "=&v"(a2), "=&v"(a3), "=&v"(a4), "=&v"(a5), "=&v"(tm), "=&v"(tn), "=&v"(e0), "=&v"(e1), "=&v"(e2)
                         : "v"(va[0][0]), "v"(va[0][1]), "v"(va[0][2]), "v"(va[0][3]), "v"(va[1][0]), "v"(va[1][1]), "v"(va[1][2]), "v"(va[1][3]),
                           "v"(va[2][0]), "v"(va[2][1]), "v"(va[2][2]), "v"(va[2][3]), "s"(0x00FF00FF00FF00FFull), "s"(SCL));
            *(LAS u32x2*)(lds + A_P + (g * 16 + (c & 7)) * 16 + (c >> 3) * 8) = (u32x2){e0, e1};
            *(LAS unsigned*)(lds + A_P2 + (g * 16 + (c & 7)) * 8 + (c >> 3) * 4) = e2;
            FT(5);
            bar();
            FT(6);
            sc.fetch_a(rw, exo, pol, max(s, 1), lane); sc.fetch_m(rw, exo, pol, max(s, 1), lane);
            NEXT_KV();
            if (s < NSEG - 1) waitmt(mt32() + KM_DELAY * 3);
#pragma unroll
            for (int ks = 0; ks < 4; ++ks) if (s < NSEG - 1) st16(xf[ks], rw, oaddr, kb + ks * 512);
        } else {
            if (W == 3) st4(s, rw, asto, C_KM * 4 + h * 124);
#pragma unroll
            for (int i = 0; i < (10 * NT - NL - VPRE > NL ? 10 * NT - NL - VPRE : NL); ++i) {
                if (i < NL) {
                    smf_v(va[i % NT], mf[P::m_of(i / NT)], ring[(NL + i) % RING], idx);
                    if (NL + i + RING < 2 * NL) ring[(NL + i) % RING] = sget(NL + i + RING);
                }
                const int r = NL + i;
                if (r < 10 * NT - VPRE) smf_a(va[r % NT], mf[P::m_of(r / NT)], wv[r % NT][r / NT], idx);
                SB();
            }
            asm volatile("s_nop 7" : "+v"(va[0]), "+v"(va[1]), "+v"(va[2]));
            float sa[4], sb[4];
#pragma unroll
            for (int r = 0; r < 4; ++r) { sa[r] = swap32_add(va[0][r], va[1][r]); if (W != 3) sb[r] = swap32_add(va[2][r], va[2][r]); }
            u32x4 vv = {pk2(sa[0], sa[1]), pk2(sa[2], sa[3]), 0u, 0u};
            if (W != 3) { vv.z = pk2(sb[0], sb[1]); vv.w = pk2(sb[2], sb[3]); }
            asm volatile("" : "+v"(vv));
            if (__builtin_expect(s == NSEG - 1, 0)) {
                st8((u32x2){nosent(vv.x), nosent(vv.y)}, rw, lane * 8, kb + 2048 + T0 * 256);
                if (W != 3) st8((u32x2){nosent(vv.z), nosent(vv.w)}, rw, g < 2 ? lane * 8 : OOB, kb + 2048 + T0 * 256 + 512);
                KM_OUT();
            }
            SB();
            FT(5);
            bar();
            FT(6);
            SB();
            if (s < NSEG - 1) {
                const u32x4 p01 = *(LAS u32x4*)(lds + pa[0]);
                const u32x2 q2 = *(LAS u32x2*)(lds + pa[1]), q2s = *(LAS u32x2*)(lds + pa[2]);
                pw[4] = q2.x; pw[5] = q2.y; pw[0] = q2s.x; pw[1] = q2s.y;
                const u32x4 p2s = __builtin_shufflevector(pw, pw, 0, 1, 2, 3), p2h = __builtin_shufflevector(pw, pw, 2, 3, 4, 5), p2 = __builtin_shufflevector(pw, pw, 4, 5, 6, 7);
                SB();
                f32x4 l, o[3];
                asm volatile("v_mfma_f32_16x16x32_bf16 %0, %1, %2, 0" : "=&v"(l) : "a"(one4), "v"(p01));
                mfma_0(o[0], pf[0], p01);
                mfma_av(l, one4, p2);
                mfma_0(o[1], pf[1 % NPF], p01);
                mfma_v(o[0], vv, p2s);
                mfma_v(o[1], vv, p2);
                float inv;
                if (W != 3) {
                    mfma_0(o[2], pf[2 % NPF], p01);
                    asm volatile("v_rcp_f32 %0, %1" : "=v"(inv) : "v"(l[0]));
                    mfma_v(o[2], vv, p2h);
                    asm volatile("s_nop 3" : "+v"(o[0]), "+v"(o[1]), "+v"(inv));
                } else {
                    asm volatile("s_nop 1\n\tv_rcp_f32 %0, %3\n\ts_nop 5" : "=&v"(inv), "+v"(o[0]), "+v"(o[1]) : "v"(l[0]));
                }
                const f32x4 n0 = o[0] * inv, n1 = o[1] * inv;
                SB();
                const unsigned a0 = pk2(n0[0], n0[1]), b0 = pk2(n1[0], n1[1]), a1 = pk2(n0[2], n0[3]), b1 = pk2(n1[2], n1[3]);
                SB();
                auto r0 = __builtin_amdgcn_permlane16_swap(a0, b0, false, false);
                auto r1 = __builtin_amdgcn_permlane16_swap(a1, b1, false, false);
                st16((u32x4){nosent(r0[0]), nosent(r1[0]), nosent(r0[1]), nosent(r1[1])}, rw, oaddr2, ob);
                if (W != 3) {
                    asm volatile("" : "+v"(o[2]));
                    const f32x4 n2 = o[2] * inv;
                    st8((u32x2){nosent(pk2(n2[0], n2[1])), nosent(pk2(n2[2], n2[3]))}, rw, oaddr, ob + 256);
                }
            }
            tpub = ck.now();
            sc.fetch_a(rw, exo, pol, max(s, 1), lane); sc.fetch_m(rw, exo, pol, max(s, 1), lane);
            NEXT_KV();
            if (W == 1) st4(mis - tw <= 120u ? tpub - tgo : 0u, rw, asto, eb + EX_AST);
            if (s < NSEG - 1) st8((u32x2){vv.x, vv.y}, rw, lane * 8, kb + 2048 + T0 * 256);
            if (W != 3 && s < NSEG - 1) st8((u32x2){vv.z, vv.w}, rw, g < 2 ? lane * 8 : OOB, kb + 2048 + T0 * 256 + 512);
        }
        if (budget == 0) set_tmo(p);
        FT(7); FDUMP(h, s, W);
        STAT(h, s, W, ck.at(mis), tpub, tgo, tries);
        if (s == NSEG - 1) break;
        ck.sync();
        if (s > 0) { sc.take_a(); sc.next(p, rw, exo, pol, s, lane, alive); }
        if ((unsigned)__builtin_amdgcn_readfirstlane(kvn) < 4u) wait_ctr(p, rw, kvw, 4);
        if (s == 2) wait_ctr(p, rw, C_PZ + 32, 56);
    }
    XST(W == 0, h);
    if (W == 0 && h == 0) {
        wait_ctr(p, rw, C_ACK, 256);
        st8(tag, rw, lane == 0 ? 0 : OOB, B_EPOCH * 4);
    }
    __builtin_amdgcn_endpgm();
}

template <int W>
DI void a_wave(const Params& p, LAS uchar* lds, int h, bool started) {
    typedef AP<W> P;
    u32x8 wq[2][10], wk[2][10], wv[3][10];
    if (W == 0 || !started) {
        a_dma(p.wq, lds, h, W, 0);
        a_dma(p.wq, lds, h, W, 1);
    }
    static_for<6>([&](auto sc) {
        constexpr int mat = sc.value >> 1, half = sc.value & 1;
        if (sc.value < 5) wait_vm<20>(); else wait_vm<0>();
        bar();
        AST_(sc.value);
        const ATake tk;
#pragma unroll
        for (int ks = 5 * half; ks < 5 * half + 5; ++ks) {
            const int m = P::m_of(ks);
#pragma unroll
            for (int t = 0; t < (mat < 2 ? 2 : P::NT); ++t) {
                if (mat == 0) { wq[t][m] = tk.get(lds, ks, 2 * W + t); pin(wq[t][m], 2 * m + t < P::QA - (W == 0 ? 10 : 0)); }
                if (mat == 1) { wk[t][m] = tk.get(lds, ks, 2 * W + t); pin(wk[t][m], W == 0); }
                if (mat == 2) { wv[t][ks] = tk.get(lds, ks, P::T0 + t); pin(wv[t][ks], true); }
            }
            SB();
        }
        bar(); SB();
        if (mat < 2) a_dma(mat == 0 ? p.wk : p.wv, lds, h, W, half);
        else {
            const int lane = fresh_lane();
            if (half == 0) {
#pragma unroll
                for (int i = 0; i < P::NL; ++i) ldsB_put(lds + P::v_slot(i) + lane * 16, wv[i % (P::NT ? P::NT : 1)][i / (P::NT ? P::NT : 1)]);
            } else {
#pragma unroll
                for (int i = 0; i < 20; ++i) if (P::k_lds(i & 1, i >> 1)) ldsB_put(lds + P::k_slot(P::k_idx(i & 1, i >> 1)) + lane * 16, wk[i & 1][i >> 1]);
            }
        }
        SB();
    });
    if (W == 0) *(LAS unsigned*)(lds + A_CNT) = 0;
    bar();
    SB();
    a_loop<W>(p, lds, h, wq, wk, wv);
}
DI void role_A(const Params& p, LAS uchar* lds, int h, bool started) {
    const int w = __builtin_amdgcn_readfirstlane(threadIdx.x >> 6);
    if (w == 0) a_wave<0>(p, lds, h, started);
    else if (w == 1) a_wave<1>(p, lds, h, started);
    else if (w == 2) a_wave<2>(p, lds, h, started);
    else a_wave<3>(p, lds, h, started);
}
DI int take_ticket(const Params& p, LAS uchar* lds, int word) {
    if (fresh_tid() == 0) *(LAS unsigned*)(lds + LDS_BYTES - 16) = __hip_atomic_fetch_add((unsigned*)p.ws + word, 1u, __ATOMIC_RELAXED, __HIP_MEMORY_SCOPE_AGENT);
    bar();
    const int t = __builtin_amdgcn_readfirstlane(*(LAS unsigned*)(lds + LDS_BYTES - 16));
    bar();
    return t;
}
#define TSTAT(i, a, b)
#define SMALL_DELAY 300
DI void warm_l2(const Params& p, int x, int j) {
    const int lane = fresh_lane(), id = j * 4 + __builtin_amdgcn_readfirstlane(threadIdx.x >> 6);
    const rsrc_t rk = mk_rsrc(p.wk, 640 * 8192), rv = mk_rsrc(p.wv, 640 * 8192), ro = mk_rsrc(p.wo, 4096 * 1280);
    unsigned sink[24];
    const unsigned vw = (lane >> 4) * 8192 + (lane & 15) * 128, vo = (lane >> 2) * 1280 + min((lane & 3) * 128, 316);
#pragma unroll
    for (int i = 0; i < 4; ++i) asm volatile("buffer_load_dword %0, %1, %2, %3 offen" : "=v"(sink[i]) : "v"(vw), "s"(rk), "s"((id + 40 * i) * 4 * 8192 + x * 2048) : "memory");
#pragma unroll
    for (int i = 0; i < 7; ++i) asm volatile("buffer_load_dword %0, %1, %2, %3 offen" : "=v"(sink[4 + i]) : "v"(vo), "s"(ro), "s"(min(id + 40 * i, 255) * 16 * 1280 + x * 320) : "memory");
#pragma unroll
    for (int i = 0; i < 4; ++i) asm volatile("buffer_load_dword %0, %1, %2, %3 offen" : "=v"(sink[11 + i]) : "v"(vw), "s"(rv), "s"((id + 40 * i) * 4 * 8192 + x * 2048) : "memory");
    wait_vm<0>();
#pragma unroll
    for (int i = 0; i < 15; ++i) asm volatile("" :: "v"(sink[i]));
}
DI void background(const Params& p, LAS uchar* lds, int x, int j) {
    const bool big = x >= 4;
    if (!big) {
        const int id = j * 4 + x;
        const rsrc_t rw = mk_rsrc(p.ws, WS_USED);
        u32x2 tag;
        if (j < 4) ln_tile<true>(p, id, tag);
        else {
            warm_l2(p, x, j - 4);
            tag = launch_tag(rw);
            wait_boot(rw, tag);
        }
        if (fresh_tid() == 0) add_ctr(p, C_ACK, 1u);
        ln_tile<false>(p, id + 16, tag);
        const unsigned pol = read_pol(rw, tag);
        if (j < 4) poison(p, pol, id, 16, 0, 80, C_PZ);
        ln_tile<false>(p, id + 72, tag);
        poison(p, pol, id, 56, 80, 1376, C_PZ + 32);
    } else {
        if (j < 12) probe(p, lds, j * 4 + x - 4);
        if (j & 2) kv0_task<true>(p, lds, 32 * (x - 4) + j); else kv0_task<false>(p, lds, 32 * (x - 4) + j);
        TSTAT((x - 4) * 128 + j, t0, 0);
    }
    if (!big) return;
    const int y = x - 4;
    for (;;) {
        int t = take_ticket(p, lds, C_Q1 + x * 32), kind = 0, chunk = 0, u0 = 0, nu = 8;
        const int wu = 192 + 8 * y, wn = y < 3 ? 4 : 2;
        if (t < 8) { chunk = t >> 1; u0 = wu + wn * (t & 1); nu = y < 3 ? 4 : 2 - (t & 1); }
        else if ((t -= 8) < 8) { chunk = 2; u0 = 32 * y + 4 * t; nu = 4; }
        else if ((t -= 8) < 8) { chunk = t >> 2; u0 = 128 + 16 * y + 4 * (t & 3); nu = 4; }
        else if ((t -= 8) < 8) { chunk = 3; u0 = 32 * y + 4 * t; nu = 4; }
        else if ((t -= 8) < 8) { chunk = 4 + (t >> 2); u0 = 32 * y + 8 * (t & 3); }
        else if ((t -= 8) < 5) { kind = 1; chunk = y; u0 = t; }
        else if ((t -= 5) < 4) { chunk = 4 + t; u0 = wu; nu = y < 3 ? 8 : 3; }
        else if ((t -= 4) < 6) { kind = 3; u0 = 6 * y + t; }
        else if ((t -= 6) < 4) { chunk = 2 + (t >> 1); u0 = 128 + 16 * y + 8 * (t & 1); }
        else if ((t -= 4) < 8) { chunk = 6 + (t >> 2); u0 = 32 * y + 8 * (t & 3); }
        else if ((t -= 8) < 8) { chunk = 4 + (t >> 1); u0 = 128 + 16 * y + 8 * (t & 1); }
        else if ((t -= 8) < 5) { kind = 1; chunk = 4 + y; u0 = t; }
        else if ((t -= 5) < 5) { kind = 2; chunk = y; u0 = t; }
        else if ((t -= 5) < 5 * N_WG) { kind = 4; u0 = t; }
        else return;
        if (kind == 0) p1_task(p, lds, chunk, u0, nu);
        else if (kind == 1) gemm_task<false>(p, lds, chunk, u0);
        else if (kind == 2) gemm_task<true>(p, lds, chunk, u0);
        else {
            wait_ctr(p, mk_rsrc(p.ws, WS_USED), C_PZ + 32, 56);
            if (kind == 3) {
                const int w = __builtin_amdgcn_readfirstlane(threadIdx.x >> 6);
                for (int s = u0 >> 3; s < NSEG; s += N_T / 8) attn_task(p, s, (u0 & 7) * 4 + w, (s & 7) < N_T / 8);
            } else { const int rank = (u0 * 52) >> 8; w_role(p, lds, 5 * y + u0 - 5 * rank, rank); }
            TSTAT(x * 128 + tk0, t0, kind + 2);
            return;
        }
        TSTAT(x * 128 + tk0, t0, kind == 0 ? (u0 < 128 ? 1 : (u0 < 192 ? 2 : 3)) : (kind == 1 ? 4 : 7));
    }
}
__global__ void __launch_bounds__(256, 1) k_main(Params p) {
    extern __shared__ __attribute__((aligned(16))) uchar smem[];
    LAS uchar* lds = (LAS uchar*)smem;
    const int w = uni(threadIdx.x >> 6), guess = uni((blockIdx.x & ~7) | xcc_id());
    if (w && (guess & 7) < 4) {
        const int x = guess & 7, t = guess >> 3;
        if (t < 8) { a_dma(p.wq, lds, x * 8 + t, w, 0); a_dma(p.wq, lds, x * 8 + t, w, 1); }
        else if (t < 18) {
            const int lane = fresh_lane(), cs = (x * 10 + t - 8) >> 1;
            wo_stage(p, (unsigned)(size_t)lds + w * 32768, 8 * w, cs, lane);
            wo_stage(p, (unsigned)(size_t)lds + w * 32768 + 16384, 8 * w + 2, cs, lane);
        }
    }
    if (w == 0) {
        int slot = guess;
        const u32x2 tag = launch_tag(mk_rsrc(p.ws, WS_USED));
        const unsigned long long t64 = ((unsigned long long)tag.y << 32) | tag.x;
        unsigned long long* cl = (unsigned long long*)(p.ws + ex_off(p) + 12 * EX_STEP);
        for (int i = 0; i < 256; ++i) {
            unsigned long long old = 0;
            if (fresh_lane() == 0) old = __hip_atomic_exchange(cl + slot, t64, __ATOMIC_RELAXED, __HIP_MEMORY_SCOPE_AGENT);
            if (uni((unsigned)old) != tag.x || uni((unsigned)(old >> 32)) != tag.y) break;
            slot = slot < 248 ? slot + 8 : (slot + 1) & 7;
        }
        *(LAS int*)lds = slot;
    }
    bar();
    const int slot = uni(*(LAS int*)lds), x = slot & 7, t = slot >> 3;
    if (slot != guess) wait_vm<0>();
    bar();
    if (x < 4 && t < 8) role_A(p, lds, x * 8 + t, slot == guess);
    else if (x < 4 && t < 18) role_M(p, lds, x * 10 + t - 8, slot == guess);
    else background(p, lds, x, x < 4 ? t - 18 : t);
}

extern "C" void kernel_launch(void* const* d_in, const int* in_sizes, int n_in, void* d_out, int out_size, void* d_ws, size_t ws_size, hipStream_t stream) {
    Params p;
    p.x = (const uchar*)d_in[0]; p.mem0 = (const uchar*)d_in[1]; p.wq = (const uchar*)d_in[2]; p.wk = (const uchar*)d_in[3];
    p.wv = (const uchar*)d_in[4]; p.wo = (const uchar*)d_in[5]; p.w1 = (const uchar*)d_in[8]; p.b1 = (const uchar*)d_in[9];
    p.w2 = (const uchar*)d_in[10]; p.out = (uchar*)d_out; p.ws = (uchar*)d_ws;
    (void)hipFuncSetAttribute((const void*)k_main, hipFuncAttributeMaxDynamicSharedMemorySize, LDS_BYTES);
    k_main<<<256, 256, LDS_BYTES, stream>>>(p);
}
```

```cpp
#include <hip/hip_runtime.h>
#include <stdint.h>

typedef __bf16 bf16x2 __attribute__((ext_vector_type(2)));
typedef __bf16 bf16x4 __attribute__((ext_vector_type(4)));
typedef __bf16 bf16x8 __attribute__((ext_vector_type(8)));
typedef __bf16 bf16x16 __attribute__((ext_vector_type(16)));
typedef short s16x4 __attribute__((ext_vector_type(4)));
typedef float f32x2 __attribute__((ext_vector_type(2)));
typedef float f32x4 __attribute__((ext_vector_type(4)));
typedef unsigned u32x2 __attribute__((ext_vector_type(2)));
typedef unsigned u32x4 __attribute__((ext_vector_type(4)));
typedef unsigned u32x8 __attribute__((ext_vector_type(8)));
typedef unsigned char uchar;
typedef __amdgpu_buffer_rsrc_t rsrc_t;
#define LAS __attribute__((address_space(3)))
#define DI __device__ __forceinline__

constexpr int S_ = 2048, D_ = 640, H_ = 32, NSEG = 64, F_ = 1728;
constexpr int LDS_BYTES = 163840;
constexpr int OFF_CTRL = 0;
constexpr int OFF_XF = 65536;
constexpr int OFF_KF = OFF_XF + 128 * 20 * 1024;
constexpr int OFF_QF = OFF_KF + 64 * 32 * 8192;
constexpr int OFF_VF = OFF_QF + 64 * 32 * 8192;
constexpr int OFF_ACT = OFF_VF + 64 * 32 * 8192;
constexpr int OFF_GF = OFF_ACT + 128 * 54 * 1024;
constexpr int OFF_OT = OFF_GF + 128 * 20 * 1024;
constexpr int OFF_KM = OFF_OT + 128 * 32 * 4096;
constexpr int OFF_EX = OFF_KM + 64 * 32 * 4096;
constexpr int EX_STEP = 163840, EX_MF = 8 * 16384, EX_AST = 9 * 16384 + 2048, EX_MST = 9 * 16384 + 2304, EX_PROBE = 9 * 16384 + 7168;
constexpr int WS_USED = OFF_EX + 7 * 2097152;
constexpr int C_TMO = 0, C_ACK = 32, C_KV = 512  , C_Q = C_KV + 4096, C_ACT = C_Q + 4096  , C_G = C_ACT + 256  ,
              C_OT = C_G + 1280  , C_KM = C_OT + 2048  , C_Q1 = C_KM + 1024  , C_KVH = C_Q1 + 256  , C_X = C_KVH + 1024  , C_PZ = C_X + 512  , C_WT = C_PZ + 64  , C_OF = C_WT + 640  ,
              C_ZEND = 16 * 1008;
constexpr int B_INIT = C_ZEND  , B_VOTE = B_INIT + 112  , B_EPOCH = 16352;
static_assert(C_OF + 128 <= C_ZEND && B_VOTE + 96 <= B_EPOCH && B_EPOCH + 2 <= 16384, "");
constexpr unsigned SENT = 0xFFFFFFFFu;
constexpr int SC1 = 16;
constexpr unsigned OOB = 0x7FFFFF00u;
constexpr unsigned SPIN_CAP = 400000;

struct Params {
    const uchar* x; const uchar* mem0; const uchar* wq; const uchar* wk; const uchar* wv; const uchar* wo;
    const uchar* w1; const uchar* b1; const uchar* w2; uchar* out; uchar* ws;
};

DI rsrc_t mk_rsrc(const void* p, unsigned bytes) { return __builtin_amdgcn_make_buffer_rsrc((void*)p, 0, bytes, 0x00020000); }
DI u32x4 ld16(rsrc_t r, unsigned v, unsigned s) { return __builtin_amdgcn_raw_buffer_load_b128(r, v, s, SC1); }
DI u32x2 ld8(rsrc_t r, unsigned v, unsigned s) { return __builtin_amdgcn_raw_buffer_load_b64(r, v, s, SC1); }
DI unsigned ld4(rsrc_t r, unsigned v, unsigned s) { return __builtin_amdgcn_raw_buffer_load_b32(r, v, s, SC1); }
DI void st16(u32x4 d, rsrc_t r, unsigned v, unsigned s) { __builtin_amdgcn_raw_buffer_store_b128(d, r, v, s, SC1); asm volatile("s_nop 1" :: "v"(d)); }
DI void st8(u32x2 d, rsrc_t r, unsigned v, unsigned s) { __builtin_amdgcn_raw_buffer_store_b64(d, r, v, s, SC1); }
DI void st4(unsigned d, rsrc_t r, unsigned v, unsigned s) { __builtin_amdgcn_raw_buffer_store_b32(d, r, v, s, SC1); }
DI unsigned pk2(float a, float b) { f32x2 v = {a, b}; return __builtin_bit_cast(unsigned, __builtin_convertvector(v, bf16x2)); }
DI unsigned nosent(unsigned v) { return min(v, 0xFFFFFFFEu); }
DI float bf_lo(unsigned v) { return __builtin_bit_cast(float, v << 16); }
DI float bf_hi(unsigned v) { return __builtin_bit_cast(float, v & 0xFFFF0000u); }
DI float rbf(float a) { return bf_lo(pk2(a, a)); }
DI bf16x8 as8(u32x4 v) { return __builtin_bit_cast(bf16x8, v); }
DI f32x4 mfma32(u32x4 a, u32x4 b, f32x4 c) { return __builtin_amdgcn_mfma_f32_16x16x32_bf16(as8(a), as8(b), c, 0, 0, 0); }
DI f32x4 mfma16(u32x2 a, u32x2 b, f32x4 c) { return mfma32((u32x4){a.x, a.y, 0u, 0u}, (u32x4){b.x, b.y, 0u, 0u}, c); }
DI f32x4 smf(u32x4 a, u32x8 b, f32x4 c, int idx) { return __builtin_amdgcn_smfmac_f32_16x16x64_bf16(as8(a), __builtin_bit_cast(bf16x16, b), c, idx, 0, 0); }
DI void smf_a(f32x4& c, u32x4 a, u32x8 b, int idx) { asm volatile("v_smfmac_f32_16x16x64_bf16 %0, %1, %2, %3" : "+v"(c) : "v"(a), "a"(b), "v"(idx)); }
DI void smf_v(f32x4& c, u32x4 a, u32x8 b, int idx) { asm volatile("v_smfmac_f32_16x16x64_bf16 %0, %1, %2, %3" : "+v"(c) : "v"(a), "v"(b), "v"(idx)); }
DI void mfma_v(f32x4& c, u32x4 a, u32x4 b) { asm volatile("v_mfma_f32_16x16x32_bf16 %0, %1, %2, %0" : "+v"(c) : "v"(a), "v"(b)); }
DI void mfma_0(f32x4& c, u32x4 a, u32x4 b) { asm volatile("v_mfma_f32_16x16x32_bf16 %0, %1, %2, 0" : "=&v"(c) : "v"(a), "v"(b)); }
DI float fmax2(float a, float b) { return __builtin_amdgcn_fmed3f(a, b, __builtin_inff()); }
DI void mfma_pad(f32x4& a, f32x4& b, f32x4& c, f32x4& d) { asm volatile("s_nop 7" : "+v"(a), "+v"(b), "+v"(c), "+v"(d)); }
DI u32x2 trd(LAS uchar* p) { return __builtin_bit_cast(u32x2, __builtin_amdgcn_ds_read_tr16_b64_v4i16((LAS s16x4*)p)); }
DI unsigned mx4(unsigned m, u32x4 v) { return max(max(max(m, v.x), max(v.y, v.z)), v.w); }
DI void mx4v(unsigned& m, u32x4 v) { asm volatile("v_max3_u32 %0, %0, %1, %2\n\tv_max3_u32 %0, %0, %3, %4" : "+v"(m) : "v"(v.x), "v"(v.y), "v"(v.z), "v"(v.w)); }
DI unsigned long long nan_lanes(const f32x4& a) { return __builtin_amdgcn_ballot_w64(__builtin_isunordered(a[0], a[1]) | __builtin_isunordered(a[2], a[3])); }
template <int N> DI bool refetch(u32x4 (&f)[N], rsrc_t r, unsigned v, const unsigned (&so)[N], unsigned& budget) {
    bool redo = false;
#pragma nounroll
    for (;;) {
        unsigned m = 0;
#pragma unroll
        for (int i = 0; i < N; ++i) m = max(max(max(m, f[i].x), max(f[i].y, f[i].z)), f[i].w);
        if (__builtin_amdgcn_ballot_w64(m == SENT) == 0 || budget == 0) break;
        --budget; redo = true;
        asm volatile("" ::: "memory");
#pragma unroll
        for (int i = 0; i < N; ++i) f[i] = __builtin_amdgcn_raw_buffer_load_b128(r, v, so[i], SC1);
    }
    return redo;
}
DI unsigned rt32() { unsigned long long t; asm volatile("s_memrealtime %0\n\ts_waitcnt lgkmcnt(0)" : "=s"(t) :: "memory"); return (unsigned)t; }
DI unsigned waitrt(unsigned t) { unsigned n; do n = rt32(); while ((int)(n - t) < 0); return n; }
DI void bar() { asm volatile("s_waitcnt lgkmcnt(0)" ::: "memory"); __builtin_amdgcn_s_barrier(); asm volatile("" ::: "memory"); }
template <int N> DI void wait_vm() { asm volatile("s_waitcnt vmcnt(%0)" :: "n"(N) : "memory"); }
DI void dma16(rsrc_t r, unsigned voff, unsigned soff, unsigned ldsaddr) {
    asm volatile("s_mov_b32 m0, %0\n\ts_nop 0\n\tbuffer_load_dwordx4 %1, %2, %3 offen lds" :: "s"(ldsaddr), "v"(voff), "s"(r), "s"(soff) : "memory");
}
DI float swap32_add(float a, float b) { auto r = __builtin_amdgcn_permlane32_swap(__builtin_bit_cast(unsigned, a), __builtin_bit_cast(unsigned, b), false, false);
    return __builtin_bit_cast(float, (unsigned)r[0]) + __builtin_bit_cast(float, (unsigned)r[1]); }
DI float swap16_add(float a, float b) { auto r = __builtin_amdgcn_permlane16_swap(__builtin_bit_cast(unsigned, a), __builtin_bit_cast(unsigned, b), false, false);
    return __builtin_bit_cast(float, (unsigned)r[0]) + __builtin_bit_cast(float, (unsigned)r[1]); }
DI float grp_max(float m) {
    auto r = __builtin_amdgcn_permlane32_swap(__builtin_bit_cast(unsigned, m), __builtin_bit_cast(unsigned, m), false, false);
    m = fmax2(__builtin_bit_cast(float, (unsigned)r[0]), __builtin_bit_cast(float, (unsigned)r[1]));
    auto q = __builtin_amdgcn_permlane16_swap(__builtin_bit_cast(unsigned, m), __builtin_bit_cast(unsigned, m), false, false);
    return fmax2(__builtin_bit_cast(float, (unsigned)q[0]), __builtin_bit_cast(float, (unsigned)q[1]));
}
DI int fresh_lane() { int l = __builtin_amdgcn_mbcnt_hi(~0u, __builtin_amdgcn_mbcnt_lo(~0u, 0u)); asm volatile("" : "+v"(l)); return l & 63; }
DI unsigned fresh_tid() { unsigned t = threadIdx.x; asm volatile("" : "+v"(t)); return t; }
DI unsigned uni(unsigned v) { asm volatile("" : "+v"(v)); return __builtin_amdgcn_readfirstlane(v); }
DI unsigned ex_off(const Params& p) { return OFF_EX + ((0u - uni((unsigned)(size_t)p.ws) - OFF_EX) & 0x1FFFFFu); }
DI unsigned ex_step(unsigned exo, unsigned pol, int s) { const int r = (s * 43) >> 9; return exo + r * 2097152 + (s - 12 * r) * EX_STEP + ((pol >> r) & 1) * 8192; }
DI constexpr unsigned mf_off(int ks) { return EX_MF + (ks >> 3) * 16384 + (ks & 7) * 1024; }
DI unsigned xcc_id() { return (unsigned)__builtin_amdgcn_s_getreg(20 | (3 << 11)) & 7u; }
DI unsigned mt32() { unsigned long long t; asm volatile("s_memtime %0\n\ts_waitcnt lgkmcnt(0)" : "=s"(t) :: "memory"); return (unsigned)t; }
DI void set_tmo(const Params& p) { __hip_atomic_store((unsigned*)p.ws + C_TMO, 1u, __ATOMIC_RELAXED, __HIP_MEMORY_SCOPE_AGENT); }

DI float row16_sum(float v) {
    v += __builtin_bit_cast(float, __builtin_amdgcn_update_dpp(0, __builtin_bit_cast(int, v), 0x121, 0xf, 0xf, true));
    v += __builtin_bit_cast(float, __builtin_amdgcn_update_dpp(0, __builtin_bit_cast(int, v), 0x122, 0xf, 0xf, true));
    v += __builtin_bit_cast(float, __builtin_amdgcn_update_dpp(0, __builtin_bit_cast(int, v), 0x124, 0xf, 0xf, true));
    v += __builtin_bit_cast(float, __builtin_amdgcn_update_dpp(0, __builtin_bit_cast(int, v), 0x128, 0xf, 0xf, true));
    return v;
}
DI void add_ctr(const Params& p, int word, unsigned v) { (void)__hip_atomic_fetch_add((unsigned*)p.ws + word, v, __ATOMIC_RELAXED, __HIP_MEMORY_SCOPE_AGENT); }
DI void wait_ctr(const Params& p, rsrc_t rw, int word, unsigned want) {
    unsigned n = SPIN_CAP;
#pragma nounroll
    for (;;) {
        asm volatile("" ::: "memory");
        if ((unsigned)__builtin_amdgcn_readfirstlane(ld4(rw, 0, word * 4)) >= want) break;
        if (--n == 0) { set_tmo(p); break; }
        if ((n & 63) == 0 && __builtin_amdgcn_readfirstlane(ld4(rw, 0, C_TMO * 4))) break;
        __builtin_amdgcn_s_sleep(1);
    }
    asm volatile("" ::: "memory");
}

template <int V> struct IC { static constexpr int value = V; };
template <int N, int I = 0, class F> DI void static_for(F&& f) { if constexpr (I < N) { f(IC<I>{}); static_for<N, I + 1>(f); } }
DI u32x2 launch_tag(rsrc_t rw) {
    const u32x2 e = ld8(rw, 0, B_EPOCH * 4);
    const unsigned lo = __builtin_amdgcn_readfirstlane(e.x) + 1u;
    return (u32x2){lo, (unsigned)__builtin_amdgcn_readfirstlane(e.y) + (lo == 0u ? 1u : 0u)};
}
DI u32x2 boot_words(rsrc_t rw) { return ld8(rw, (fresh_lane() & 15) * 8, B_INIT * 4); }
DI u32x2 vote_words(rsrc_t rw) { return ld8(rw, min(fresh_lane(), 47) * 8, B_VOTE * 4); }
DI void wait_boot(rsrc_t rw, u32x2 tag, u32x2 v) {
    for (unsigned n = SPIN_CAP; n; --n) {
        if (__builtin_amdgcn_ballot_w64(v.x != tag.x || v.y != tag.y) == 0) break;
        __builtin_amdgcn_s_sleep(1);
        asm volatile("" ::: "memory");
        v = boot_words(rw);
    }
    asm volatile("" ::: "memory");
}
DI void probe(const Params& p, LAS uchar* lds, int v, u32x2 tag) {
    const int lane = fresh_lane(), w = __builtin_amdgcn_readfirstlane(threadIdx.x >> 6);
    const rsrc_t rw = mk_rsrc(p.ws, WS_USED);
    unsigned* a = (unsigned*)(p.ws + ex_off(p) + (v >> 3) * 2097152 + EX_PROBE + (w >> 1) * 8192 + (w & 1) * EX_STEP + (v & 7) * 128);
    unsigned best = ~0u;
    for (int i = 0; i < 4; ++i) {
        const unsigned t0 = mt32();
        unsigned r = 0;
        if (lane == 0) r = __hip_atomic_fetch_add(a, 1u, __ATOMIC_RELAXED, __HIP_MEMORY_SCOPE_AGENT);
        asm volatile("s_waitcnt vmcnt(0)" :: "v"(r) : "memory");
        best = min(best, mt32() - t0);
    }
    ((LAS unsigned*)lds)[w] = best;
    bar();
    const LAS unsigned* lat = (const LAS unsigned*)lds;
    const int d = (int)min(lat[0], lat[1]) - (int)min(lat[2], lat[3]);
    st8((u32x2){tag.x, tag.y ^ ((abs(d) < 120 ? 0u : ((d > 0) == (xcc_id() >= 4) ? 1u : 2u)) << 30)}, rw, fresh_tid() == 0 ? v * 8 : OOB, B_VOTE * 4);
    bar();
}
DI unsigned read_pol(rsrc_t rw, u32x2 tag, u32x2 v) {
    for (unsigned n = SPIN_CAP; n; --n) {
        if (__builtin_amdgcn_ballot_w64(v.x != tag.x || ((v.y ^ tag.y) << 2) != 0) == 0) break;
        __builtin_amdgcn_s_sleep(1);
        asm volatile("" ::: "memory");
        v = vote_words(rw);
    }
    const unsigned c = (v.y ^ tag.y) >> 30;
    const unsigned long long m1 = __builtin_amdgcn_ballot_w64(c == 2), m0 = __builtin_amdgcn_ballot_w64(c == 1);
    unsigned pol = 0;
#pragma unroll
    for (int r = 0; r < 6; ++r) pol |= (__builtin_popcountll((m1 >> (8 * r)) & 255) > __builtin_popcountll((m0 >> (8 * r)) & 255) ? 1u : 0u) << r;
    return uni(pol);
}

template <bool FIRST>
DI void ln_tile(const Params& p, int b, u32x2 tag) {
    const int lane = fresh_lane(), w = __builtin_amdgcn_readfirstlane(threadIdx.x >> 6);
    const rsrc_t rw = mk_rsrc(p.ws, WS_USED);
    const int c = w * 4 + (lane >> 4), sub = lane & 15, row = b * 16 + c;
    u32x4 v[5]; float sum = 0.f;
#pragma unroll
    for (int k = 0; k < 5; ++k) v[k] = *(const u32x4*)(p.x + (size_t)row * 1280 + (k * 16 + sub) * 16);
    if (FIRST) {
        const unsigned tid = fresh_tid();
        st16((u32x4){0u, 0u, 0u, 0u}, rw, tid < 252 ? tid * 16 : OOB, b * 4032);
        wait_vm<0>();
        bar();
        st8(tag, rw, tid == 0 ? b * 8 : OOB, B_INIT * 4);
    }
#pragma unroll
    for (int k = 0; k < 5; ++k)
#pragma unroll
        for (int j = 0; j < 4; ++j) sum += bf_lo(v[k][j]) + bf_hi(v[k][j]);
    const float mean = row16_sum(sum) * (1.f / 640.f);
    float sq = 0.f;
#pragma unroll
    for (int k = 0; k < 5; ++k)
#pragma unroll
        for (int j = 0; j < 4; ++j) { const float a = bf_lo(v[k][j]) - mean, bb = bf_hi(v[k][j]) - mean; sq += a * a + bb * bb; }
    const float mb = rbf(mean), sd = rbf(sqrtf(rbf(rbf(row16_sum(sq) * (1.f / 640.f)) + 1e-5f))), ri = 1.f / sd;
#pragma unroll
    for (int k = 0; k < 5; ++k) {
        u32x4 o;
#pragma unroll
        for (int j = 0; j < 4; ++j) { const unsigned d = pk2(bf_lo(v[k][j]) - mb, bf_hi(v[k][j]) - mb); o[j] = pk2(bf_lo(d) * ri, bf_hi(d) * ri); }
        st16(o, rw, (k * 16 + sub) * 256 + c * 16, OFF_XF + b * 20480);
    }
    if (FIRST && w == 0) wait_boot(rw, tag, boot_words(rw));
    wait_vm<0>();
    bar();
    if (fresh_tid() == 0) add_ctr(p, C_X + (b >> 3) * 32, 1u);
}
DI void poison(const Params& p, unsigned pol, int j, int nj, int n0, int n1, int word) {
    const int tid = fresh_tid();
    const rsrc_t rw = mk_rsrc(p.ws, WS_USED);
    const unsigned exo = ex_off(p);
    const u32x4 sv = {SENT, SENT, SENT, SENT};
    for (int n = n0 + j; n < n1; n += nj) {
        const int s = min((n * 3277) >> 16, 63), i = n - 20 * s;
        u32x4 v = sv;
        if (s == 0 && i >= 16 && (i < 18 || (i == 18 && tid < 128))) {
            const int f = (i - 16) * 256 + tid, ks = f >> 6, l = f & 63, r = (l & 3) | ((l >> 1) & 4), half = (l >> 2) & 1, g = l >> 4;
            v = *(const u32x4*)(p.mem0 + (r * 640 + 64 * ks + 16 * g + 8 * half) * 2);
            v.x = nosent(v.x); v.y = nosent(v.y); v.z = nosent(v.z); v.w = nosent(v.w);
        }
        st16(v, rw, tid * 16, n < 1280 ? ex_step(exo, pol, s) + (i >> 1) * 16384 + (i & 1) * 4096
                                       : (n < 1312 ? OFF_KM + 63 * 131072 + (n - 1280) * 4096 : OFF_OT + 126 * 131072 + (n - 1312) * 4096));
    }
    wait_vm<0>();
    bar();
    if (fresh_tid() == 0) add_ctr(p, word, 1u);
}

#define SB() __builtin_amdgcn_sched_barrier(0)
DI void mfma_va(f32x4& c, u32x4 a, u32x4 b) { asm volatile("v_mfma_f32_16x16x32_bf16 %0, %1, %2, %0" : "+v"(c) : "v"(a), "a"(b)); }
DI void mfma_av(f32x4& c, u32x4 a, u32x4 b) { asm volatile("v_mfma_f32_16x16x32_bf16 %0, %1, %2, %0" : "+v"(c) : "a"(a), "v"(b)); }
DI void mfma_cacc(f32x4& c, u32x4 a, u32x4 b) { asm volatile("v_mfma_f32_16x16x32_bf16 %0, %1, %2, %0" : "+a"(c) : "v"(a), "v"(b)); }
struct P1Src {
    rsrc_t r; unsigned v0, v1, so, step;
    DI void set(const Params& p, int u, int kh, int w, int lane) {
        const uchar* base; unsigned stride, col, bytes;
        if (u < 128) { base = (u & 2) ? p.wv : p.wk; stride = 8192; col = ((u >> 2) * 128 + (u & 1) * 64) * 2; bytes = 640 * 8192; }
        else if (u < 192) { base = p.wq; stride = 8192; col = (u - 128) * 128; bytes = 640 * 8192; }
        else { base = p.w1; stride = 3456; col = (u - 192) * 128; bytes = u < 219 ? 640 * 3456 : 0; }
        r = mk_rsrc(base, bytes);
        const int rl = lane >> 3, f0 = ((rl >> 1) & 1) | (((rl >> 2) & 1) << 1);
        v0 = rl * stride + (((lane & 7) ^ (2 * f0)) << 4); v1 = v0 ^ 64;
        so = (kh * 320 + 80 * w) * stride + col; step = 8 * stride;
    }
    DI void piece(int i, unsigned ldsw) const { dma16(r, (i & 1) ? v1 : v0, so + i * step, ldsw + i * 1024); }
};
DI float mish(float a, float bias) {
    const float h = a + bias;
    const float e = __builtin_amdgcn_exp2f(fminf(h, 30.f) * 1.44269504f);
    const float n = e * (e + 2.f);
    return h * n * __builtin_amdgcn_rcpf(n + 2.f);
}
#define PT(i)
constexpr int XA_ACC = 15;
template <bool ISV, int KH>
DI void p1_slab(LAS uchar* sb, const unsigned (&tb)[4], const u32x4 (&xa)[4][20], f32x4 (&acc)[4][4], const P1Src& src, unsigned ldsn) {
    u32x4 wf[2][4];
    auto rd = [&](int kk, int nt) { const u32x2 lo = trd(sb + tb[nt] + kk * 4096), hi = trd(sb + tb[nt ^ 2] + kk * 4096 + 512); return (u32x4){lo.x, lo.y, hi.x, hi.y}; };
#pragma unroll
    for (int nt = 0; nt < 4; ++nt) wf[0][nt] = rd(0, nt);
    SB();
#pragma unroll
    for (int kk = 0; kk < 10; ++kk) {
#pragma unroll
        for (int nt = 0; nt < 4; ++nt) {
            if (kk + 1 < 10) wf[(kk + 1) & 1][nt] = rd(kk + 1, nt);
            if (kk < 5 && (nt & 1)) src.piece(2 * kk + (nt >> 1), ldsn);
#pragma unroll
            for (int t = 0; t < 4; ++t) {
                const int ks = KH * 10 + kk;
                if (ISV) { if (ks < XA_ACC) mfma_av(acc[t][nt], xa[t][ks], wf[kk & 1][nt]); else mfma_v(acc[t][nt], xa[t][ks], wf[kk & 1][nt]); }
                else { if (ks < XA_ACC) mfma_va(acc[t][nt], wf[kk & 1][nt], xa[t][ks]); else mfma_v(acc[t][nt], wf[kk & 1][nt], xa[t][ks]); }
            }
            SB();
        }
    }
    asm volatile("s_nop 7" : "+v"(acc[3][3]), "+v"(acc[2][3]), "+v"(acc[1][3]), "+v"(acc[0][3]), "+v"(acc[3][2]), "+v"(acc[2][2]), "+v"(acc[1][2]), "+v"(acc[0][2]));
#pragma unroll
    for (int t = 0; t < 4; ++t) asm volatile("" : "+v"(acc[t][0]), "+v"(acc[t][1]));
}
template <bool ISV>
DI void p1_group(const Params& p, LAS uchar* lds, rsrc_t rw, unsigned lbase, const unsigned (&tb)[4], const u32x4 (&xa)[4][20], P1Src& src, int buf,
                 int chunk, int u, int un, bool first, int w, int lane, unsigned (&pt)[5]) {
    const int g = lane >> 4;
    f32x4 acc[4][4];
#pragma unroll
    for (int t = 0; t < 4; ++t)
#pragma unroll
        for (int nt = 0; nt < 4; ++nt) { acc[t][nt] = (f32x4){0.f, 0.f, 0.f, 0.f}; asm volatile("" : "+v"(acc[t][nt])); }
    PT(0);
    if (first) wait_vm<50>(); else wait_vm<10>();
    PT(1);
    bar();
    PT(2);
    src.set(p, un, 0, w, lane);
    {
        const unsigned ln = lbase + (buf == 0 ? 2 : buf - 1) * 40960;
        p1_slab<ISV, 0>(lds + buf * 40960, tb, xa, acc, src, ln);
    }
    buf = buf == 2 ? 0 : buf + 1;
    PT(3);
    wait_vm<10>();
    PT(1);
    bar();
    PT(2);
    src.set(p, un, 1, w, lane);
    {
        const unsigned ln = lbase + (buf == 0 ? 2 : buf - 1) * 40960;
        p1_slab<ISV, 1>(lds + buf * 40960, tb, xa, acc, src, ln);
    }
    PT(3);
    const int tt0 = chunk * 16 + w * 4;
    if (ISV) {
        const int h = u >> 2;
#pragma unroll
        for (int sp = 0; sp < 2; ++sp)
#pragma unroll
            for (int nt = 0; nt < 4; ++nt) {
                const f32x4 a = acc[2 * sp][nt], b = acc[2 * sp + 1][nt];
                const u32x4 o = {pk2(a[0], a[1]), pk2(a[2], a[3]), pk2(b[0], b[1]), pk2(b[2], b[3])};
                st16(o, rw, lane * 16, OFF_VF + (((tt0 >> 1) + sp) * 32 + h) * 8192 + ((u & 1) * 4 + nt) * 1024);
            }
    } else if (u < 192) {
        const int h = u < 128 ? u >> 2 : (u - 128) >> 1;
        const int ob = u < 128 ? OFF_KF : OFF_QF;
#pragma unroll
        for (int t = 0; t < 4; ++t)
#pragma unroll
            for (int m = 0; m < 2; ++m) {
                const f32x4 a = acc[t][2 * m], b = acc[t][2 * m + 1];
                const u32x4 o = {pk2(a[0], a[1]), pk2(a[2], a[3]), pk2(b[0], b[1]), pk2(b[2], b[3])};
                st16(o, rw, lane * 16, ob + (((tt0 + t) >> 1) * 32 + h) * 8192 + ((t & 1) * 4 + (u & 1) * 2 + m) * 1024);
            }
    } else {
        const int fg = u - 192;
        u32x2 bv[4];
#pragma unroll
        for (int nt = 0; nt < 4; ++nt) bv[nt] = *(const u32x2*)(p.b1 + (fg * 64 + nt * 16 + 4 * g) * 2);
#pragma unroll
        for (int t = 0; t < 4; ++t)
#pragma unroll
            for (int m = 0; m < 2; ++m) {
                const f32x4 a = acc[t][2 * m], b = acc[t][2 * m + 1];
                const u32x2 ba = bv[2 * m], bb = bv[2 * m + 1];
                const u32x4 o = {pk2(mish(a[0], bf_lo(ba.x)), mish(a[1], bf_hi(ba.x))), pk2(mish(a[2], bf_lo(ba.y)), mish(a[3], bf_hi(ba.y))),
                                 pk2(mish(b[0], bf_lo(bb.x)), mish(b[1], bf_hi(bb.x))), pk2(mish(b[2], bf_lo(bb.y)), mish(b[3], bf_hi(bb.y)))};
                st16(o, rw, lane * 16, OFF_ACT + ((tt0 + t) * 54 + fg * 2 + m) * 1024);
                SB();
            }
    }
}
DI void ld16_a(u32x4& d, rsrc_t r, unsigned v, unsigned s) { asm volatile("buffer_load_dwordx4 %0, %1, %2, %3 offen sc1" : "=a"(d) : "v"(v), "s"(r), "s"(s) : "memory"); }
DI void ld16_v(u32x4& d, rsrc_t r, unsigned v, unsigned s) { asm volatile("buffer_load_dwordx4 %0, %1, %2, %3 offen sc1" : "=v"(d) : "v"(v), "s"(r), "s"(s) : "memory"); }
DI void ld16_s(u32x4& d, rsrc_t r, unsigned v, unsigned s) { asm volatile("buffer_load_dwordx4 %0, %1, %2, %3 offen sc1" : "=v"(d) : "v"(v), "s"(r), "s"(s) : "memory"); }
DI void p1_task(const Params& p, LAS uchar* lds, int chunk, int u0, int nu) {
    const int lane = fresh_lane(), w = __builtin_amdgcn_readfirstlane(threadIdx.x >> 6), c = lane & 15, g = lane >> 4;
    const rsrc_t rw = mk_rsrc(p.ws, WS_USED);
    const unsigned lbase = (unsigned)(size_t)lds + w * 10240;
    P1Src src;
    u32x4 xa[4][20];
    src.set(p, u0, 0, w, lane);
    if (w == 0) { wait_ctr(p, rw, C_X + chunk * 64, 8); wait_ctr(p, rw, C_X + chunk * 64 + 32, 8); }
    bar();
#pragma unroll
    for (int i = 0; i < 10; ++i) src.piece(i, lbase);
#pragma unroll
    for (int ks = 0; ks < 20; ++ks) {
        if (ks == 10) {
            src.set(p, u0, 1, w, lane);
#pragma unroll
            for (int i = 0; i < 10; ++i) src.piece(i, lbase + 40960);
        }
#pragma unroll
        for (int t = 0; t < 4; ++t) {
            const unsigned so = OFF_XF + ((chunk * 16 + w * 4 + t) * 20 + ks) * 1024;
            if (ks < XA_ACC) ld16_a(xa[t][ks], rw, lane * 16, so); else ld16_v(xa[t][ks], rw, lane * 16, so);
        }
        SB();
    }
    const int q = c >> 2, pp = c & 3, sw = (q >> 1) | ((g & 1) << 1);
    unsigned tb[4];
#pragma unroll
    for (int nt = 0; nt < 4; ++nt) tb[nt] = (8 * g + q) * 128 + 32 * (nt ^ sw) + 16 * (pp >> 1) + 8 * (pp & 1);
    int buf = 0;
    unsigned pt[5] = {0, 0, 0, 0, mt32()};
    for (int gi = 0; gi < nu; ++gi) {
        const int u = u0 + gi, un = gi + 1 < nu ? u + 1 : 219;
        if (u < 128 && (u & 2)) p1_group<true>(p, lds, rw, lbase, tb, xa, src, buf, chunk, u, un, gi == 0, w, lane, pt);
        else p1_group<false>(p, lds, rw, lbase, tb, xa, src, buf, chunk, u, un, gi == 0, w, lane, pt);
        buf = buf == 0 ? 2 : buf - 1;
    }
    wait_vm<0>();
    bar();
    const unsigned tid = fresh_tid();
    if (tid < (unsigned)nu) {
        const int u = u0 + tid;
        add_ctr(p, u < 128 ? C_KV + (chunk * 32 + (u >> 2)) * 16 : (u < 192 ? C_Q + (chunk * 32 + ((u - 128) >> 1)) * 16 : C_ACT + chunk * 32), 1u);
    }
}

#define KST(i)
#define AST_(i)
template <bool ISV>
DI void kv0_task(const Params& p, LAS uchar* lds, int u, u32x2 tag) {
    const int lane = fresh_lane(), w = __builtin_amdgcn_readfirstlane(threadIdx.x >> 6), c = lane & 15, g = lane >> 4;
    const rsrc_t rw = mk_rsrc(p.ws, WS_USED);
    const unsigned lbase = (unsigned)(size_t)lds + w * 10240;
    P1Src src;
    u32x4 xa[2][2][20];
    auto fetch = [&](int part, int i) {
        const unsigned so = OFF_XF + ((part * 8 + w * 2 + (i & 1)) * 20 + (i >> 1)) * 1024;
        if (part & 1) ld16_a(xa[1][i & 1][i >> 1], rw, lane * 16, so); else ld16_v(xa[0][i & 1][i >> 1], rw, lane * 16, so);
    };
#pragma unroll
    for (int kh = 0; kh < 2; ++kh) {
        src.set(p, u, kh, w, lane);
#pragma unroll
        for (int i = 0; i < 10; ++i) src.piece(i, lbase + kh * 40960);
    }
    KST(8);
    KST(9);
    if (w == 0) {
        wait_boot(rw, tag, boot_words(rw));
        if (fresh_tid() == 0) add_ctr(p, C_ACK, 1u);
        wait_ctr(p, rw, C_X, 8);
    }
    bar();
    KST(10);
#pragma unroll
    for (int i = 0; i < 40; ++i) fetch(0, i);
    const int q = c >> 2, pp = c & 3, sw = (q >> 1) | ((g & 1) << 1), h = u >> 2;
    unsigned tb[4];
#pragma unroll
    for (int nt = 0; nt < 4; ++nt) tb[nt] = (8 * g + q) * 128 + 32 * (nt ^ sw) + 16 * (pp >> 1) + 8 * (pp & 1);
    auto rd = [&](int i) {
        const int ks = i >> 2, nt = i & 3; LAS uchar* sb = lds + (ks / 10) * 40960 + (ks % 10) * 4096;
        const u32x2 lo = trd(sb + tb[nt]), hi = trd(sb + tb[nt ^ 2] + 512); return (u32x4){lo.x, lo.y, hi.x, hi.y};
    };
    static_for<4>([&](auto pc) {
        constexpr int part = pc.value, b = part & 1;
        f32x4 acc[2][4];
#pragma unroll
        for (int i = 0; i < 8; ++i) { acc[i >> 2][i & 3] = (f32x4){0.f, 0.f, 0.f, 0.f}; asm volatile("" : "+v"(acc[i >> 2][i & 3])); }
        if (part < 2) KST(part * 4 + 0);
        if (part < 3 && w == 0) wait_ctr(p, rw, C_X + 32 * (part + 1), 8);
        wait_vm<0>();
#pragma unroll
        for (int ks = 0; ks < 20; ++ks) { if (b) asm volatile("" : "+a"(xa[b][0][ks]), "+a"(xa[b][1][ks])); else asm volatile("" : "+v"(xa[b][0][ks]), "+v"(xa[b][1][ks])); }
        bar();
        if (part > 0 && fresh_tid() == 0) add_ctr(p, (b ? C_KVH : C_KV) + (((part - 1) >> 1) * 32 + h) * 16, 1u);
        if (part < 2) KST(part * 4 + 1);
        u32x4 wf[8];
#pragma unroll
        for (int i = 0; i < 8; ++i) wf[i] = rd(i);
        SB();
#pragma unroll
        for (int i = 0; i < 80; ++i) {
#pragma unroll
            for (int t = 0; t < 2; ++t) {
                if (ISV) { if (b) mfma_av(acc[t][i & 3], xa[b][t][i >> 2], wf[i & 7]); else mfma_v(acc[t][i & 3], xa[b][t][i >> 2], wf[i & 7]); }
                else { if (b) mfma_va(acc[t][i & 3], wf[i & 7], xa[b][t][i >> 2]); else mfma_v(acc[t][i & 3], wf[i & 7], xa[b][t][i >> 2]); }
            }
            if (i + 8 < 80) wf[i & 7] = rd(i + 8);
            if (part < 3 && i < 40) fetch(part + 1, i);
            SB();
        }
        asm volatile("s_nop 7" : "+v"(acc[0][0]), "+v"(acc[0][1]), "+v"(acc[0][2]), "+v"(acc[0][3]), "+v"(acc[1][0]), "+v"(acc[1][1]), "+v"(acc[1][2]), "+v"(acc[1][3]));
        if (part < 2) KST(part * 4 + 2);
        const unsigned ob = (ISV ? OFF_VF : OFF_KF) + ((part * 4 + w) * 32 + h) * 8192;
#pragma unroll
        for (int i = 0; i < 4; ++i) {
            const f32x4 a = ISV ? acc[0][i] : acc[i >> 1][2 * (i & 1)], bb = ISV ? acc[1][i] : acc[i >> 1][2 * (i & 1) + 1];
            const u32x4 o = {pk2(a[0], a[1]), pk2(a[2], a[3]), pk2(bb[0], bb[1]), pk2(bb[2], bb[3])};
            st16(o, rw, lane * 16, ob + (ISV ? (u & 1) * 4 + i : (i >> 1) * 4 + (u & 1) * 2 + (i & 1)) * 1024);
        }
        if (part < 2) KST(part * 4 + 3);
        SB();
    });
    wait_vm<0>();
    bar();
    if (fresh_tid() == 0) add_ctr(p, C_KV + (32 + h) * 16, 1u);
}

template <bool WO>
DI void gemm_task(const Params& p, LAS uchar* lds, int chunk, int cg) {
    constexpr int KS = WO ? 4 : 3, NS = WO ? 32 : 18, NP = 2 * KS, IMG = KS * 4096, NKF = KS * NS, XOFF = WO ? OFF_OT : OFF_ACT;
    const int lane = fresh_lane(), w = __builtin_amdgcn_readfirstlane(threadIdx.x >> 6), c = lane & 15, g = lane >> 4;
    const rsrc_t rw = mk_rsrc(p.ws, WS_USED), r2 = mk_rsrc(WO ? p.wo : p.w2, NKF * 32 * 1280);
    const unsigned lbase = (unsigned)(size_t)lds + (w >> 1) * IMG + (w & 1) * NP * 1024;
    const int rl = lane >> 3, f0 = ((rl >> 1) & 1) | (((rl >> 2) & 1) << 1);
    const unsigned v0 = rl * 1280 + (((lane & 7) ^ (2 * f0)) << 4), v1 = v0 ^ 64;
    const unsigned wbase = (w & 1) * NP * 8 * 1280 + cg * 256 + (w >> 1) * 128;
    auto piece = [&](int sl, int buf, int i) { dma16(r2, (i & 1) ? v1 : v0, wbase + (sl * KS * 32 + 8 * i) * 1280, lbase + buf * 2 * IMG + i * 1024); };
    const int tt0 = chunk * 16 + w * 4;
    u32x4 xb[2][KS][4];
    auto frag = [&](int par, int sl, int i) { ld16_s(xb[par][i >> 2][i & 3], rw, lane * 16, XOFF + ((tt0 + (i & 3)) * NKF + sl * KS + (i >> 2)) * 1024); };
#pragma unroll
    for (int i = 0; i < NP; ++i) piece(0, 0, i);
    if (w == 0) {
        if (WO) {
            wait_ctr(p, rw, C_G + (chunk * 5 + cg) * 32, 1);
            for (int i = 0; i < 8; ++i) wait_ctr(p, rw, C_OT + (chunk * 8 + i) * 32, 32);
        } else wait_ctr(p, rw, C_ACT + chunk * 32, 27);
    }
    bar();
#pragma unroll
    for (int i = 0; i < 4 * KS; ++i) frag(0, 0, i);
#pragma unroll
    for (int i = 0; i < NP; ++i) piece(1, 1, i);
    const int q = c >> 2, pp = c & 3, sw = (q >> 1) | (((g & 1) ^ (g >> 1)) << 1);
    unsigned tb[4];
#pragma unroll
    for (int nt = 0; nt < 4; ++nt) tb[nt] = (4 * g + q) * 128 + 32 * (nt ^ sw) + 16 * (pp >> 1) + 8 * (pp & 1);
    f32x4 acc[4][8];
#pragma unroll
    for (int t = 0; t < 4; ++t)
#pragma unroll
        for (int nt = 0; nt < 8; ++nt) { acc[t][nt] = (f32x4){0.f, 0.f, 0.f, 0.f}; asm volatile("" : "+a"(acc[t][nt])); }
    int buf = 0;
    for (int s2 = 0; s2 < NS / 2; ++s2) {
#pragma unroll
        for (int par = 0; par < 2; ++par) {
            const int sl = 2 * s2 + par;
            wait_vm<NP>();
#pragma unroll
            for (int kk = 0; kk < KS; ++kk) asm volatile("" : "+v"(xb[par][kk][0]), "+v"(xb[par][kk][1]), "+v"(xb[par][kk][2]), "+v"(xb[par][kk][3]));
            bar();
            const int sn = min(sl + 1, NS - 1), sp = min(sl + 2, NS - 1), bn = buf == 0 ? 2 : buf - 1;
            LAS uchar* sb = lds + buf * 2 * IMG;
            auto rd = [&](int i) { LAS uchar* a = sb + ((i >> 2) & 1) * IMG + tb[i & 3] + (i >> 3) * 4096; const u32x2 lo = trd(a), hi = trd(a + 2048); return (u32x4){lo.x, lo.y, hi.x, hi.y}; };
            u32x4 wf[4];
#pragma unroll
            for (int i = 0; i < 4; ++i) wf[i] = rd(i);
            SB();
#pragma unroll
            for (int i = 0; i < 8 * KS; ++i) {
                const u32x4 cur = wf[i & 3];
                mfma_cacc(acc[0][i & 7], cur, xb[par][i >> 3][0]);
                if (i < 4 * KS) frag(par ^ 1, sn, i); else if (i < 4 * KS + NP) piece(sp, bn, i - 4 * KS);
#pragma unroll
                for (int t = 1; t < 4; ++t) mfma_cacc(acc[t][i & 7], cur, xb[par][i >> 3][t]);
                if (i + 4 < 8 * KS) wf[i & 3] = rd(i + 4);
                SB();
            }
            if (par) asm volatile("s_nop 7");
            SB();
            buf = buf == 2 ? 0 : buf + 1;
        }
    }
    wait_vm<0>();
#pragma unroll
    for (int kk = 0; kk < KS; ++kk) asm volatile("" :: "v"(xb[0][kk][0]), "v"(xb[0][kk][1]), "v"(xb[0][kk][2]), "v"(xb[0][kk][3]));
    SB();
#pragma unroll
    for (int t = 0; t < 4; ++t) {
        if (WO) {
            const size_t xo = (size_t)((tt0 + t) * 16 + c) * 1280 + cg * 256 + g * 8;
            u32x4 gv[4]; u32x2 xv[8];
#pragma unroll
            for (int m = 0; m < 4; ++m) gv[m] = ld16(rw, lane * 16, OFF_GF + ((tt0 + t) * 20 + cg * 4 + m) * 1024);
#pragma unroll
            for (int nt = 0; nt < 8; ++nt) xv[nt] = *(const u32x2*)(p.x + xo + nt * 32);
#pragma unroll
            for (int nt = 0; nt < 8; ++nt) {
                const f32x4 a = acc[t][nt];
                const unsigned g0 = gv[nt >> 1][2 * (nt & 1)], g1 = gv[nt >> 1][2 * (nt & 1) + 1], s0 = pk2(a[0], a[1]), s1 = pk2(a[2], a[3]);
                const unsigned m0 = pk2(bf_lo(s0) * bf_lo(g0), bf_hi(s0) * bf_hi(g0)), m1 = pk2(bf_lo(s1) * bf_lo(g1), bf_hi(s1) * bf_hi(g1));
                *(u32x2*)(p.out + xo + nt * 32) = (u32x2){pk2(bf_lo(xv[nt].x) + bf_lo(m0), bf_hi(xv[nt].x) + bf_hi(m0)), pk2(bf_lo(xv[nt].y) + bf_lo(m1), bf_hi(xv[nt].y) + bf_hi(m1))};
            }
        } else {
#pragma unroll
            for (int m = 0; m < 4; ++m) {
                float y[8];
#pragma unroll
                for (int r = 0; r < 8; ++r) { const float a = rbf(acc[t][2 * m + (r >> 2)][r & 3]); y[r] = a * __builtin_amdgcn_rcpf(1.f + __builtin_amdgcn_exp2f(-a * 1.44269504f)); }
                const u32x4 o = {pk2(y[0], y[1]), pk2(y[2], y[3]), pk2(y[4], y[5]), pk2(y[6], y[7])};
                st16(o, rw, lane * 16, OFF_GF + ((tt0 + t) * 20 + cg * 4 + m) * 1024);
            }
        }
        SB();
    }
    wait_vm<0>();
    bar();
    if (!WO && fresh_tid() == 0) add_ctr(p, C_G + (chunk * 5 + cg) * 32, 1u);
}

#define XST(cond, idx)
constexpr float SCL = 0.08838834764831845f * 1.4426950408889634f;
DI void attn_task(const Params& p, int s, int h, bool new_chunk) {
    const int lane = fresh_lane(), c = lane & 15, g = lane >> 4;
    const rsrc_t rw = mk_rsrc(p.ws, WS_USED);
    const int sh = (s * 32 + h);
    u32x4 qf[2][4], kf[2][4], vf[8], km[4]; u32x2 vm[8];
    if (new_chunk) {
        wait_ctr(p, rw, C_Q + ((s >> 3) * 32 + h) * 16, 2);
        wait_ctr(p, rw, C_KV + ((s >> 3) * 32 + h) * 16, 4);
    }
#pragma unroll
    for (int i = 0; i < 8; ++i) qf[i >> 2][i & 3] = ld16(rw, lane * 16, OFF_QF + sh * 8192 + i * 1024);
#pragma unroll
    for (int i = 0; i < 8; ++i) kf[i >> 2][i & 3] = ld16(rw, lane * 16, OFF_KF + sh * 8192 + i * 1024);
#pragma unroll
    for (int i = 0; i < 8; ++i) vf[i] = ld16(rw, lane * 16, OFF_VF + sh * 8192 + i * 1024);
    const float NINF = -__builtin_inff();
    const f32x4 zero = {0.f, 0.f, 0.f, 0.f};
    float t[2][12], m[2];
    {
        f32x4 s0[2] = {zero, zero}, s1 = zero;
#pragma unroll
        for (int ks = 0; ks < 4; ++ks) { s0[0] = mfma32(kf[0][ks], qf[0][ks], s0[0]); s0[1] = mfma32(kf[0][ks], qf[1][ks], s0[1]); s1 = mfma32(kf[1][ks], qf[1][ks], s1); }
#pragma unroll
        for (int r = 0; r < 4; ++r) {
            const bool diag = 4 * g + r <= c;
            t[0][r] = diag ? s0[0][r] * SCL : NINF; t[1][r] = s0[1][r] * SCL; t[1][4 + r] = diag ? s1[r] * SCL : NINF;
        }
        m[0] = fmaxf(fmaxf(t[0][0], t[0][1]), fmaxf(t[0][2], t[0][3]));
        m[1] = fmaxf(fmaxf(fmaxf(t[1][0], t[1][1]), fmaxf(t[1][2], t[1][3])), fmaxf(fmaxf(t[1][4], t[1][5]), fmaxf(t[1][6], t[1][7])));
#pragma unroll
        for (int r = 0; r < 4; ++r) asm volatile("" : "+v"(t[0][r]), "+v"(t[1][r]), "+v"(t[1][4 + r]));
        asm volatile("" : "+v"(m[0]), "+v"(m[1]));
#pragma unroll
        for (int i = 0; i < 8; ++i) asm volatile("" : "+v"(vf[i]));
    }
    u32x4 one4 = {0x3F803F80u, 0x3F803F80u, 0x3F803F80u, 0x3F803F80u};
    const float z2 = g >= 2 ? 0.f : NINF;
    f32x4 s2[2] = {{z2, z2, z2, z2}, {z2, z2, z2, z2}};
    asm volatile("" : "+v"(s2[0]), "+v"(s2[1]));
    XST(s == 63, 64 + h * 4);
    wait_ctr(p, rw, C_KM + h * 32 + (s == NSEG - 1), s + 1);
    XST(s == 63, 64 + h * 4 + 1);
    for (unsigned n = SPIN_CAP;;) {
        asm volatile("" ::: "memory");
#pragma unroll
        for (int i = 0; i < 4; ++i) km[i] = ld16(rw, (g * 8 + (c & 7)) * 16, OFF_KM + sh * 4096 + i * 512);
#pragma unroll
        for (int i = 0; i < 8; ++i) vm[i] = ld8(rw, (lane & 31) * 8, OFF_KM + sh * 4096 + 2048 + i * 256);
        if (s < NSEG - 1) break;
        unsigned mx = 0;
#pragma unroll
        for (int i = 0; i < 4; ++i) mx = mx4(mx, km[i]);
#pragma unroll
        for (int i = 0; i < 8; ++i) mx = max(mx, max(vm[i].x, vm[i].y));
        if (__builtin_amdgcn_ballot_w64(mx == SENT) == 0) break;
        if (--n == 0) { set_tmo(p); break; }
    }
    SB();
    SB();
#pragma unroll
    for (int ks = 0; ks < 4; ++ks)
#pragma unroll
        for (int qt = 0; qt < 2; ++qt) mfma_v(s2[qt], km[ks], qf[qt][ks]);
    asm volatile("s_nop 7" : "+v"(s2[0]), "+v"(s2[1]));
    u32x4 p01[2], p2[2];
#pragma unroll
    for (int qt = 0; qt < 2; ++qt) {
#pragma unroll
        for (int r = 0; r < 4; ++r) t[qt][8 + r] = s2[qt][r] * SCL;
        m[qt] = fmaxf(fmaxf(m[qt], fmaxf(t[qt][8], t[qt][9])), fmaxf(t[qt][10], t[qt][11]));
    }
#pragma unroll
    for (int qt = 0; qt < 2; ++qt) m[qt] = grp_max(m[qt]);
#pragma unroll
    for (int qt = 0; qt < 2; ++qt) {
#pragma unroll
        for (int r = 0; r < 12; ++r) if (qt == 1 || r < 4 || r >= 8) t[qt][r] = __builtin_amdgcn_exp2f(t[qt][r] - m[qt]);
        p01[qt] = (u32x4){pk2(t[qt][0], t[qt][1]), pk2(t[qt][2], t[qt][3]), qt ? pk2(t[qt][4], t[qt][5]) : 0u, qt ? pk2(t[qt][6], t[qt][7]) : 0u};
        p2[qt] = (u32x4){pk2(t[qt][8], t[qt][9]), pk2(t[qt][10], t[qt][11]), 0u, 0u};
    }
    u32x4 vq[4];
#pragma unroll
    for (int i = 0; i < 4; ++i) vq[i] = (u32x4){vm[2 * i].x, vm[2 * i].y, vm[2 * i + 1].x, vm[2 * i + 1].y};
    u32x4 p2h[2] = {{0u, 0u, p2[0].x, p2[0].y}, {0u, 0u, p2[1].x, p2[1].y}};
    asm volatile("s_nop 1" : "+v"(p01[0]), "+v"(p01[1]), "+v"(p2[0]), "+v"(p2[1]), "+v"(p2h[0]), "+v"(p2h[1]), "+v"(vq[0]), "+v"(vq[1]), "+v"(vq[2]), "+v"(vq[3]), "+v"(one4));
    f32x4 l[2], o[2][8];
#pragma unroll
    for (int qt = 0; qt < 2; ++qt) mfma_0(l[qt], one4, p01[qt]);
#pragma unroll
    for (int qt = 0; qt < 2; ++qt) mfma_v(l[qt], one4, p2[qt]);
#pragma unroll
    for (int et = 0; et < 8; ++et)
#pragma unroll
        for (int qt = 0; qt < 2; ++qt) { mfma_0(o[qt][et], vf[et], p01[qt]); mfma_v(o[qt][et], vq[et >> 1], et & 1 ? p2h[qt] : p2[qt]); }
    asm volatile("s_nop 3" : "+v"(l[0]), "+v"(l[1]), "+v"(o[0][0]), "+v"(o[0][1]), "+v"(o[1][0]), "+v"(o[1][1]));
    const float inv[2] = {__builtin_amdgcn_rcpf(l[0][0]), __builtin_amdgcn_rcpf(l[1][0])};
#pragma unroll
    for (int tp = 0; tp < 4; ++tp) {
        asm volatile("" : "+v"(o[0][2 * tp]), "+v"(o[0][2 * tp + 1]), "+v"(o[1][2 * tp]), "+v"(o[1][2 * tp + 1]));
#pragma unroll
        for (int qt = 0; qt < 2; ++qt) {
            const f32x4 a = o[qt][2 * tp] * inv[qt], b = o[qt][2 * tp + 1] * inv[qt];
            const u32x4 ov = {nosent(pk2(a[0], a[1])), nosent(pk2(a[2], a[3])), nosent(pk2(b[0], b[1])), nosent(pk2(b[2], b[3]))};
            st16(ov, rw, lane * 16, OFF_OT + ((2 * s + qt) * 32 + h) * 4096 + tp * 1024);
        }
        SB();
    }
    if (s == NSEG - 1) st4(1u, rw, lane == 0 ? (C_OF + (h >> 3) * 32 + (h & 7)) * 4 : OOB, 0);
    XST(s == 63, 64 + h * 4 + 2);
    wait_vm<0>();
    XST(s == 63, 64 + h * 4 + 3);
    if (lane == 0) add_ctr(p, C_OT + s * 32, 1u);
}

constexpr int N_T = 24, N_WG = 4, W_FIRST = 64;
DI void wo_stage(const Params& p, unsigned ldsw, int h0, int cs, int lane) {
    const rsrc_t ro = mk_rsrc(p.wo, 4096 * 1280);
    const unsigned v = (lane >> 2) * 1280 + (lane & 3) * 16;
    const unsigned so = uni(h0 * 128 * 1280 + cs * 64);
    ldsw = uni(ldsw);
#pragma unroll
    for (int i = 0; i < 16; ++i) dma16(ro, v, so + 16 * i * 1280, ldsw + i * 1024);
}
DI void w_role(const Params& p, LAS uchar* lds, int cs, int rank) {
    const int lane = fresh_lane(), w = __builtin_amdgcn_readfirstlane(threadIdx.x >> 6), c = lane & 15, g = lane >> 4;
    const rsrc_t rw = mk_rsrc(p.ws, WS_USED);
    u32x4 wf[8][4][2];
    {
        LAS uchar* lw = lds + w * 32768;
        const int q = c >> 2, pp = c & 3;
        wo_stage(p, (unsigned)(size_t)lw, 8 * w, cs, lane);
        wo_stage(p, (unsigned)(size_t)lw + 16384, 8 * w + 2, cs, lane);
#pragma unroll
        for (int rd = 0; rd < 4; ++rd) {
            if (rd < 3) wait_vm<16>(); else wait_vm<0>();
#pragma unroll
            for (int i = 0; i < 2; ++i)
#pragma unroll
                for (int tp = 0; tp < 4; ++tp)
#pragma unroll
                    for (int nt = 0; nt < 2; ++nt) {
                        LAS uchar* a = lw + (rd & 1) * 16384 + i * 8192 + (32 * tp + 4 * g + q) * 64 + nt * 32 + pp * 8;
                        const u32x2 lo = trd(a), hi = trd(a + 1024);
                        wf[2 * rd + i][tp][nt] = (u32x4){lo.x, lo.y, hi.x, hi.y};
                    }
            SB();
#pragma unroll
            for (int i = 0; i < 16; ++i) { if (2 * rd + (i >> 3) < 7) asm volatile("" : "+a"(wf[2 * rd + (i >> 3)][(i >> 1) & 3][i & 1])); else asm volatile("" : "+v"(wf[2 * rd + (i >> 3)][(i >> 1) & 3][i & 1])); }
            asm volatile("s_waitcnt lgkmcnt(0)" ::: "memory");
            SB();
            if (rd < 2) wo_stage(p, (unsigned)(size_t)lw + (rd & 1) * 16384, 8 * w + 2 * rd + 4, cs, lane);
        }
    }
    bar();
    LAS f32x2* red = (LAS f32x2*)lds;
    volatile LAS int* nxt = (volatile LAS int*)(lds + 16384);
    const rsrc_t rx = mk_rsrc(p.x, S_ * 1280), ro = mk_rsrc(p.out, S_ * 1280);
    unsigned tk = 0;
    const unsigned wto = lane == 0 ? (C_WT + cs * 32) * 4 : OOB;
    auto draw = [&]() { tk = 1; asm volatile("buffer_atomic_add %0, %1, %2, 0 offen sc0" : "+v"(tk) : "v"(wto), "s"(rw) : "memory"); };
    auto tile_of = [&](int ticket, int cur) { return ticket + W_FIRST < 120 ? ticket + W_FIRST : (cur < 120 ? 120 + rank : (cur < 124 ? (rank < 2 ? 126 : 122) + rank : 128)); };
    u32x4 ob[8][4];
    int tt = 0;
    bool have = false, careful = false;
    if (w == 0) { draw(); asm volatile("s_waitcnt vmcnt(0)" : "+v"(tk)); *nxt = tile_of(__builtin_amdgcn_readfirstlane(tk), 0); draw(); }
    bar();
    tt = __builtin_amdgcn_readfirstlane(*nxt);
    while (tt < 2 * NSEG) {
        if (!have) {
            if (w == 0) { wait_ctr(p, rw, C_G + ((tt >> 4) * 5 + (cs >> 2)) * 32, 1); if (tt < 126 || careful) wait_ctr(p, rw, C_OT + (tt >> 1) * 32, 32); }
            bar();
            if (tt >= 126 && !careful) {
                for (unsigned n = SPIN_CAP;;) {
                    asm volatile("" ::: "memory");
                    if (__builtin_amdgcn_ballot_w64(ld4(rw, (lane & 7) * 4, (C_OF + w * 32) * 4) == 0) == 0) break;
                    if (--n == 0) { set_tmo(p); break; }
                }
            }
            XST(w == 0 && tt >= 126, 256 + ((tt - 126) * 20 + cs) * 4);
#pragma unroll
            for (int i = 0; i < 32; ++i) ld16_s(ob[i >> 2][i & 3], rw, lane * 16, OFF_OT + (tt * 32 + 8 * w) * 4096 + i * 1024);
        }
        f32x4 acc[2][2];
#pragma unroll
        for (int i = 0; i < 4; ++i) { acc[i >> 1][i & 1] = (f32x4){0.f, 0.f, 0.f, 0.f}; asm volatile("" : "+v"(acc[i >> 1][i & 1])); }
        const unsigned xo = c * 1280 + g * 8, xs = tt * 16 * 1280 + cs * 64 + (w >> 1) * 32 + (w & 1) * 4;
        unsigned xv, gv;
        asm volatile("buffer_load_dword %0, %1, %2, %3 offen" : "=v"(xv) : "v"(xo), "s"(rx), "s"(xs) : "memory");
        asm volatile("buffer_load_dword %0, %1, %2, %3 offen sc1" : "=v"(gv) : "v"(lane * 16), "s"(rw), "s"(OFF_GF + (tt * 20 + cs) * 1024 + w * 4) : "memory");
        if (w == 0) {
            asm volatile("s_waitcnt vmcnt(32)" : "+v"(tk));
            const int tn = tile_of(__builtin_amdgcn_readfirstlane(tk), tt);
            int rd = 0;
            if (tn < 126) {
                const unsigned f = ld4(rw, (lane == 0 ? C_G + ((tn >> 4) * 5 + (cs >> 2)) * 32 : C_OT + (tn >> 1) * 32) * 4, 0);
                rd = __builtin_amdgcn_ballot_w64(f < (lane == 0 ? 1u : 32u)) == 0 ? 0x10000 : 0;
            }
            nxt[1] = tn | rd;
        }
        bar();
        XST(w == 0 && tt >= 126, 256 + ((tt - 126) * 20 + cs) * 4 + 1);
        const int nx = __builtin_amdgcn_readfirstlane(nxt[1]);
        have = (nx >> 16) != 0;
        const rsrc_t rn = mk_rsrc(p.ws, have ? WS_USED : 0);
        const unsigned nb = OFF_OT + ((nx & 127) * 32 + 8 * w) * 4096;
        if (w == 0) draw();
#pragma unroll
        for (int i = 0; i < 8; ++i) {
            asm volatile("s_waitcnt vmcnt(28)" : "+v"(ob[i][0]), "+v"(ob[i][1]), "+v"(ob[i][2]), "+v"(ob[i][3]));
#pragma unroll
            for (int tp = 0; tp < 4; ++tp)
#pragma unroll
                for (int n = 0; n < 2; ++n) { if (i < 7) mfma_av(acc[tp & 1][n], wf[i][tp][n], ob[i][tp]); else mfma_v(acc[tp & 1][n], wf[i][tp][n], ob[i][tp]); }
#pragma unroll
            for (int tp = 0; tp < 4; ++tp) ld16_s(ob[i][tp], rn, lane * 16, nb + (i * 4 + tp) * 1024);
            SB();
        }
        mfma_pad(acc[0][0], acc[0][1], acc[1][0], acc[1][1]);
#pragma unroll
        for (int n = 0; n < 2; ++n) {
            const f32x4 a = acc[0][n] + acc[1][n];
            red[((2 * n) * 4 + w) * 64 + lane] = (f32x2){a[0], a[1]};
            red[((2 * n + 1) * 4 + w) * 64 + lane] = (f32x2){a[2], a[3]};
        }
        bar();
        const f32x2 r = (red[(w * 4 + 0) * 64 + lane] + red[(w * 4 + 1) * 64 + lane]) + (red[(w * 4 + 2) * 64 + lane] + red[(w * 4 + 3) * 64 + lane]);
        asm volatile("s_waitcnt vmcnt(32)" : "+v"(xv), "+v"(gv));
        const bool redo = tt >= 126 && !careful && __builtin_amdgcn_ballot_w64(r[0] != r[0]) != 0;
        const unsigned sv = pk2(r[0], r[1]), m = pk2(bf_lo(sv) * bf_lo(gv), bf_hi(sv) * bf_hi(gv));
        __builtin_amdgcn_raw_buffer_store_b32(pk2(bf_lo(xv) + bf_lo(m), bf_hi(xv) + bf_hi(m)), ro, xo, xs, 0);
        XST(w == 0 && tt >= 126, 256 + ((tt - 126) * 20 + cs) * 4 + 2);
        if (!have) {
            wait_vm<0>();
#pragma unroll
            for (int i = 0; i < 8; ++i) asm volatile("" : "+v"(ob[i][0]), "+v"(ob[i][1]), "+v"(ob[i][2]), "+v"(ob[i][3]));
        }
        careful = redo;
        if (!redo) tt = nx & 0xFFFF;
    }
}

DI void smf_x(f32x4& c, u32x4 a, u32x8 b, int idx, bool acc) { if (acc) smf_a(c, a, b, idx); else smf_v(c, a, b, idx); }
#define TRY()
#define V_A 80
#define V_M 52
#define KM_DELAY 240
#define PAD0 0
#define PAD_UP 0
#define PAD_MAX 48
#define RISE 64
#define DECAY 8
#ifndef M_DELAY
#define M_DELAY 0
#endif
#define L_SMALL 96
#define F_SMALL 280
#ifndef M_TAIL
#define M_TAIL 40
#endif
#define D_MST 300
#define F_LATE 1000
#define V_LATE 8
#define DUR_A0 980
#define DUR_M0 700
#define FT(i)
#define FDUMP(blk, s, w)
#define STAT(blk, s, w, a, b, c, d)
DI int wave_max(int d) {
    d = max(d, __builtin_amdgcn_update_dpp(0, d, 0x121, 0xf, 0xf, true));
    d = max(d, __builtin_amdgcn_update_dpp(0, d, 0x122, 0xf, 0xf, true));
    d = max(d, __builtin_amdgcn_update_dpp(0, d, 0x124, 0xf, 0xf, true));
    d = max(d, __builtin_amdgcn_update_dpp(0, d, 0x128, 0xf, 0xf, true));
    auto r = __builtin_amdgcn_permlane32_swap((unsigned)d, (unsigned)d, false, false); d = max((int)r[0], (int)r[1]);
    auto q = __builtin_amdgcn_permlane16_swap((unsigned)d, (unsigned)d, false, false); d = max((int)q[0], (int)q[1]);
    return __builtin_amdgcn_readfirstlane(d);
}
struct Clock {
    unsigned r0, m0, cq, qc;
    DI void edge(unsigned& r, unsigned& m) {
        unsigned long long a, b;
        const unsigned first = rt32();
        do asm volatile("s_memrealtime %0\n\ts_memtime %1\n\ts_waitcnt lgkmcnt(0)" : "=s"(a), "=s"(b) :: "memory"); while ((unsigned)a == first);
        r = (unsigned)a << 3; m = (unsigned)b - 24;
    }
    DI void start() { edge(r0, m0); cq = 3000; qc = 22370; }
    DI void sync() {
        unsigned r, m; edge(r, m);
        const float f = (float)(m - m0) * __builtin_amdgcn_rcpf((float)(r - r0));
        asm volatile("" : "+s"(r), "+s"(m));
        cq = __builtin_amdgcn_readfirstlane((unsigned)(f * 1024.f));
        qc = __builtin_amdgcn_readfirstlane((unsigned)(65536.f * __builtin_amdgcn_rcpf(f)));
        r0 = r; m0 = m;
    }
    DI unsigned at(unsigned m) const { return r0 + (unsigned)(((unsigned long long)(m - m0) * qc) >> 16); }
    DI static void sample(unsigned long long& a, unsigned long long& b) { asm volatile("s_memrealtime %0\n\ts_memtime %1\n\ts_waitcnt lgkmcnt(0)" : "=s"(a), "=s"(b) :: "memory"); }
    DI unsigned value(unsigned long long a, unsigned long long b) const {
        const unsigned lo = (unsigned)a << 3;
        return lo + __builtin_amdgcn_readfirstlane(min(max((int)(at((unsigned)b) - lo), 0), 31));
    }
    DI unsigned now() const { unsigned long long a, b; sample(a, b); return value(a, b); }
    DI unsigned cycles(unsigned t) const { const int d = __builtin_amdgcn_readfirstlane(min(max((int)(t - r0), 0), 1 << 20)); return m0 + (((unsigned)d * cq) >> 10) - 26; }
};
DI unsigned waitmt(unsigned t) { unsigned n; do n = mt32(); while ((int)(n - t) < 0); return n; }
#define MST_SH 2
DI unsigned mst_off(int m) { return (m >> MST_SH) * 128 + (m & ((1 << MST_SH) - 1)) * 4; }
struct Sched {
    unsigned tgo, tau, durA, durM, pad;
    unsigned a, v;
    DI void fetch_a(rsrc_t rw, unsigned exo, unsigned pol, int s, int lane) { a = ld4(rw, s >= 2 ? (lane & 31) * 4 : OOB, ex_step(exo, pol, max(s - 2, 0)) + EX_AST); }
    DI void fetch_m(rsrc_t rw, unsigned exo, unsigned pol, int s, int lane) { v = ld4(rw, mst_off(min(lane, 39)), ex_step(exo, pol, s - 1) + EX_MST); }
    DI void take_a() { const int m = wave_max((int)a); durA += __builtin_amdgcn_ballot_w64(a - 1u >= 0xFFFFu) == 0 ? (min(m - (int)durA, RISE) + 2) >> 2 : 0; }
    DI void next(const Params& p, rsrc_t rw, unsigned exo, unsigned pol, int s, int lane, unsigned long long& alive) {
        if ((__builtin_amdgcn_ballot_w64(v == SENT) & alive) != 0) {
            const unsigned mo = ex_step(exo, pol, s - 1) + EX_MST;
            unsigned n = SPIN_CAP; bool bad;
#pragma nounroll
            do {
                asm volatile("" ::: "memory");
                v = ld4(rw, mst_off(min(lane, 39)), mo);
                bad = __builtin_amdgcn_ballot_w64(v == SENT) != 0;
            } while (bad && --n);
            if (bad) { alive = 0; set_tmo(p); }
        }
        const unsigned ref = __builtin_amdgcn_readfirstlane(v);
        const unsigned pm = ref + (unsigned)wave_max((int)(v - ref));
        if (s >= 2 && __builtin_amdgcn_ballot_w64(v & 1) == 0) durM += (min((int)(pm - tau - durM), RISE) + 2) >> 2;
        const bool late = s == 1 || (int)(pm + V_LATE - tgo) > 0;
        pad = late || __builtin_amdgcn_ballot_w64(v & 1) != 0 ? min(pad + PAD_UP, (unsigned)PAD_MAX) : (pad > 4 ? pad - 4 : 0);
        tau = (late ? pm + F_LATE : tgo) + durA + pad;
        tgo = tau + durM + V_A + pad;
    }
};

#define PACE 8
DI void pace() { asm volatile("s_nop %0" :: "n"(PACE)); }
DI void role_M(const Params& p, LAS uchar* lds, int mi, bool started, u32x2 tag) {
    const int tid = threadIdx.x, lane = tid & 63, w = __builtin_amdgcn_readfirstlane(tid >> 6), c = lane & 15, g = lane >> 4;
    const int qh = mi & 1, cs = mi >> 1;
    const rsrc_t rw = mk_rsrc(p.ws, WS_USED);
    u32x8 wf[8][2][2];
    {
        LAS uchar* lw = lds + w * 32768;
        const int q = c >> 2, pp = c & 3;
        unsigned ra[4];
#pragma unroll
        for (int m = 0; m < 4; ++m) {
            const int k0 = m < 2 ? 8 * g + 4 * m : 32 + 8 * g + 4 * (m - 2), ga = k0 >> 4, qq = (k0 >> 2) & 3;
            const int e = 32 * ga + 2 * qq + (q & 1) + 8 * (q >> 1);
            ra[m] = e * 64 + pp * 8;
        }
        if (w == 0 || !started) {
            wo_stage(p, (unsigned)(size_t)lw, 8 * w, cs, lane);
            wo_stage(p, (unsigned)(size_t)lw + 16384, 8 * w + 2, cs, lane);
        }
#pragma unroll
        for (int rd = 0; rd < 4; ++rd) {
            if (rd < 3) wait_vm<16>(); else wait_vm<0>();
#pragma unroll
            for (int i = 0; i < 2; ++i)
#pragma unroll
                for (int sel = 0; sel < 2; ++sel)
#pragma unroll
                    for (int nt = 0; nt < 2; ++nt) {
                        LAS uchar* a = lw + (rd & 1) * 16384 + i * 8192 + sel * 1024 + nt * 32;
                        const u32x2 t0 = trd(a + ra[0]), t1 = trd(a + ra[1]), t2 = trd(a + ra[2]), t3 = trd(a + ra[3]);
                        wf[2 * rd + i][sel][nt] = (u32x8){t0.x, t0.y, t1.x, t1.y, t2.x, t2.y, t3.x, t3.y};
                    }
            SB();
#pragma unroll
            for (int i = 0; i < 8; ++i) { if (rd < 3) asm volatile("" : "+a"(wf[2 * rd + (i >> 2)][(i >> 1) & 1][i & 1])); else asm volatile("" : "+v"(wf[2 * rd + (i >> 2)][(i >> 1) & 1][i & 1])); }
            asm volatile("s_waitcnt lgkmcnt(0)" ::: "memory");
            SB();
            if (rd < 2) wo_stage(p, (unsigned)(size_t)lw + (rd & 1) * 16384, 8 * w + 2 * rd + 4, cs, lane);
        }
    }
    bar();
    if (tid < 2) *(LAS unsigned*)(lds + 4096 + tid * 4) = 0;
    bar();
    const int idx = (c & 4) ? 0xEEEE : 0x4444;
    const int dl = 32 * cs + 2 * c;
    const unsigned so = mf_off(dl >> 6) + (((dl >> 4) & 3) * 16 + qh * 8 + ((dl >> 3) & 1) * 4 + g) * 16 + (dl & 7) * 2;
    const unsigned rwo = (w * 128 + (2 * (g & 1)) * 32 + 16 * (g >> 1) + c) * 4, rro = (g * 32 + 2 * c) * 4;
    const unsigned lo16 = lane * 16, sto = lane == 0 ? mst_off(mi) : OOB;
    unsigned long long alive = ~0ull;
    unsigned budget = SPIN_CAP;
    const u32x2 bv = vote_words(rw);
    wait_boot(rw, tag, boot_words(rw));
    if (fresh_tid() == 0) add_ctr(p, C_ACK, 1u);
    const unsigned exo = ex_off(p), pol = read_pol(rw, tag, bv);
    wait_ctr(p, rw, C_PZ, 16);
    const unsigned obo = uni(w * 32768 + qh * 1024);
    Clock ck; ck.start();
    unsigned tdone = ck.now();
    Sched sc = {0u, tdone - V_M, DUR_A0, DUR_M0, PAD0, 0u, 0u};
    for (int s = 0; s < NSEG - 1; ++s) {
        const unsigned eb = ex_step(exo, pol, s);
        const unsigned ob = eb + obo;
        if (s == 3) wait_ctr(p, rw, C_PZ + 32, 56);
        if (s > 0) {
            sc.fetch_a(rw, exo, pol, s, lane);
            ck.sync();
            sc.take_a();
            SB();
            waitmt(ck.cycles(tdone + D_MST));
            sc.fetch_m(rw, exo, pol, s, lane);
            sc.next(p, rw, exo, pol, s, lane, alive);
        }
        const unsigned tau = sc.tau;
        unsigned obs[8], mb = ex_step(exo, pol, s + 1);
#pragma unroll
        for (int i = 0; i < 8; ++i) { obs[i] = ob + (i >> 2) * 16384 + (i & 3) * 2048; asm volatile("" : "+s"(obs[i])); }
        unsigned rwa = rwo + (s & 1) * 2048, rra = rro + (s & 1) * 2048;
        f32x4 acc[2][2];
#pragma unroll
        for (int i = 0; i < 4; ++i) { acc[i >> 1][i & 1] = (f32x4){0.f, 0.f, 0.f, 0.f}; asm volatile("" : "+v"(acc[i >> 1][i & 1])); }
        unsigned tw = ck.cycles(tau + V_M);
        asm volatile("" : "+s"(mb), "+v"(rwa), "+v"(rra), "+s"(tw));
        waitmt(tw);
        FT(0);
        u32x4 of[8];
#pragma unroll
        for (int i = 0; i < 8; ++i) { of[i] = ld16(rw, lo16, obs[i]); SB(); if (i < 7) pace(); SB(); }
        auto mix = [&]() {
#pragma unroll
            for (int i = 0; i < 8; ++i) {
                smf_x(acc[0][1], of[i], wf[i][0][1], idx, i < 6);
                smf_x(acc[1][0], of[i], wf[i][1][0], idx, i < 6);
                smf_x(acc[0][0], of[i], wf[i][0][0], idx, i < 6);
                smf_x(acc[1][1], of[i], wf[i][1][1], idx, i < 6);
                if (i == 0) FT(1);
                if (i == 7) FT(2);
                SB();
            }
            asm volatile("s_nop 2" : "+v"(acc[0][1]), "+v"(acc[1][0]));
            float u[4], v[4], y[4];
#pragma unroll
            for (int r = 0; r < 4; ++r) {
                const float fa = acc[1][0][r], fb = acc[0][1][r];
                auto q = __builtin_amdgcn_permlane32_swap(__builtin_bit_cast(unsigned, fa), __builtin_bit_cast(unsigned, fb), false, false);
                u[r] = __builtin_bit_cast(float, (unsigned)q[0]); v[r] = __builtin_bit_cast(float, (unsigned)q[1]);
            }
            SB();
            asm volatile("s_nop 0" : "+v"(acc[0][0]), "+v"(acc[1][1]));
#pragma unroll
            for (int r = 0; r < 4; ++r) { float lo = acc[0][0][r] + v[r], hi = acc[1][1][r] + u[r]; asm("" : "+v"(lo), "+v"(hi)); y[r] = g < 2 ? lo : hi; }
            const float z0 = swap16_add(y[0], y[2]), z1 = swap16_add(y[1], y[3]);
            *(LAS float*)(lds + rwa) = z0;
            *(LAS float*)(lds + rwa + 128) = z1;
            SB();
            return __builtin_amdgcn_ballot_w64(__builtin_isunordered(z0, z1));
        };
        if (__builtin_expect(mix() != 0, 0)) {
            if (refetch<8>(of, rw, lo16, obs, budget)) {
                TRY();
                *(LAS unsigned*)(lds + 4096 + (s & 1) * 4) = s + 1;
#pragma unroll
                for (int i = 0; i < 4; ++i) { acc[i >> 1][i & 1] = (f32x4){0.f, 0.f, 0.f, 0.f}; asm volatile("" : "+v"(acc[i >> 1][i & 1])); }
                asm volatile("s_nop 1" ::: "memory");
                mix();
            }
        }
        FT(3);
        bar();
        FT(4);
        if (__builtin_expect(w == 0, 1)) {
            LAS f32x2* r2 = (LAS f32x2*)(lds + rra);
            const f32x2 a0 = r2[0], a1 = r2[64], a2 = r2[128], a3 = r2[192];
            const unsigned fl = *(LAS unsigned*)(lds + 4096 + (s & 1) * 4);
            SB();
            unsigned long long ca, cb;
            Clock::sample(ca, cb);
            SB();
            const f32x2 v = (a0 + a1) + (a2 + a3);
            st4(nosent(pk2(v[0], v[1])), rw, so, mb);
            SB();
            tdone = ck.value(ca, cb) + M_TAIL;
            st4(nosent((tdone & ~1u) | (fl == (unsigned)(s + 1))), rw, sto, eb + EX_MST);
        } else tdone = ck.now() + M_TAIL;
        if (budget == 0) set_tmo(p);
        FT(5); FDUMP(32 + mi, s, w);
    }
}

constexpr int RING = 4, VPRE = 4;
constexpr int a_slot(int ks, int tile) { return ks * 16384 + tile * 2048; }
constexpr int a_xblk(int i) { return a_slot(i & 1 ? 9 : 4, i & 2 ? 5 : 2); }
constexpr int A_P = a_slot(4, 7), A_P2 = A_P + 1024, A_CNT = A_P2 + 512;
template <int W> struct AP {
    static constexpr int ROT = 3 * W, NT = W == 0 ? 0 : (W == 3 ? 2 : 3), T0 = W == 0 ? 0 : 3 * (W - 1);
    static constexpr int NL = W == 0 ? 0 : (W == 3 ? 9 : 14);
    static constexpr int QA = W == 0 || W == 3 ? 20 : 15;
    static constexpr bool k_lds(int t, int m) { return W == 0 ? false : (W == 3 ? (t == 1 && m >= 1) : (t == 1 || (m > 0 && (m & 1) == 0))); }
    static constexpr bool k_acc(int t, int m) { return W == 0 && m < 5; }
    static constexpr bool v_lds(int t, int ks) { return W == 3 ? ks < 4 : (ks < 4 || (ks == 4 && t == 0)); }
    static constexpr int k_idx(int t, int m) { int n = 0; for (int i = 0; i < 2 * m + 1 - t; ++i) n += k_lds(1 - (i & 1), i >> 1) ? 1 : 0; return n; }
    static constexpr int k_slot(int i) { return a_slot(5 + i / (NT ? NT : 1), T0 + i % (NT ? NT : 1)); }
    static constexpr int v_slot(int i) { return a_slot(i / (NT ? NT : 1), T0 + i % (NT ? NT : 1)); }
    static constexpr int stream(int i) { return i < NL ? k_slot(i) : v_slot(i - NL); }
    static constexpr int m_of(int ks) { return (ks + 10 - ROT % 10) % 10; }
};
DI void a_dma(const uchar* wsrc, LAS uchar* lds, int h, int w, int ks0, int ks1) {
    const int lane = fresh_lane();
    const rsrc_t r = mk_rsrc(wsrc, 640 * 8192);
    const unsigned v = (lane >> 4) * 8192 + (((lane & 15) ^ (2 * (((lane >> 4) & 1) | (w << 1)))) << 4);
    const unsigned lb = uni((unsigned)(size_t)lds + w * 1024), so = uni(4 * w * 8192 + h * 256);
#pragma unroll
    for (int i = 4 * ks0; i < 4 * ks1; ++i) dma16(r, v, so + 16 * i * 8192, lb + i * 4096);
}
struct ATake {
    unsigned row[4], f[4];
    DI ATake() {
        const int lane = fresh_lane(), c = lane & 15, g = lane >> 4, q = c >> 2, pp = c & 3;
#pragma unroll
        for (int m = 0; m < 4; ++m) {
            const int k0 = m < 2 ? 8 * g + 4 * m : 32 + 8 * g + 4 * (m - 2), ga = k0 >> 4, qq = (k0 >> 2) & 3;
            const int r = 16 * ga + 2 * qq + (q & 1) + 8 * (q >> 1);
            row[m] = r * 256 + pp * 8; f[m] = 32 * ((r & 1) | ((r >> 1) & 6));
        }
    }
    DI u32x8 get(LAS uchar* lds, int ks, int tile) const {
        LAS uchar* a = lds + ks * 16384;
        const u32x2 t0 = trd(a + row[0] + (f[0] ^ (32 * tile))), t1 = trd(a + row[1] + (f[1] ^ (32 * tile))), t2 = trd(a + row[2] + (f[2] ^ (32 * tile))), t3 = trd(a + row[3] + (f[3] ^ (32 * tile)));
        return (u32x8){t0.x, t0.y, t1.x, t1.y, t2.x, t2.y, t3.x, t3.y};
    }
};
DI u32x8 ldsB(LAS uchar* a) { const u32x4 lo = *(LAS u32x4*)a, hi = *(LAS u32x4*)(a + 1024); return (u32x8){lo.x, lo.y, lo.z, lo.w, hi.x, hi.y, hi.z, hi.w}; }
DI void ldsB_put(LAS uchar* a, u32x8 v) { *(LAS u32x4*)a = (u32x4){v[0], v[1], v[2], v[3]}; *(LAS u32x4*)(a + 1024) = (u32x4){v[4], v[5], v[6], v[7]}; }
DI void pin(u32x8& v, bool acc) { if (acc) asm volatile("" : "+a"(v)); else asm volatile("" : "+v"(v)); }

#define NEXT_KV() do { SB(); const int sn_ = min(s + 1, NSEG - 1); kvn = ld4(rw, 0, kvw * 4); \
    _Pragma("unroll") for (int i = 0; i < NPF; ++i) pf[i] = ld16(rw, lo16, pfb + sn_ * 32 * 8192 + i * 1024); SB(); } while (0)
#define KM_OUT() do { unsigned n_ = 0; if (lane == 0) n_ = __hip_atomic_fetch_add((LAS unsigned*)(lds + A_CNT), 1u, __ATOMIC_RELAXED, __HIP_MEMORY_SCOPE_WORKGROUP); \
    if ((__builtin_amdgcn_readfirstlane(n_) & 3) == 3) st4(s + 1, rw, lane == 0 ? (C_KM + h * 32 + 1) * 4 : OOB, 0); } while (0)
template <int W>
DI void a_loop(const Params& p, LAS uchar* lds, int h, u32x2 tag, unsigned pol, u32x2 early, const u32x8 (&wq)[2][10], const u32x8 (&wk)[2][10], const u32x8 (&wv)[3][10]) {
    typedef AP<W> P;
    const int lane = fresh_lane(), c = lane & 15, g = lane >> 4;
    const rsrc_t rw = mk_rsrc(p.ws, WS_USED);
    constexpr int ROT = P::ROT, NT = P::NT, T0 = P::T0, NPF = W == 0 ? 8 : NT, NL = P::NL;
    const int idx = (c & 8) ? 0xEEEE : 0x4444;
    const unsigned lo16 = lane * 16, mo16 = (g * 16 + ((c >> 2) & 1) * 8 + (c >> 3) * 4 + (c & 3)) * 16;
    const unsigned oaddr = W == 0 ? (c >= 8 ? (g * 8 + (c & 7)) * 16 : OOB) : (c < 8 ? (c >> 2) * 1024 + (g >> 1) * 64 + (c & 3) * 16 + (g & 1) * 8 : OOB);
    const unsigned oaddr2 = c < 8 ? (c >> 2) * 1024 + (g & 1) * 128 + (g >> 1) * 64 + (c & 3) * 16 : OOB;
    const unsigned xw = a_xblk(W) + c * 40 + g * 8;
    const unsigned asto = lane == 0 ? h * 4 : OOB;
    const unsigned pfb = W == 0 ? OFF_KF + h * 8192 : OFF_VF + h * 8192 + T0 * 1024;
    u32x4 pf[NPF];
    unsigned long long alive = ~0ull;
    unsigned budget = SPIN_CAP;
    const unsigned exo = ex_off(p);
    const unsigned obo = uni((h >> 2) * 16384 + (h & 3) * 2048 + T0 * 128);
    Clock ck; ck.start();
    if (uni(early.x) < 16u) wait_ctr(p, rw, C_PZ, 16);
    if (uni(early.y) < 4u) wait_ctr(p, rw, C_KVH + h * 16, 4);
#pragma unroll
    for (int i = 0; i < NPF; ++i) pf[i] = ld16(rw, lo16, pfb + i * 1024);
    Sched sc = {ck.now(), 0u, DUR_A0, DUR_M0, PAD0, 0u, 0u};
    const float ninf = g < 2 ? -__builtin_inff() : 0.f;
    unsigned xra[4], xqa[4];
    if (W == 0) {
#pragma unroll
        for (int ks = 0; ks < 4; ++ks) {
            xra[ks] = a_xblk(ks) + (4 * g + (c >> 2)) * 40 + (c & 3) * 8; xqa[ks] = a_xblk(ks) + (4 * g + (c >> 2)) * 40 + (c & 1) * 8;
            asm volatile("" : "+v"(xra[ks]), "+v"(xqa[ks]));
        }
    }
    u32x4 one4 = {0x3F803F80u, 0x3F803F80u, 0x3F803F80u, 0x3F803F80u};
    u32x8 pw = {0u, 0u, 0u, 0u, 0u, 0u, 0u, 0u};
    unsigned pa[3] = {A_P + lo16, A_P2 + lane * 8u, A_P2 + (lane ^ 32) * 8u};
    if (W) asm volatile("" : "+a"(one4), "+v"(pw), "+v"(pa[0]), "+v"(pa[1]), "+v"(pa[2]));
    unsigned lb[3] = {lo16, lo16 + 65536, lo16 + 131072};
    asm volatile("" : "+v"(lb[0]), "+v"(lb[1]), "+v"(lb[2]));
    auto sget = [&](int i) -> u32x8 { const int a = P::stream(i); return ldsB(lds + lb[a >> 16] + (a & 65535)); };
    for (int s = 0; s < NSEG; ++s) {
        const unsigned eb = ex_step(exo, pol, s);
        unsigned mb[10], ob = eb + obo;
#pragma unroll
        for (int m = 0; m < 10; ++m) { mb[m] = eb + mf_off((m + ROT) % 10); asm volatile("" : "+s"(mb[m])); }
        f32x4 aq[2], ak[2], va[3];
        u32x8 ring[RING];
#pragma unroll
        for (int t = 0; t < 2; ++t) { aq[t] = (f32x4){0.f, 0.f, 0.f, 0.f}; ak[t] = aq[t]; asm volatile("" : "+v"(aq[t]), "+v"(ak[t])); }
#pragma unroll
        for (int t = 0; t < 3; ++t) {
            const float z = W == 0 && t == 2 ? ninf : 0.f;
            va[t] = (f32x4){z, z, z, z}; asm volatile("" : "+v"(va[t]));
        }
        if (W) {
#pragma unroll
            for (int i = 0; i < RING; ++i) ring[i] = sget(i);
        }
        const unsigned tgo = sc.tgo;
        unsigned tw = ck.cycles(tgo);
        asm volatile("" : "+s"(ob), "+s"(tw));
        SB();
        unsigned mis = waitmt(tw);
        FT(0);
        u32x4 mf[10];
#pragma unroll
        for (int m = 0; m < 10; ++m) {
            mf[m] = ld16(rw, mo16, mb[m]);
            SB();
            if (m < 9) pace();
            SB();
        }
        auto project = [&]() {
            static_for<10>([&](auto mc) {
                constexpr int m = mc.value;
                auto qprod = [&](auto tc) { constexpr int t = tc.value; smf_x(aq[t], mf[m], wq[t][m], idx, 2 * m + t < P::QA - (W == 0 ? 10 : 0)); };
                auto kprod = [&](auto tc) {
                    constexpr int t = tc.value;
                    if constexpr (P::k_lds(t, m)) {
                        constexpr int i = P::k_idx(t, m);
                        smf_v(ak[t], mf[m], ring[i % RING], idx);
                        ring[i % RING] = sget(i + RING);
                    } else smf_x(ak[t], mf[m], wk[t][m], idx, W == 0);
                };
                kprod(IC<1>{});
                qprod(IC<m == 9 ? 1 : 0>{});
                if constexpr (m == 9) qprod(IC<0>{});
                kprod(IC<0>{});
                if constexpr (m < 9) qprod(IC<1>{});
                if (m == 0) FT(1);
                if (m == 9) FT(2);
                SB();
            });
            float x[4], y[4];
            asm volatile("s_nop 2" : "+v"(aq[1]), "+v"(ak[1]));
#pragma unroll
            for (int r = 0; r < 4; ++r) y[r] = swap32_add(aq[1][r], ak[1][r]);
            *(LAS u32x2*)(lds + xw + 640) = (u32x2){pk2(y[0], y[1]), pk2(y[2], y[3])};
            SB();
            asm volatile("" : "+v"(aq[0]), "+v"(ak[0]));
#pragma unroll
            for (int r = 0; r < 4; ++r) x[r] = swap32_add(aq[0][r], ak[0][r]);
            *(LAS u32x2*)(lds + xw) = (u32x2){pk2(x[0], x[1]), pk2(x[2], x[3])};
            SB();
            return nan_lanes((f32x4){x[0], x[1], x[2], x[3]});
        };
        if (__builtin_expect(project() != 0, 0)) {
            if (refetch<10>(mf, rw, mo16, mb, budget)) {
                mis = tw + 100000; TRY();
#pragma unroll
                for (int t = 0; t < 2; ++t) { aq[t] = (f32x4){0.f, 0.f, 0.f, 0.f}; ak[t] = aq[t]; asm volatile("" : "+v"(aq[t]), "+v"(ak[t])); }
                if (W) {
#pragma unroll
                    for (int i = 0; i < RING; ++i) ring[i] = sget(i);
                }
                asm volatile("s_nop 1" ::: "memory");
                project();
            }
        }
        SB();
        if (W) {
#pragma unroll
            for (int i = 0; i < VPRE; ++i) { const int r = 10 * NT - 1 - i; smf_a(va[r % NT], mf[P::m_of(r / NT)], wv[r % NT][r / NT], idx); }
        }
        bar();
        SB();
        FT(3);
        const unsigned kb = OFF_KM + (s * 32 + h) * 4096;
        const int kvs = min(s + 2, NSEG - 1), kvw = (kvs < 16 && (kvs & 4) == 0 ? C_KVH : C_KV) + ((kvs >> 3) * 32 + h) * 16;
        unsigned tpub = 0, kvn;
        if (W == 0) {
            u32x4 xf[4], xd[4];
#pragma unroll
            for (int ks = 0; ks < 4; ++ks) {
                const u32x2 a = trd(lds + xqa[ks]), b = trd(lds + xqa[ks] + 640), lo = trd(lds + xra[ks]), hi = trd(lds + xra[ks] + 640);
                xd[ks] = (u32x4){a.x, a.y, b.x, b.y}; xf[ks] = (u32x4){lo.x, lo.y, hi.x, hi.y};
            }
            SB();
            if (__builtin_expect(s == NSEG - 1, 0)) {
#pragma unroll
                for (int ks = 0; ks < 4; ++ks) st16((u32x4){nosent(xf[ks].x), nosent(xf[ks].y), nosent(xf[ks].z), nosent(xf[ks].w)}, rw, oaddr, kb + ks * 512);
                KM_OUT();
            }
#pragma unroll
            for (int ks = 0; ks < 4; ++ks) { mfma_v(va[0], pf[ks], xd[ks]); mfma_v(va[1], pf[(4 + ks) % NPF], xd[ks]); mfma_v(va[2], xf[ks], xd[ks]); }
            FT(4);
            float a0, a1, a2, a3, a4, a5, tm, tn;
            unsigned e0, e1, e2;
            asm volatile("s_nop 3\n\t"
                         "v_cndmask_b32_e64 %0, %15, %11, %23\n\tv_cndmask_b32_e64 %1, %16, %12, %23\n\tv_cndmask_b32_e64 %2, %17, %13, %23\n\t"
                         "v_cndmask_b32_e64 %3, %18, %14, %23\n\tv_cndmask_b32_e64 %4, %21, %19, %23\n\tv_cndmask_b32_e64 %5, %22, %20, %23\n\t"
                         "v_max3_f32 %6, %0, %1, %2\n\tv_max3_f32 %7, %3, %4, %5\n\tv_max_f32 %6, %6, %7\n\ts_nop 1\n\t"
                         "v_max_f32_dpp %6, %6, %6 row_ror:8 row_mask:0xf bank_mask:0xf\n\t"
                         "v_mov_b32 %7, %6\n\ts_nop 1\n\tv_permlane32_swap_b32 %6, %7\n\tv_max_f32 %6, %6, %7\n\t"
                         "v_mov_b32 %7, %6\n\ts_nop 1\n\tv_permlane16_swap_b32 %6, %7\n\tv_max_f32 %6, %6, %7\n\t"
                         "v_mul_f32 %6, %24, %6\n\t"
                         "v_fma_f32 %0, %0, %24, -%6\n\tv_fma_f32 %1, %1, %24, -%6\n\tv_fma_f32 %2, %2, %24, -%6\n\t"
                         "v_fma_f32 %3, %3, %24, -%6\n\tv_fma_f32 %4, %4, %24, -%6\n\tv_fma_f32 %5, %5, %24, -%6\n\t"
                         "v_exp_f32 %0, %0\n\tv_exp_f32 %1, %1\n\tv_exp_f32 %2, %2\n\tv_exp_f32 %3, %3\n\tv_exp_f32 %4, %4\n\tv_exp_f32 %5, %5\n\t"
                         "v_cvt_pk_bf16_f32 %8, %0, %1\n\tv_cvt_pk_bf16_f32 %9, %2, %3\n\tv_cvt_pk_bf16_f32 %10, %4, %5"
                         : "=&v"(a0), "=&v"(a1), "=&v"(a2), "=&v"(a3), "=&v"(a4), "=&v"(a5), "=&v"(tm), "=&v"(tn), "=&v"(e0), "=&v"(e1), "=&v"(e2)
                         : "v"(va[0][0]), "v"(va[0][1]), "v"(va[0][2]), "v"(va[0][3]), "v"(va[1][0]), "v"(va[1][1]), "v"(va[1][2]), "v"(va[1][3]),
                           "v"(va[2][0]), "v"(va[2][1]), "v"(va[2][2]), "v"(va[2][3]), "s"(0x00FF00FF00FF00FFull), "s"(SCL));
            *(LAS u32x2*)(lds + A_P + (g * 16 + (c & 7)) * 16 + (c >> 3) * 8) = (u32x2){e0, e1};
            *(LAS unsigned*)(lds + A_P2 + (g * 16 + (c & 7)) * 8 + (c >> 3) * 4) = e2;
            FT(5);
            bar();
            FT(6);
            sc.fetch_a(rw, exo, pol, max(s, 1), lane); sc.fetch_m(rw, exo, pol, max(s, 1), lane);
            NEXT_KV();
            if (s < NSEG - 1) waitmt(mt32() + KM_DELAY * 3);
#pragma unroll
            for (int ks = 0; ks < 4; ++ks) if (s < NSEG - 1) st16(xf[ks], rw, oaddr, kb + ks * 512);
        } else {
            if (W == 3) st4(s, rw, asto, C_KM * 4 + h * 124);
#pragma unroll
            for (int i = 0; i < (10 * NT - NL - VPRE > NL ? 10 * NT - NL - VPRE : NL); ++i) {
                if (i < NL) {
                    smf_v(va[i % NT], mf[P::m_of(i / NT)], ring[(NL + i) % RING], idx);
                    if (NL + i + RING < 2 * NL) ring[(NL + i) % RING] = sget(NL + i + RING);
                }
                const int r = NL + i;
                if (r < 10 * NT - VPRE) smf_a(va[r % NT], mf[P::m_of(r / NT)], wv[r % NT][r / NT], idx);
                SB();
            }
            asm volatile("s_nop 7" : "+v"(va[0]), "+v"(va[1]), "+v"(va[2]));
            float sa[4], sb[4];
#pragma unroll
            for (int r = 0; r < 4; ++r) { sa[r] = swap32_add(va[0][r], va[1][r]); if (W != 3) sb[r] = swap32_add(va[2][r], va[2][r]); }
            u32x4 vv = {pk2(sa[0], sa[1]), pk2(sa[2], sa[3]), 0u, 0u};
            if (W != 3) { vv.z = pk2(sb[0], sb[1]); vv.w = pk2(sb[2], sb[3]); }
            asm volatile("" : "+v"(vv));
            if (__builtin_expect(s == NSEG - 1, 0)) {
                st8((u32x2){nosent(vv.x), nosent(vv.y)}, rw, lane * 8, kb + 2048 + T0 * 256);
                if (W != 3) st8((u32x2){nosent(vv.z), nosent(vv.w)}, rw, g < 2 ? lane * 8 : OOB, kb + 2048 + T0 * 256 + 512);
                KM_OUT();
            }
            SB();
            FT(5);
            bar();
            FT(6);
            SB();
            if (s < NSEG - 1) {
                const u32x4 p01 = *(LAS u32x4*)(lds + pa[0]);
                const u32x2 q2 = *(LAS u32x2*)(lds + pa[1]), q2s = *(LAS u32x2*)(lds + pa[2]);
                pw[4] = q2.x; pw[5] = q2.y; pw[0] = q2s.x; pw[1] = q2s.y;
                const u32x4 p2s = __builtin_shufflevector(pw, pw, 0, 1, 2, 3), p2h = __builtin_shufflevector(pw, pw, 2, 3, 4, 5), p2 = __builtin_shufflevector(pw, pw, 4, 5, 6, 7);
                SB();
                f32x4 l, o[3];
                asm volatile("v_mfma_f32_16x16x32_bf16 %0, %1, %2, 0" : "=&v"(l) : "a"(one4), "v"(p01));
                mfma_0(o[0], pf[0], p01);
                mfma_av(l, one4, p2);
                mfma_0(o[1], pf[1 % NPF], p01);
                mfma_v(o[0], vv, p2s);
                mfma_v(o[1], vv, p2);
                float inv;
                if (W != 3) {
                    mfma_0(o[2], pf[2 % NPF], p01);
                    asm volatile("v_rcp_f32 %0, %1" : "=v"(inv) : "v"(l[0]));
                    mfma_v(o[2], vv, p2h);
                    asm volatile("s_nop 3" : "+v"(o[0]), "+v"(o[1]), "+v"(inv));
                } else {
                    asm volatile("s_nop 1\n\tv_rcp_f32 %0, %3\n\ts_nop 5" : "=&v"(inv), "+v"(o[0]), "+v"(o[1]) : "v"(l[0]));
                }
                const f32x4 n0 = o[0] * inv, n1 = o[1] * inv;
                SB();
                const unsigned a0 = pk2(n0[0], n0[1]), b0 = pk2(n1[0], n1[1]), a1 = pk2(n0[2], n0[3]), b1 = pk2(n1[2], n1[3]);
                SB();
                auto r0 = __builtin_amdgcn_permlane16_swap(a0, b0, false, false);
                auto r1 = __builtin_amdgcn_permlane16_swap(a1, b1, false, false);
                st16((u32x4){nosent(r0[0]), nosent(r1[0]), nosent(r0[1]), nosent(r1[1])}, rw, oaddr2, ob);
                if (W != 3) {
                    asm volatile("" : "+v"(o[2]));
                    const f32x4 n2 = o[2] * inv;
                    st8((u32x2){nosent(pk2(n2[0], n2[1])), nosent(pk2(n2[2], n2[3]))}, rw, oaddr, ob + 256);
                }
            }
            tpub = ck.now();
            sc.fetch_a(rw, exo, pol, max(s, 1), lane); sc.fetch_m(rw, exo, pol, max(s, 1), lane);
            NEXT_KV();
            if (W == 1) st4(mis - tw <= 120u ? tpub - tgo : 0u, rw, asto, eb + EX_AST);
            if (s < NSEG - 1) st8((u32x2){vv.x, vv.y}, rw, lane * 8, kb + 2048 + T0 * 256);
            if (W != 3 && s < NSEG - 1) st8((u32x2){vv.z, vv.w}, rw, g < 2 ? lane * 8 : OOB, kb + 2048 + T0 * 256 + 512);
        }
        if (budget == 0) set_tmo(p);
        FT(7); FDUMP(h, s, W);
        STAT(h, s, W, ck.at(mis), tpub, tgo, tries);
        if (s == NSEG - 1) break;
        ck.sync();
        if (s > 0) { sc.take_a(); sc.next(p, rw, exo, pol, s, lane, alive); }
        if ((unsigned)__builtin_amdgcn_readfirstlane(kvn) < 4u) wait_ctr(p, rw, kvw, 4);
        if (s == 2) wait_ctr(p, rw, C_PZ + 32, 56);
    }
    XST(W == 0, h);
    if (W == 0 && h == 0) {
        wait_ctr(p, rw, C_ACK, 256);
        st8(tag, rw, lane == 0 ? 0 : OOB, B_EPOCH * 4);
    }
    __builtin_amdgcn_endpgm();
}

template <int W>
DI void a_wave(const Params& p, LAS uchar* lds, int h, bool started, u32x2 tag) {
    typedef AP<W> P;
    u32x8 wq[2][10], wk[2][10], wv[3][10];
    if (W == 0 || !started) a_dma(p.wq, lds, h, W, 0, 10);
    const rsrc_t rw = mk_rsrc(p.ws, WS_USED);
    u32x2 bi, bv;
    const ATake tk;
    static_for<15>([&](auto sc) {
        constexpr int mat = sc.value / 5, k0 = 2 * (sc.value % 5), NTL = mat < 2 ? 2 : P::NT, NTD = P::NT ? P::NT : 1;
        wait_vm<sc.value < 10 ? 32 : 8 * (14 - sc.value)>();
        bar();
        if (k0 == 0 || k0 == 4) AST_(mat * 2 + k0 / 4);
#pragma unroll
        for (int i = 0; i < 2 * NTL; ++i) {
            const int ks = k0 + i / NTL, t = i % NTL, m = P::m_of(ks);
            if (mat == 0) wq[t][m] = tk.get(lds, ks, 2 * W + t);
            if (mat == 1) wk[t][m] = tk.get(lds, ks, 2 * W + t);
            if (mat == 2) wv[t][ks] = tk.get(lds, ks, P::T0 + t);
        }
        SB();
#pragma unroll
        for (int i = 0; i < 2 * NTL; ++i) {
            const int ks = k0 + i / NTL, t = i % NTL, m = P::m_of(ks);
            if (mat == 0) pin(wq[t][m], 2 * m + t < P::QA - (W == 0 ? 10 : 0));
            if (mat == 1) pin(wk[t][m], W == 0);
            if (mat == 2) pin(wv[t][ks], true);
        }
        bar(); SB();
        if (sc.value == 9) { bi = boot_words(rw); bv = vote_words(rw); SB(); }
        if (mat < 2) a_dma(mat == 0 ? p.wk : p.wv, lds, h, W, k0, k0 + 2);
        else {
            const int lane = fresh_lane();
#pragma unroll
            for (int i = 0; i < P::NL; ++i) if (i / NTD / 2 == k0 / 2) ldsB_put(lds + P::v_slot(i) + lane * 16, wv[i % NTD][i / NTD]);
#pragma unroll
            for (int i = 0; i < 20; ++i)
                if (P::k_lds(i & 1, i >> 1) && (5 + P::k_idx(i & 1, i >> 1) / NTD) / 2 == k0 / 2) ldsB_put(lds + P::k_slot(P::k_idx(i & 1, i >> 1)) + lane * 16, wk[i & 1][i >> 1]);
        }
        if (sc.value == 12) {
            wait_boot(rw, tag, bi);
            if (W == 0 && fresh_tid() == 0) add_ctr(p, C_ACK, 1u);
            bi = (u32x2){ld4(rw, 0, C_PZ * 4), ld4(rw, 0, (C_KVH + h * 16) * 4)};
        }
        SB();
    });
    const unsigned pol = read_pol(rw, tag, bv);
    if (W == 0) *(LAS unsigned*)(lds + A_CNT) = 0;
    bar();
    SB();
    a_loop<W>(p, lds, h, tag, pol, bi, wq, wk, wv);
}
DI void role_A(const Params& p, LAS uchar* lds, int h, bool started, u32x2 tag) {
    const int w = __builtin_amdgcn_readfirstlane(threadIdx.x >> 6);
    if (w == 0) a_wave<0>(p, lds, h, started, tag);
    else if (w == 1) a_wave<1>(p, lds, h, started, tag);
    else if (w == 2) a_wave<2>(p, lds, h, started, tag);
    else a_wave<3>(p, lds, h, started, tag);
}
DI int take_ticket(const Params& p, LAS uchar* lds, int word) {
    if (fresh_tid() == 0) *(LAS unsigned*)(lds + LDS_BYTES - 16) = __hip_atomic_fetch_add((unsigned*)p.ws + word, 1u, __ATOMIC_RELAXED, __HIP_MEMORY_SCOPE_AGENT);
    bar();
    const int t = __builtin_amdgcn_readfirstlane(*(LAS unsigned*)(lds + LDS_BYTES - 16));
    bar();
    return t;
}
#define TSTAT(i, a, b)
#define SMALL_DELAY 300
DI void warm_l2(const Params& p, int x, int j) {
    const int lane = fresh_lane(), id = j * 4 + __builtin_amdgcn_readfirstlane(threadIdx.x >> 6);
    const rsrc_t rk = mk_rsrc(p.wk, 640 * 8192), rv = mk_rsrc(p.wv, 640 * 8192), ro = mk_rsrc(p.wo, 4096 * 1280);
    unsigned sink[24];
    const unsigned vw = (lane >> 4) * 8192 + (lane & 15) * 128, vo = (lane >> 2) * 1280 + min((lane & 3) * 128, 316);
#pragma unroll
    for (int i = 0; i < 4; ++i) asm volatile("buffer_load_dword %0, %1, %2, %3 offen" : "=v"(sink[i]) : "v"(vw), "s"(rk), "s"((id + 40 * i) * 4 * 8192 + x * 2048) : "memory");
#pragma unroll
    for (int i = 0; i < 7; ++i) asm volatile("buffer_load_dword %0, %1, %2, %3 offen" : "=v"(sink[4 + i]) : "v"(vo), "s"(ro), "s"(min(id + 40 * i, 255) * 16 * 1280 + x * 320) : "memory");
#pragma unroll
    for (int i = 0; i < 4; ++i) asm volatile("buffer_load_dword %0, %1, %2, %3 offen" : "=v"(sink[11 + i]) : "v"(vw), "s"(rv), "s"((id + 40 * i) * 4 * 8192 + x * 2048) : "memory");
    wait_vm<0>();
#pragma unroll
    for (int i = 0; i < 15; ++i) asm volatile("" :: "v"(sink[i]));
}
DI void background(const Params& p, LAS uchar* lds, int x, int j, u32x2 tag) {
    const bool big = x >= 4;
    if (!big) {
        const int id = j * 4 + x;
        const rsrc_t rw = mk_rsrc(p.ws, WS_USED);
        if (j < 4) ln_tile<true>(p, id, tag);
        else {
            warm_l2(p, x, j - 4);
            wait_boot(rw, tag, boot_words(rw));
        }
        if (fresh_tid() == 0) add_ctr(p, C_ACK, 1u);
        ln_tile<false>(p, id + 16, tag);
        const unsigned pol = read_pol(rw, tag, vote_words(rw));
        if (j < 4) poison(p, pol, id, 16, 0, 80, C_PZ);
        ln_tile<false>(p, id + 72, tag);
        poison(p, pol, id, 56, 80, 1376, C_PZ + 32);
    } else {
        if (j < 12) probe(p, lds, j * 4 + x - 4, tag);
        if (j & 2) kv0_task<true>(p, lds, 32 * (x - 4) + j, tag); else kv0_task<false>(p, lds, 32 * (x - 4) + j, tag);
        TSTAT((x - 4) * 128 + j, t0, 0);
    }
    if (!big) return;
    const int y = x - 4;
    for (;;) {
        int t = take_ticket(p, lds, C_Q1 + x * 32), kind = 0, chunk = 0, u0 = 0, nu = 8;
        const int wu = 192 + 8 * y, wn = y < 3 ? 4 : 2;
        if (t < 8) { chunk = t >> 1; u0 = wu + wn * (t & 1); nu = y < 3 ? 4 : 2 - (t & 1); }
        else if ((t -= 8) < 8) { chunk = 2; u0 = 32 * y + 4 * t; nu = 4; }
        else if ((t -= 8) < 8) { chunk = t >> 2; u0 = 128 + 16 * y + 4 * (t & 3); nu = 4; }
        else if ((t -= 8) < 8) { chunk = 3; u0 = 32 * y + 4 * t; nu = 4; }
        else if ((t -= 8) < 8) { chunk = 4 + (t >> 2); u0 = 32 * y + 8 * (t & 3); }
        else if ((t -= 8) < 5) { kind = 1; chunk = y; u0 = t; }
        else if ((t -= 5) < 4) { chunk = 4 + t; u0 = wu; nu = y < 3 ? 8 : 3; }
        else if ((t -= 4) < 6) { kind = 3; u0 = 6 * y + t; }
        else if ((t -= 6) < 4) { chunk = 2 + (t >> 1); u0 = 128 + 16 * y + 8 * (t & 1); }
        else if ((t -= 4) < 8) { chunk = 6 + (t >> 2); u0 = 32 * y + 8 * (t & 3); }
        else if ((t -= 8) < 8) { chunk = 4 + (t >> 1); u0 = 128 + 16 * y + 8 * (t & 1); }
        else if ((t -= 8) < 5) { kind = 1; chunk = 4 + y; u0 = t; }
        else if ((t -= 5) < 5) { kind = 2; chunk = y; u0 = t; }
        else if ((t -= 5) < 5 * N_WG) { kind = 4; u0 = t; }
        else return;
        if (kind == 0) p1_task(p, lds, chunk, u0, nu);
        else if (kind == 1) gemm_task<false>(p, lds, chunk, u0);
        else if (kind == 2) gemm_task<true>(p, lds, chunk, u0);
        else {
            wait_ctr(p, mk_rsrc(p.ws, WS_USED), C_PZ + 32, 56);
            if (kind == 3) {
                const int w = __builtin_amdgcn_readfirstlane(threadIdx.x >> 6);
                for (int s = u0 >> 3; s < NSEG; s += N_T / 8) attn_task(p, s, (u0 & 7) * 4 + w, (s & 7) < N_T / 8);
            } else { const int rank = (u0 * 52) >> 8; w_role(p, lds, 5 * y + u0 - 5 * rank, rank); }
            TSTAT(x * 128 + tk0, t0, kind + 2);
            return;
        }
        TSTAT(x * 128 + tk0, t0, kind == 0 ? (u0 < 128 ? 1 : (u0 < 192 ? 2 : 3)) : (kind == 1 ? 4 : 7));
    }
}
__global__ void __launch_bounds__(256, 1) k_main(Params p) {
    extern __shared__ __attribute__((aligned(16))) uchar smem[];
    LAS uchar* lds = (LAS uchar*)smem;
    const int w = uni(threadIdx.x >> 6), guess = uni((blockIdx.x & ~7) | xcc_id());
    if (w && (guess & 7) < 4) {
        const int x = guess & 7, t = guess >> 3;
        if (t < 8) a_dma(p.wq, lds, x * 8 + t, w, 0, 10);
        else if (t < 18) {
            const int lane = fresh_lane(), cs = (x * 10 + t - 8) >> 1;
            wo_stage(p, (unsigned)(size_t)lds + w * 32768, 8 * w, cs, lane);
            wo_stage(p, (unsigned)(size_t)lds + w * 32768 + 16384, 8 * w + 2, cs, lane);
        }
    }
    if (w == 0) {
        int slot = guess;
        const u32x2 tag = launch_tag(mk_rsrc(p.ws, WS_USED));
        const unsigned long long t64 = ((unsigned long long)tag.y << 32) | tag.x;
        unsigned long long* cl = (unsigned long long*)(p.ws + ex_off(p) + 12 * EX_STEP);
        for (int i = 0; i < 256; ++i) {
            unsigned long long old = 0;
            if (fresh_lane() == 0) old = __hip_atomic_exchange(cl + slot, t64, __ATOMIC_RELAXED, __HIP_MEMORY_SCOPE_AGENT);
            if (uni((unsigned)old) != tag.x || uni((unsigned)(old >> 32)) != tag.y) break;
            slot = slot < 248 ? slot + 8 : (slot + 1) & 7;
        }
        *(LAS u32x4*)lds = (u32x4){(unsigned)slot, tag.x, tag.y, 0u};
    }
    bar();
    const u32x4 got = *(LAS u32x4*)lds;
    const int slot = uni(got.x), x = slot & 7, t = slot >> 3;
    const u32x2 tag = {uni(got.y), uni(got.z)};
    if (slot != guess) wait_vm<0>();
    bar();
    if (x < 4 && t < 8) role_A(p, lds, x * 8 + t, slot == guess, tag);
    else if (x < 4 && t < 18) role_M(p, lds, x * 10 + t - 8, slot == guess, tag);
    else background(p, lds, x, x < 4 ? t - 18 : t, tag);
}

extern "C" void kernel_launch(void* const* d_in, const int* in_sizes, int n_in, void* d_out, int out_size, void* d_ws, size_t ws_size, hipStream_t stream) {
    Params p;
    p.x = (const uchar*)d_in[0]; p.mem0 = (const uchar*)d_in[1]; p.wq = (const uchar*)d_in[2]; p.wk = (const uchar*)d_in[3];
    p.wv = (const uchar*)d_in[4]; p.wo = (const uchar*)d_in[5]; p.w1 = (const uchar*)d_in[8]; p.b1 = (const uchar*)d_in[9];
    p.w2 = (const uchar*)d_in[10]; p.out = (uchar*)d_out; p.ws = (uchar*)d_ws;
    (void)hipFuncSetAttribute((const void*)k_main, hipFuncAttributeMaxDynamicSharedMemorySize, LDS_BYTES);
    k_main<<<256, 256, LDS_BYTES, stream>>>(p);
}
```

```cpp
#include <hip/hip_runtime.h>
#include <stdint.h>

typedef __bf16 bf16x2 __attribute__((ext_vector_type(2)));
typedef __bf16 bf16x4 __attribute__((ext_vector_type(4)));
typedef __bf16 bf16x8 __attribute__((ext_vector_type(8)));
typedef __bf16 bf16x16 __attribute__((ext_vector_type(16)));
typedef short s16x4 __attribute__((ext_vector_type(4)));
typedef float f32x2 __attribute__((ext_vector_type(2)));
typedef float f32x4 __attribute__((ext_vector_type(4)));
typedef unsigned u32x2 __attribute__((ext_vector_type(2)));
typedef unsigned u32x4 __attribute__((ext_vector_type(4)));
typedef unsigned u32x8 __attribute__((ext_vector_type(8)));
typedef unsigned char uchar;
typedef __amdgpu_buffer_rsrc_t rsrc_t;
#define LAS __attribute__((address_space(3)))
#define DI __device__ __forceinline__

constexpr int S_ = 2048, D_ = 640, H_ = 32, NSEG = 64, F_ = 1728;
constexpr int LDS_BYTES = 163840;
constexpr int OFF_CTRL = 0;
constexpr int OFF_XF = 65536;
constexpr int OFF_KF = OFF_XF + 128 * 20 * 1024;
constexpr int OFF_QF = OFF_KF + 64 * 32 * 8192;
constexpr int OFF_VF = OFF_QF + 64 * 32 * 8192;
constexpr int OFF_ACT = OFF_VF + 64 * 32 * 8192;
constexpr int OFF_GF = OFF_ACT + 128 * 54 * 1024;
constexpr int OFF_OT = OFF_GF + 128 * 20 * 1024;
constexpr int OFF_KM = OFF_OT + 128 * 32 * 4096;
constexpr int OFF_EX = OFF_KM + 64 * 32 * 4096;
constexpr int EX_STEP = 163840, EX_MF = 8 * 16384, EX_AST = 9 * 16384 + 2048, EX_MST = 9 * 16384 + 2304, EX_PROBE = 9 * 16384 + 7168;
constexpr int WS_USED = OFF_EX + 7 * 2097152;
constexpr int C_TMO = 0, C_ACK = 32, C_KV = 512  , C_Q = C_KV + 4096, C_ACT = C_Q + 4096  , C_G = C_ACT + 256  ,
              C_OT = C_G + 1280  , C_KM = C_OT + 2048  , C_Q1 = C_KM + 1024  , C_KVH = C_Q1 + 256  , C_X = C_KVH + 1024  , C_PZ = C_X + 512  , C_WT = C_PZ + 64  , C_OF = C_WT + 640  ,
              C_ZEND = 16 * 1008;
constexpr int B_INIT = C_ZEND  , B_VOTE = B_INIT + 112  , B_EPOCH = 16352;
static_assert(C_OF + 128 <= C_ZEND && B_VOTE + 96 <= B_EPOCH && B_EPOCH + 2 <= 16384, "");
constexpr unsigned SENT = 0xFFFFFFFFu;
constexpr int SC1 = 16;
constexpr unsigned OOB = 0x7FFFFF00u;
constexpr unsigned SPIN_CAP = 400000;

struct Params {
    const uchar* x; const uchar* mem0; const uchar* wq; const uchar* wk; const uchar* wv; const uchar* wo;
    const uchar* w1; const uchar* b1; const uchar* w2; uchar* out; uchar* ws;
};

DI unsigned uni(unsigned v) { asm volatile("" : "+v"(v)); return __builtin_amdgcn_readfirstlane(v); }
DI rsrc_t mk_rsrc(const void* p, unsigned bytes) {
    return __builtin_amdgcn_make_buffer_rsrc((void*)(((size_t)uni((unsigned)((size_t)p >> 32)) << 32) | uni((unsigned)(size_t)p)), 0, bytes, 0x00020000);
}
DI u32x4 ld16(rsrc_t r, unsigned v, unsigned s) { return __builtin_amdgcn_raw_buffer_load_b128(r, v, s, SC1); }
DI u32x2 ld8(rsrc_t r, unsigned v, unsigned s) { return __builtin_amdgcn_raw_buffer_load_b64(r, v, s, SC1); }
DI unsigned ld4(rsrc_t r, unsigned v, unsigned s) { return __builtin_amdgcn_raw_buffer_load_b32(r, v, s, SC1); }
DI void st16(u32x4 d, rsrc_t r, unsigned v, unsigned s) { __builtin_amdgcn_raw_buffer_store_b128(d, r, v, s, SC1); asm volatile("s_nop 1" :: "v"(d)); }
DI void st8(u32x2 d, rsrc_t r, unsigned v, unsigned s) { __builtin_amdgcn_raw_buffer_store_b64(d, r, v, s, SC1); }
DI void st4(unsigned d, rsrc_t r, unsigned v, unsigned s) { __builtin_amdgcn_raw_buffer_store_b32(d, r, v, s, SC1); }
DI unsigned pk2(float a, float b) { f32x2 v = {a, b}; return __builtin_bit_cast(unsigned, __builtin_convertvector(v, bf16x2)); }
DI unsigned nosent(unsigned v) { return min(v, 0xFFFFFFFEu); }
DI float bf_lo(unsigned v) { return __builtin_bit_cast(float, v << 16); }
DI float bf_hi(unsigned v) { return __builtin_bit_cast(float, v & 0xFFFF0000u); }
DI float rbf(float a) { return bf_lo(pk2(a, a)); }
DI bf16x8 as8(u32x4 v) { return __builtin_bit_cast(bf16x8, v); }
DI f32x4 mfma32(u32x4 a, u32x4 b, f32x4 c) { return __builtin_amdgcn_mfma_f32_16x16x32_bf16(as8(a), as8(b), c, 0, 0, 0); }
DI f32x4 mfma16(u32x2 a, u32x2 b, f32x4 c) { return mfma32((u32x4){a.x, a.y, 0u, 0u}, (u32x4){b.x, b.y, 0u, 0u}, c); }
DI f32x4 smf(u32x4 a, u32x8 b, f32x4 c, int idx) { return __builtin_amdgcn_smfmac_f32_16x16x64_bf16(as8(a), __builtin_bit_cast(bf16x16, b), c, idx, 0, 0); }
DI void smf_a(f32x4& c, u32x4 a, u32x8 b, int idx) { asm volatile("v_smfmac_f32_16x16x64_bf16 %0, %1, %2, %3" : "+v"(c) : "v"(a), "a"(b), "v"(idx)); }
DI void smf_v(f32x4& c, u32x4 a, u32x8 b, int idx) { asm volatile("v_smfmac_f32_16x16x64_bf16 %0, %1, %2, %3" : "+v"(c) : "v"(a), "v"(b), "v"(idx)); }
DI void mfma_v(f32x4& c, u32x4 a, u32x4 b) { asm volatile("v_mfma_f32_16x16x32_bf16 %0, %1, %2, %0" : "+v"(c) : "v"(a), "v"(b)); }
DI void mfma_0(f32x4& c, u32x4 a, u32x4 b) { asm volatile("v_mfma_f32_16x16x32_bf16 %0, %1, %2, 0" : "=&v"(c) : "v"(a), "v"(b)); }
DI float fmax2(float a, float b) { return __builtin_amdgcn_fmed3f(a, b, __builtin_inff()); }
DI void mfma_pad(f32x4& a, f32x4& b, f32x4& c, f32x4& d) { asm volatile("s_nop 7" : "+v"(a), "+v"(b), "+v"(c), "+v"(d)); }
DI u32x2 trd(LAS uchar* p) { return __builtin_bit_cast(u32x2, __builtin_amdgcn_ds_read_tr16_b64_v4i16((LAS s16x4*)p)); }
DI unsigned mx4(unsigned m, u32x4 v) { return max(max(max(m, v.x), max(v.y, v.z)), v.w); }
DI void mx4v(unsigned& m, u32x4 v) { asm volatile("v_max3_u32 %0, %0, %1, %2\n\tv_max3_u32 %0, %0, %3, %4" : "+v"(m) : "v"(v.x), "v"(v.y), "v"(v.z), "v"(v.w)); }
DI unsigned long long nan_lanes(const f32x4& a) { return __builtin_amdgcn_ballot_w64(__builtin_isunordered(a[0], a[1]) | __builtin_isunordered(a[2], a[3])); }
template <int N> DI bool refetch(u32x4 (&f)[N], rsrc_t r, unsigned v, const unsigned (&so)[N], unsigned& budget) {
    bool redo = false;
#pragma nounroll
    for (;;) {
        unsigned m = 0;
#pragma unroll
        for (int i = 0; i < N; ++i) m = max(max(max(m, f[i].x), max(f[i].y, f[i].z)), f[i].w);
        if (__builtin_amdgcn_ballot_w64(m == SENT) == 0 || budget == 0) break;
        --budget; redo = true;
        asm volatile("" ::: "memory");
#pragma unroll
        for (int i = 0; i < N; ++i) f[i] = __builtin_amdgcn_raw_buffer_load_b128(r, v, so[i], SC1);
    }
    return redo;
}
DI unsigned rt32() { unsigned long long t; asm volatile("s_memrealtime %0\n\ts_waitcnt lgkmcnt(0)" : "=s"(t) :: "memory"); return (unsigned)t; }
DI unsigned waitrt(unsigned t) { unsigned n; do n = rt32(); while ((int)(n - t) < 0); return n; }
DI void bar() { asm volatile("s_waitcnt lgkmcnt(0)" ::: "memory"); __builtin_amdgcn_s_barrier(); asm volatile("" ::: "memory"); }
template <int N> DI void wait_vm() { asm volatile("s_waitcnt vmcnt(%0)" :: "n"(N) : "memory"); }
DI void dma16(rsrc_t r, unsigned voff, unsigned soff, unsigned ldsaddr) {
    asm volatile("s_mov_b32 m0, %0\n\ts_nop 0\n\tbuffer_load_dwordx4 %1, %2, %3 offen lds" :: "s"(ldsaddr), "v"(voff), "s"(r), "s"(soff) : "memory");
}
DI float swap32_add(float a, float b) { auto r = __builtin_amdgcn_permlane32_swap(__builtin_bit_cast(unsigned, a), __builtin_bit_cast(unsigned, b), false, false);
    return __builtin_bit_cast(float, (unsigned)r[0]) + __builtin_bit_cast(float, (unsigned)r[1]); }
DI float swap16_add(float a, float b) { auto r = __builtin_amdgcn_permlane16_swap(__builtin_bit_cast(unsigned, a), __builtin_bit_cast(unsigned, b), false, false);
    return __builtin_bit_cast(float, (unsigned)r[0]) + __builtin_bit_cast(float, (unsigned)r[1]); }
DI float grp_max(float m) {
    auto r = __builtin_amdgcn_permlane32_swap(__builtin_bit_cast(unsigned, m), __builtin_bit_cast(unsigned, m), false, false);
    m = fmax2(__builtin_bit_cast(float, (unsigned)r[0]), __builtin_bit_cast(float, (unsigned)r[1]));
    auto q = __builtin_amdgcn_permlane16_swap(__builtin_bit_cast(unsigned, m), __builtin_bit_cast(unsigned, m), false, false);
    return fmax2(__builtin_bit_cast(float, (unsigned)q[0]), __builtin_bit_cast(float, (unsigned)q[1]));
}
DI int fresh_lane() { int l = __builtin_amdgcn_mbcnt_hi(~0u, __builtin_amdgcn_mbcnt_lo(~0u, 0u)); asm volatile("" : "+v"(l)); return l & 63; }
DI unsigned fresh_tid() { unsigned t = threadIdx.x; asm volatile("" : "+v"(t)); return t; }
DI unsigned ex_off(const Params& p) { return OFF_EX + ((0u - uni((unsigned)(size_t)p.ws) - OFF_EX) & 0x1FFFFFu); }
DI unsigned ex_step(unsigned exo, unsigned pol, int s) { const int r = (s * 43) >> 9; return exo + r * 2097152 + (s - 12 * r) * EX_STEP + ((pol >> r) & 1) * 8192; }
DI constexpr unsigned mf_off(int ks) { return EX_MF + (ks >> 3) * 16384 + (ks & 7) * 1024; }
DI unsigned xcc_id() { return (unsigned)__builtin_amdgcn_s_getreg(20 | (3 << 11)) & 7u; }
DI unsigned mt32() { unsigned long long t; asm volatile("s_memtime %0\n\ts_waitcnt lgkmcnt(0)" : "=s"(t) :: "memory"); return (unsigned)t; }
DI void set_tmo(const Params& p) { __hip_atomic_store((unsigned*)p.ws + C_TMO, 1u, __ATOMIC_RELAXED, __HIP_MEMORY_SCOPE_AGENT); }

DI float row16_sum(float v) {
    v += __builtin_bit_cast(float, __builtin_amdgcn_update_dpp(0, __builtin_bit_cast(int, v), 0x121, 0xf, 0xf, true));
    v += __builtin_bit_cast(float, __builtin_amdgcn_update_dpp(0, __builtin_bit_cast(int, v), 0x122, 0xf, 0xf, true));
    v += __builtin_bit_cast(float, __builtin_amdgcn_update_dpp(0, __builtin_bit_cast(int, v), 0x124, 0xf, 0xf, true));
    v += __builtin_bit_cast(float, __builtin_amdgcn_update_dpp(0, __builtin_bit_cast(int, v), 0x128, 0xf, 0xf, true));
    return v;
}
DI void add_ctr(const Params& p, int word, unsigned v) { (void)__hip_atomic_fetch_add((unsigned*)p.ws + word, v, __ATOMIC_RELAXED, __HIP_MEMORY_SCOPE_AGENT); }
DI void wait_ctr(const Params& p, rsrc_t rw, int word, unsigned want) {
    unsigned n = SPIN_CAP;
#pragma nounroll
    for (;;) {
        asm volatile("" ::: "memory");
        if ((unsigned)__builtin_amdgcn_readfirstlane(ld4(rw, 0, word * 4)) >= want) break;
        if (--n == 0) { set_tmo(p); break; }
        if ((n & 63) == 0 && __builtin_amdgcn_readfirstlane(ld4(rw, 0, C_TMO * 4))) break;
        __builtin_amdgcn_s_sleep(1);
    }
    asm volatile("" ::: "memory");
}

template <int V> struct IC { static constexpr int value = V; };
template <int N, int I = 0, class F> DI void static_for(F&& f) { if constexpr (I < N) { f(IC<I>{}); static_for<N, I + 1>(f); } }
DI u32x2 launch_tag(rsrc_t rw) {
    const u32x2 e = ld8(rw, 0, B_EPOCH * 4);
    const unsigned lo = __builtin_amdgcn_readfirstlane(e.x) + 1u;
    return (u32x2){lo, (unsigned)__builtin_amdgcn_readfirstlane(e.y) + (lo == 0u ? 1u : 0u)};
}
DI u32x2 boot_words(rsrc_t rw) { return ld8(rw, (fresh_lane() & 15) * 8, B_INIT * 4); }
DI u32x2 vote_words(rsrc_t rw) { return ld8(rw, min(fresh_lane(), 47) * 8, B_VOTE * 4); }
DI void wait_boot(rsrc_t rw, u32x2 tag, u32x2 v) {
    for (unsigned n = SPIN_CAP; n; --n) {
        if (__builtin_amdgcn_ballot_w64(v.x != tag.x || v.y != tag.y) == 0) break;
        __builtin_amdgcn_s_sleep(1);
        asm volatile("" ::: "memory");
        v = boot_words(rw);
    }
    asm volatile("" ::: "memory");
}
DI void probe(const Params& p, LAS uchar* lds, int v, u32x2 tag) {
    const int lane = fresh_lane(), w = __builtin_amdgcn_readfirstlane(threadIdx.x >> 6);
    const rsrc_t rw = mk_rsrc(p.ws, WS_USED);
    unsigned* a = (unsigned*)(p.ws + ex_off(p) + (v >> 3) * 2097152 + EX_PROBE + (w >> 1) * 8192 + (w & 1) * EX_STEP + (v & 7) * 128);
    unsigned best = ~0u;
    for (int i = 0; i < 4; ++i) {
        const unsigned t0 = mt32();
        unsigned r = 0;
        if (lane == 0) r = __hip_atomic_fetch_add(a, 1u, __ATOMIC_RELAXED, __HIP_MEMORY_SCOPE_AGENT);
        asm volatile("s_waitcnt vmcnt(0)" :: "v"(r) : "memory");
        best = min(best, mt32() - t0);
    }
    ((LAS unsigned*)lds)[w] = best;
    bar();
    const LAS unsigned* lat = (const LAS unsigned*)lds;
    const int d = (int)min(lat[0], lat[1]) - (int)min(lat[2], lat[3]);
    st8((u32x2){tag.x, tag.y ^ ((abs(d) < 120 ? 0u : ((d > 0) == (xcc_id() >= 4) ? 1u : 2u)) << 30)}, rw, fresh_tid() == 0 ? v * 8 : OOB, B_VOTE * 4);
    bar();
}
DI unsigned read_pol(rsrc_t rw, u32x2 tag, u32x2 v) {
    for (unsigned n = SPIN_CAP; n; --n) {
        if (__builtin_amdgcn_ballot_w64(v.x != tag.x || ((v.y ^ tag.y) << 2) != 0) == 0) break;
        __builtin_amdgcn_s_sleep(1);
        asm volatile("" ::: "memory");
        v = vote_words(rw);
    }
    const unsigned c = (v.y ^ tag.y) >> 30;
    const unsigned long long m1 = __builtin_amdgcn_ballot_w64(c == 2), m0 = __builtin_amdgcn_ballot_w64(c == 1);
    int sc[6], all = 0;
#pragma unroll
    for (int r = 0; r < 6; ++r) {
        sc[r] = __builtin_popcountll((m1 >> (8 * r)) & 255) - __builtin_popcountll((m0 >> (8 * r)) & 255);
        all += r & 1 ? -sc[r] : sc[r];
    }
    const bool joint = 2 * abs(all) >= __builtin_popcountll((m0 | m1) & 0xFFFFFFFFFFFFull);
    unsigned pol = 0;
#pragma unroll
    for (int r = 0; r < 6; ++r) pol |= ((joint || sc[r] == 0 ? (r & 1 ? -all : all) : sc[r]) > 0 ? 1u : 0u) << r;
    return uni(pol);
}

template <bool FIRST>
DI void ln_tile(const Params& p, int b, u32x2 tag) {
    const int lane = fresh_lane(), w = __builtin_amdgcn_readfirstlane(threadIdx.x >> 6);
    const rsrc_t rw = mk_rsrc(p.ws, WS_USED);
    const int c = w * 4 + (lane >> 4), sub = lane & 15, row = b * 16 + c;
    u32x4 v[5]; float sum = 0.f;
#pragma unroll
    for (int k = 0; k < 5; ++k) v[k] = *(const u32x4*)(p.x + (size_t)row * 1280 + (k * 16 + sub) * 16);
    if (FIRST) {
        const unsigned tid = fresh_tid();
        st16((u32x4){0u, 0u, 0u, 0u}, rw, tid < 252 ? tid * 16 : OOB, b * 4032);
        wait_vm<0>();
        bar();
        st8(tag, rw, tid == 0 ? b * 8 : OOB, B_INIT * 4);
    }
#pragma unroll
    for (int k = 0; k < 5; ++k)
#pragma unroll
        for (int j = 0; j < 4; ++j) sum += bf_lo(v[k][j]) + bf_hi(v[k][j]);
    const float mean = row16_sum(sum) * (1.f / 640.f);
    float sq = 0.f;
#pragma unroll
    for (int k = 0; k < 5; ++k)
#pragma unroll
        for (int j = 0; j < 4; ++j) { const float a = bf_lo(v[k][j]) - mean, bb = bf_hi(v[k][j]) - mean; sq += a * a + bb * bb; }
    const float mb = rbf(mean), sd = rbf(sqrtf(rbf(rbf(row16_sum(sq) * (1.f / 640.f)) + 1e-5f))), ri = 1.f / sd;
#pragma unroll
    for (int k = 0; k < 5; ++k) {
        u32x4 o;
#pragma unroll
        for (int j = 0; j < 4; ++j) { const unsigned d = pk2(bf_lo(v[k][j]) - mb, bf_hi(v[k][j]) - mb); o[j] = pk2(bf_lo(d) * ri, bf_hi(d) * ri); }
        st16(o, rw, (k * 16 + sub) * 256 + c * 16, OFF_XF + b * 20480);
    }
    if (FIRST && w == 0) wait_boot(rw, tag, boot_words(rw));
    wait_vm<0>();
    bar();
    if (fresh_tid() == 0) add_ctr(p, C_X + (b >> 3) * 32, 1u);
}
DI void poison(const Params& p, unsigned pol, int j, int nj, int n0, int n1, int word) {
    const int tid = fresh_tid();
    const rsrc_t rw = mk_rsrc(p.ws, WS_USED);
    const unsigned exo = ex_off(p);
    const u32x4 sv = {SENT, SENT, SENT, SENT};
    for (int n = n0 + j; n < n1; n += nj) {
        const int s = min((n * 3277) >> 16, 63), i = n - 20 * s;
        u32x4 v = sv;
        if (s == 0 && i >= 16 && (i < 18 || (i == 18 && tid < 128))) {
            const int f = (i - 16) * 256 + tid, ks = f >> 6, l = f & 63, r = (l & 3) | ((l >> 1) & 4), half = (l >> 2) & 1, g = l >> 4;
            v = *(const u32x4*)(p.mem0 + (r * 640 + 64 * ks + 16 * g + 8 * half) * 2);
            v.x = nosent(v.x); v.y = nosent(v.y); v.z = nosent(v.z); v.w = nosent(v.w);
        }
        st16(v, rw, tid * 16, n < 1280 ? ex_step(exo, pol, s) + (i >> 1) * 16384 + (i & 1) * 4096
                                       : (n < 1312 ? OFF_KM + 63 * 131072 + (n - 1280) * 4096 : OFF_OT + 126 * 131072 + (n - 1312) * 4096));
    }
    wait_vm<0>();
    bar();
    if (fresh_tid() == 0) add_ctr(p, word, 1u);
}

#define SB() __builtin_amdgcn_sched_barrier(0)
DI void mfma_va(f32x4& c, u32x4 a, u32x4 b) { asm volatile("v_mfma_f32_16x16x32_bf16 %0, %1, %2, %0" : "+v"(c) : "v"(a), "a"(b)); }
DI void mfma_av(f32x4& c, u32x4 a, u32x4 b) { asm volatile("v_mfma_f32_16x16x32_bf16 %0, %1, %2, %0" : "+v"(c) : "a"(a), "v"(b)); }
DI void mfma_cacc(f32x4& c, u32x4 a, u32x4 b) { asm volatile("v_mfma_f32_16x16x32_bf16 %0, %1, %2, %0" : "+a"(c) : "v"(a), "v"(b)); }
struct P1Src {
    rsrc_t r; unsigned v0, v1, so, step;
    DI void set(const Params& p, int u, int kh, int w, int lane) {
        const uchar* base; unsigned stride, col, bytes;
        if (u < 128) { base = (u & 2) ? p.wv : p.wk; stride = 8192; col = ((u >> 2) * 128 + (u & 1) * 64) * 2; bytes = 640 * 8192; }
        else if (u < 192) { base = p.wq; stride = 8192; col = (u - 128) * 128; bytes = 640 * 8192; }
        else { base = p.w1; stride = 3456; col = (u - 192) * 128; bytes = u < 219 ? 640 * 3456 : 0; }
        r = mk_rsrc(base, bytes);
        const int rl = lane >> 3, f0 = ((rl >> 1) & 1) | (((rl >> 2) & 1) << 1);
        v0 = rl * stride + (((lane & 7) ^ (2 * f0)) << 4); v1 = v0 ^ 64;
        so = (kh * 320 + 80 * w) * stride + col; step = 8 * stride;
    }
    DI void piece(int i, unsigned ldsw) const { dma16(r, (i & 1) ? v1 : v0, so + i * step, ldsw + i * 1024); }
};
DI float mish(float a, float bias) {
    const float h = a + bias;
    const float e = __builtin_amdgcn_exp2f(fminf(h, 30.f) * 1.44269504f);
    const float n = e * (e + 2.f);
    return h * n * __builtin_amdgcn_rcpf(n + 2.f);
}
#define PT(i)
constexpr int XA_ACC = 15;
template <bool ISV, int KH>
DI void p1_slab(LAS uchar* sb, const unsigned (&tb)[4], const u32x4 (&xa)[4][20], f32x4 (&acc)[4][4], const P1Src& src, unsigned ldsn) {
    u32x4 wf[2][4];
    auto rd = [&](int kk, int nt) { const u32x2 lo = trd(sb + tb[nt] + kk * 4096), hi = trd(sb + tb[nt ^ 2] + kk * 4096 + 512); return (u32x4){lo.x, lo.y, hi.x, hi.y}; };
#pragma unroll
    for (int nt = 0; nt < 4; ++nt) wf[0][nt] = rd(0, nt);
    SB();
#pragma unroll
    for (int kk = 0; kk < 10; ++kk) {
#pragma unroll
        for (int nt = 0; nt < 4; ++nt) {
            if (kk + 1 < 10) wf[(kk + 1) & 1][nt] = rd(kk + 1, nt);
            if (kk < 5 && (nt & 1)) src.piece(2 * kk + (nt >> 1), ldsn);
#pragma unroll
            for (int t = 0; t < 4; ++t) {
                const int ks = KH * 10 + kk;
                if (ISV) { if (ks < XA_ACC) mfma_av(acc[t][nt], xa[t][ks], wf[kk & 1][nt]); else mfma_v(acc[t][nt], xa[t][ks], wf[kk & 1][nt]); }
                else { if (ks < XA_ACC) mfma_va(acc[t][nt], wf[kk & 1][nt], xa[t][ks]); else mfma_v(acc[t][nt], wf[kk & 1][nt], xa[t][ks]); }
            }
            SB();
        }
    }
    asm volatile("s_nop 7" : "+v"(acc[3][3]), "+v"(acc[2][3]), "+v"(acc[1][3]), "+v"(acc[0][3]), "+v"(acc[3][2]), "+v"(acc[2][2]), "+v"(acc[1][2]), "+v"(acc[0][2]));
#pragma unroll
    for (int t = 0; t < 4; ++t) asm volatile("" : "+v"(acc[t][0]), "+v"(acc[t][1]));
}
template <bool ISV>
DI void p1_group(const Params& p, LAS uchar* lds, rsrc_t rw, unsigned lbase, const unsigned (&tb)[4], const u32x4 (&xa)[4][20], P1Src& src, int buf,
                 int chunk, int u, int un, bool first, int w, int lane, unsigned (&pt)[5]) {
    const int g = lane >> 4;
    f32x4 acc[4][4];
#pragma unroll
    for (int t = 0; t < 4; ++t)
#pragma unroll
        for (int nt = 0; nt < 4; ++nt) { acc[t][nt] = (f32x4){0.f, 0.f, 0.f, 0.f}; asm volatile("" : "+v"(acc[t][nt])); }
    PT(0);
    if (first) wait_vm<50>(); else wait_vm<10>();
    PT(1);
    bar();
    PT(2);
    src.set(p, un, 0, w, lane);
    {
        const unsigned ln = lbase + (buf == 0 ? 2 : buf - 1) * 40960;
        p1_slab<ISV, 0>(lds + buf * 40960, tb, xa, acc, src, ln);
    }
    buf = buf == 2 ? 0 : buf + 1;
    PT(3);
    wait_vm<10>();
    PT(1);
    bar();
    PT(2);
    src.set(p, un, 1, w, lane);
    {
        const unsigned ln = lbase + (buf == 0 ? 2 : buf - 1) * 40960;
        p1_slab<ISV, 1>(lds + buf * 40960, tb, xa, acc, src, ln);
    }
    PT(3);
    const int tt0 = chunk * 16 + w * 4;
    if (ISV) {
        const int h = u >> 2;
#pragma unroll
        for (int sp = 0; sp < 2; ++sp)
#pragma unroll
            for (int nt = 0; nt < 4; ++nt) {
                const f32x4 a = acc[2 * sp][nt], b = acc[2 * sp + 1][nt];
                const u32x4 o = {pk2(a[0], a[1]), pk2(a[2], a[3]), pk2(b[0], b[1]), pk2(b[2], b[3])};
                st16(o, rw, lane * 16, OFF_VF + (((tt0 >> 1) + sp) * 32 + h) * 8192 + ((u & 1) * 4 + nt) * 1024);
            }
    } else if (u < 192) {
        const int h = u < 128 ? u >> 2 : (u - 128) >> 1;
        const int ob = u < 128 ? OFF_KF : OFF_QF;
#pragma unroll
        for (int t = 0; t < 4; ++t)
#pragma unroll
            for (int m = 0; m < 2; ++m) {
                const f32x4 a = acc[t][2 * m], b = acc[t][2 * m + 1];
                const u32x4 o = {pk2(a[0], a[1]), pk2(a[2], a[3]), pk2(b[0], b[1]), pk2(b[2], b[3])};
                st16(o, rw, lane * 16, ob + (((tt0 + t) >> 1) * 32 + h) * 8192 + ((t & 1) * 4 + (u & 1) * 2 + m) * 1024);
            }
    } else {
        const int fg = u - 192;
        u32x2 bv[4];
#pragma unroll
        for (int nt = 0; nt < 4; ++nt) bv[nt] = *(const u32x2*)(p.b1 + (fg * 64 + nt * 16 + 4 * g) * 2);
#pragma unroll
        for (int t = 0; t < 4; ++t)
#pragma unroll
            for (int m = 0; m < 2; ++m) {
                const f32x4 a = acc[t][2 * m], b = acc[t][2 * m + 1];
                const u32x2 ba = bv[2 * m], bb = bv[2 * m + 1];
                const u32x4 o = {pk2(mish(a[0], bf_lo(ba.x)), mish(a[1], bf_hi(ba.x))), pk2(mish(a[2], bf_lo(ba.y)), mish(a[3], bf_hi(ba.y))),
                                 pk2(mish(b[0], bf_lo(bb.x)), mish(b[1], bf_hi(bb.x))), pk2(mish(b[2], bf_lo(bb.y)), mish(b[3], bf_hi(bb.y)))};
                st16(o, rw, lane * 16, OFF_ACT + ((tt0 + t) * 54 + fg * 2 + m) * 1024);
                SB();
            }
    }
}
DI void ld16_a(u32x4& d, rsrc_t r, unsigned v, unsigned s) { asm volatile("buffer_load_dwordx4 %0, %1, %2, %3 offen sc1" : "=a"(d) : "v"(v), "s"(r), "s"(s) : "memory"); }
DI void ld16_v(u32x4& d, rsrc_t r, unsigned v, unsigned s) { asm volatile("buffer_load_dwordx4 %0, %1, %2, %3 offen sc1" : "=v"(d) : "v"(v), "s"(r), "s"(s) : "memory"); }
DI void ld16_s(u32x4& d, rsrc_t r, unsigned v, unsigned s) { asm volatile("buffer_load_dwordx4 %0, %1, %2, %3 offen sc1" : "=v"(d) : "v"(v), "s"(r), "s"(s) : "memory"); }
DI void p1_task(const Params& p, LAS uchar* lds, int chunk, int u0, int nu) {
    const int lane = fresh_lane(), w = __builtin_amdgcn_readfirstlane(threadIdx.x >> 6), c = lane & 15, g = lane >> 4;
    const rsrc_t rw = mk_rsrc(p.ws, WS_USED);
    const unsigned lbase = (unsigned)(size_t)lds + w * 10240;
    P1Src src;
    u32x4 xa[4][20];
    src.set(p, u0, 0, w, lane);
    if (w == 0) { wait_ctr(p, rw, C_X + chunk * 64, 8); wait_ctr(p, rw, C_X + chunk * 64 + 32, 8); }
    bar();
#pragma unroll
    for (int i = 0; i < 10; ++i) src.piece(i, lbase);
#pragma unroll
    for (int ks = 0; ks < 20; ++ks) {
        if (ks == 10) {
            src.set(p, u0, 1, w, lane);
#pragma unroll
            for (int i = 0; i < 10; ++i) src.piece(i, lbase + 40960);
        }
#pragma unroll
        for (int t = 0; t < 4; ++t) {
            const unsigned so = OFF_XF + ((chunk * 16 + w * 4 + t) * 20 + ks) * 1024;
            if (ks < XA_ACC) ld16_a(xa[t][ks], rw, lane * 16, so); else ld16_v(xa[t][ks], rw, lane * 16, so);
        }
        SB();
    }
    const int q = c >> 2, pp = c & 3, sw = (q >> 1) | ((g & 1) << 1);
    unsigned tb[4];
#pragma unroll
    for (int nt = 0; nt < 4; ++nt) tb[nt] = (8 * g + q) * 128 + 32 * (nt ^ sw) + 16 * (pp >> 1) + 8 * (pp & 1);
    int buf = 0;
    unsigned pt[5] = {0, 0, 0, 0, mt32()};
    for (int gi = 0; gi < nu; ++gi) {
        const int u = u0 + gi, un = gi + 1 < nu ? u + 1 : 219;
        if (u < 128 && (u & 2)) p1_group<true>(p, lds, rw, lbase, tb, xa, src, buf, chunk, u, un, gi == 0, w, lane, pt);
        else p1_group<false>(p, lds, rw, lbase, tb, xa, src, buf, chunk, u, un, gi == 0, w, lane, pt);
        buf = buf == 0 ? 2 : buf - 1;
    }
    wait_vm<0>();
    bar();
    const unsigned tid = fresh_tid();
    if (tid < (unsigned)nu) {
        const int u = u0 + tid;
        add_ctr(p, u < 128 ? C_KV + (chunk * 32 + (u >> 2)) * 16 : (u < 192 ? C_Q + (chunk * 32 + ((u - 128) >> 1)) * 16 : C_ACT + chunk * 32), 1u);
    }
}

#define KST(i)
#define AST_(i)
template <bool ISV>
DI void kv0_task(const Params& p, LAS uchar* lds, int u, u32x2 tag) {
    const int lane = fresh_lane(), w = __builtin_amdgcn_readfirstlane(threadIdx.x >> 6), c = lane & 15, g = lane >> 4;
    const rsrc_t rw = mk_rsrc(p.ws, WS_USED);
    const unsigned lbase = (unsigned)(size_t)lds + w * 10240;
    P1Src src;
    u32x4 xa[2][2][20];
    auto fetch = [&](int part, int i) {
        const unsigned so = OFF_XF + ((part * 8 + w * 2 + (i & 1)) * 20 + (i >> 1)) * 1024;
        if (part & 1) ld16_a(xa[1][i & 1][i >> 1], rw, lane * 16, so); else ld16_v(xa[0][i & 1][i >> 1], rw, lane * 16, so);
    };
#pragma unroll
    for (int kh = 0; kh < 2; ++kh) {
        src.set(p, u, kh, w, lane);
#pragma unroll
        for (int i = 0; i < 10; ++i) src.piece(i, lbase + kh * 40960);
    }
    KST(8);
    KST(9);
    if (w == 0) {
        wait_boot(rw, tag, boot_words(rw));
        if (fresh_tid() == 0) add_ctr(p, C_ACK, 1u);
        wait_ctr(p, rw, C_X, 8);
    }
    bar();
    KST(10);
#pragma unroll
    for (int i = 0; i < 40; ++i) fetch(0, i);
    const int q = c >> 2, pp = c & 3, sw = (q >> 1) | ((g & 1) << 1), h = u >> 2;
    unsigned tb[4];
#pragma unroll
    for (int nt = 0; nt < 4; ++nt) tb[nt] = (8 * g + q) * 128 + 32 * (nt ^ sw) + 16 * (pp >> 1) + 8 * (pp & 1);
    auto rd = [&](int i) {
        const int ks = i >> 2, nt = i & 3; LAS uchar* sb = lds + (ks / 10) * 40960 + (ks % 10) * 4096;
        const u32x2 lo = trd(sb + tb[nt]), hi = trd(sb + tb[nt ^ 2] + 512); return (u32x4){lo.x, lo.y, hi.x, hi.y};
    };
    static_for<4>([&](auto pc) {
        constexpr int part = pc.value, b = part & 1;
        f32x4 acc[2][4];
#pragma unroll
        for (int i = 0; i < 8; ++i) { acc[i >> 2][i & 3] = (f32x4){0.f, 0.f, 0.f, 0.f}; asm volatile("" : "+v"(acc[i >> 2][i & 3])); }
        if (part < 2) KST(part * 4 + 0);
        if (part < 3 && w == 0) wait_ctr(p, rw, C_X + 32 * (part + 1), 8);
        wait_vm<0>();
#pragma unroll
        for (int ks = 0; ks < 20; ++ks) { if (b) asm volatile("" : "+a"(xa[b][0][ks]), "+a"(xa[b][1][ks])); else asm volatile("" : "+v"(xa[b][0][ks]), "+v"(xa[b][1][ks])); }
        bar();
        if (part > 0 && fresh_tid() == 0) add_ctr(p, (b ? C_KVH : C_KV) + (((part - 1) >> 1) * 32 + h) * 16, 1u);
        if (part < 2) KST(part * 4 + 1);
        u32x4 wf[8];
#pragma unroll
        for (int i = 0; i < 8; ++i) wf[i] = rd(i);
        SB();
#pragma unroll
        for (int i = 0; i < 80; ++i) {
#pragma unroll
            for (int t = 0; t < 2; ++t) {
                if (ISV) { if (b) mfma_av(acc[t][i & 3], xa[b][t][i >> 2], wf[i & 7]); else mfma_v(acc[t][i & 3], xa[b][t][i >> 2], wf[i & 7]); }
                else { if (b) mfma_va(acc[t][i & 3], wf[i & 7], xa[b][t][i >> 2]); else mfma_v(acc[t][i & 3], wf[i & 7], xa[b][t][i >> 2]); }
            }
            if (i + 8 < 80) wf[i & 7] = rd(i + 8);
            if (part < 3 && i < 40) fetch(part + 1, i);
            SB();
        }
        asm volatile("s_nop 7" : "+v"(acc[0][0]), "+v"(acc[0][1]), "+v"(acc[0][2]), "+v"(acc[0][3]), "+v"(acc[1][0]), "+v"(acc[1][1]), "+v"(acc[1][2]), "+v"(acc[1][3]));
        if (part < 2) KST(part * 4 + 2);
        const unsigned ob = (ISV ? OFF_VF : OFF_KF) + ((part * 4 + w) * 32 + h) * 8192;
#pragma unroll
        for (int i = 0; i < 4; ++i) {
            const f32x4 a = ISV ? acc[0][i] : acc[i >> 1][2 * (i & 1)], bb = ISV ? acc[1][i] : acc[i >> 1][2 * (i & 1) + 1];
            const u32x4 o = {pk2(a[0], a[1]), pk2(a[2], a[3]), pk2(bb[0], bb[1]), pk2(bb[2], bb[3])};
            st16(o, rw, lane * 16, ob + (ISV ? (u & 1) * 4 + i : (i >> 1) * 4 + (u & 1) * 2 + (i & 1)) * 1024);
        }
        if (part < 2) KST(part * 4 + 3);
        SB();
    });
    wait_vm<0>();
    bar();
    if (fresh_tid() == 0) add_ctr(p, C_KV + (32 + h) * 16, 1u);
}

template <bool WO>
DI void gemm_task(const Params& p, LAS uchar* lds, int chunk, int cg) {
    constexpr int KS = WO ? 4 : 3, NS = WO ? 32 : 18, NP = 2 * KS, IMG = KS * 4096, NKF = KS * NS, XOFF = WO ? OFF_OT : OFF_ACT;
    const int lane = fresh_lane(), w = __builtin_amdgcn_readfirstlane(threadIdx.x >> 6), c = lane & 15, g = lane >> 4;
    const rsrc_t rw = mk_rsrc(p.ws, WS_USED), r2 = mk_rsrc(WO ? p.wo : p.w2, NKF * 32 * 1280);
    const unsigned lbase = (unsigned)(size_t)lds + (w >> 1) * IMG + (w & 1) * NP * 1024;
    const int rl = lane >> 3, f0 = ((rl >> 1) & 1) | (((rl >> 2) & 1) << 1);
    const unsigned v0 = rl * 1280 + (((lane & 7) ^ (2 * f0)) << 4), v1 = v0 ^ 64;
    const unsigned wbase = (w & 1) * NP * 8 * 1280 + cg * 256 + (w >> 1) * 128;
    auto piece = [&](int sl, int buf, int i) { dma16(r2, (i & 1) ? v1 : v0, wbase + (sl * KS * 32 + 8 * i) * 1280, lbase + buf * 2 * IMG + i * 1024); };
    const int tt0 = chunk * 16 + w * 4;
    u32x4 xb[2][KS][4];
    auto frag = [&](int par, int sl, int i) { ld16_s(xb[par][i >> 2][i & 3], rw, lane * 16, XOFF + ((tt0 + (i & 3)) * NKF + sl * KS + (i >> 2)) * 1024); };
#pragma unroll
    for (int i = 0; i < NP; ++i) piece(0, 0, i);
    if (w == 0) {
        if (WO) {
            wait_ctr(p, rw, C_G + (chunk * 5 + cg) * 32, 1);
            for (int i = 0; i < 8; ++i) wait_ctr(p, rw, C_OT + (chunk * 8 + i) * 32, 32);
        } else wait_ctr(p, rw, C_ACT + chunk * 32, 27);
    }
    bar();
#pragma unroll
    for (int i = 0; i < 4 * KS; ++i) frag(0, 0, i);
#pragma unroll
    for (int i = 0; i < NP; ++i) piece(1, 1, i);
    const int q = c >> 2, pp = c & 3, sw = (q >> 1) | (((g & 1) ^ (g >> 1)) << 1);
    unsigned tb[4];
#pragma unroll
    for (int nt = 0; nt < 4; ++nt) tb[nt] = (4 * g + q) * 128 + 32 * (nt ^ sw) + 16 * (pp >> 1) + 8 * (pp & 1);
    f32x4 acc[4][8];
#pragma unroll
    for (int t = 0; t < 4; ++t)
#pragma unroll
        for (int nt = 0; nt < 8; ++nt) { acc[t][nt] = (f32x4){0.f, 0.f, 0.f, 0.f}; asm volatile("" : "+a"(acc[t][nt])); }
    int buf = 0;
    for (int s2 = 0; s2 < NS / 2; ++s2) {
#pragma unroll
        for (int par = 0; par < 2; ++par) {
            const int sl = 2 * s2 + par;
            wait_vm<NP>();
#pragma unroll
            for (int kk = 0; kk < KS; ++kk) asm volatile("" : "+v"(xb[par][kk][0]), "+v"(xb[par][kk][1]), "+v"(xb[par][kk][2]), "+v"(xb[par][kk][3]));
            bar();
            const int sn = min(sl + 1, NS - 1), sp = min(sl + 2, NS - 1), bn = buf == 0 ? 2 : buf - 1;
            LAS uchar* sb = lds + buf * 2 * IMG;
            auto rd = [&](int i) { LAS uchar* a = sb + ((i >> 2) & 1) * IMG + tb[i & 3] + (i >> 3) * 4096; const u32x2 lo = trd(a), hi = trd(a + 2048); return (u32x4){lo.x, lo.y, hi.x, hi.y}; };
            u32x4 wf[4];
#pragma unroll
            for (int i = 0; i < 4; ++i) wf[i] = rd(i);
            SB();
#pragma unroll
            for (int i = 0; i < 8 * KS; ++i) {
                const u32x4 cur = wf[i & 3];
                mfma_cacc(acc[0][i & 7], cur, xb[par][i >> 3][0]);
                if (i < 4 * KS) frag(par ^ 1, sn, i); else if (i < 4 * KS + NP) piece(sp, bn, i - 4 * KS);
#pragma unroll
                for (int t = 1; t < 4; ++t) mfma_cacc(acc[t][i & 7], cur, xb[par][i >> 3][t]);
                if (i + 4 < 8 * KS) wf[i & 3] = rd(i + 4);
                SB();
            }
            if (par) asm volatile("s_nop 7");
            SB();
            buf = buf == 2 ? 0 : buf + 1;
        }
    }
    wait_vm<0>();
#pragma unroll
    for (int kk = 0; kk < KS; ++kk) asm volatile("" :: "v"(xb[0][kk][0]), "v"(xb[0][kk][1]), "v"(xb[0][kk][2]), "v"(xb[0][kk][3]));
    SB();
#pragma unroll
    for (int t = 0; t < 4; ++t) {
        if (WO) {
            const size_t xo = (size_t)((tt0 + t) * 16 + c) * 1280 + cg * 256 + g * 8;
            u32x4 gv[4]; u32x2 xv[8];
#pragma unroll
            for (int m = 0; m < 4; ++m) gv[m] = ld16(rw, lane * 16, OFF_GF + ((tt0 + t) * 20 + cg * 4 + m) * 1024);
#pragma unroll
            for (int nt = 0; nt < 8; ++nt) xv[nt] = *(const u32x2*)(p.x + xo + nt * 32);
#pragma unroll
            for (int nt = 0; nt < 8; ++nt) {
                const f32x4 a = acc[t][nt];
                const unsigned g0 = gv[nt >> 1][2 * (nt & 1)], g1 = gv[nt >> 1][2 * (nt & 1) + 1], s0 = pk2(a[0], a[1]), s1 = pk2(a[2], a[3]);
                const unsigned m0 = pk2(bf_lo(s0) * bf_lo(g0), bf_hi(s0) * bf_hi(g0)), m1 = pk2(bf_lo(s1) * bf_lo(g1), bf_hi(s1) * bf_hi(g1));
                *(u32x2*)(p.out + xo + nt * 32) = (u32x2){pk2(bf_lo(xv[nt].x) + bf_lo(m0), bf_hi(xv[nt].x) + bf_hi(m0)), pk2(bf_lo(xv[nt].y) + bf_lo(m1), bf_hi(xv[nt].y) + bf_hi(m1))};
            }
        } else {
#pragma unroll
            for (int m = 0; m < 4; ++m) {
                float y[8];
#pragma unroll
                for (int r = 0; r < 8; ++r) { const float a = rbf(acc[t][2 * m + (r >> 2)][r & 3]); y[r] = a * __builtin_amdgcn_rcpf(1.f + __builtin_amdgcn_exp2f(-a * 1.44269504f)); }
                const u32x4 o = {pk2(y[0], y[1]), pk2(y[2], y[3]), pk2(y[4], y[5]), pk2(y[6], y[7])};
                st16(o, rw, lane * 16, OFF_GF + ((tt0 + t) * 20 + cg * 4 + m) * 1024);
            }
        }
        SB();
    }
    wait_vm<0>();
    bar();
    if (!WO && fresh_tid() == 0) add_ctr(p, C_G + (chunk * 5 + cg) * 32, 1u);
}

#define XST(cond, idx)
constexpr float SCL = 0.08838834764831845f * 1.4426950408889634f;
DI void attn_task(const Params& p, int s, int h, bool new_chunk) {
    const int lane = fresh_lane(), c = lane & 15, g = lane >> 4;
    const rsrc_t rw = mk_rsrc(p.ws, WS_USED);
    const int sh = (s * 32 + h);
    u32x4 qf[2][4], kf[2][4], vf[8], km[4]; u32x2 vm[8];
    if (new_chunk) {
        wait_ctr(p, rw, C_Q + ((s >> 3) * 32 + h) * 16, 2);
        wait_ctr(p, rw, C_KV + ((s >> 3) * 32 + h) * 16, 4);
    }
#pragma unroll
    for (int i = 0; i < 8; ++i) qf[i >> 2][i & 3] = ld16(rw, lane * 16, OFF_QF + sh * 8192 + i * 1024);
#pragma unroll
    for (int i = 0; i < 8; ++i) kf[i >> 2][i & 3] = ld16(rw, lane * 16, OFF_KF + sh * 8192 + i * 1024);
#pragma unroll
    for (int i = 0; i < 8; ++i) vf[i] = ld16(rw, lane * 16, OFF_VF + sh * 8192 + i * 1024);
    const float NINF = -__builtin_inff();
    const f32x4 zero = {0.f, 0.f, 0.f, 0.f};
    float t[2][12], m[2];
    {
        f32x4 s0[2] = {zero, zero}, s1 = zero;
#pragma unroll
        for (int ks = 0; ks < 4; ++ks) { s0[0] = mfma32(kf[0][ks], qf[0][ks], s0[0]); s0[1] = mfma32(kf[0][ks], qf[1][ks], s0[1]); s1 = mfma32(kf[1][ks], qf[1][ks], s1); }
#pragma unroll
        for (int r = 0; r < 4; ++r) {
            const bool diag = 4 * g + r <= c;
            t[0][r] = diag ? s0[0][r] * SCL : NINF; t[1][r] = s0[1][r] * SCL; t[1][4 + r] = diag ? s1[r] * SCL : NINF;
        }
        m[0] = fmaxf(fmaxf(t[0][0], t[0][1]), fmaxf(t[0][2], t[0][3]));
        m[1] = fmaxf(fmaxf(fmaxf(t[1][0], t[1][1]), fmaxf(t[1][2], t[1][3])), fmaxf(fmaxf(t[1][4], t[1][5]), fmaxf(t[1][6], t[1][7])));
#pragma unroll
        for (int r = 0; r < 4; ++r) asm volatile("" : "+v"(t[0][r]), "+v"(t[1][r]), "+v"(t[1][4 + r]));
        asm volatile("" : "+v"(m[0]), "+v"(m[1]));
#pragma unroll
        for (int i = 0; i < 8; ++i) asm volatile("" : "+v"(vf[i]));
    }
    u32x4 one4 = {0x3F803F80u, 0x3F803F80u, 0x3F803F80u, 0x3F803F80u};
    const float z2 = g >= 2 ? 0.f : NINF;
    f32x4 s2[2] = {{z2, z2, z2, z2}, {z2, z2, z2, z2}};
    asm volatile("" : "+v"(s2[0]), "+v"(s2[1]));
    XST(s == 63, 64 + h * 4);
    wait_ctr(p, rw, C_KM + h * 32 + (s == NSEG - 1), s + 1);
    XST(s == 63, 64 + h * 4 + 1);
    for (unsigned n = SPIN_CAP;;) {
        asm volatile("" ::: "memory");
#pragma unroll
        for (int i = 0; i < 4; ++i) km[i] = ld16(rw, (g * 8 + (c & 7)) * 16, OFF_KM + sh * 4096 + i * 512);
#pragma unroll
        for (int i = 0; i < 8; ++i) vm[i] = ld8(rw, (lane & 31) * 8, OFF_KM + sh * 4096 + 2048 + i * 256);
        if (s < NSEG - 1) break;
        unsigned mx = 0;
#pragma unroll
        for (int i = 0; i < 4; ++i) mx = mx4(mx, km[i]);
#pragma unroll
        for (int i = 0; i < 8; ++i) mx = max(mx, max(vm[i].x, vm[i].y));
        if (__builtin_amdgcn_ballot_w64(mx == SENT) == 0) break;
        if (--n == 0) { set_tmo(p); break; }
    }
    SB();
    SB();
#pragma unroll
    for (int ks = 0; ks < 4; ++ks)
#pragma unroll
        for (int qt = 0; qt < 2; ++qt) mfma_v(s2[qt], km[ks], qf[qt][ks]);
    asm volatile("s_nop 7" : "+v"(s2[0]), "+v"(s2[1]));
    u32x4 p01[2], p2[2];
#pragma unroll
    for (int qt = 0; qt < 2; ++qt) {
#pragma unroll
        for (int r = 0; r < 4; ++r) t[qt][8 + r] = s2[qt][r] * SCL;
        m[qt] = fmaxf(fmaxf(m[qt], fmaxf(t[qt][8], t[qt][9])), fmaxf(t[qt][10], t[qt][11]));
    }
#pragma unroll
    for (int qt = 0; qt < 2; ++qt) m[qt] = grp_max(m[qt]);
#pragma unroll
    for (int qt = 0; qt < 2; ++qt) {
#pragma unroll
        for (int r = 0; r < 12; ++r) if (qt == 1 || r < 4 || r >= 8) t[qt][r] = __builtin_amdgcn_exp2f(t[qt][r] - m[qt]);
        p01[qt] = (u32x4){pk2(t[qt][0], t[qt][1]), pk2(t[qt][2], t[qt][3]), qt ? pk2(t[qt][4], t[qt][5]) : 0u, qt ? pk2(t[qt][6], t[qt][7]) : 0u};
        p2[qt] = (u32x4){pk2(t[qt][8], t[qt][9]), pk2(t[qt][10], t[qt][11]), 0u, 0u};
    }
    u32x4 vq[4];
#pragma unroll
    for (int i = 0; i < 4; ++i) vq[i] = (u32x4){vm[2 * i].x, vm[2 * i].y, vm[2 * i + 1].x, vm[2 * i + 1].y};
    u32x4 p2h[2] = {{0u, 0u, p2[0].x, p2[0].y}, {0u, 0u, p2[1].x, p2[1].y}};
    asm volatile("s_nop 1" : "+v"(p01[0]), "+v"(p01[1]), "+v"(p2[0]), "+v"(p2[1]), "+v"(p2h[0]), "+v"(p2h[1]), "+v"(vq[0]), "+v"(vq[1]), "+v"(vq[2]), "+v"(vq[3]), "+v"(one4));
    f32x4 l[2], o[2][8];
#pragma unroll
    for (int qt = 0; qt < 2; ++qt) mfma_0(l[qt], one4, p01[qt]);
#pragma unroll
    for (int qt = 0; qt < 2; ++qt) mfma_v(l[qt], one4, p2[qt]);
#pragma unroll
    for (int et = 0; et < 8; ++et)
#pragma unroll
        for (int qt = 0; qt < 2; ++qt) { mfma_0(o[qt][et], vf[et], p01[qt]); mfma_v(o[qt][et], vq[et >> 1], et & 1 ? p2h[qt] : p2[qt]); }
    asm volatile("s_nop 3" : "+v"(l[0]), "+v"(l[1]), "+v"(o[0][0]), "+v"(o[0][1]), "+v"(o[1][0]), "+v"(o[1][1]));
    const float inv[2] = {__builtin_amdgcn_rcpf(l[0][0]), __builtin_amdgcn_rcpf(l[1][0])};
#pragma unroll
    for (int tp = 0; tp < 4; ++tp) {
        asm volatile("" : "+v"(o[0][2 * tp]), "+v"(o[0][2 * tp + 1]), "+v"(o[1][2 * tp]), "+v"(o[1][2 * tp + 1]));
#pragma unroll
        for (int qt = 0; qt < 2; ++qt) {
            const f32x4 a = o[qt][2 * tp] * inv[qt], b = o[qt][2 * tp + 1] * inv[qt];
            const u32x4 ov = {nosent(pk2(a[0], a[1])), nosent(pk2(a[2], a[3])), nosent(pk2(b[0], b[1])), nosent(pk2(b[2], b[3]))};
            st16(ov, rw, lane * 16, OFF_OT + ((2 * s + qt) * 32 + h) * 4096 + tp * 1024);
        }
        SB();
    }
    if (s == NSEG - 1) st4(1u, rw, lane == 0 ? (C_OF + (h >> 3) * 32 + (h & 7)) * 4 : OOB, 0);
    XST(s == 63, 64 + h * 4 + 2);
    wait_vm<0>();
    XST(s == 63, 64 + h * 4 + 3);
    if (lane == 0) add_ctr(p, C_OT + s * 32, 1u);
}

constexpr int N_T = 24, N_WG = 4, W_FIRST = 64;
DI void wo_stage(const Params& p, unsigned ldsw, int h0, int cs, int lane) {
    const rsrc_t ro = mk_rsrc(p.wo, 4096 * 1280);
    const unsigned v = (lane >> 2) * 1280 + (lane & 3) * 16;
    const unsigned so = uni(h0 * 128 * 1280 + cs * 64);
    ldsw = uni(ldsw);
#pragma unroll
    for (int i = 0; i < 16; ++i) dma16(ro, v, so + 16 * i * 1280, ldsw + i * 1024);
}
DI void w_role(const Params& p, LAS uchar* lds, int cs, int rank) {
    const int lane = fresh_lane(), w = __builtin_amdgcn_readfirstlane(threadIdx.x >> 6), c = lane & 15, g = lane >> 4;
    const rsrc_t rw = mk_rsrc(p.ws, WS_USED);
    u32x4 wf[8][4][2];
    {
        LAS uchar* lw = lds + w * 32768;
        const int q = c >> 2, pp = c & 3;
        wo_stage(p, (unsigned)(size_t)lw, 8 * w, cs, lane);
        wo_stage(p, (unsigned)(size_t)lw + 16384, 8 * w + 2, cs, lane);
#pragma unroll
        for (int rd = 0; rd < 4; ++rd) {
            if (rd < 3) wait_vm<16>(); else wait_vm<0>();
#pragma unroll
            for (int i = 0; i < 2; ++i)
#pragma unroll
                for (int tp = 0; tp < 4; ++tp)
#pragma unroll
                    for (int nt = 0; nt < 2; ++nt) {
                        LAS uchar* a = lw + (rd & 1) * 16384 + i * 8192 + (32 * tp + 4 * g + q) * 64 + nt * 32 + pp * 8;
                        const u32x2 lo = trd(a), hi = trd(a + 1024);
                        wf[2 * rd + i][tp][nt] = (u32x4){lo.x, lo.y, hi.x, hi.y};
                    }
            SB();
#pragma unroll
            for (int i = 0; i < 16; ++i) { if (2 * rd + (i >> 3) < 7) asm volatile("" : "+a"(wf[2 * rd + (i >> 3)][(i >> 1) & 3][i & 1])); else asm volatile("" : "+v"(wf[2 * rd + (i >> 3)][(i >> 1) & 3][i & 1])); }
            asm volatile("s_waitcnt lgkmcnt(0)" ::: "memory");
            SB();
            if (rd < 2) wo_stage(p, (unsigned)(size_t)lw + (rd & 1) * 16384, 8 * w + 2 * rd + 4, cs, lane);
        }
    }
    bar();
    LAS f32x2* red = (LAS f32x2*)lds;
    volatile LAS int* nxt = (volatile LAS int*)(lds + 16384);
    const rsrc_t rx = mk_rsrc(p.x, S_ * 1280), ro = mk_rsrc(p.out, S_ * 1280);
    unsigned tk = 0;
    const unsigned wto = lane == 0 ? (C_WT + cs * 32) * 4 : OOB;
    auto draw = [&]() { tk = 1; asm volatile("buffer_atomic_add %0, %1, %2, 0 offen sc0" : "+v"(tk) : "v"(wto), "s"(rw) : "memory"); };
    auto tile_of = [&](int ticket, int cur) { return ticket + W_FIRST < 120 ? ticket + W_FIRST : (cur < 120 ? 120 + rank : (cur < 124 ? (rank < 2 ? 126 : 122) + rank : 128)); };
    u32x4 ob[8][4];
    int tt = 0;
    bool have = false, careful = false;
    if (w == 0) { draw(); asm volatile("s_waitcnt vmcnt(0)" : "+v"(tk)); *nxt = tile_of(__builtin_amdgcn_readfirstlane(tk), 0); draw(); }
    bar();
    tt = __builtin_amdgcn_readfirstlane(*nxt);
    while (tt < 2 * NSEG) {
        if (!have) {
            if (w == 0) { wait_ctr(p, rw, C_G + ((tt >> 4) * 5 + (cs >> 2)) * 32, 1); if (tt < 126 || careful) wait_ctr(p, rw, C_OT + (tt >> 1) * 32, 32); }
            bar();
            if (tt >= 126 && !careful) {
                for (unsigned n = SPIN_CAP;;) {
                    asm volatile("" ::: "memory");
                    if (__builtin_amdgcn_ballot_w64(ld4(rw, (lane & 7) * 4, (C_OF + w * 32) * 4) == 0) == 0) break;
                    if (--n == 0) { set_tmo(p); break; }
                }
            }
            XST(w == 0 && tt >= 126, 256 + ((tt - 126) * 20 + cs) * 4);
#pragma unroll
            for (int i = 0; i < 32; ++i) ld16_s(ob[i >> 2][i & 3], rw, lane * 16, OFF_OT + (tt * 32 + 8 * w) * 4096 + i * 1024);
        }
        f32x4 acc[2][2];
#pragma unroll
        for (int i = 0; i < 4; ++i) { acc[i >> 1][i & 1] = (f32x4){0.f, 0.f, 0.f, 0.f}; asm volatile("" : "+v"(acc[i >> 1][i & 1])); }
        const unsigned xo = c * 1280 + g * 8, xs = tt * 16 * 1280 + cs * 64 + (w >> 1) * 32 + (w & 1) * 4;
        unsigned xv, gv;
        asm volatile("buffer_load_dword %0, %1, %2, %3 offen" : "=v"(xv) : "v"(xo), "s"(rx), "s"(xs) : "memory");
        asm volatile("buffer_load_dword %0, %1, %2, %3 offen sc1" : "=v"(gv) : "v"(lane * 16), "s"(rw), "s"(OFF_GF + (tt * 20 + cs) * 1024 + w * 4) : "memory");
        if (w == 0) {
            asm volatile("s_waitcnt vmcnt(32)" : "+v"(tk));
            const int tn = tile_of(__builtin_amdgcn_readfirstlane(tk), tt);
            int rd = 0;
            if (tn < 126) {
                const unsigned f = ld4(rw, (lane == 0 ? C_G + ((tn >> 4) * 5 + (cs >> 2)) * 32 : C_OT + (tn >> 1) * 32) * 4, 0);
                rd = __builtin_amdgcn_ballot_w64(f < (lane == 0 ? 1u : 32u)) == 0 ? 0x10000 : 0;
            }
            nxt[1] = tn | rd;
        }
        bar();
        XST(w == 0 && tt >= 126, 256 + ((tt - 126) * 20 + cs) * 4 + 1);
        const int nx = __builtin_amdgcn_readfirstlane(nxt[1]);
        have = (nx >> 16) != 0;
        const rsrc_t rn = mk_rsrc(p.ws, have ? WS_USED : 0);
        const unsigned nb = OFF_OT + ((nx & 127) * 32 + 8 * w) * 4096;
        if (w == 0) draw();
#pragma unroll
        for (int i = 0; i < 8; ++i) {
            asm volatile("s_waitcnt vmcnt(28)" : "+v"(ob[i][0]), "+v"(ob[i][1]), "+v"(ob[i][2]), "+v"(ob[i][3]));
#pragma unroll
            for (int tp = 0; tp < 4; ++tp)
#pragma unroll
                for (int n = 0; n < 2; ++n) { if (i < 7) mfma_av(acc[tp & 1][n], wf[i][tp][n], ob[i][tp]); else mfma_v(acc[tp & 1][n], wf[i][tp][n], ob[i][tp]); }
#pragma unroll
            for (int tp = 0; tp < 4; ++tp) ld16_s(ob[i][tp], rn, lane * 16, nb + (i * 4 + tp) * 1024);
            SB();
        }
        mfma_pad(acc[0][0], acc[0][1], acc[1][0], acc[1][1]);
#pragma unroll
        for (int n = 0; n < 2; ++n) {
            const f32x4 a = acc[0][n] + acc[1][n];
            red[((2 * n) * 4 + w) * 64 + lane] = (f32x2){a[0], a[1]};
            red[((2 * n + 1) * 4 + w) * 64 + lane] = (f32x2){a[2], a[3]};
        }
        bar();
        const f32x2 r = (red[(w * 4 + 0) * 64 + lane] + red[(w * 4 + 1) * 64 + lane]) + (red[(w * 4 + 2) * 64 + lane] + red[(w * 4 + 3) * 64 + lane]);
        asm volatile("s_waitcnt vmcnt(32)" : "+v"(xv), "+v"(gv));
        const bool redo = tt >= 126 && !careful && __builtin_amdgcn_ballot_w64(r[0] != r[0]) != 0;
        const unsigned sv = pk2(r[0], r[1]), m = pk2(bf_lo(sv) * bf_lo(gv), bf_hi(sv) * bf_hi(gv));
        __builtin_amdgcn_raw_buffer_store_b32(pk2(bf_lo(xv) + bf_lo(m), bf_hi(xv) + bf_hi(m)), ro, xo, xs, 0);
        XST(w == 0 && tt >= 126, 256 + ((tt - 126) * 20 + cs) * 4 + 2);
        if (!have) {
            wait_vm<0>();
#pragma unroll
            for (int i = 0; i < 8; ++i) asm volatile("" : "+v"(ob[i][0]), "+v"(ob[i][1]), "+v"(ob[i][2]), "+v"(ob[i][3]));
        }
        careful = redo;
        if (!redo) tt = nx & 0xFFFF;
    }
}

DI void smf_x(f32x4& c, u32x4 a, u32x8 b, int idx, bool acc) { if (acc) smf_a(c, a, b, idx); else smf_v(c, a, b, idx); }
#define TRY()
#define V_A 64
#define V_M 56
#define KM_DELAY 240
#define PAD0 0
#define PAD_UP 0
#define PAD_MAX 48
#define RISE 64
#define DECAY 8
#ifndef M_DELAY
#define M_DELAY 0
#endif
#define L_SMALL 96
#define F_SMALL 280
#ifndef M_TAIL
#define M_TAIL 40
#endif
#define EDGES 2
#define VPRE 2
#define D_MST 300
#define F_LATE 800
#define V_LATE 8
#define DUR_A0 940
#define DUR_M0 680
#define FT(i)
#define FDUMP(blk, s, w)
#define STAT(blk, s, w, a, b, c, d)
DI int wave_max(int d) {
    d = max(d, __builtin_amdgcn_update_dpp(0, d, 0x121, 0xf, 0xf, true));
    d = max(d, __builtin_amdgcn_update_dpp(0, d, 0x122, 0xf, 0xf, true));
    d = max(d, __builtin_amdgcn_update_dpp(0, d, 0x124, 0xf, 0xf, true));
    d = max(d, __builtin_amdgcn_update_dpp(0, d, 0x128, 0xf, 0xf, true));
    auto r = __builtin_amdgcn_permlane32_swap((unsigned)d, (unsigned)d, false, false); d = max((int)r[0], (int)r[1]);
    auto q = __builtin_amdgcn_permlane16_swap((unsigned)d, (unsigned)d, false, false); d = max((int)q[0], (int)q[1]);
    return __builtin_amdgcn_readfirstlane(d);
}
struct Clock {
    unsigned r0, m0, cq, qc;
    DI void edge(unsigned& r, unsigned& m) {
        unsigned long long a, b;
        unsigned last = rt32();
#pragma nounroll
        for (int i = 0; i < EDGES; ++i) {
            do asm volatile("s_memrealtime %0\n\ts_memtime %1\n\ts_waitcnt lgkmcnt(0)" : "=s"(a), "=s"(b) :: "memory"); while ((unsigned)a == last);
            last = (unsigned)a;
            if (i == 0) { r = last << 3; m = (unsigned)b; }
            const unsigned c = (unsigned)b - ((((last << 3) - r) * cq) >> 10);
            if ((int)(c - m) < 0) m = c;
        }
        m -= 8;
    }
    DI void start() { cq = 3000; qc = 22370; edge(r0, m0); }
    DI void sync() {
        unsigned r, m; edge(r, m);
        const float f = (float)(m - m0) * __builtin_amdgcn_rcpf((float)(r - r0));
        asm volatile("" : "+s"(r), "+s"(m));
        cq = __builtin_amdgcn_readfirstlane((unsigned)(f * 1024.f));
        qc = __builtin_amdgcn_readfirstlane((unsigned)(65536.f * __builtin_amdgcn_rcpf(f)));
        r0 = r; m0 = m;
    }
    DI unsigned at(unsigned m) const { return r0 + (unsigned)(((unsigned long long)(m - m0) * qc) >> 16); }
    DI static void sample(unsigned long long& a, unsigned long long& b) { asm volatile("s_memrealtime %0\n\ts_memtime %1\n\ts_waitcnt lgkmcnt(0)" : "=s"(a), "=s"(b) :: "memory"); }
    DI unsigned value(unsigned long long a, unsigned long long b) const {
        const unsigned lo = (unsigned)a << 3;
        return lo + __builtin_amdgcn_readfirstlane(min(max((int)(at((unsigned)b) - lo), 0), 31));
    }
    DI unsigned now() const { unsigned long long a, b; sample(a, b); return value(a, b); }
    DI unsigned cycles(unsigned t) const { const int d = __builtin_amdgcn_readfirstlane(min(max((int)(t - r0), 0), 1 << 12)); return m0 + (((unsigned)d * cq) >> 10); }
};
DI unsigned waitmt(unsigned t) { unsigned n; do n = mt32(); while ((int)(n - t) < 0); return n; }
DI unsigned wait_exact(unsigned t) {
    unsigned n;
    do n = mt32(); while ((int)(t - n) > 160);
    const int k = min(max((int)(t - n - 60) >> 2, 0), 32);
    asm volatile("s_getpc_b64 s[20:21]\n\ts_add_u32 s20, s20, %0\n\ts_addc_u32 s21, s21, 0\n\ts_setpc_b64 s[20:21]\n\t.rept 32\n\ts_nop 0\n\t.endr" :: "s"(uni(140 - 4 * k)) : "s20", "s21", "scc");
    return n;
}
#define MST_SH 2
DI unsigned mst_off(int m) { return (m >> MST_SH) * 128 + (m & ((1 << MST_SH) - 1)) * 4; }
struct Sched {
    unsigned tgo, tau, durA, durM;
    unsigned a, v;
    DI void fetch_a(rsrc_t rw, unsigned exo, unsigned pol, int s, int lane) { a = ld4(rw, s >= 2 ? (lane & 31) * 4 : OOB, ex_step(exo, pol, max(s - 2, 0)) + EX_AST); }
    DI void fetch_m(rsrc_t rw, unsigned exo, unsigned pol, int s, int lane) { v = ld4(rw, mst_off(min(lane, 39)), ex_step(exo, pol, s - 1) + EX_MST); }
    DI void take_a() { const int m = wave_max((int)a); durA += __builtin_amdgcn_ballot_w64(a - 1u >= 0xFFFFu) == 0 ? (min(m - (int)durA, RISE) + 2) >> 2 : 0; }
    DI void next(const Params& p, rsrc_t rw, unsigned exo, unsigned pol, int s, int lane, unsigned long long& alive) {
        if ((__builtin_amdgcn_ballot_w64(v == SENT) & alive) != 0) {
            const unsigned mo = ex_step(exo, pol, s - 1) + EX_MST;
            unsigned n = SPIN_CAP; bool bad;
#pragma nounroll
            do {
                asm volatile("" ::: "memory");
                v = ld4(rw, mst_off(min(lane, 39)), mo);
                bad = __builtin_amdgcn_ballot_w64(v == SENT) != 0;
            } while (bad && --n);
            if (bad) { alive = 0; set_tmo(p); }
        }
        const unsigned ref = __builtin_amdgcn_readfirstlane(v);
        const unsigned pm = ref + (unsigned)wave_max((int)(v - ref));
        if (s >= 2 && __builtin_amdgcn_ballot_w64(v & 1) == 0) durM += (min((int)(pm - tau - durM), RISE) + 2) >> 2;
        const bool late = s == 1 || (int)(pm + V_LATE - tgo) > 0;
        tau = (late ? pm + F_LATE : tgo) + durA;
        tgo = tau + durM + V_A;
    }
};

#define PACE 8
DI void pace() { asm volatile("s_nop %0" :: "n"(PACE)); }
DI void role_M(const Params& p, LAS uchar* lds, int mi, bool started, u32x2 tag) {
    const int tid = threadIdx.x, lane = tid & 63, w = __builtin_amdgcn_readfirstlane(tid >> 6), c = lane & 15, g = lane >> 4;
    const int qh = mi & 1, cs = mi >> 1;
    const rsrc_t rw = mk_rsrc(p.ws, WS_USED);
    u32x8 wf[8][2][2];
    {
        LAS uchar* lw = lds + w * 32768;
        const int q = c >> 2, pp = c & 3;
        unsigned ra[4];
#pragma unroll
        for (int m = 0; m < 4; ++m) {
            const int k0 = m < 2 ? 8 * g + 4 * m : 32 + 8 * g + 4 * (m - 2), ga = k0 >> 4, qq = (k0 >> 2) & 3;
            const int e = 32 * ga + 2 * qq + (q & 1) + 8 * (q >> 1);
            ra[m] = e * 64 + pp * 8;
        }
        if (w == 0 || !started) {
            wo_stage(p, (unsigned)(size_t)lw, 8 * w, cs, lane);
            wo_stage(p, (unsigned)(size_t)lw + 16384, 8 * w + 2, cs, lane);
        }
#pragma unroll
        for (int rd = 0; rd < 4; ++rd) {
            if (rd < 3) wait_vm<16>(); else wait_vm<0>();
#pragma unroll
            for (int i = 0; i < 2; ++i)
#pragma unroll
                for (int sel = 0; sel < 2; ++sel)
#pragma unroll
                    for (int nt = 0; nt < 2; ++nt) {
                        LAS uchar* a = lw + (rd & 1) * 16384 + i * 8192 + sel * 1024 + nt * 32;
                        const u32x2 t0 = trd(a + ra[0]), t1 = trd(a + ra[1]), t2 = trd(a + ra[2]), t3 = trd(a + ra[3]);
                        wf[2 * rd + i][sel][nt] = (u32x8){t0.x, t0.y, t1.x, t1.y, t2.x, t2.y, t3.x, t3.y};
                    }
            SB();
#pragma unroll
            for (int i = 0; i < 8; ++i) { if (rd < 3) asm volatile("" : "+a"(wf[2 * rd + (i >> 2)][(i >> 1) & 1][i & 1])); else asm volatile("" : "+v"(wf[2 * rd + (i >> 2)][(i >> 1) & 1][i & 1])); }
            asm volatile("s_waitcnt lgkmcnt(0)" ::: "memory");
            SB();
            if (rd < 2) wo_stage(p, (unsigned)(size_t)lw + (rd & 1) * 16384, 8 * w + 2 * rd + 4, cs, lane);
        }
    }
    bar();
    if (tid < 2) *(LAS unsigned*)(lds + 4096 + tid * 4) = 0;
    bar();
    const int idx = (c & 4) ? 0xEEEE : 0x4444;
    const int dl = 32 * cs + 2 * c;
    const unsigned so = mf_off(dl >> 6) + (((dl >> 4) & 3) * 16 + qh * 8 + ((dl >> 3) & 1) * 4 + g) * 16 + (dl & 7) * 2;
    unsigned rwo = (w * 128 + (2 * (g & 1)) * 32 + 16 * (g >> 1) + c) * 4, rro = (g * 32 + 2 * c) * 4, fla = 4096;
    asm volatile("" : "+v"(rwo), "+v"(rro), "+v"(fla));
    const unsigned lo16 = lane * 16, sto = lane == 0 ? mst_off(mi) : OOB;
    unsigned long long alive = ~0ull;
    unsigned budget = SPIN_CAP;
    const u32x2 bv = vote_words(rw);
    wait_boot(rw, tag, boot_words(rw));
    if (fresh_tid() == 0) add_ctr(p, C_ACK, 1u);
    const unsigned exo = ex_off(p), pol = read_pol(rw, tag, bv);
    wait_ctr(p, rw, C_PZ, 16);
    const unsigned obo = uni(w * 32768 + qh * 1024);
    auto loop = [&](auto lead) {
        constexpr bool LEAD = lead.value;
        Clock ck;
        if (LEAD) ck.start();
        unsigned tdone = LEAD ? ck.now() : 0u;
        Sched sc = {0u, tdone - V_M, DUR_A0, DUR_M0, 0u, 0u};
        for (int s = 0; s < NSEG - 1; ++s) {
            const unsigned eb = ex_step(exo, pol, s);
            unsigned obs[8], mb = ex_step(exo, pol, s + 1);
#pragma unroll
            for (int i = 0; i < 8; ++i) { obs[i] = eb + obo + (i >> 2) * 16384 + (i & 3) * 2048; asm volatile("" : "+s"(obs[i])); }
            f32x4 acc[2][2];
#pragma unroll
            for (int i = 0; i < 4; ++i) { acc[i >> 1][i & 1] = (f32x4){0.f, 0.f, 0.f, 0.f}; asm volatile("" : "+v"(acc[i >> 1][i & 1])); }
            if (LEAD) {
                if (s == 3) wait_ctr(p, rw, C_PZ + 32, 56);
                if (s > 0) {
                    sc.fetch_a(rw, exo, pol, s, lane);
                    ck.sync();
                    sc.take_a();
                    SB();
                    waitmt(ck.cycles(tdone + D_MST));
                    sc.fetch_m(rw, exo, pol, s, lane);
                    sc.next(p, rw, exo, pol, s, lane, alive);
                }
                unsigned tw = ck.cycles(sc.tau + V_M) - 12;
                asm volatile("" : "+s"(mb), "+s"(tw));
                wait_exact(tw);
            }
            SB();
            bar();
            FT(0);
            u32x4 of[8];
#pragma unroll
            for (int i = 0; i < 8; ++i) { of[i] = ld16(rw, lo16, obs[i]); SB(); if (i < 7) pace(); SB(); }
            auto mix = [&]() {
#pragma unroll
                for (int i = 0; i < 8; ++i) {
                    smf_x(acc[0][1], of[i], wf[i][0][1], idx, i < 6);
                    smf_x(acc[1][0], of[i], wf[i][1][0], idx, i < 6);
                    smf_x(acc[0][0], of[i], wf[i][0][0], idx, i < 6);
                    smf_x(acc[1][1], of[i], wf[i][1][1], idx, i < 6);
                    if (i == 0) FT(1);
                    if (i == 7) FT(2);
                    SB();
                }
                asm volatile("s_nop 2" : "+v"(acc[0][1]), "+v"(acc[1][0]));
                f32x4 u, v;
#pragma unroll
                for (int r = 0; r < 4; ++r) {
                    const float fa = acc[1][0][r], fb = acc[0][1][r];
                    auto q = __builtin_amdgcn_permlane32_swap(__builtin_bit_cast(unsigned, fa), __builtin_bit_cast(unsigned, fb), false, false);
                    u[r] = __builtin_bit_cast(float, (unsigned)q[0]); v[r] = __builtin_bit_cast(float, (unsigned)q[1]);
                }
                SB();
                asm volatile("" : "+v"(acc[0][0]), "+v"(acc[1][1]));
                f32x4 lo = acc[0][0] + v, hi = acc[1][1] + u;
                asm volatile("" : "+v"(lo), "+v"(hi));
                float y[4];
#pragma unroll
                for (int r = 0; r < 4; ++r) y[r] = g < 2 ? lo[r] : hi[r];
                SB();
                const float z0 = swap16_add(y[0], y[2]), z1 = swap16_add(y[1], y[3]);
                *(LAS float*)(lds + rwo) = z0;
                *(LAS float*)(lds + rwo + 128) = z1;
                SB();
                return __builtin_amdgcn_ballot_w64(__builtin_isunordered(z0, z1));
            };
            if (__builtin_expect(mix() != 0, 0)) {
                if (refetch<8>(of, rw, lo16, obs, budget)) {
                    TRY();
                    *(LAS unsigned*)(lds + 4096) = s + 1;
#pragma unroll
                    for (int i = 0; i < 4; ++i) { acc[i >> 1][i & 1] = (f32x4){0.f, 0.f, 0.f, 0.f}; asm volatile("" : "+v"(acc[i >> 1][i & 1])); }
                    asm volatile("s_nop 1" ::: "memory");
                    mix();
                }
            }
            FT(3);
            bar();
            FT(4);
            if (LEAD) {
                LAS f32x2* r2 = (LAS f32x2*)(lds + rro);
                const f32x2 a0 = r2[0], a1 = r2[64], a2 = r2[128], a3 = r2[192];
                const unsigned fl = *(LAS unsigned*)(lds + fla);
                SB();
                unsigned long long ca, cb;
                Clock::sample(ca, cb);
                SB();
                const f32x2 v = (a0 + a1) + (a2 + a3);
                st4(nosent(pk2(v[0], v[1])), rw, so, mb);
                SB();
                tdone = ck.value(ca, cb) + M_TAIL;
                st4(nosent((tdone & ~1u) | (fl == (unsigned)(s + 1))), rw, sto, eb + EX_MST);
            }
            if (budget == 0) set_tmo(p);
            FT(5); FDUMP(32 + mi, s, w);
            STAT(32 + mi, s, w, sc.tau, tb, tdone, tries);
        }
        __builtin_amdgcn_endpgm();
    };
    if (w == 0) loop(IC<1>{}); else loop(IC<0>{});
}

constexpr int RING = 4;
constexpr int a_slot(int ks, int tile) { return ks * 16384 + tile * 2048; }
constexpr int a_xblk(int i) { return a_slot(i & 1 ? 9 : 4, i & 2 ? 5 : 2); }
constexpr int A_P = a_slot(4, 7), A_P2 = A_P + 1024, A_CNT = A_P2 + 512, A_BAD = A_CNT + 8, A_V2 = a_slot(9, 7);
template <int W> struct AP {
    static constexpr int ROT = 3 * W, NT = W == 0 ? 0 : (W == 3 ? 2 : 3), T0 = W == 0 ? 0 : 3 * (W - 1);
    static constexpr int NL = W == 0 ? 0 : (W == 3 ? 9 : 14);
    static constexpr int QA = W == 0 || W == 3 ? 20 : 15;
    static constexpr bool k_lds(int t, int m) { return W == 0 ? false : (W == 3 ? (t == 1 && m >= 1) : (t == 1 || (m > 0 && (m & 1) == 0))); }
    static constexpr bool k_acc(int t, int m) { return W == 0 && m < 5; }
    static constexpr bool v_lds(int t, int ks) { return W == 3 ? ks < 4 : (ks < 4 || (ks == 4 && t == 0)); }
    static constexpr int k_idx(int t, int m) { int n = 0; for (int i = 0; i < 2 * m + 1 - t; ++i) n += k_lds(1 - (i & 1), i >> 1) ? 1 : 0; return n; }
    static constexpr int k_slot(int i) { return a_slot(5 + i / (NT ? NT : 1), T0 + i % (NT ? NT : 1)); }
    static constexpr int v_slot(int i) { return a_slot(i / (NT ? NT : 1), T0 + i % (NT ? NT : 1)); }
    static constexpr int v_ord(int i) { return NT < 3 ? i : (i < 4 ? 3 * i + 2 : 3 * ((i - 4) / 2) + (i - 4) % 2); }
    static constexpr int stream(int i) { return i < NL ? k_slot(i) : v_slot(v_ord(i - NL)); }
    static constexpr int m_of(int ks) { return (ks + 10 - ROT % 10) % 10; }
};
DI void a_dma(const uchar* wsrc, LAS uchar* lds, int h, int w, int ks0, int ks1) {
    const int lane = fresh_lane();
    const rsrc_t r = mk_rsrc(wsrc, 640 * 8192);
    const unsigned v = (lane >> 4) * 8192 + (((lane & 15) ^ (2 * (((lane >> 4) & 1) | (w << 1)))) << 4);
    const unsigned lb = uni((unsigned)(size_t)lds + w * 1024), so = uni(4 * w * 8192 + h * 256);
#pragma unroll
    for (int i = 4 * ks0; i < 4 * ks1; ++i) dma16(r, v, so + 16 * i * 8192, lb + i * 4096);
}
struct ATake {
    unsigned row[4], f[4];
    DI ATake() {
        const int lane = fresh_lane(), c = lane & 15, g = lane >> 4, q = c >> 2, pp = c & 3;
#pragma unroll
        for (int m = 0; m < 4; ++m) {
            const int k0 = m < 2 ? 8 * g + 4 * m : 32 + 8 * g + 4 * (m - 2), ga = k0 >> 4, qq = (k0 >> 2) & 3;
            const int r = 16 * ga + 2 * qq + (q & 1) + 8 * (q >> 1);
            row[m] = r * 256 + pp * 8; f[m] = 32 * ((r & 1) | ((r >> 1) & 6));
        }
    }
    DI u32x8 get(LAS uchar* lds, int ks, int tile) const {
        LAS uchar* a = lds + ks * 16384;
        const u32x2 t0 = trd(a + row[0] + (f[0] ^ (32 * tile))), t1 = trd(a + row[1] + (f[1] ^ (32 * tile))), t2 = trd(a + row[2] + (f[2] ^ (32 * tile))), t3 = trd(a + row[3] + (f[3] ^ (32 * tile)));
        return (u32x8){t0.x, t0.y, t1.x, t1.y, t2.x, t2.y, t3.x, t3.y};
    }
};
DI u32x8 ldsB(LAS uchar* a) { const u32x4 lo = *(LAS u32x4*)a, hi = *(LAS u32x4*)(a + 1024); return (u32x8){lo.x, lo.y, lo.z, lo.w, hi.x, hi.y, hi.z, hi.w}; }
DI void ldsB_put(LAS uchar* a, u32x8 v) { *(LAS u32x4*)a = (u32x4){v[0], v[1], v[2], v[3]}; *(LAS u32x4*)(a + 1024) = (u32x4){v[4], v[5], v[6], v[7]}; }
DI void pin(u32x8& v, bool acc) { if (acc) asm volatile("" : "+a"(v)); else asm volatile("" : "+v"(v)); }

#define KM_OUT() do { unsigned n_ = 0; if (lane == 0) n_ = __hip_atomic_fetch_add((LAS unsigned*)(lds + A_CNT), 1u, __ATOMIC_RELAXED, __HIP_MEMORY_SCOPE_WORKGROUP); \
    if ((__builtin_amdgcn_readfirstlane(n_) & 3) == 3) st4(s + 1, rw, lane == 0 ? (C_KM + h * 32 + 1) * 4 : OOB, 0); } while (0)
template <int W>
DI void a_loop(const Params& p, LAS uchar* lds, int h, u32x2 tag, unsigned pol, u32x2 early, const u32x8 (&wq)[2][10], const u32x8 (&wk)[2][10], const u32x8 (&wv)[3][10]) {
    typedef AP<W> P;
    const int lane = fresh_lane(), c = lane & 15, g = lane >> 4;
    const rsrc_t rw = mk_rsrc(p.ws, WS_USED);
    constexpr int ROT = P::ROT, NT = P::NT, T0 = P::T0, NL = P::NL, E0 = W == 0 ? 2 : T0, E1 = W == 0 ? 5 : T0 + 1;
    constexpr bool TK = W == 3;
    const int idx = (c & 8) ? 0xEEEE : 0x4444;
    const unsigned lo16 = lane * 16, mo16 = (g * 16 + ((c >> 2) & 1) * 8 + (c >> 3) * 4 + (c & 3)) * 16;
    const unsigned kaddr = c >= 8 ? (g * 8 + (c & 7)) * 16 : OOB;
    const unsigned oaddr = c < 8 ? (c >> 2) * 1024 + (g & 1) * (E1 - E0) * 128 + (g >> 1) * 64 + (c & 3) * 16 : OOB;
    const unsigned xw = a_xblk(W) + c * 40 + g * 8;
    const unsigned asto = lane == 0 ? h * 4 : OOB;
    const unsigned sca = (lane & 31) * 4, scm = mst_off(min(lane, 39));
    u32x4 kf[8], vf[2];
    auto next_kv = [&](int sn) {
        if (W == 0) {
#pragma unroll
            for (int i = 0; i < 8; ++i) kf[i] = ld16(rw, lo16, OFF_KF + (sn * 32 + h) * 8192 + i * 1024);
        }
        vf[0] = ld16(rw, lo16, OFF_VF + (sn * 32 + h) * 8192 + E0 * 1024);
        vf[1] = ld16(rw, lo16, OFF_VF + (sn * 32 + h) * 8192 + E1 * 1024);
    };
    unsigned long long alive = ~0ull;
    unsigned budget = SPIN_CAP;
    const unsigned exo = ex_off(p);
    const unsigned obo = uni((h >> 2) * 16384 + (h & 3) * 2048 + E0 * 128), h16 = uni(h * 16);
    Clock ck;
    if (TK) ck.start();
    if (uni(early.x) < 16u) wait_ctr(p, rw, C_PZ, 16);
    if (uni(early.y) < 4u) wait_ctr(p, rw, C_KVH + h * 16, 4);
    next_kv(0);
    Sched sc = {TK ? ck.now() : 0u, 0u, DUR_A0, DUR_M0, 0u, 0u};
    const float ninf = g < 2 ? -__builtin_inff() : 0.f;
    unsigned xra[4], xqa[4];
    if (W == 0) {
#pragma unroll
        for (int ks = 0; ks < 4; ++ks) {
            xra[ks] = a_xblk(ks) + (4 * g + (c >> 2)) * 40 + (c & 3) * 8; xqa[ks] = a_xblk(ks) + (4 * g + (c >> 2)) * 40 + (c & 1) * 8;
            asm volatile("" : "+v"(xra[ks]), "+v"(xqa[ks]));
        }
    }
    u32x4 one4 = {0x3F803F80u, 0x3F803F80u, 0x3F803F80u, 0x3F803F80u};
    u32x8 pw = {0u, 0u, 0u, 0u, 0u, 0u, 0u, 0u};
    unsigned pa[3] = {A_P + lo16, A_P2 + lane * 8u, W == 0 ? A_V2 + lane * 8u : A_P2 + (lane ^ 32) * 8u};
    asm volatile("" : "+a"(one4), "+v"(pw), "+v"(pa[0]), "+v"(pa[1]), "+v"(pa[2]));
    unsigned lb[3] = {lo16, lo16 + 65536, lo16 + 131072};
    asm volatile("" : "+v"(lb[0]), "+v"(lb[1]), "+v"(lb[2]));
    auto sget = [&](int i) -> u32x8 { const int a = P::stream(i); return ldsB(lds + lb[a >> 16] + (a & 65535)); };
    for (int s = 0;; ++s) {
        const unsigned eb = ex_step(exo, pol, s);
        unsigned mb[10], ob = eb + obo;
#pragma unroll
        for (int m = 0; m < 10; ++m) { mb[m] = eb + mf_off((m + ROT) % 10); asm volatile("" : "+s"(mb[m])); }
        f32x4 aq[2], ak[2], va[3];
        u32x8 ring[RING];
#pragma unroll
        for (int t = 0; t < 2; ++t) { aq[t] = (f32x4){0.f, 0.f, 0.f, 0.f}; ak[t] = aq[t]; asm volatile("" : "+v"(aq[t]), "+v"(ak[t])); }
#pragma unroll
        for (int t = 0; t < 3; ++t) {
            const float z = W == 0 && t == 2 ? ninf : 0.f;
            va[t] = (f32x4){z, z, z, z}; asm volatile("" : "+v"(va[t]));
        }
        if (W) {
#pragma unroll
            for (int i = 0; i < RING; ++i) ring[i] = sget(i);
        }
        asm volatile("" : "+s"(ob));
        const int kvs = min(s + 2, NSEG - 1);
        const unsigned kvw = (kvs < 16 && (kvs & 4) == 0 ? C_KVH : C_KV) + (kvs >> 3) * 512 + h16;
        unsigned oa = ex_step(exo, pol, max(s - 2, 0)) + EX_AST, om = ex_step(exo, pol, max(s - 1, 0)) + EX_MST, kvo = kvw * 4, kvn = 0;
        if (TK) { oa = uni(oa); om = uni(om); kvo = uni(kvo); }
        const unsigned tgo = sc.tgo;
        bool late = false;
        if (TK) {
            unsigned tw = ck.cycles(tgo) - 12;
            asm volatile("" : "+s"(tw));
            SB();
            late = (int)(wait_exact(tw) - tw) > 0;
        }
        SB();
        bar();
        FT(0);
        u32x4 mf[10];
#pragma unroll
        for (int m = 0; m < 10; ++m) {
            mf[m] = ld16(rw, mo16, mb[m]);
            SB();
            if (m < 9) pace();
            SB();
        }
        auto project = [&]() {
            static_for<10>([&](auto mc) {
                constexpr int m = mc.value;
                auto qprod = [&](auto tc) { constexpr int t = tc.value; smf_x(aq[t], mf[m], wq[t][m], idx, 2 * m + t < P::QA - (W == 0 ? 10 : 0)); };
                auto kprod = [&](auto tc) {
                    constexpr int t = tc.value;
                    if constexpr (P::k_lds(t, m)) {
                        constexpr int i = P::k_idx(t, m);
                        smf_v(ak[t], mf[m], ring[i % RING], idx);
                        ring[i % RING] = sget(i + RING);
                    } else smf_x(ak[t], mf[m], wk[t][m], idx, W == 0);
                };
                kprod(IC<1>{});
                qprod(IC<m == 9 ? 1 : 0>{});
                if constexpr (m == 9) qprod(IC<0>{});
                kprod(IC<0>{});
                if constexpr (m < 9) qprod(IC<1>{});
                if (m == 0) FT(1);
                if (m == 9) FT(2);
                SB();
            });
            float x[4], y[4];
            asm volatile("s_nop 2" : "+v"(aq[1]), "+v"(ak[1]));
#pragma unroll
            for (int r = 0; r < 4; ++r) y[r] = swap32_add(aq[1][r], ak[1][r]);
            *(LAS u32x2*)(lds + xw + 640) = (u32x2){pk2(y[0], y[1]), pk2(y[2], y[3])};
            SB();
            asm volatile("" : "+v"(aq[0]), "+v"(ak[0]));
#pragma unroll
            for (int r = 0; r < 4; ++r) x[r] = swap32_add(aq[0][r], ak[0][r]);
            *(LAS u32x2*)(lds + xw) = (u32x2){pk2(x[0], x[1]), pk2(x[2], x[3])};
            SB();
            return nan_lanes((f32x4){x[0], x[1], x[2], x[3]});
        };
        if (__builtin_expect(project() != 0, 0)) {
            if (refetch<10>(mf, rw, mo16, mb, budget)) {
                *(LAS unsigned*)(lds + A_BAD) = s + 1; TRY();
#pragma unroll
                for (int t = 0; t < 2; ++t) { aq[t] = (f32x4){0.f, 0.f, 0.f, 0.f}; ak[t] = aq[t]; asm volatile("" : "+v"(aq[t]), "+v"(ak[t])); }
                if (W) {
#pragma unroll
                    for (int i = 0; i < RING; ++i) ring[i] = sget(i);
                }
                asm volatile("s_nop 1" ::: "memory");
                project();
            }
        }
        SB();
        if (W) { smf_a(va[NT - 1], mf[P::m_of(9)], wv[NT - 1][9], idx); smf_a(va[NT - 1], mf[P::m_of(8)], wv[NT - 1][8], idx); }
        bar();
        SB();
        FT(3);
        const unsigned kb = OFF_KM + (s * 32 + h) * 4096;
        u32x4 xf[4], vv;
        u32x2 v2 = {0u, 0u};
        if (W == 0) {
            u32x4 xd[4];
#pragma unroll
            for (int ks = 0; ks < 4; ++ks) {
                const u32x2 a = trd(lds + xqa[ks]), b = trd(lds + xqa[ks] + 640), lo = trd(lds + xra[ks]), hi = trd(lds + xra[ks] + 640);
                xd[ks] = (u32x4){a.x, a.y, b.x, b.y}; xf[ks] = (u32x4){lo.x, lo.y, hi.x, hi.y};
            }
            SB();
            if (__builtin_expect(s == NSEG - 1, 0)) {
#pragma unroll
                for (int ks = 0; ks < 4; ++ks) st16((u32x4){nosent(xf[ks].x), nosent(xf[ks].y), nosent(xf[ks].z), nosent(xf[ks].w)}, rw, kaddr, kb + ks * 512);
                KM_OUT();
                break;
            }
#pragma unroll
            for (int ks = 0; ks < 4; ++ks) { mfma_v(va[0], kf[ks], xd[ks]); mfma_v(va[1], kf[4 + ks], xd[ks]); mfma_v(va[2], xf[ks], xd[ks]); }
            FT(4);
            float a0, a1, a2, a3, a4, a5, tm, tn;
            unsigned e0, e1, e2;
            asm volatile("s_nop 3\n\t"
                         "v_cndmask_b32_e64 %0, %15, %11, %23\n\tv_cndmask_b32_e64 %1, %16, %12, %23\n\tv_cndmask_b32_e64 %2, %17, %13, %23\n\t"
                         "v_cndmask_b32_e64 %3, %18, %14, %23\n\tv_cndmask_b32_e64 %4, %21, %19, %23\n\tv_cndmask_b32_e64 %5, %22, %20, %23\n\t"
                         "v_max3_f32 %6, %0, %1, %2\n\tv_max3_f32 %7, %3, %4, %5\n\tv_max_f32 %6, %6, %7\n\ts_nop 1\n\t"
                         "v_max_f32_dpp %6, %6, %6 row_ror:8 row_mask:0xf bank_mask:0xf\n\t"
                         "v_mov_b32 %7, %6\n\ts_nop 1\n\tv_permlane32_swap_b32 %6, %7\n\tv_max_f32 %6, %6, %7\n\t"
                         "v_mov_b32 %7, %6\n\ts_nop 1\n\tv_permlane16_swap_b32 %6, %7\n\tv_max_f32 %6, %6, %7\n\t"
                         "v_mul_f32 %6, %24, %6\n\t"
                         "v_fma_f32 %0, %0, %24, -%6\n\tv_fma_f32 %1, %1, %24, -%6\n\tv_fma_f32 %2, %2, %24, -%6\n\t"
                         "v_fma_f32 %3, %3, %24, -%6\n\tv_fma_f32 %4, %4, %24, -%6\n\tv_fma_f32 %5, %5, %24, -%6\n\t"
                         "v_exp_f32 %0, %0\n\tv_exp_f32 %1, %1\n\tv_exp_f32 %2, %2\n\tv_exp_f32 %3, %3\n\tv_exp_f32 %4, %4\n\tv_exp_f32 %5, %5\n\t"
                         "v_cvt_pk_bf16_f32 %8, %0, %1\n\tv_cvt_pk_bf16_f32 %9, %2, %3\n\tv_cvt_pk_bf16_f32 %10, %4, %5"
                         : "=&v"(a0), "=&v"(a1), "=&v"(a2), "=&v"(a3), "=&v"(a4), "=&v"(a5), "=&v"(tm), "=&v"(tn), "=&v"(e0), "=&v"(e1), "=&v"(e2)
                         : "v"(va[0][0]), "v"(va[0][1]), "v"(va[0][2]), "v"(va[0][3]), "v"(va[1][0]), "v"(va[1][1]), "v"(va[1][2]), "v"(va[1][3]),
                           "v"(va[2][0]), "v"(va[2][1]), "v"(va[2][2]), "v"(va[2][3]), "s"(0x00FF00FF00FF00FFull), "s"(SCL));
            *(LAS u32x2*)(lds + A_P + (g * 16 + (c & 7)) * 16 + (c >> 3) * 8) = (u32x2){e0, e1};
            *(LAS unsigned*)(lds + A_P2 + (g * 16 + (c & 7)) * 8 + (c >> 3) * 4) = e2;
        } else {
            if (W == 3) st4(s, rw, asto, C_KM * 4 + h * 124);
            auto from_lds = [&](int i) {
                const int o = P::v_ord(i);
                smf_v(va[o % NT], mf[P::m_of(o / NT)], ring[(NL + i) % RING], idx);
                if (NL + i + RING < 2 * NL) ring[(NL + i) % RING] = sget(NL + i + RING);
            };
            if (NT == 3) {
#pragma unroll
                for (int i = 0; i < 4; ++i) { from_lds(i); smf_a(va[2], mf[P::m_of(4 + i)], wv[2][4 + i], idx); SB(); }
#pragma unroll
                for (int i = 0; i < 10; ++i) {
                    from_lds(4 + i);
                    smf_a(va[i & 1], mf[P::m_of(5 + i / 2)], wv[i & 1][5 + i / 2], idx);
                    if (i == 1) {
                        asm volatile("" : "+v"(va[2]));
                        float sb[4];
#pragma unroll
                        for (int r = 0; r < 4; ++r) sb[r] = swap32_add(va[2][r], va[2][r]);
                        v2 = (u32x2){pk2(sb[0], sb[1]), pk2(sb[2], sb[3])};
                        *(LAS u32x2*)(lds + A_V2 + (W - 1) * 512 + lane * 8) = v2;
                    }
                    SB();
                }
            } else {
#pragma unroll
                for (int i = 0; i < 9; ++i) {
                    from_lds(i);
                    const int r = i < 7 ? 9 + i : 2 * i + 2;
                    smf_a(va[r & 1], mf[P::m_of(r >> 1)], wv[r & 1][r >> 1], idx);
                    SB();
                }
            }
            asm volatile("s_nop 7" : "+v"(va[0]), "+v"(va[1]));
            float sa[4];
#pragma unroll
            for (int r = 0; r < 4; ++r) sa[r] = swap32_add(va[0][r], va[1][r]);
            vv = (u32x4){pk2(sa[0], sa[1]), pk2(sa[2], sa[3]), 0u, 0u};
            asm volatile("" : "+v"(vv));
            if (__builtin_expect(s == NSEG - 1, 0)) {
                st8((u32x2){nosent(vv.x), nosent(vv.y)}, rw, lane * 8, kb + 2048 + T0 * 256);
                if (NT == 3) st8((u32x2){nosent(v2.x), nosent(v2.y)}, rw, g < 2 ? lane * 8 : OOB, kb + 2048 + T0 * 256 + 512);
                KM_OUT();
                break;
            }
        }
        SB();
        FT(5);
        bar();
        FT(6);
        SB();
        {
            const u32x4 p01 = *(LAS u32x4*)(lds + pa[0]);
            const u32x2 q2 = *(LAS u32x2*)(lds + pa[1]), q3 = *(LAS u32x2*)(lds + pa[2]);
            pw[4] = q2.x; pw[5] = q2.y;
            if (W == 0) { const u32x2 q4 = *(LAS u32x2*)(lds + pa[2] + 512); vv = (u32x4){q3.x, q3.y, q4.x, q4.y}; }
            else { pw[0] = q3.x; pw[1] = q3.y; }
            const u32x4 p2s = __builtin_shufflevector(pw, pw, 0, 1, 2, 3), p2h = __builtin_shufflevector(pw, pw, 2, 3, 4, 5), p2 = __builtin_shufflevector(pw, pw, 4, 5, 6, 7);
            SB();
            if (TK) { sc.a = ld4(rw, sca, oa); sc.v = ld4(rw, scm, om); kvn = ld4(rw, 0, kvo); }
            SB();
            f32x4 l, o[2];
            asm volatile("v_mfma_f32_16x16x32_bf16 %0, %1, %2, 0" : "=&v"(l) : "a"(one4), "v"(p01));
            mfma_0(o[0], vf[0], p01);
            mfma_av(l, one4, p2);
            mfma_0(o[1], vf[1], p01);
            mfma_v(o[0], vv, W == 0 ? p2 : p2s);
            mfma_v(o[1], vv, W == 0 ? p2h : p2);
            float inv;
            asm volatile("s_nop 1\n\tv_rcp_f32 %0, %3\n\ts_nop 4" : "=&v"(inv), "+v"(o[0]), "+v"(o[1]) : "v"(l[0]));
            const f32x4 n0 = o[0] * inv, n1 = o[1] * inv;
            SB();
            const unsigned a0 = pk2(n0[0], n0[1]), b0 = pk2(n1[0], n1[1]), a1 = pk2(n0[2], n0[3]), b1 = pk2(n1[2], n1[3]);
            SB();
            auto r0 = __builtin_amdgcn_permlane16_swap(a0, b0, false, false);
            auto r1 = __builtin_amdgcn_permlane16_swap(a1, b1, false, false);
            st16((u32x4){nosent(r0[0]), nosent(r1[0]), nosent(r0[1]), nosent(r1[1])}, rw, oaddr, ob);
        }
        SB();
        unsigned tpub = 0;
        if (TK) tpub = ck.now();
        next_kv(s + 1);
        if (W == 0) {
#pragma unroll
            for (int ks = 0; ks < 4; ++ks) st16(xf[ks], rw, kaddr, kb + ks * 512);
        } else {
            st8((u32x2){vv.x, vv.y}, rw, lane * 8, kb + 2048 + T0 * 256);
            if (NT == 3) st8(v2, rw, g < 2 ? lane * 8 : OOB, kb + 2048 + T0 * 256 + 512);
        }
        if (TK) {
            st4(late || *(LAS unsigned*)(lds + A_BAD) == (unsigned)(s + 1) ? 0u : tpub - tgo, rw, asto, eb + EX_AST);
            ck.sync();
            if (s >= 2) sc.take_a();
            if (s > 0) sc.next(p, rw, exo, pol, s, lane, alive);
            if (uni(kvn) < 4u) wait_ctr(p, rw, kvw, 4);
            if (s == 2) wait_ctr(p, rw, C_PZ + 32, 56);
        }
        if (budget == 0) set_tmo(p);
        FT(7); FDUMP(h, s, W);
        STAT(h, s, W, tgo + (late ? 80 : 0), tpub, tgo, tries);
    }
    XST(W == 0, h);
    if (W == 0 && h == 0) {
        wait_ctr(p, rw, C_ACK, 256);
        st8(tag, rw, lane == 0 ? 0 : OOB, B_EPOCH * 4);
    }
    __builtin_amdgcn_endpgm();
}

template <int W>
DI void a_wave(const Params& p, LAS uchar* lds, int h, bool started, u32x2 tag) {
    typedef AP<W> P;
    u32x8 wq[2][10], wk[2][10], wv[3][10];
    if (W == 0 || !started) a_dma(p.wq, lds, h, W, 0, 10);
    const rsrc_t rw = mk_rsrc(p.ws, WS_USED);
    u32x2 bi, bv;
    const ATake tk;
    static_for<15>([&](auto sc) {
        constexpr int mat = sc.value / 5, k0 = 2 * (sc.value % 5), NTL = mat < 2 ? 2 : P::NT, NTD = P::NT ? P::NT : 1;
        wait_vm<sc.value < 10 ? 32 : 8 * (14 - sc.value)>();
        bar();
        if (k0 == 0 || k0 == 4) AST_(mat * 2 + k0 / 4);
#pragma unroll
        for (int i = 0; i < 2 * NTL; ++i) {
            const int ks = k0 + i / NTL, t = i % NTL, m = P::m_of(ks);
            if (mat == 0) wq[t][m] = tk.get(lds, ks, 2 * W + t);
            if (mat == 1) wk[t][m] = tk.get(lds, ks, 2 * W + t);
            if (mat == 2) wv[t][ks] = tk.get(lds, ks, P::T0 + t);
        }
        SB();
#pragma unroll
        for (int i = 0; i < 2 * NTL; ++i) {
            const int ks = k0 + i / NTL, t = i % NTL, m = P::m_of(ks);
            if (mat == 0) pin(wq[t][m], 2 * m + t < P::QA - (W == 0 ? 10 : 0));
            if (mat == 1) pin(wk[t][m], W == 0);
            if (mat == 2) pin(wv[t][ks], true);
        }
        bar(); SB();
        if (sc.value == 9) { bi = boot_words(rw); bv = vote_words(rw); SB(); }
        if (mat < 2) a_dma(mat == 0 ? p.wk : p.wv, lds, h, W, k0, k0 + 2);
        else {
            const int lane = fresh_lane();
#pragma unroll
            for (int i = 0; i < P::NL; ++i) if (i / NTD / 2 == k0 / 2) ldsB_put(lds + P::v_slot(i) + lane * 16, wv[i % NTD][i / NTD]);
#pragma unroll
            for (int i = 0; i < 20; ++i)
                if (P::k_lds(i & 1, i >> 1) && (5 + P::k_idx(i & 1, i >> 1) / NTD) / 2 == k0 / 2) ldsB_put(lds + P::k_slot(P::k_idx(i & 1, i >> 1)) + lane * 16, wk[i & 1][i >> 1]);
        }
        if (sc.value == 12) {
            wait_boot(rw, tag, bi);
            if (W == 0 && fresh_tid() == 0) add_ctr(p, C_ACK, 1u);
            bi = (u32x2){ld4(rw, 0, C_PZ * 4), ld4(rw, 0, (C_KVH + h * 16) * 4)};
        }
        SB();
    });
    const unsigned pol = read_pol(rw, tag, bv);
    if (W == 0) *(LAS u32x4*)(lds + A_CNT) = (u32x4){0u, 0u, 0u, 0u};
    bar();
    SB();
    a_loop<W>(p, lds, h, tag, pol, bi, wq, wk, wv);
}
DI void role_A(const Params& p, LAS uchar* lds, int h, bool started, u32x2 tag) {
    const int w = __builtin_amdgcn_readfirstlane(threadIdx.x >> 6);
    if (w == 0) a_wave<0>(p, lds, h, started, tag);
    else if (w == 1) a_wave<1>(p, lds, h, started, tag);
    else if (w == 2) a_wave<2>(p, lds, h, started, tag);
    else a_wave<3>(p, lds, h, started, tag);
}
DI int take_ticket(const Params& p, LAS uchar* lds, int word) {
    if (fresh_tid() == 0) *(LAS unsigned*)(lds + LDS_BYTES - 16) = __hip_atomic_fetch_add((unsigned*)p.ws + word, 1u, __ATOMIC_RELAXED, __HIP_MEMORY_SCOPE_AGENT);
    bar();
    const int t = __builtin_amdgcn_readfirstlane(*(LAS unsigned*)(lds + LDS_BYTES - 16));
    bar();
    return t;
}
#define TSTAT(i, a, b)
#define SMALL_DELAY 300
DI void warm_l2(const Params& p, int x, int j) {
    const int lane = fresh_lane(), id = j * 4 + __builtin_amdgcn_readfirstlane(threadIdx.x >> 6);
    const rsrc_t rk = mk_rsrc(p.wk, 640 * 8192), rv = mk_rsrc(p.wv, 640 * 8192), ro = mk_rsrc(p.wo, 4096 * 1280);
    unsigned sink[24];
    const unsigned vw = (lane >> 4) * 8192 + (lane & 15) * 128, vo = (lane >> 2) * 1280 + min((lane & 3) * 128, 316);
#pragma unroll
    for (int i = 0; i < 4; ++i) asm volatile("buffer_load_dword %0, %1, %2, %3 offen" : "=v"(sink[i]) : "v"(vw), "s"(rk), "s"((id + 40 * i) * 4 * 8192 + x * 2048) : "memory");
#pragma unroll
    for (int i = 0; i < 7; ++i) asm volatile("buffer_load_dword %0, %1, %2, %3 offen" : "=v"(sink[4 + i]) : "v"(vo), "s"(ro), "s"(min(id + 40 * i, 255) * 16 * 1280 + x * 320) : "memory");
#pragma unroll
    for (int i = 0; i < 4; ++i) asm volatile("buffer_load_dword %0, %1, %2, %3 offen" : "=v"(sink[11 + i]) : "v"(vw), "s"(rv), "s"((id + 40 * i) * 4 * 8192 + x * 2048) : "memory");
    wait_vm<0>();
#pragma unroll
    for (int i = 0; i < 15; ++i) asm volatile("" :: "v"(sink[i]));
}
DI void background(const Params& p, LAS uchar* lds, int x, int j, u32x2 tag) {
    const bool big = x >= 4;
    if (!big) {
        const int id = j * 4 + x;
        const rsrc_t rw = mk_rsrc(p.ws, WS_USED);
        if (j < 4) ln_tile<true>(p, id, tag);
        else {
            warm_l2(p, x, j - 4);
            wait_boot(rw, tag, boot_words(rw));
        }
        if (fresh_tid() == 0) add_ctr(p, C_ACK, 1u);
        ln_tile<false>(p, id + 16, tag);
        const unsigned pol = read_pol(rw, tag, vote_words(rw));
        if (j < 4) poison(p, pol, id, 16, 0, 80, C_PZ);
        ln_tile<false>(p, id + 72, tag);
        poison(p, pol, id, 56, 80, 1376, C_PZ + 32);
    } else {
        if (j < 12) probe(p, lds, j * 4 + x - 4, tag);
        if (j & 2) kv0_task<true>(p, lds, 32 * (x - 4) + j, tag); else kv0_task<false>(p, lds, 32 * (x - 4) + j, tag);
        TSTAT((x - 4) * 128 + j, t0, 0);
    }
    if (!big) return;
    const int y = x - 4;
    for (;;) {
        int t = take_ticket(p, lds, C_Q1 + x * 32), kind = 0, chunk = 0, u0 = 0, nu = 8;
        const int wu = 192 + 8 * y, wn = y < 3 ? 4 : 2;
        if (t < 8) { chunk = t >> 1; u0 = wu + wn * (t & 1); nu = y < 3 ? 4 : 2 - (t & 1); }
        else if ((t -= 8) < 8) { chunk = 2; u0 = 32 * y + 4 * t; nu = 4; }
        else if ((t -= 8) < 8) { chunk = t >> 2; u0 = 128 + 16 * y + 4 * (t & 3); nu = 4; }
        else if ((t -= 8) < 8) { chunk = 3; u0 = 32 * y + 4 * t; nu = 4; }
        else if ((t -= 8) < 8) { chunk = 4 + (t >> 2); u0 = 32 * y + 8 * (t & 3); }
        else if ((t -= 8) < 5) { kind = 1; chunk = y; u0 = t; }
        else if ((t -= 5) < 4) { chunk = 4 + t; u0 = wu; nu = y < 3 ? 8 : 3; }
        else if ((t -= 4) < 6) { kind = 3; u0 = 6 * y + t; }
        else if ((t -= 6) < 4) { chunk = 2 + (t >> 1); u0 = 128 + 16 * y + 8 * (t & 1); }
        else if ((t -= 4) < 8) { chunk = 6 + (t >> 2); u0 = 32 * y + 8 * (t & 3); }
        else if ((t -= 8) < 8) { chunk = 4 + (t >> 1); u0 = 128 + 16 * y + 8 * (t & 1); }
        else if ((t -= 8) < 5) { kind = 1; chunk = 4 + y; u0 = t; }
        else if ((t -= 5) < 5) { kind = 2; chunk = y; u0 = t; }
        else if ((t -= 5) < 5 * N_WG) { kind = 4; u0 = t; }
        else return;
        if (kind == 0) p1_task(p, lds, chunk, u0, nu);
        else if (kind == 1) gemm_task<false>(p, lds, chunk, u0);
        else if (kind == 2) gemm_task<true>(p, lds, chunk, u0);
        else {
            wait_ctr(p, mk_rsrc(p.ws, WS_USED), C_PZ + 32, 56);
            if (kind == 3) {
                const int w = __builtin_amdgcn_readfirstlane(threadIdx.x >> 6);
                for (int s = u0 >> 3; s < NSEG; s += N_T / 8) attn_task(p, s, (u0 & 7) * 4 + w, (s & 7) < N_T / 8);
            } else { const int rank = (u0 * 52) >> 8; w_role(p, lds, 5 * y + u0 - 5 * rank, rank); }
            TSTAT(x * 128 + tk0, t0, kind + 2);
            return;
        }
        TSTAT(x * 128 + tk0, t0, kind == 0 ? (u0 < 128 ? 1 : (u0 < 192 ? 2 : 3)) : (kind == 1 ? 4 : 7));
    }
}
__global__ void __launch_bounds__(256, 1) k_main(Params p) {
    extern __shared__ __attribute__((aligned(16))) uchar smem[];
    LAS uchar* lds = (LAS uchar*)smem;
    const int w = uni(threadIdx.x >> 6), guess = uni((blockIdx.x & ~7) | xcc_id());
    if (w && (guess & 7) < 4) {
        const int x = guess & 7, t = guess >> 3;
        if (t < 8) a_dma(p.wq, lds, x * 8 + t, w, 0, 10);
        else if (t < 18) {
            const int lane = fresh_lane(), cs = (x * 10 + t - 8) >> 1;
            wo_stage(p, (unsigned)(size_t)lds + w * 32768, 8 * w, cs, lane);
            wo_stage(p, (unsigned)(size_t)lds + w * 32768 + 16384, 8 * w + 2, cs, lane);
        }
    }
    if (w == 0) {
        int slot = guess;
        const u32x2 tag = launch_tag(mk_rsrc(p.ws, WS_USED));
        const unsigned long long t64 = ((unsigned long long)tag.y << 32) | tag.x;
        unsigned long long* cl = (unsigned long long*)(p.ws + ex_off(p) + 12 * EX_STEP);
        for (int i = 0; i < 256; ++i) {
            unsigned long long old = 0;
            if (fresh_lane() == 0) old = __hip_atomic_exchange(cl + slot, t64, __ATOMIC_RELAXED, __HIP_MEMORY_SCOPE_AGENT);
            if (uni((unsigned)old) != tag.x || uni((unsigned)(old >> 32)) != tag.y) break;
            slot = slot < 248 ? slot + 8 : (slot + 1) & 7;
        }
        *(LAS u32x4*)lds = (u32x4){(unsigned)slot, tag.x, tag.y, 0u};
    }
    bar();
    const u32x4 got = *(LAS u32x4*)lds;
    const int slot = uni(got.x), x = slot & 7, t = slot >> 3;
    const u32x2 tag = {uni(got.y), uni(got.z)};
    if (slot != guess) wait_vm<0>();
    bar();
    if (x < 4 && t < 8) role_A(p, lds, x * 8 + t, slot == guess, tag);
    else if (x < 4 && t < 18) role_M(p, lds, x * 10 + t - 8, slot == guess, tag);
    else background(p, lds, x, x < 4 ? t - 18 : t, tag);
}

extern "C" void kernel_launch(void* const* d_in, const int* in_sizes, int n_in, void* d_out, int out_size, void* d_ws, size_t ws_size, hipStream_t stream) {
    Params p;
    p.x = (const uchar*)d_in[0]; p.mem0 = (const uchar*)d_in[1]; p.wq = (const uchar*)d_in[2]; p.wk = (const uchar*)d_in[3];
    p.wv = (const uchar*)d_in[4]; p.wo = (const uchar*)d_in[5]; p.w1 = (const uchar*)d_in[8]; p.b1 = (const uchar*)d_in[9];
    p.w2 = (const uchar*)d_in[10]; p.out = (uchar*)d_out; p.ws = (uchar*)d_ws;
    (void)hipFuncSetAttribute((const void*)k_main, hipFuncAttributeMaxDynamicSharedMemorySize, LDS_BYTES);
    k_main<<<256, 256, LDS_BYTES, stream>>>(p);
}
```
